```python
import torch

def _softmax(x, axis=-1):
    return torch.softmax(x, dim=axis)
def _gelu(x):
    return torch.nn.functional.gelu(x, approximate="tanh")
def _silu(x):
    return torch.nn.functional.silu(x)
def _sigmoid(x):
    return torch.sigmoid(x)
def _layernorm(x, axis=-1, eps=1e-5):
    m = x.mean(dim=axis, keepdim=True)
    v = x.var(dim=axis, keepdim=True, unbiased=False)
    return (x - m) / torch.sqrt(v + eps)
def _scatter_add(tgt, idx, src, axis=0):
    return tgt.clone().index_add_(axis, idx, src)
def _segment_sum(src, seg, n):
    out = torch.zeros((n,) + src.shape[1:], dtype=src.dtype)
    return out.index_add_(0, seg, src)
def _pad(x, pw):
    flat = tuple(v for pair in reversed(pw) for v in pair)
    return torch.nn.functional.pad(x, flat)

kernel_name = 'concat_heads__mha_block_cached__gqa_block_cached__near_ridge'

def setup_inputs():

    _w = (lambda a: (lambda t: t.to(torch.bfloat16) if t.is_floating_point() else t)(torch.as_tensor(a)))
    def _A_setup():
        B, Sq, D = 8, 176, 1024
        H, Sk, Dh = 32, 512, 128
        return {
            "x": _w((lambda *s: torch.randn(*s, dtype=torch.bfloat16) * 0.8)(B, Sq, D)),
            "k_cache": _w((lambda *s: torch.randn(*s, dtype=torch.bfloat16) * 0.8)(B, H, Sk, Dh)),
            "v_cache": _w((lambda *s: torch.randn(*s, dtype=torch.bfloat16) * 0.05)(B, H, Sk, Dh)),
            "wq": _w((lambda *s: torch.randn(*s, dtype=torch.bfloat16) * 0.8)(D, H * Dh) * (D ** -0.5)),
            "wo": _w((lambda *s: torch.randn(*s, dtype=torch.bfloat16) * 0.05)(H * Dh, D) * ((H * Dh) ** -0.5)),
        }
    _A_in = _A_setup()

    def _B_setup():
        B, Sq, D = 8, 176, 1024
        Hq, Hkv, Sk, Dh = 32, 32, 512, 128
        assert Hq % Hkv == 0, (Hq, Hkv)
        return {
            "x": _w((lambda *s: torch.randn(*s, dtype=torch.bfloat16) * 0.8)(B, Sq, D)),
            "k_cache": _w((lambda *s: torch.randn(*s, dtype=torch.bfloat16) * 0.8)(B, Hkv, Sk, Dh)),
            "v_cache": _w((lambda *s: torch.randn(*s, dtype=torch.bfloat16) * 0.05)(B, Hkv, Sk, Dh)),
            "wq": _w((lambda *s: torch.randn(*s, dtype=torch.bfloat16) * 0.8)(D, Hq * Dh) * (D ** -0.5)),
            "wo": _w((lambda *s: torch.randn(*s, dtype=torch.bfloat16) * 0.05)(Hq * Dh, D) * ((Hq * Dh) ** -0.5)),
        }
    _B_in = _B_setup()

    return {
        "A_x": _A_in["x"],
        "A_k_cache": _A_in["k_cache"],
        "A_v_cache": _A_in["v_cache"],
        "A_wq": _A_in["wq"],
        "A_wo": _A_in["wo"],
        "B_x": _B_in["x"],
        "B_k_cache": _B_in["k_cache"],
        "B_v_cache": _B_in["v_cache"],
        "B_wq": _B_in["wq"],
        "B_wo": _B_in["wo"],
    }

def reference(A_x, A_k_cache, A_v_cache, A_wq, A_wo, B_x, B_k_cache, B_v_cache, B_wq, B_wo):
    def _A_ref(x, k_cache, v_cache, wq, wo):
        B, Sq, D = x.shape
        H, Dh = 32, 128
        q = (lambda x, *d: x.permute(*d))((x @ wq).reshape(B, Sq, H, Dh), 0, 2, 1, 3)
        scores = torch.einsum("bhqd,bhkd->bhqk", q, k_cache) * (Dh ** -0.5)
        p = _softmax(scores, axis=-1)
        o = torch.einsum("bhqk,bhkd->bhqd", p, v_cache)
        return (lambda x, *d: x.permute(*d))(o, 0, 2, 1, 3).reshape(B, Sq, H * Dh) @ wo

    def _B_ref(x, k_cache, v_cache, wq, wo):
        B, Sq, D = x.shape
        Hq, Dh = 32, 128
        Hkv = k_cache.shape[1]
        rep = Hq // Hkv
        q = (lambda x, *d: x.permute(*d))((x @ wq).reshape(B, Sq, Hq, Dh), 0, 2, 1, 3)
        k = (lambda x, n, axis: torch.repeat_interleave(x, n, dim=axis))(k_cache, rep, axis=1)
        v = (lambda x, n, axis: torch.repeat_interleave(x, n, dim=axis))(v_cache, rep, axis=1)
        scores = torch.einsum("bhqd,bhkd->bhqk", q, k) * (Dh ** -0.5)
        p = _softmax(scores, axis=-1)
        o = torch.einsum("bhqk,bhkd->bhqd", p, v)
        return (lambda x, *d: x.permute(*d))(o, 0, 2, 1, 3).reshape(B, Sq, Hq * Dh) @ wo

    a = _A_ref(A_x, A_k_cache, A_v_cache, A_wq, A_wo)
    b = _B_ref(B_x, B_k_cache, B_v_cache, B_wq, B_wo)
    return torch.concatenate([a, b], axis=-1)
```

```cpp
#include <hip/hip_runtime.h>
#include <hip/hip_bf16.h>
typedef unsigned short u16;
__device__ __forceinline__ float bf2f(u16 x){ return __uint_as_float(((unsigned)x)<<16); }
__device__ __forceinline__ u16 f2bf(float f){ unsigned u=__float_as_uint(f); u += 0x7fffu + ((u>>16)&1u); return (u16)(u>>16); }
__global__ __launch_bounds__(256) void gemm_naive(const u16* A, const u16* B, u16* C, int M, int N, int K, int lda, int ldb, int ldc){
  __shared__ float As[32][33]; __shared__ float Bs[32][33];
  int tx=threadIdx.x&15, ty=threadIdx.x>>4;
  int m0=blockIdx.y*32, n0=blockIdx.x*32;
  float acc[2][2]={{0,0},{0,0}};
  for(int k0=0;k0<K;k0+=32){
    for(int i=threadIdx.x;i<1024;i+=256){ int r=i>>5,c=i&31; As[r][c]=bf2f(A[(long)(m0+r)*lda+k0+c]); Bs[r][c]=bf2f(B[(long)(k0+r)*ldb+n0+c]); }
    __syncthreads();
    for(int k=0;k<32;++k){ float a0=As[ty][k],a1=As[ty+16][k],b0=Bs[k][tx],b1=Bs[k][tx+16];
      acc[0][0]+=a0*b0; acc[0][1]+=a0*b1; acc[1][0]+=a1*b0; acc[1][1]+=a1*b1; }
    __syncthreads();
  }
  for(int i=0;i<2;++i)for(int j=0;j<2;++j) C[(long)(m0+ty+16*i)*ldc+n0+tx+16*j]=f2bf(acc[i][j]);
}
__global__ __launch_bounds__(256) void attn_naive(const u16* Q, const u16* Kc, const u16* Vc, u16* O){
  __shared__ float qs[16][128]; __shared__ float s[16][512]; __shared__ float inv[16];
  int qb=blockIdx.x, h=blockIdx.y, b=blockIdx.z; int t=threadIdx.x;
  for(int i=t;i<16*128;i+=256){ int r=i>>7,c=i&127; qs[r][c]=bf2f(Q[((long)(b*176+qb*16+r))*4096+h*128+c]); }
  __syncthreads();
  const u16* Kh=Kc+((long)(b*32+h))*512*128; const u16* Vh=Vc+((long)(b*32+h))*512*128;
  for(int key=t;key<512;key+=256){ float a[16]; for(int r=0;r<16;++r)a[r]=0;
    for(int d=0;d<128;++d){ float kv=bf2f(Kh[key*128+d]); for(int r=0;r<16;++r)a[r]+=qs[r][d]*kv; }
    for(int r=0;r<16;++r)s[r][key]=a[r]*0.08838834764831845f; }
  __syncthreads();
  if(t<16){ float m=-1e30f; for(int k=0;k<512;++k)m=fmaxf(m,s[t][k]); float l=0; for(int k=0;k<512;++k){ float p=__expf(s[t][k]-m); s[t][k]=p; l+=p;} inv[t]=1.f/l; }
  __syncthreads();
  { int d=t&127, rh=t>>7; float a[8]; for(int r=0;r<8;++r)a[r]=0;
    for(int k=0;k<512;++k){ float v=bf2f(Vh[k*128+d]); for(int r=0;r<8;++r)a[r]+=s[rh*8+r][k]*v; }
    for(int r=0;r<8;++r) O[((long)(b*176+qb*16+rh*8+r))*4096+h*128+d]=f2bf(a[r]*inv[rh*8+r]); }
}
extern "C" void kernel_launch(void* const* d_in, const int* in_sizes, int n_in, void* d_out, int out_size, void* d_ws, size_t ws_size, hipStream_t stream){
  u16* ws=(u16*)d_ws; u16* out=(u16*)d_out;
  for(int br=0;br<2;++br){
    const u16* x=(const u16*)d_in[br*5+0]; const u16* k=(const u16*)d_in[br*5+1]; const u16* v=(const u16*)d_in[br*5+2];
    const u16* wq=(const u16*)d_in[br*5+3]; const u16* wo=(const u16*)d_in[br*5+4];
    u16* q=ws+(long)br*2*1408*4096; u16* o=q+(long)1408*4096;
    gemm_naive<<<dim3(4096/32,1408/32),256,0,stream>>>(x,wq,q,1408,4096,1024,1024,4096,4096);
    attn_naive<<<dim3(11,32,8),256,0,stream>>>(q,k,v,o);
    gemm_naive<<<dim3(1024/32,1408/32),256,0,stream>>>(o,wo,out+br*1024,1408,1024,4096,4096,1024,2048);
  }
}
```

```cpp
#include <hip/hip_runtime.h>
#include <hip/hip_bf16.h>

#define LDS_BYTES 163840

__global__ __launch_bounds__(256, 1) void fused_attn_pair(const char* xA, const char* kA, const char* vA, const char* wqA, const char* woA,
                                                          const char* xB, const char* kB, const char* vB, const char* wqB, const char* woB,
                                                          char* ows, char* out) {
  asm volatile(
    "s_bfe_u32 s13, %2, 0x10002\n\t"
    "s_mul_i32 s68, s13, 40\n\t"
    "s_load_dwordx8 s[16:23], %1, s68\n\t"
    "s_add_u32 s68, s68, 32\n\t"
    "s_load_dwordx2 s[24:25], %1, s68\n\t"
    "s_load_dwordx4 s[28:31], %1, 0x50\n\t"
    "v_readfirstlane_b32 s8, %0\n\t"
    "v_and_b32_e32 v1, 63, %0\n\t"
    "s_lshr_b32 s10, %2, 3\n\t"
    "s_lshr_b32 s12, %2, 5\n\t"
    "s_and_b32 s9, %2, 3\n\t"
    "s_lshl_b32 s9, s9, 2\n\t"
    "s_and_b32 s68, s10, 3\n\t"
    "s_or_b32 s9, s9, s68\n\t"
    "s_lshr_b32 s8, s8, 6\n\t"
    "s_add_u32 s11, s12, s9\n\t"
    "s_and_b32 s11, s11, 3\n\t"
    "v_lshrrev_b32_e32 v2, 4, v1\n\t"
    "v_bfe_u32 v3, v1, 2, 2\n\t"
    "v_and_b32_e32 v4, 3, v1\n\t"
    "v_lshlrev_b32_e32 v31, 4, v1\n\t"
    "s_getpc_b64 s[90:91]\n\t"
    ".Lfa_pc:\n\t"
    "v_lshlrev_b32_e32 v37, 7, v1\n\t"
    "s_lshl_b32 s68, s10, 2\n\t"
    "s_add_u32 s68, s68, s8\n\t"
    "s_lshl_b32 s68, s68, 10\n\t"
    "s_min_u32 s68, s68, .Lfa_end-.Lfa_pc-1024\n\t"
    "v_add_u32_e32 v37, s68, v37\n\t"
    "s_mov_b64 exec, 0xff\n\t"
    "global_load_dword v254, v37, s[90:91]\n\t"
    "s_mov_b64 exec, -1\n\t"
    "s_lshl_b32 s68, s8, 1\n\t"
    "v_and_b32_e32 v5, 1, v2\n\t"
    "v_add_u32_e32 v5, s68, v5\n\t"
    "v_sub_u32_e32 v5, 0, v5\n\t"
    "v_xor_b32_e32 v5, v5, v4\n\t"
    "v_and_b32_e32 v5, 3, v5\n\t"
    "v_lshlrev_b32_e32 v5, 4, v5\n\t"
    "v_lshrrev_b32_e32 v6, 5, v1\n\t"
    "v_lshl_or_b32 v5, v6, 6, v5\n\t"
    "v_bfe_u32 v6, v1, 2, 3\n\t"
    "v_lshl_or_b32 v5, v6, 11, v5\n\t"
    "s_lshl_b32 s68, s8, 14\n\t"
    "v_add_u32_e32 v19, s68, v5\n\t"
    "s_sub_u32 s68, 0, s8\n\t"
    "s_and_b32 s68, s68, 3\n\t"
    "v_xor_b32_e32 v5, s68, v4\n\t"
    "v_lshlrev_b32_e32 v5, 4, v5\n\t"
    "s_lshl_b32 s68, s8, 3\n\t"
    "v_add_u32_e32 v6, s68, v3\n\t"
    "v_lshl_or_b32 v16, v6, 8, v5\n\t"
    "v_lshl_or_b32 v17, v6, 13, v5\n\t"
    "v_lshl_or_b32 v16, v2, 6, v16\n\t"
    "v_and_b32_e32 v5, 1, v2\n\t"
    "v_lshl_or_b32 v17, v5, 6, v17\n\t"
    "v_lshrrev_b32_e32 v5, 1, v2\n\t"
    "v_lshl_or_b32 v17, v5, 8, v17\n\t"
    "v_add_u32_e32 v18, 0x8000, v17\n\t"
    "v_add_u32_e32 v20, 0x10000, v19\n\t"
    "v_add_u32_e32 v21, 0x20000, v19\n\t"
    "v_add_u32_e32 v32, 0x30000, v19\n\t"
    "v_add_u32_e32 v33, 0x40000, v19\n\t"
    "v_add_u32_e32 v34, 0x50000, v19\n\t"
    "s_mov_b32 s84, 0x8653421f\n\t"
    "s_mov_b32 s85, 0xedbca97\n\t"
    "s_cmp_eq_u32 s11, 1\n\t"
    "s_cselect_b32 s84, 0x76453201, s84\n\t"
    "s_cselect_b32 s85, 0xfecdba89, s85\n\t"
    "s_cmp_eq_u32 s11, 2\n\t"
    "s_cselect_b32 s84, 0x75643120, s84\n\t"
    "s_cselect_b32 s85, 0xfdecb9a8, s85\n\t"
    "s_cmp_eq_u32 s11, 3\n\t"
    "s_cselect_b32 s84, 0x67542310, s84\n\t"
    "s_cselect_b32 s85, 0xefdcab98, s85\n\t"
    "s_lshl_b32 s65, s8, 11\n\t"
    "s_lshl_b32 s63, s8, 10\n\t"
    "s_mov_b32 s38, 0x58000\n\t"
    "s_mov_b32 s39, 0x20000\n\t"
    "s_mov_b32 s42, 0x7fe200\n\t"
    "s_mov_b32 s43, 0x20000\n\t"
    "s_mul_i32 s68, s12, 0x58000\n\t"
    "s_lshl_b32 s75, s9, 9\n\t"
    "s_waitcnt lgkmcnt(0)\n\t"
    "s_add_u32 s36, s16, s68\n\t"
    "s_addc_u32 s37, s17, 0\n\t"
    "s_and_b32 s37, s37, 0xffff\n\t"
    "s_add_u32 s40, s22, s75\n\t"
    "s_addc_u32 s41, s23, 0\n\t"
    "s_and_b32 s41, s41, 0xffff\n\t"
    "s_and_b32 s86, s84, 15\n\t"
    "s_lshr_b64 s[84:85], s[84:85], 4\n\t"
    "s_lshl_b32 s61, s86, 19\n\t"
    "s_lshl_b32 s60, s86, 7\n\t"
    "s_add_u32 s62, s61, 0x40000\n\t"
    "s_add_u32 m0, s63, 0xc000\n\t"
    "s_nop 0\n\t"
    "buffer_load_dwordx4 v19, s[36:39], s60 offen lds\n\t"
    "s_add_u32 m0, s65, 0x12000\n\t"
    "s_nop 0\n\t"
    "buffer_load_dwordx4 v17, s[40:43], s61 offen lds\n\t"
    "s_add_u32 m0, s63, 0xd000\n\t"
    "s_nop 0\n\t"
    "buffer_load_dwordx4 v20, s[36:39], s60 offen lds\n\t"
    "s_add_u32 m0, s63, 0xe000\n\t"
    "s_nop 0\n\t"
    "buffer_load_dwordx4 v21, s[36:39], s60 offen lds\n\t"
    "s_add_u32 m0, s65, 0x12400\n\t"
    "s_nop 0\n\t"
    "buffer_load_dwordx4 v18, s[40:43], s61 offen lds\n\t"
    "s_add_u32 m0, s63, 0xf000\n\t"
    "s_nop 0\n\t"
    "buffer_load_dwordx4 v32, s[36:39], s60 offen lds\n\t"
    "s_add_u32 m0, s65, 0x14000\n\t"
    "s_nop 0\n\t"
    "buffer_load_dwordx4 v17, s[40:43], s62 offen lds\n\t"
    "s_add_u32 m0, s63, 0x10000\n\t"
    "s_nop 0\n\t"
    "buffer_load_dwordx4 v33, s[36:39], s60 offen lds\n\t"
    "s_add_u32 m0, s63, 0x11000\n\t"
    "s_nop 0\n\t"
    "buffer_load_dwordx4 v34, s[36:39], s60 offen lds\n\t"
    "s_add_u32 m0, s65, 0x14400\n\t"
    "s_nop 0\n\t"
    "buffer_load_dwordx4 v18, s[40:43], s62 offen lds\n\t"
    "s_lshl_b32 s68, s12, 5\n\t"
    "s_lshl_b32 s75, s9, 1\n\t"
    "s_add_u32 s68, s68, s75\n\t"
    "s_lshl_b32 s68, s68, 17\n\t"
    "s_add_u32 s44, s18, s68\n\t"
    "s_addc_u32 s45, s19, 0\n\t"
    "s_and_b32 s45, s45, 0xffff\n\t"
    "s_add_u32 s48, s20, s68\n\t"
    "s_addc_u32 s49, s21, 0\n\t"
    "s_and_b32 s49, s49, 0xffff\n\t"
    "s_mul_i32 s68, s13, 0x58\n\t"
    "s_mul_i32 s75, s12, 11\n\t"
    "s_add_u32 s68, s68, s75\n\t"
    "s_lshl_b32 s68, s68, 17\n\t"
    "s_add_u32 s52, s28, s68\n\t"
    "s_addc_u32 s53, s29, 0\n\t"
    "s_and_b32 s53, s53, 0xffff\n\t"
    "s_mov_b32 s46, 0x40000\n\t"
    "s_mov_b32 s47, 0x20000\n\t"
    "s_mov_b32 s50, 0x40000\n\t"
    "s_mov_b32 s51, 0x20000\n\t"
    "s_mov_b32 s54, 0x160000\n\t"
    "s_mov_b32 s55, 0x20000\n\t"
    "s_lshl_b32 s68, s13, 3\n\t"
    "s_add_u32 s68, s68, s12\n\t"
    "s_lshl_b32 s68, s68, 13\n\t"
    "s_add_u32 s68, s68, 0x2000000\n\t"
    "s_add_u32 s26, s28, s68\n\t"
    "s_addc_u32 s27, s29, 0\n\t"
    "s_load_dwordx2 s[96:97], s[26:27], 0x0\n\t"
    "s_lshl_b32 s76, s9, 13\n\t"
    "s_lshr_b32 s86, s8, 1\n\t"
    "s_lshl_b32 s68, s86, 12\n\t"
    "s_add_u32 s76, s76, s68\n\t"
    "s_and_b32 s77, s8, 1\n\t"
    "s_mul_i32 s68, s77, 0xc0000\n\t"
    "s_add_u32 s76, s76, s68\n\t"
    "v_and_b32_e32 v7, 15, v1\n\t"
    "v_sub_u32_e32 v5, 0, v2\n\t"
    "v_lshrrev_b32_e32 v6, 1, v4\n\t"
    "v_xor_b32_e32 v5, v5, v6\n\t"
    "s_lshl_b32 s68, s77, 1\n\t"
    "v_xor_b32_e32 v5, s68, v5\n\t"
    "v_and_b32_e32 v5, 3, v5\n\t"
    "v_lshlrev_b32_e32 v5, 4, v5\n\t"
    "v_and_b32_e32 v6, 1, v4\n\t"
    "v_lshl_or_b32 v5, v6, 3, v5\n\t"
    "v_lshl_or_b32 v5, v3, 6, v5\n\t"
    "v_lshl_or_b32 v5, v2, 11, v5\n\t"
    "s_lshl_b32 s75, s86, 9\n\t"
    "s_add_u32 s75, s75, 0x12000\n\t"
    "v_add_u32_e32 v22, s75, v5\n\t"
    "v_xor_b32_e32 v23, 32, v22\n\t"
    "v_sub_u32_e32 v5, 0, v3\n\t"
    "v_xor_b32_e32 v5, v5, v2\n\t"
    "v_and_b32_e32 v5, 3, v5\n\t"
    "v_lshlrev_b32_e32 v5, 4, v5\n\t"
    "v_and_b32_e32 v6, 7, v7\n\t"
    "v_lshl_or_b32 v24, v6, 6, v5\n\t"
    "v_lshrrev_b32_e32 v6, 3, v7\n\t"
    "v_lshl_or_b32 v24, v6, 10, v24\n\t"
    "v_add_u32_e32 v25, 0xe800, v24\n\t"
    "s_mul_i32 s75, s77, 0x3000\n\t"
    "s_add_u32 s75, s75, 0xc000\n\t"
    "v_add_u32_e32 v24, s75, v24\n\t"
    "s_lshl_b32 s68, s77, 5\n\t"
    "v_xor_b32_e32 v5, s68, v5\n\t"
    "v_lshl_or_b32 v5, v4, 6, v5\n\t"
    "v_lshl_or_b32 v5, v3, 11, v5\n\t"
    "s_lshl_b32 s75, s86, 13\n\t"
    "v_add_u32_e32 v30, s75, v5\n\t"
    "v_add_u32_e32 v26, 0xe000, v22\n\t"
    "v_add_u32_e32 v27, 0xe000, v23\n\t"
    "v_add_u32_e32 v28, 0xe000, v24\n\t"
    "v_add_u32_e32 v35, 0xe000, v25\n\t"
    "s_mov_b32 s66, 0x3e0293ee\n\t"
    "s_mov_b32 s67, 0x3e0293ee\n\t"
    "s_lshl_b32 s89, s8, 13\n\t"
    "s_add_u32 s89, s89, 0xa000\n\t"
    "s_lshr_b32 s93, s9, 3\n\t"
    "s_lshr_b32 s86, s8, 1\n\t"
    "s_lshl1_add_u32 s92, s93, s86\n\t"
    "s_mov_b32 s15, 0\n\t"
    "s_lshl_b32 s14, s8, 13\n\t"
    "s_lshl_b32 s79, s77, 12\n\t"
    "v_xor_b32_e32 v5, s92, v2\n\t"
    "v_lshlrev_b32_e32 v5, 6, v5\n\t"
    "v_lshl_or_b32 v5, v4, 4, v5\n\t"
    "v_lshl_or_b32 v29, v3, 11, v5\n\t"
    "s_and_b32 s68, s9, 4\n\t"
    "s_lshl_b32 s68, s68, 8\n\t"
    "s_lshl_b32 s75, s93, 22\n\t"
    "s_add_u32 s68, s68, s75\n\t"
    "s_add_u32 s56, s24, s68\n\t"
    "s_addc_u32 s57, s25, 0\n\t"
    "s_and_b32 s57, s57, 0xffff\n\t"
    "s_mov_b32 s58, 0x3ffc00\n\t"
    "s_mov_b32 s59, 0x20000\n\t"
    "s_and_b32 s68, s9, 7\n\t"
    "s_lshl_b32 s68, s68, 8\n\t"
    "s_add_u32 s16, s24, s68\n\t"
    "s_addc_u32 s17, s25, 0\n\t"
    "s_and_b32 s17, s17, 0xffff\n\t"
    "s_mov_b32 s18, 0x7ff900\n\t"
    "s_mov_b32 s19, 0x20000\n\t"
    "s_lshl_b32 s25, s86, 12\n\t"
    "s_lshl_b32 s75, s93, 16\n\t"
    "s_add_u32 s25, s25, s75\n\t"
    "s_mul_i32 s75, s12, 11\n\t"
    "s_mul_i32 s86, s77, 6\n\t"
    "s_add_u32 s75, s75, s86\n\t"
    "s_lshl_b32 s75, s75, 16\n\t"
    "s_add_u32 s68, s68, s75\n\t"
    "s_lshl_b32 s75, s13, 11\n\t"
    "s_add_u32 s68, s68, s75\n\t"
    "s_lshl_b32 s75, s92, 6\n\t"
    "s_add_u32 s68, s68, s75\n\t"
    "s_add_u32 s4, s30, s68\n\t"
    "s_addc_u32 s5, s31, 0\n\t"
    "s_and_b32 s5, s5, 0xffff\n\t"
    "s_mov_b32 s6, 0x5f040\n\t"
    "s_mov_b32 s7, 0x20000\n\t"
    "s_lshl_b32 s86, s86, 17\n\t"
    "s_add_u32 s20, s52, s86\n\t"
    "s_addc_u32 s21, s53, 0\n\t"
    "s_and_b32 s21, s21, 0xffff\n\t"
    "s_cmp_lg_u32 s77, 0\n\t"
    "s_mov_b32 s22, 0xc0000\n\t"
    "s_cselect_b32 s22, 0xa0000, s22\n\t"
    "s_mov_b32 s23, 0x20000\n\t"
    "s_and_b32 s68, s9, 12\n\t"
    "s_lshl_b32 s24, 15, s68\n\t"
    "s_xor_b32 s68, s9, 8\n\t"
    "s_lshl_b32 s78, 0x10000, s68\n\t"
    "s_lshl2_add_u32 s74, s68, s8\n\t"
    "s_mul_i32 s74, s74, 0x3000\n\t"
    "s_lshl2_add_u32 s73, s9, s8\n\t"
    "s_mul_i32 s73, s73, 0x3000\n\t"
    "s_lshl_b32 s68, s13, 3\n\t"
    "s_add_u32 s68, s68, s12\n\t"
    "s_mul_i32 s68, s68, 0xc0000\n\t"
    "s_add_u32 s68, s68, 0x2800000\n\t"
    "s_add_u32 s100, s28, s68\n\t"
    "s_addc_u32 s101, s29, 0\n\t"
    "s_and_b32 s68, s9, 7\n\t"
    "s_mov_b32 s71, 0x86543210\n\t"
    "s_mov_b32 s72, 0xfedbca97\n\t"
    "s_cmp_eq_u32 s68, 1\n\t"
    "s_cselect_b32 s71, 0x76541032, s71\n\t"
    "s_cselect_b32 s72, 0xfecdba89, s72\n\t"
    "s_cmp_eq_u32 s68, 2\n\t"
    "s_cselect_b32 s71, 0x76321054, s71\n\t"
    "s_cselect_b32 s72, 0xfdecb9a8, s72\n\t"
    "s_cmp_eq_u32 s68, 3\n\t"
    "s_cselect_b32 s71, 0x54321076, s71\n\t"
    "s_cselect_b32 s72, 0xefdcab98, s72\n\t"
    "s_cmp_eq_u32 s68, 4\n\t"
    "s_cselect_b32 s71, 0xedcba98, s71\n\t"
    "s_cselect_b32 s72, 0x7653421f, s72\n\t"
    "s_cmp_eq_u32 s68, 5\n\t"
    "s_cselect_b32 s71, 0xfedc98ba, s71\n\t"
    "s_cselect_b32 s72, 0x76453201, s72\n\t"
    "s_cmp_eq_u32 s68, 6\n\t"
    "s_cselect_b32 s71, 0xfeba98dc, s71\n\t"
    "s_cselect_b32 s72, 0x75643120, s72\n\t"
    "s_cmp_eq_u32 s68, 7\n\t"
    "s_cselect_b32 s71, 0xdcba98fe, s71\n\t"
    "s_cselect_b32 s72, 0x67542310, s72\n\t"
    "s_lshl_b32 s88, s10, 2\n\t"
    "s_add_u32 s88, s88, s8\n\t"
    "s_mul_i32 s88, s88, 0x1000\n\t"
    "s_and_b32 s75, s9, 4\n\t"
    "s_lshl_b32 s75, s75, 19\n\t"
    "s_add_u32 s88, s88, s75\n\t"
    "s_sub_u32 s88, s88, 0x80000\n\t"
    "s_add_u32 s61, s61, 0x80\n\t"
    "s_add_u32 s62, s62, 0x80\n\t"
    "s_add_u32 m0, s65, 0x16000\n\t"
    "s_nop 0\n\t"
    "buffer_load_dwordx4 v17, s[40:43], s61 offen lds\n\t"
    "s_add_u32 m0, s65, 0x16400\n\t"
    "s_nop 0\n\t"
    "buffer_load_dwordx4 v18, s[40:43], s61 offen lds\n\t"
    "s_add_u32 m0, s65, 0x18000\n\t"
    "s_nop 0\n\t"
    "buffer_load_dwordx4 v17, s[40:43], s62 offen lds\n\t"
    "s_add_u32 m0, s65, 0x18400\n\t"
    "s_nop 0\n\t"
    "buffer_load_dwordx4 v18, s[40:43], s62 offen lds\n\t"
    "s_and_b32 s86, s84, 15\n\t"
    "s_lshr_b64 s[84:85], s[84:85], 4\n\t"
    "s_lshl_b32 s61, s86, 19\n\t"
    "s_lshl_b32 s60, s86, 7\n\t"
    "s_add_u32 s62, s61, 0x40000\n\t"
    "s_add_u32 m0, s63, 0x1a000\n\t"
    "s_nop 0\n\t"
    "buffer_load_dwordx4 v19, s[36:39], s60 offen lds\n\t"
    "s_add_u32 m0, s65, 0x20000\n\t"
    "s_nop 0\n\t"
    "buffer_load_dwordx4 v17, s[40:43], s61 offen lds\n\t"
    "s_add_u32 m0, s63, 0x1b000\n\t"
    "s_nop 0\n\t"
    "buffer_load_dwordx4 v20, s[36:39], s60 offen lds\n\t"
    "s_add_u32 m0, s63, 0x1c000\n\t"
    "s_nop 0\n\t"
    "buffer_load_dwordx4 v21, s[36:39], s60 offen lds\n\t"
    "s_add_u32 m0, s65, 0x20400\n\t"
    "s_nop 0\n\t"
    "buffer_load_dwordx4 v18, s[40:43], s61 offen lds\n\t"
    "s_add_u32 m0, s63, 0x1d000\n\t"
    "s_nop 0\n\t"
    "buffer_load_dwordx4 v32, s[36:39], s60 offen lds\n\t"
    "s_add_u32 m0, s65, 0x22000\n\t"
    "s_nop 0\n\t"
    "buffer_load_dwordx4 v17, s[40:43], s62 offen lds\n\t"
    "s_add_u32 m0, s63, 0x1e000\n\t"
    "s_nop 0\n\t"
    "buffer_load_dwordx4 v33, s[36:39], s60 offen lds\n\t"
    "s_add_u32 m0, s63, 0x1f000\n\t"
    "s_nop 0\n\t"
    "buffer_load_dwordx4 v34, s[36:39], s60 offen lds\n\t"
    "s_add_u32 m0, s65, 0x22400\n\t"
    "s_nop 0\n\t"
    "buffer_load_dwordx4 v18, s[40:43], s62 offen lds\n\t"
    "s_add_u32 s61, s61, 0x80\n\t"
    "s_add_u32 s62, s62, 0x80\n\t"
    "s_add_u32 m0, s65, 0x24000\n\t"
    "s_nop 0\n\t"
    "buffer_load_dwordx4 v17, s[40:43], s61 offen lds\n\t"
    "s_add_u32 m0, s65, 0x24400\n\t"
    "s_nop 0\n\t"
    "buffer_load_dwordx4 v18, s[40:43], s61 offen lds\n\t"
    "s_add_u32 m0, s65, 0x26000\n\t"
    "s_nop 0\n\t"
    "buffer_load_dwordx4 v17, s[40:43], s62 offen lds\n\t"
    "s_add_u32 m0, s65, 0x26400\n\t"
    "s_nop 0\n\t"
    "buffer_load_dwordx4 v18, s[40:43], s62 offen lds\n\t"
    "s_waitcnt vmcnt(18)\n\t"
    "s_barrier\n\t"
    "ds_read_b64_tr_b16 a[192:193], v22 offset:0\n\t"
    "ds_read_b64_tr_b16 a[194:195], v22 offset:1024\n\t"
    "ds_read_b64_tr_b16 a[196:197], v22 offset:8192\n\t"
    "ds_read_b64_tr_b16 a[198:199], v22 offset:9216\n\t"
    "ds_read_b64_tr_b16 a[200:201], v23 offset:0\n\t"
    "ds_read_b64_tr_b16 a[202:203], v23 offset:1024\n\t"
    "ds_read_b64_tr_b16 a[204:205], v23 offset:8192\n\t"
    "ds_read_b64_tr_b16 a[206:207], v23 offset:9216\n\t"
    "ds_read_b64_tr_b16 a[208:209], v22 offset:256\n\t"
    "ds_read_b64_tr_b16 a[210:211], v22 offset:1280\n\t"
    "ds_read_b64_tr_b16 a[212:213], v22 offset:8448\n\t"
    "ds_read_b64_tr_b16 a[214:215], v22 offset:9472\n\t"
    "ds_read_b64_tr_b16 a[216:217], v23 offset:256\n\t"
    "ds_read_b64_tr_b16 a[218:219], v23 offset:1280\n\t"
    "ds_read_b64_tr_b16 a[220:221], v23 offset:8448\n\t"
    "ds_read_b64_tr_b16 a[222:223], v23 offset:9472\n\t"
    "ds_read_b128 a[224:227], v24 offset:0\n\t"
    "ds_read_b128 a[228:231], v24 offset:2048\n\t"
    "ds_read_b128 a[232:235], v24 offset:4096\n\t"
    "ds_read_b128 a[236:239], v24 offset:6144\n\t"
    "ds_read_b128 a[240:243], v24 offset:8192\n\t"
    "ds_read_b128 a[244:247], v25 offset:0\n\t"
    "ds_read_b128 v[40:43], v24 offset:512\n\t"
    "ds_read_b128 v[44:47], v24 offset:2560\n\t"
    "ds_read_b128 v[48:51], v24 offset:4608\n\t"
    "ds_read_b128 v[52:55], v24 offset:6656\n\t"
    "ds_read_b128 v[56:59], v24 offset:8704\n\t"
    "ds_read_b128 v[60:63], v25 offset:512\n\t"
    "s_waitcnt lgkmcnt(0)\n\t"
    "s_add_u32 s96, s96, 0x7f4a7c15\n\t"
    "s_addc_u32 s97, s97, 0x9e3779b9\n\t"
    "v_mfma_f32_16x16x32_bf16 v[64:67], a[192:195], a[224:227], 0\n\t"
    "v_mfma_f32_16x16x32_bf16 v[64:67], a[196:199], v[40:43], v[64:67]\n\t"
    "v_mfma_f32_16x16x32_bf16 v[96:99], a[196:199], v[44:47], 0\n\t"
    "v_mfma_f32_16x16x32_bf16 v[96:99], a[192:195], a[228:231], v[96:99]\n\t"
    "s_and_b32 s86, s84, 15\n\t"
    "s_lshr_b64 s[84:85], s[84:85], 4\n\t"
    "v_mfma_f32_16x16x32_bf16 v[128:131], a[192:195], a[232:235], 0\n\t"
    "s_lshl_b32 s61, s86, 19\n\t"
    "s_lshl_b32 s60, s86, 7\n\t"
    "s_add_u32 s62, s61, 0x40000\n\t"
    "v_mfma_f32_16x16x32_bf16 v[128:131], a[196:199], v[48:51], v[128:131]\n\t"
    "v_mfma_f32_16x16x32_bf16 v[160:163], a[196:199], v[52:55], 0\n\t"
    "v_mfma_f32_16x16x32_bf16 v[160:163], a[192:195], a[236:239], v[160:163]\n\t"
    "v_mfma_f32_16x16x32_bf16 v[192:195], a[192:195], a[240:243], 0\n\t"
    "s_waitcnt vmcnt(14) lgkmcnt(0)\n\t"
    "s_barrier\n\t"
    "v_mfma_f32_16x16x32_bf16 v[192:195], a[196:199], v[56:59], v[192:195]\n\t"
    "s_add_u32 m0, s63, 0xc000\n\t"
    "v_mfma_f32_16x16x32_bf16 v[224:227], a[196:199], v[60:63], 0\n\t"
    "buffer_load_dwordx4 v19, s[36:39], s60 offen lds\n\t"
    "v_mfma_f32_16x16x32_bf16 v[224:227], a[192:195], a[244:247], v[224:227]\n\t"
    "ds_read_b64_tr_b16 a[192:193], v22 offset:16384\n\t"
    "v_mfma_f32_16x16x32_bf16 v[196:199], a[200:203], a[240:243], 0\n\t"
    "ds_read_b64_tr_b16 a[194:195], v22 offset:17408\n\t"
    "v_mfma_f32_16x16x32_bf16 v[196:199], a[204:207], v[56:59], v[196:199]\n\t"
    "ds_read_b64_tr_b16 a[196:197], v22 offset:24576\n\t"
    "v_mfma_f32_16x16x32_bf16 v[164:167], a[204:207], v[52:55], 0\n\t"
    "ds_read_b64_tr_b16 a[198:199], v22 offset:25600\n\t"
    "s_add_u32 m0, s65, 0x12000\n\t"
    "v_mfma_f32_16x16x32_bf16 v[164:167], a[200:203], a[236:239], v[164:167]\n\t"
    "buffer_load_dwordx4 v17, s[40:43], s61 offen lds\n\t"
    "v_mfma_f32_16x16x32_bf16 v[132:135], a[200:203], a[232:235], 0\n\t"
    "s_add_u32 m0, s63, 0xd000\n\t"
    "v_mfma_f32_16x16x32_bf16 v[132:135], a[204:207], v[48:51], v[132:135]\n\t"
    "buffer_load_dwordx4 v20, s[36:39], s60 offen lds\n\t"
    "v_mfma_f32_16x16x32_bf16 v[100:103], a[204:207], v[44:47], 0\n\t"
    "v_mfma_f32_16x16x32_bf16 v[100:103], a[200:203], a[228:231], v[100:103]\n\t"
    "s_add_u32 m0, s63, 0xe000\n\t"
    "v_mfma_f32_16x16x32_bf16 v[68:71], a[200:203], a[224:227], 0\n\t"
    "buffer_load_dwordx4 v21, s[36:39], s60 offen lds\n\t"
    "v_mfma_f32_16x16x32_bf16 v[68:71], a[204:207], v[40:43], v[68:71]\n\t"
    "ds_read_b64_tr_b16 a[200:201], v23 offset:16384\n\t"
    "v_mfma_f32_16x16x32_bf16 v[72:75], a[208:211], a[224:227], 0\n\t"
    "ds_read_b64_tr_b16 a[202:203], v23 offset:17408\n\t"
    "v_mfma_f32_16x16x32_bf16 v[72:75], a[212:215], v[40:43], v[72:75]\n\t"
    "ds_read_b64_tr_b16 a[204:205], v23 offset:24576\n\t"
    "v_mfma_f32_16x16x32_bf16 v[104:107], a[212:215], v[44:47], 0\n\t"
    "ds_read_b64_tr_b16 a[206:207], v23 offset:25600\n\t"
    "s_add_u32 m0, s65, 0x12400\n\t"
    "v_mfma_f32_16x16x32_bf16 v[104:107], a[208:211], a[228:231], v[104:107]\n\t"
    "buffer_load_dwordx4 v18, s[40:43], s61 offen lds\n\t"
    "v_mfma_f32_16x16x32_bf16 v[136:139], a[208:211], a[232:235], 0\n\t"
    "s_add_u32 m0, s63, 0xf000\n\t"
    "v_mfma_f32_16x16x32_bf16 v[136:139], a[212:215], v[48:51], v[136:139]\n\t"
    "buffer_load_dwordx4 v32, s[36:39], s60 offen lds\n\t"
    "v_mfma_f32_16x16x32_bf16 v[168:171], a[212:215], v[52:55], 0\n\t"
    "v_mfma_f32_16x16x32_bf16 v[168:171], a[208:211], a[236:239], v[168:171]\n\t"
    "v_mfma_f32_16x16x32_bf16 v[200:203], a[208:211], a[240:243], 0\n\t"
    "s_add_u32 m0, s65, 0x14000\n\t"
    "v_mfma_f32_16x16x32_bf16 v[200:203], a[212:215], v[56:59], v[200:203]\n\t"
    "buffer_load_dwordx4 v17, s[40:43], s62 offen lds\n\t"
    "v_mfma_f32_16x16x32_bf16 v[232:235], a[212:215], v[60:63], 0\n\t"
    "v_mfma_f32_16x16x32_bf16 v[232:235], a[208:211], a[244:247], v[232:235]\n\t"
    "ds_read_b64_tr_b16 a[208:209], v22 offset:16640\n\t"
    "v_mfma_f32_16x16x32_bf16 v[204:207], a[216:219], a[240:243], 0\n\t"
    "ds_read_b64_tr_b16 a[210:211], v22 offset:17664\n\t"
    "v_mfma_f32_16x16x32_bf16 v[204:207], a[220:223], v[56:59], v[204:207]\n\t"
    "ds_read_b64_tr_b16 a[212:213], v22 offset:24832\n\t"
    "v_mfma_f32_16x16x32_bf16 v[172:175], a[220:223], v[52:55], 0\n\t"
    "ds_read_b64_tr_b16 a[214:215], v22 offset:25856\n\t"
    "s_add_u32 m0, s63, 0x10000\n\t"
    "v_mfma_f32_16x16x32_bf16 v[172:175], a[216:219], a[236:239], v[172:175]\n\t"
    "buffer_load_dwordx4 v33, s[36:39], s60 offen lds\n\t"
    "v_mfma_f32_16x16x32_bf16 v[140:143], a[216:219], a[232:235], 0\n\t"
    "s_add_u32 m0, s63, 0x11000\n\t"
    "v_mfma_f32_16x16x32_bf16 v[140:143], a[220:223], v[48:51], v[140:143]\n\t"
    "buffer_load_dwordx4 v34, s[36:39], s60 offen lds\n\t"
    "v_mfma_f32_16x16x32_bf16 v[108:111], a[220:223], v[44:47], 0\n\t"
    "s_add_u32 m0, s65, 0x14400\n\t"
    "v_mfma_f32_16x16x32_bf16 v[108:111], a[216:219], a[228:231], v[108:111]\n\t"
    "buffer_load_dwordx4 v18, s[40:43], s62 offen lds\n\t"
    "v_mfma_f32_16x16x32_bf16 v[76:79], a[216:219], a[224:227], 0\n\t"
    "v_mfma_f32_16x16x32_bf16 v[76:79], a[220:223], v[40:43], v[76:79]\n\t"
    "ds_read_b64_tr_b16 a[216:217], v23 offset:16640\n\t"
    "s_waitcnt lgkmcnt(5)\n\t"
    "v_mfma_f32_16x16x32_bf16 v[80:83], a[192:195], a[224:227], 0\n\t"
    "ds_read_b64_tr_b16 a[218:219], v23 offset:17664\n\t"
    "v_mfma_f32_16x16x32_bf16 v[80:83], a[196:199], v[40:43], v[80:83]\n\t"
    "ds_read_b64_tr_b16 a[220:221], v23 offset:24832\n\t"
    "v_mfma_f32_16x16x32_bf16 v[112:115], a[196:199], v[44:47], 0\n\t"
    "ds_read_b64_tr_b16 a[222:223], v23 offset:25856\n\t"
    "v_mfma_f32_16x16x32_bf16 v[112:115], a[192:195], a[228:231], v[112:115]\n\t"
    "s_add_u32 s61, s61, 0x80\n\t"
    "s_add_u32 s62, s62, 0x80\n\t"
    "v_mfma_f32_16x16x32_bf16 v[144:147], a[192:195], a[232:235], 0\n\t"
    "v_mfma_f32_16x16x32_bf16 v[144:147], a[196:199], v[48:51], v[144:147]\n\t"
    "v_mfma_f32_16x16x32_bf16 v[176:179], a[196:199], v[52:55], 0\n\t"
    "v_mfma_f32_16x16x32_bf16 v[176:179], a[192:195], a[236:239], v[176:179]\n\t"
    "v_mfma_f32_16x16x32_bf16 v[208:211], a[192:195], a[240:243], 0\n\t"
    "s_waitcnt vmcnt(14) lgkmcnt(0)\n\t"
    "s_barrier\n\t"
    "v_mfma_f32_16x16x32_bf16 v[208:211], a[196:199], v[56:59], v[208:211]\n\t"
    "ds_read_b128 a[248:251], v28 offset:0\n\t"
    "s_add_u32 m0, s65, 0x16000\n\t"
    "v_mfma_f32_16x16x32_bf16 v[240:243], a[196:199], v[60:63], 0\n\t"
    "buffer_load_dwordx4 v17, s[40:43], s61 offen lds\n\t"
    "v_mfma_f32_16x16x32_bf16 v[240:243], a[192:195], a[244:247], v[240:243]\n\t"
    "ds_read_b64_tr_b16 a[192:193], v26 offset:0\n\t"
    "v_mfma_f32_16x16x32_bf16 v[212:215], a[200:203], a[240:243], 0\n\t"
    "ds_read_b64_tr_b16 a[194:195], v26 offset:1024\n\t"
    "v_mfma_f32_16x16x32_bf16 v[212:215], a[204:207], v[56:59], v[212:215]\n\t"
    "ds_read_b64_tr_b16 a[196:197], v26 offset:8192\n\t"
    "v_mfma_f32_16x16x32_bf16 v[180:183], a[204:207], v[52:55], 0\n\t"
    "ds_read_b64_tr_b16 a[198:199], v26 offset:9216\n\t"
    "s_add_u32 m0, s65, 0x16400\n\t"
    "v_mfma_f32_16x16x32_bf16 v[180:183], a[200:203], a[236:239], v[180:183]\n\t"
    "buffer_load_dwordx4 v18, s[40:43], s61 offen lds\n\t"
    "v_mfma_f32_16x16x32_bf16 v[148:151], a[200:203], a[232:235], 0\n\t"
    "ds_read_b128 a[252:255], v28 offset:2048\n\t"
    "v_mfma_f32_16x16x32_bf16 v[148:151], a[204:207], v[48:51], v[148:151]\n\t"
    "ds_read_b128 v[0:3], v28 offset:4096\n\t"
    "v_mfma_f32_16x16x32_bf16 v[116:119], a[204:207], v[44:47], 0\n\t"
    "ds_read_b128 v[4:7], v28 offset:6144\n\t"
    "s_add_u32 m0, s65, 0x18000\n\t"
    "v_mfma_f32_16x16x32_bf16 v[116:119], a[200:203], a[228:231], v[116:119]\n\t"
    "buffer_load_dwordx4 v17, s[40:43], s62 offen lds\n\t"
    "v_mfma_f32_16x16x32_bf16 v[84:87], a[200:203], a[224:227], 0\n\t"
    "ds_read_b128 v[8:11], v28 offset:8192\n\t"
    "v_mfma_f32_16x16x32_bf16 v[84:87], a[204:207], v[40:43], v[84:87]\n\t"
    "ds_read_b64_tr_b16 a[200:201], v27 offset:0\n\t"
    "v_mfma_f32_16x16x32_bf16 v[88:91], a[208:211], a[224:227], 0\n\t"
    "ds_read_b64_tr_b16 a[202:203], v27 offset:1024\n\t"
    "v_mfma_f32_16x16x32_bf16 v[88:91], a[212:215], v[40:43], v[88:91]\n\t"
    "ds_read_b64_tr_b16 a[204:205], v27 offset:8192\n\t"
    "v_mfma_f32_16x16x32_bf16 v[120:123], a[212:215], v[44:47], 0\n\t"
    "ds_read_b64_tr_b16 a[206:207], v27 offset:9216\n\t"
    "s_add_u32 m0, s65, 0x18400\n\t"
    "v_mfma_f32_16x16x32_bf16 v[120:123], a[208:211], a[228:231], v[120:123]\n\t"
    "buffer_load_dwordx4 v18, s[40:43], s62 offen lds\n\t"
    "v_mfma_f32_16x16x32_bf16 v[152:155], a[208:211], a[232:235], 0\n\t"
    "ds_read_b128 v[12:15], v35 offset:0\n\t"
    "v_mfma_f32_16x16x32_bf16 v[152:155], a[212:215], v[48:51], v[152:155]\n\t"
    "ds_read_b128 a[104:107], v28 offset:512\n\t"
    "s_add_u32 m0, s65, 0\n\t"
    "s_mov_b32 s68, 0\n\t"
    "v_mfma_f32_16x16x32_bf16 v[184:187], a[212:215], v[52:55], 0\n\t"
    "buffer_load_dwordx4 v16, s[44:47], s68 offen nt lds\n\t"
    "v_mfma_f32_16x16x32_bf16 v[184:187], a[208:211], a[236:239], v[184:187]\n\t"
    "ds_read_b128 a[108:111], v28 offset:2560\n\t"
    "v_mfma_f32_16x16x32_bf16 v[216:219], a[208:211], a[240:243], 0\n\t"
    "ds_read_b128 a[112:115], v28 offset:4608\n\t"
    "v_mfma_f32_16x16x32_bf16 v[216:219], a[212:215], v[56:59], v[216:219]\n\t"
    "ds_read_b128 a[116:119], v28 offset:6656\n\t"
    "v_mfma_f32_16x16x32_bf16 v[248:251], a[212:215], v[60:63], 0\n\t"
    "buffer_load_dwordx4 v16, s[44:47], s68 offen offset:1024 nt lds\n\t"
    "v_mfma_f32_16x16x32_bf16 v[248:251], a[208:211], a[244:247], v[248:251]\n\t"
    "ds_read_b64_tr_b16 a[208:209], v26 offset:256\n\t"
    "v_mfma_f32_16x16x32_bf16 v[220:223], a[216:219], a[240:243], 0\n\t"
    "ds_read_b64_tr_b16 a[210:211], v26 offset:1280\n\t"
    "v_mfma_f32_16x16x32_bf16 v[220:223], a[220:223], v[56:59], v[220:223]\n\t"
    "ds_read_b64_tr_b16 a[212:213], v26 offset:8448\n\t"
    "v_mfma_f32_16x16x32_bf16 v[188:191], a[220:223], v[52:55], 0\n\t"
    "ds_read_b64_tr_b16 a[214:215], v26 offset:9472\n\t"
    "s_add_u32 m0, s65, 0x2000\n\t"
    "s_mov_b32 s68, 0x20000\n\t"
    "v_mfma_f32_16x16x32_bf16 v[188:191], a[216:219], a[236:239], v[188:191]\n\t"
    "buffer_load_dwordx4 v16, s[44:47], s68 offen nt lds\n\t"
    "v_mfma_f32_16x16x32_bf16 v[156:159], a[216:219], a[232:235], 0\n\t"
    "ds_read_b128 a[120:123], v28 offset:8704\n\t"
    "v_mfma_f32_16x16x32_bf16 v[156:159], a[220:223], v[48:51], v[156:159]\n\t"
    "ds_read_b128 a[124:127], v35 offset:512\n\t"
    "v_mfma_f32_16x16x32_bf16 v[124:127], a[220:223], v[44:47], 0\n\t"
    "buffer_load_dwordx4 v16, s[44:47], s68 offen offset:1024 nt lds\n\t"
    "v_mfma_f32_16x16x32_bf16 v[124:127], a[216:219], a[228:231], v[124:127]\n\t"
    "v_mfma_f32_16x16x32_bf16 v[92:95], a[216:219], a[224:227], 0\n\t"
    "v_mfma_f32_16x16x32_bf16 v[92:95], a[220:223], v[40:43], v[92:95]\n\t"
    "ds_read_b64_tr_b16 a[216:217], v27 offset:256\n\t"
    "s_waitcnt lgkmcnt(1)\n\t"
    "v_mfma_f32_16x16x32_bf16 v[64:67], a[192:195], a[248:251], v[64:67]\n\t"
    "ds_read_b64_tr_b16 a[218:219], v27 offset:1280\n\t"
    "v_mfma_f32_16x16x32_bf16 v[64:67], a[196:199], a[104:107], v[64:67]\n\t"
    "ds_read_b64_tr_b16 a[220:221], v27 offset:8448\n\t"
    "v_mfma_f32_16x16x32_bf16 v[96:99], a[196:199], a[108:111], v[96:99]\n\t"
    "ds_read_b64_tr_b16 a[222:223], v27 offset:9472\n\t"
    "v_mfma_f32_16x16x32_bf16 v[96:99], a[192:195], a[252:255], v[96:99]\n\t"
    "s_and_b32 s86, s84, 15\n\t"
    "s_lshr_b64 s[84:85], s[84:85], 4\n\t"
    "v_mfma_f32_16x16x32_bf16 v[128:131], a[192:195], v[0:3], v[128:131]\n\t"
    "s_lshl_b32 s61, s86, 19\n\t"
    "s_lshl_b32 s60, s86, 7\n\t"
    "s_add_u32 s62, s61, 0x40000\n\t"
    "v_mfma_f32_16x16x32_bf16 v[128:131], a[196:199], a[112:115], v[128:131]\n\t"
    "v_mfma_f32_16x16x32_bf16 v[160:163], a[196:199], a[116:119], v[160:163]\n\t"
    "v_mfma_f32_16x16x32_bf16 v[160:163], a[192:195], v[4:7], v[160:163]\n\t"
    "v_mfma_f32_16x16x32_bf16 v[192:195], a[192:195], v[8:11], v[192:195]\n\t"
    "s_waitcnt vmcnt(18) lgkmcnt(0)\n\t"
    "s_barrier\n\t"
    "v_mfma_f32_16x16x32_bf16 v[192:195], a[196:199], a[120:123], v[192:195]\n\t"
    "s_add_u32 m0, s63, 0x1a000\n\t"
    "v_mfma_f32_16x16x32_bf16 v[224:227], a[196:199], a[124:127], v[224:227]\n\t"
    "buffer_load_dwordx4 v19, s[36:39], s60 offen lds\n\t"
    "v_mfma_f32_16x16x32_bf16 v[224:227], a[192:195], v[12:15], v[224:227]\n\t"
    "ds_read_b64_tr_b16 a[192:193], v26 offset:16384\n\t"
    "v_mfma_f32_16x16x32_bf16 v[196:199], a[200:203], v[8:11], v[196:199]\n\t"
    "ds_read_b64_tr_b16 a[194:195], v26 offset:17408\n\t"
    "v_mfma_f32_16x16x32_bf16 v[196:199], a[204:207], a[120:123], v[196:199]\n\t"
    "ds_read_b64_tr_b16 a[196:197], v26 offset:24576\n\t"
    "v_mfma_f32_16x16x32_bf16 v[164:167], a[204:207], a[116:119], v[164:167]\n\t"
    "ds_read_b64_tr_b16 a[198:199], v26 offset:25600\n\t"
    "s_add_u32 m0, s65, 0x20000\n\t"
    "v_mfma_f32_16x16x32_bf16 v[164:167], a[200:203], v[4:7], v[164:167]\n\t"
    "buffer_load_dwordx4 v17, s[40:43], s61 offen lds\n\t"
    "v_mfma_f32_16x16x32_bf16 v[132:135], a[200:203], v[0:3], v[132:135]\n\t"
    "s_add_u32 m0, s63, 0x1b000\n\t"
    "v_mfma_f32_16x16x32_bf16 v[132:135], a[204:207], a[112:115], v[132:135]\n\t"
    "buffer_load_dwordx4 v20, s[36:39], s60 offen lds\n\t"
    "v_mfma_f32_16x16x32_bf16 v[100:103], a[204:207], a[108:111], v[100:103]\n\t"
    "v_mfma_f32_16x16x32_bf16 v[100:103], a[200:203], a[252:255], v[100:103]\n\t"
    "s_add_u32 m0, s63, 0x1c000\n\t"
    "v_mfma_f32_16x16x32_bf16 v[68:71], a[200:203], a[248:251], v[68:71]\n\t"
    "buffer_load_dwordx4 v21, s[36:39], s60 offen lds\n\t"
    "v_mfma_f32_16x16x32_bf16 v[68:71], a[204:207], a[104:107], v[68:71]\n\t"
    "ds_read_b64_tr_b16 a[200:201], v27 offset:16384\n\t"
    "v_mfma_f32_16x16x32_bf16 v[72:75], a[208:211], a[248:251], v[72:75]\n\t"
    "ds_read_b64_tr_b16 a[202:203], v27 offset:17408\n\t"
    "v_mfma_f32_16x16x32_bf16 v[72:75], a[212:215], a[104:107], v[72:75]\n\t"
    "ds_read_b64_tr_b16 a[204:205], v27 offset:24576\n\t"
    "v_mfma_f32_16x16x32_bf16 v[104:107], a[212:215], a[108:111], v[104:107]\n\t"
    "ds_read_b64_tr_b16 a[206:207], v27 offset:25600\n\t"
    "s_add_u32 m0, s65, 0x20400\n\t"
    "v_mfma_f32_16x16x32_bf16 v[104:107], a[208:211], a[252:255], v[104:107]\n\t"
    "buffer_load_dwordx4 v18, s[40:43], s61 offen lds\n\t"
    "v_mfma_f32_16x16x32_bf16 v[136:139], a[208:211], v[0:3], v[136:139]\n\t"
    "s_add_u32 m0, s63, 0x1d000\n\t"
    "v_mfma_f32_16x16x32_bf16 v[136:139], a[212:215], a[112:115], v[136:139]\n\t"
    "buffer_load_dwordx4 v32, s[36:39], s60 offen lds\n\t"
    "v_mfma_f32_16x16x32_bf16 v[168:171], a[212:215], a[116:119], v[168:171]\n\t"
    "v_mfma_f32_16x16x32_bf16 v[168:171], a[208:211], v[4:7], v[168:171]\n\t"
    "v_mfma_f32_16x16x32_bf16 v[200:203], a[208:211], v[8:11], v[200:203]\n\t"
    "s_add_u32 m0, s65, 0x22000\n\t"
    "v_mfma_f32_16x16x32_bf16 v[200:203], a[212:215], a[120:123], v[200:203]\n\t"
    "buffer_load_dwordx4 v17, s[40:43], s62 offen lds\n\t"
    "v_mfma_f32_16x16x32_bf16 v[232:235], a[212:215], a[124:127], v[232:235]\n\t"
    "v_mfma_f32_16x16x32_bf16 v[232:235], a[208:211], v[12:15], v[232:235]\n\t"
    "ds_read_b64_tr_b16 a[208:209], v26 offset:16640\n\t"
    "v_mfma_f32_16x16x32_bf16 v[204:207], a[216:219], v[8:11], v[204:207]\n\t"
    "ds_read_b64_tr_b16 a[210:211], v26 offset:17664\n\t"
    "v_mfma_f32_16x16x32_bf16 v[204:207], a[220:223], a[120:123], v[204:207]\n\t"
    "ds_read_b64_tr_b16 a[212:213], v26 offset:24832\n\t"
    "v_mfma_f32_16x16x32_bf16 v[172:175], a[220:223], a[116:119], v[172:175]\n\t"
    "ds_read_b64_tr_b16 a[214:215], v26 offset:25856\n\t"
    "s_add_u32 m0, s63, 0x1e000\n\t"
    "v_mfma_f32_16x16x32_bf16 v[172:175], a[216:219], v[4:7], v[172:175]\n\t"
    "buffer_load_dwordx4 v33, s[36:39], s60 offen lds\n\t"
    "v_mfma_f32_16x16x32_bf16 v[140:143], a[216:219], v[0:3], v[140:143]\n\t"
    "s_add_u32 m0, s63, 0x1f000\n\t"
    "v_mfma_f32_16x16x32_bf16 v[140:143], a[220:223], a[112:115], v[140:143]\n\t"
    "buffer_load_dwordx4 v34, s[36:39], s60 offen lds\n\t"
    "v_mfma_f32_16x16x32_bf16 v[108:111], a[220:223], a[108:111], v[108:111]\n\t"
    "s_add_u32 m0, s65, 0x22400\n\t"
    "v_mfma_f32_16x16x32_bf16 v[108:111], a[216:219], a[252:255], v[108:111]\n\t"
    "buffer_load_dwordx4 v18, s[40:43], s62 offen lds\n\t"
    "v_mfma_f32_16x16x32_bf16 v[76:79], a[216:219], a[248:251], v[76:79]\n\t"
    "v_mfma_f32_16x16x32_bf16 v[76:79], a[220:223], a[104:107], v[76:79]\n\t"
    "ds_read_b64_tr_b16 a[216:217], v27 offset:16640\n\t"
    "s_waitcnt lgkmcnt(5)\n\t"
    "v_mfma_f32_16x16x32_bf16 v[80:83], a[192:195], a[248:251], v[80:83]\n\t"
    "ds_read_b64_tr_b16 a[218:219], v27 offset:17664\n\t"
    "v_mfma_f32_16x16x32_bf16 v[80:83], a[196:199], a[104:107], v[80:83]\n\t"
    "ds_read_b64_tr_b16 a[220:221], v27 offset:24832\n\t"
    "v_mfma_f32_16x16x32_bf16 v[112:115], a[196:199], a[108:111], v[112:115]\n\t"
    "ds_read_b64_tr_b16 a[222:223], v27 offset:25856\n\t"
    "v_mfma_f32_16x16x32_bf16 v[112:115], a[192:195], a[252:255], v[112:115]\n\t"
    "s_add_u32 s61, s61, 0x80\n\t"
    "s_add_u32 s62, s62, 0x80\n\t"
    "v_mfma_f32_16x16x32_bf16 v[144:147], a[192:195], v[0:3], v[144:147]\n\t"
    "v_mfma_f32_16x16x32_bf16 v[144:147], a[196:199], a[112:115], v[144:147]\n\t"
    "v_mfma_f32_16x16x32_bf16 v[176:179], a[196:199], a[116:119], v[176:179]\n\t"
    "v_mfma_f32_16x16x32_bf16 v[176:179], a[192:195], v[4:7], v[176:179]\n\t"
    "v_mfma_f32_16x16x32_bf16 v[208:211], a[192:195], v[8:11], v[208:211]\n\t"
    "s_waitcnt vmcnt(18) lgkmcnt(0)\n\t"
    "s_barrier\n\t"
    "v_mfma_f32_16x16x32_bf16 v[208:211], a[196:199], a[120:123], v[208:211]\n\t"
    "ds_read_b128 a[224:227], v24 offset:0\n\t"
    "s_add_u32 m0, s65, 0x24000\n\t"
    "v_mfma_f32_16x16x32_bf16 v[240:243], a[196:199], a[124:127], v[240:243]\n\t"
    "buffer_load_dwordx4 v17, s[40:43], s61 offen lds\n\t"
    "v_mfma_f32_16x16x32_bf16 v[240:243], a[192:195], v[12:15], v[240:243]\n\t"
    "ds_read_b64_tr_b16 a[192:193], v22 offset:0\n\t"
    "v_mfma_f32_16x16x32_bf16 v[212:215], a[200:203], v[8:11], v[212:215]\n\t"
    "ds_read_b64_tr_b16 a[194:195], v22 offset:1024\n\t"
    "v_mfma_f32_16x16x32_bf16 v[212:215], a[204:207], a[120:123], v[212:215]\n\t"
    "ds_read_b64_tr_b16 a[196:197], v22 offset:8192\n\t"
    "v_mfma_f32_16x16x32_bf16 v[180:183], a[204:207], a[116:119], v[180:183]\n\t"
    "ds_read_b64_tr_b16 a[198:199], v22 offset:9216\n\t"
    "v_mfma_f32_16x16x32_bf16 v[180:183], a[200:203], v[4:7], v[180:183]\n\t"
    "ds_read_b128 a[228:231], v24 offset:2048\n\t"
    "v_mfma_f32_16x16x32_bf16 v[148:151], a[200:203], v[0:3], v[148:151]\n\t"
    "ds_read_b128 a[232:235], v24 offset:4096\n\t"
    "s_add_u32 m0, s65, 0x24400\n\t"
    "v_mfma_f32_16x16x32_bf16 v[148:151], a[204:207], a[112:115], v[148:151]\n\t"
    "buffer_load_dwordx4 v18, s[40:43], s61 offen lds\n\t"
    "v_mfma_f32_16x16x32_bf16 v[116:119], a[204:207], a[108:111], v[116:119]\n\t"
    "ds_read_b128 a[236:239], v24 offset:6144\n\t"
    "v_mfma_f32_16x16x32_bf16 v[116:119], a[200:203], a[252:255], v[116:119]\n\t"
    "ds_read_b128 a[240:243], v24 offset:8192\n\t"
    "v_mfma_f32_16x16x32_bf16 v[84:87], a[200:203], a[248:251], v[84:87]\n\t"
    "ds_read_b128 a[244:247], v25 offset:0\n\t"
    "v_mfma_f32_16x16x32_bf16 v[84:87], a[204:207], a[104:107], v[84:87]\n\t"
    "ds_read_b64_tr_b16 a[200:201], v23 offset:0\n\t"
    "v_mfma_f32_16x16x32_bf16 v[88:91], a[208:211], a[248:251], v[88:91]\n\t"
    "ds_read_b64_tr_b16 a[202:203], v23 offset:1024\n\t"
    "v_mfma_f32_16x16x32_bf16 v[88:91], a[212:215], a[104:107], v[88:91]\n\t"
    "ds_read_b64_tr_b16 a[204:205], v23 offset:8192\n\t"
    "v_mfma_f32_16x16x32_bf16 v[120:123], a[212:215], a[108:111], v[120:123]\n\t"
    "ds_read_b64_tr_b16 a[206:207], v23 offset:9216\n\t"
    "s_add_u32 m0, s65, 0x26000\n\t"
    "v_mfma_f32_16x16x32_bf16 v[120:123], a[208:211], a[252:255], v[120:123]\n\t"
    "buffer_load_dwordx4 v17, s[40:43], s62 offen lds\n\t"
    "v_mfma_f32_16x16x32_bf16 v[152:155], a[208:211], v[0:3], v[152:155]\n\t"
    "ds_read_b128 v[40:43], v24 offset:512\n\t"
    "v_mfma_f32_16x16x32_bf16 v[152:155], a[212:215], a[112:115], v[152:155]\n\t"
    "ds_read_b128 v[44:47], v24 offset:2560\n\t"
    "s_add_u32 m0, s65, 0x26400\n\t"
    "v_mfma_f32_16x16x32_bf16 v[184:187], a[212:215], a[116:119], v[184:187]\n\t"
    "buffer_load_dwordx4 v18, s[40:43], s62 offen lds\n\t"
    "v_mfma_f32_16x16x32_bf16 v[184:187], a[208:211], v[4:7], v[184:187]\n\t"
    "ds_read_b128 v[48:51], v24 offset:4608\n\t"
    "v_mfma_f32_16x16x32_bf16 v[216:219], a[208:211], v[8:11], v[216:219]\n\t"
    "ds_read_b128 v[52:55], v24 offset:6656\n\t"
    "v_mfma_f32_16x16x32_bf16 v[216:219], a[212:215], a[120:123], v[216:219]\n\t"
    "ds_read_b128 v[56:59], v24 offset:8704\n\t"
    "s_add_u32 m0, s65, 0x4000\n\t"
    "s_mov_b32 s68, 0\n\t"
    "v_mfma_f32_16x16x32_bf16 v[248:251], a[212:215], a[124:127], v[248:251]\n\t"
    "buffer_load_dwordx4 v16, s[48:51], s68 offen nt lds\n\t"
    "v_mfma_f32_16x16x32_bf16 v[248:251], a[208:211], v[12:15], v[248:251]\n\t"
    "ds_read_b64_tr_b16 a[208:209], v22 offset:256\n\t"
    "v_mfma_f32_16x16x32_bf16 v[220:223], a[216:219], v[8:11], v[220:223]\n\t"
    "ds_read_b64_tr_b16 a[210:211], v22 offset:1280\n\t"
    "v_mfma_f32_16x16x32_bf16 v[220:223], a[220:223], a[120:123], v[220:223]\n\t"
    "ds_read_b64_tr_b16 a[212:213], v22 offset:8448\n\t"
    "v_mfma_f32_16x16x32_bf16 v[188:191], a[220:223], a[116:119], v[188:191]\n\t"
    "ds_read_b64_tr_b16 a[214:215], v22 offset:9472\n\t"
    "v_mfma_f32_16x16x32_bf16 v[188:191], a[216:219], v[4:7], v[188:191]\n\t"
    "ds_read_b128 v[60:63], v25 offset:512\n\t"
    "v_mfma_f32_16x16x32_bf16 v[156:159], a[216:219], v[0:3], v[156:159]\n\t"
    "v_mfma_f32_16x16x32_bf16 v[156:159], a[220:223], a[112:115], v[156:159]\n\t"
    "v_mfma_f32_16x16x32_bf16 v[124:127], a[220:223], a[108:111], v[124:127]\n\t"
    "buffer_load_dwordx4 v16, s[48:51], s68 offen offset:1024 nt lds\n\t"
    "v_mfma_f32_16x16x32_bf16 v[124:127], a[216:219], a[252:255], v[124:127]\n\t"
    "v_mfma_f32_16x16x32_bf16 v[92:95], a[216:219], a[248:251], v[92:95]\n\t"
    "v_mfma_f32_16x16x32_bf16 v[92:95], a[220:223], a[104:107], v[92:95]\n\t"
    "ds_read_b64_tr_b16 a[216:217], v23 offset:256\n\t"
    "s_waitcnt lgkmcnt(1)\n\t"
    "v_mfma_f32_16x16x32_bf16 v[64:67], a[192:195], a[224:227], v[64:67]\n\t"
    "ds_read_b64_tr_b16 a[218:219], v23 offset:1280\n\t"
    "v_mfma_f32_16x16x32_bf16 v[64:67], a[196:199], v[40:43], v[64:67]\n\t"
    "ds_read_b64_tr_b16 a[220:221], v23 offset:8448\n\t"
    "v_mfma_f32_16x16x32_bf16 v[96:99], a[196:199], v[44:47], v[96:99]\n\t"
    "ds_read_b64_tr_b16 a[222:223], v23 offset:9472\n\t"
    "v_mfma_f32_16x16x32_bf16 v[96:99], a[192:195], a[228:231], v[96:99]\n\t"
    "s_and_b32 s86, s84, 15\n\t"
    "s_lshr_b64 s[84:85], s[84:85], 4\n\t"
    "v_mfma_f32_16x16x32_bf16 v[128:131], a[192:195], a[232:235], v[128:131]\n\t"
    "s_lshl_b32 s61, s86, 19\n\t"
    "s_lshl_b32 s60, s86, 7\n\t"
    "s_add_u32 s62, s61, 0x40000\n\t"
    "v_mfma_f32_16x16x32_bf16 v[128:131], a[196:199], v[48:51], v[128:131]\n\t"
    "v_mfma_f32_16x16x32_bf16 v[160:163], a[196:199], v[52:55], v[160:163]\n\t"
    "v_mfma_f32_16x16x32_bf16 v[160:163], a[192:195], a[236:239], v[160:163]\n\t"
    "v_mfma_f32_16x16x32_bf16 v[192:195], a[192:195], a[240:243], v[192:195]\n\t"
    "s_waitcnt vmcnt(20) lgkmcnt(0)\n\t"
    "s_barrier\n\t"
    "v_mfma_f32_16x16x32_bf16 v[192:195], a[196:199], v[56:59], v[192:195]\n\t"
    "s_add_u32 m0, s63, 0xc000\n\t"
    "v_mfma_f32_16x16x32_bf16 v[224:227], a[196:199], v[60:63], v[224:227]\n\t"
    "buffer_load_dwordx4 v19, s[36:39], s60 offen lds\n\t"
    "v_mfma_f32_16x16x32_bf16 v[224:227], a[192:195], a[244:247], v[224:227]\n\t"
    "ds_read_b64_tr_b16 a[192:193], v22 offset:16384\n\t"
    "v_mfma_f32_16x16x32_bf16 v[196:199], a[200:203], a[240:243], v[196:199]\n\t"
    "ds_read_b64_tr_b16 a[194:195], v22 offset:17408\n\t"
    "v_mfma_f32_16x16x32_bf16 v[196:199], a[204:207], v[56:59], v[196:199]\n\t"
    "ds_read_b64_tr_b16 a[196:197], v22 offset:24576\n\t"
    "v_mfma_f32_16x16x32_bf16 v[164:167], a[204:207], v[52:55], v[164:167]\n\t"
    "ds_read_b64_tr_b16 a[198:199], v22 offset:25600\n\t"
    "s_add_u32 m0, s65, 0x12000\n\t"
    "v_mfma_f32_16x16x32_bf16 v[164:167], a[200:203], a[236:239], v[164:167]\n\t"
    "buffer_load_dwordx4 v17, s[40:43], s61 offen lds\n\t"
    "v_mfma_f32_16x16x32_bf16 v[132:135], a[200:203], a[232:235], v[132:135]\n\t"
    "s_add_u32 m0, s63, 0xd000\n\t"
    "v_mfma_f32_16x16x32_bf16 v[132:135], a[204:207], v[48:51], v[132:135]\n\t"
    "buffer_load_dwordx4 v20, s[36:39], s60 offen lds\n\t"
    "v_mfma_f32_16x16x32_bf16 v[100:103], a[204:207], v[44:47], v[100:103]\n\t"
    "v_mfma_f32_16x16x32_bf16 v[100:103], a[200:203], a[228:231], v[100:103]\n\t"
    "s_add_u32 m0, s63, 0xe000\n\t"
    "v_mfma_f32_16x16x32_bf16 v[68:71], a[200:203], a[224:227], v[68:71]\n\t"
    "buffer_load_dwordx4 v21, s[36:39], s60 offen lds\n\t"
    "v_mfma_f32_16x16x32_bf16 v[68:71], a[204:207], v[40:43], v[68:71]\n\t"
    "ds_read_b64_tr_b16 a[200:201], v23 offset:16384\n\t"
    "v_mfma_f32_16x16x32_bf16 v[72:75], a[208:211], a[224:227], v[72:75]\n\t"
    "ds_read_b64_tr_b16 a[202:203], v23 offset:17408\n\t"
    "v_mfma_f32_16x16x32_bf16 v[72:75], a[212:215], v[40:43], v[72:75]\n\t"
    "ds_read_b64_tr_b16 a[204:205], v23 offset:24576\n\t"
    "v_mfma_f32_16x16x32_bf16 v[104:107], a[212:215], v[44:47], v[104:107]\n\t"
    "ds_read_b64_tr_b16 a[206:207], v23 offset:25600\n\t"
    "s_add_u32 m0, s65, 0x12400\n\t"
    "v_mfma_f32_16x16x32_bf16 v[104:107], a[208:211], a[228:231], v[104:107]\n\t"
    "buffer_load_dwordx4 v18, s[40:43], s61 offen lds\n\t"
    "v_mfma_f32_16x16x32_bf16 v[136:139], a[208:211], a[232:235], v[136:139]\n\t"
    "s_add_u32 m0, s63, 0xf000\n\t"
    "v_mfma_f32_16x16x32_bf16 v[136:139], a[212:215], v[48:51], v[136:139]\n\t"
    "buffer_load_dwordx4 v32, s[36:39], s60 offen lds\n\t"
    "v_mfma_f32_16x16x32_bf16 v[168:171], a[212:215], v[52:55], v[168:171]\n\t"
    "v_mfma_f32_16x16x32_bf16 v[168:171], a[208:211], a[236:239], v[168:171]\n\t"
    "v_mfma_f32_16x16x32_bf16 v[200:203], a[208:211], a[240:243], v[200:203]\n\t"
    "s_add_u32 m0, s65, 0x14000\n\t"
    "v_mfma_f32_16x16x32_bf16 v[200:203], a[212:215], v[56:59], v[200:203]\n\t"
    "buffer_load_dwordx4 v17, s[40:43], s62 offen lds\n\t"
    "v_mfma_f32_16x16x32_bf16 v[232:235], a[212:215], v[60:63], v[232:235]\n\t"
    "v_mfma_f32_16x16x32_bf16 v[232:235], a[208:211], a[244:247], v[232:235]\n\t"
    "ds_read_b64_tr_b16 a[208:209], v22 offset:16640\n\t"
    "v_mfma_f32_16x16x32_bf16 v[204:207], a[216:219], a[240:243], v[204:207]\n\t"
    "ds_read_b64_tr_b16 a[210:211], v22 offset:17664\n\t"
    "v_mfma_f32_16x16x32_bf16 v[204:207], a[220:223], v[56:59], v[204:207]\n\t"
    "ds_read_b64_tr_b16 a[212:213], v22 offset:24832\n\t"
    "v_mfma_f32_16x16x32_bf16 v[172:175], a[220:223], v[52:55], v[172:175]\n\t"
    "ds_read_b64_tr_b16 a[214:215], v22 offset:25856\n\t"
    "s_add_u32 m0, s63, 0x10000\n\t"
    "v_mfma_f32_16x16x32_bf16 v[172:175], a[216:219], a[236:239], v[172:175]\n\t"
    "buffer_load_dwordx4 v33, s[36:39], s60 offen lds\n\t"
    "v_mfma_f32_16x16x32_bf16 v[140:143], a[216:219], a[232:235], v[140:143]\n\t"
    "s_add_u32 m0, s63, 0x11000\n\t"
    "v_mfma_f32_16x16x32_bf16 v[140:143], a[220:223], v[48:51], v[140:143]\n\t"
    "buffer_load_dwordx4 v34, s[36:39], s60 offen lds\n\t"
    "v_mfma_f32_16x16x32_bf16 v[108:111], a[220:223], v[44:47], v[108:111]\n\t"
    "s_add_u32 m0, s65, 0x14400\n\t"
    "v_mfma_f32_16x16x32_bf16 v[108:111], a[216:219], a[228:231], v[108:111]\n\t"
    "buffer_load_dwordx4 v18, s[40:43], s62 offen lds\n\t"
    "v_mfma_f32_16x16x32_bf16 v[76:79], a[216:219], a[224:227], v[76:79]\n\t"
    "v_mfma_f32_16x16x32_bf16 v[76:79], a[220:223], v[40:43], v[76:79]\n\t"
    "ds_read_b64_tr_b16 a[216:217], v23 offset:16640\n\t"
    "s_waitcnt lgkmcnt(5)\n\t"
    "v_mfma_f32_16x16x32_bf16 v[80:83], a[192:195], a[224:227], v[80:83]\n\t"
    "ds_read_b64_tr_b16 a[218:219], v23 offset:17664\n\t"
    "v_mfma_f32_16x16x32_bf16 v[80:83], a[196:199], v[40:43], v[80:83]\n\t"
    "ds_read_b64_tr_b16 a[220:221], v23 offset:24832\n\t"
    "v_mfma_f32_16x16x32_bf16 v[112:115], a[196:199], v[44:47], v[112:115]\n\t"
    "ds_read_b64_tr_b16 a[222:223], v23 offset:25856\n\t"
    "v_mfma_f32_16x16x32_bf16 v[112:115], a[192:195], a[228:231], v[112:115]\n\t"
    "s_add_u32 s61, s61, 0x80\n\t"
    "s_add_u32 s62, s62, 0x80\n\t"
    "v_mfma_f32_16x16x32_bf16 v[144:147], a[192:195], a[232:235], v[144:147]\n\t"
    "v_mfma_f32_16x16x32_bf16 v[144:147], a[196:199], v[48:51], v[144:147]\n\t"
    "v_mfma_f32_16x16x32_bf16 v[176:179], a[196:199], v[52:55], v[176:179]\n\t"
    "v_mfma_f32_16x16x32_bf16 v[176:179], a[192:195], a[236:239], v[176:179]\n\t"
    "v_mfma_f32_16x16x32_bf16 v[208:211], a[192:195], a[240:243], v[208:211]\n\t"
    "s_waitcnt vmcnt(16) lgkmcnt(0)\n\t"
    "s_barrier\n\t"
    "v_mfma_f32_16x16x32_bf16 v[208:211], a[196:199], v[56:59], v[208:211]\n\t"
    "ds_read_b128 a[248:251], v28 offset:0\n\t"
    "s_add_u32 m0, s65, 0x16000\n\t"
    "v_mfma_f32_16x16x32_bf16 v[240:243], a[196:199], v[60:63], v[240:243]\n\t"
    "buffer_load_dwordx4 v17, s[40:43], s61 offen lds\n\t"
    "v_mfma_f32_16x16x32_bf16 v[240:243], a[192:195], a[244:247], v[240:243]\n\t"
    "ds_read_b64_tr_b16 a[192:193], v26 offset:0\n\t"
    "v_mfma_f32_16x16x32_bf16 v[212:215], a[200:203], a[240:243], v[212:215]\n\t"
    "ds_read_b64_tr_b16 a[194:195], v26 offset:1024\n\t"
    "v_mfma_f32_16x16x32_bf16 v[212:215], a[204:207], v[56:59], v[212:215]\n\t"
    "ds_read_b64_tr_b16 a[196:197], v26 offset:8192\n\t"
    "v_mfma_f32_16x16x32_bf16 v[180:183], a[204:207], v[52:55], v[180:183]\n\t"
    "ds_read_b64_tr_b16 a[198:199], v26 offset:9216\n\t"
    "s_add_u32 m0, s65, 0x16400\n\t"
    "v_mfma_f32_16x16x32_bf16 v[180:183], a[200:203], a[236:239], v[180:183]\n\t"
    "buffer_load_dwordx4 v18, s[40:43], s61 offen lds\n\t"
    "v_mfma_f32_16x16x32_bf16 v[148:151], a[200:203], a[232:235], v[148:151]\n\t"
    "ds_read_b128 a[252:255], v28 offset:2048\n\t"
    "v_mfma_f32_16x16x32_bf16 v[148:151], a[204:207], v[48:51], v[148:151]\n\t"
    "ds_read_b128 v[0:3], v28 offset:4096\n\t"
    "v_mfma_f32_16x16x32_bf16 v[116:119], a[204:207], v[44:47], v[116:119]\n\t"
    "ds_read_b128 v[4:7], v28 offset:6144\n\t"
    "s_add_u32 m0, s65, 0x18000\n\t"
    "v_mfma_f32_16x16x32_bf16 v[116:119], a[200:203], a[228:231], v[116:119]\n\t"
    "buffer_load_dwordx4 v17, s[40:43], s62 offen lds\n\t"
    "v_mfma_f32_16x16x32_bf16 v[84:87], a[200:203], a[224:227], v[84:87]\n\t"
    "ds_read_b128 v[8:11], v28 offset:8192\n\t"
    "v_mfma_f32_16x16x32_bf16 v[84:87], a[204:207], v[40:43], v[84:87]\n\t"
    "ds_read_b64_tr_b16 a[200:201], v27 offset:0\n\t"
    "v_mfma_f32_16x16x32_bf16 v[88:91], a[208:211], a[224:227], v[88:91]\n\t"
    "ds_read_b64_tr_b16 a[202:203], v27 offset:1024\n\t"
    "v_mfma_f32_16x16x32_bf16 v[88:91], a[212:215], v[40:43], v[88:91]\n\t"
    "ds_read_b64_tr_b16 a[204:205], v27 offset:8192\n\t"
    "v_mfma_f32_16x16x32_bf16 v[120:123], a[212:215], v[44:47], v[120:123]\n\t"
    "ds_read_b64_tr_b16 a[206:207], v27 offset:9216\n\t"
    "s_add_u32 m0, s65, 0x18400\n\t"
    "v_mfma_f32_16x16x32_bf16 v[120:123], a[208:211], a[228:231], v[120:123]\n\t"
    "buffer_load_dwordx4 v18, s[40:43], s62 offen lds\n\t"
    "v_mfma_f32_16x16x32_bf16 v[152:155], a[208:211], a[232:235], v[152:155]\n\t"
    "ds_read_b128 v[12:15], v35 offset:0\n\t"
    "v_mfma_f32_16x16x32_bf16 v[152:155], a[212:215], v[48:51], v[152:155]\n\t"
    "ds_read_b128 a[104:107], v28 offset:512\n\t"
    "s_add_u32 m0, s65, 0x6000\n\t"
    "s_mov_b32 s68, 0x20000\n\t"
    "v_mfma_f32_16x16x32_bf16 v[184:187], a[212:215], v[52:55], v[184:187]\n\t"
    "buffer_load_dwordx4 v16, s[48:51], s68 offen nt lds\n\t"
    "v_mfma_f32_16x16x32_bf16 v[184:187], a[208:211], a[236:239], v[184:187]\n\t"
    "ds_read_b128 a[108:111], v28 offset:2560\n\t"
    "v_mfma_f32_16x16x32_bf16 v[216:219], a[208:211], a[240:243], v[216:219]\n\t"
    "ds_read_b128 a[112:115], v28 offset:4608\n\t"
    "v_mfma_f32_16x16x32_bf16 v[216:219], a[212:215], v[56:59], v[216:219]\n\t"
    "ds_read_b128 a[116:119], v28 offset:6656\n\t"
    "v_mfma_f32_16x16x32_bf16 v[248:251], a[212:215], v[60:63], v[248:251]\n\t"
    "buffer_load_dwordx4 v16, s[48:51], s68 offen offset:1024 nt lds\n\t"
    "v_mfma_f32_16x16x32_bf16 v[248:251], a[208:211], a[244:247], v[248:251]\n\t"
    "ds_read_b64_tr_b16 a[208:209], v26 offset:256\n\t"
    "v_mfma_f32_16x16x32_bf16 v[220:223], a[216:219], a[240:243], v[220:223]\n\t"
    "ds_read_b64_tr_b16 a[210:211], v26 offset:1280\n\t"
    "v_mfma_f32_16x16x32_bf16 v[220:223], a[220:223], v[56:59], v[220:223]\n\t"
    "ds_read_b64_tr_b16 a[212:213], v26 offset:8448\n\t"
    "v_mfma_f32_16x16x32_bf16 v[188:191], a[220:223], v[52:55], v[188:191]\n\t"
    "ds_read_b64_tr_b16 a[214:215], v26 offset:9472\n\t"
    "s_add_u32 m0, s65, 0x8000\n\t"
    "s_mov_b32 s68, 0x2000\n\t"
    "v_mfma_f32_16x16x32_bf16 v[188:191], a[216:219], a[236:239], v[188:191]\n\t"
    "buffer_load_dwordx4 v16, s[44:47], s68 offen nt lds\n\t"
    "v_mfma_f32_16x16x32_bf16 v[156:159], a[216:219], a[232:235], v[156:159]\n\t"
    "ds_read_b128 a[120:123], v28 offset:8704\n\t"
    "v_mfma_f32_16x16x32_bf16 v[156:159], a[220:223], v[48:51], v[156:159]\n\t"
    "ds_read_b128 a[124:127], v35 offset:512\n\t"
    "v_mfma_f32_16x16x32_bf16 v[124:127], a[220:223], v[44:47], v[124:127]\n\t"
    "buffer_load_dwordx4 v16, s[44:47], s68 offen offset:1024 nt lds\n\t"
    "v_mfma_f32_16x16x32_bf16 v[124:127], a[216:219], a[228:231], v[124:127]\n\t"
    "v_mfma_f32_16x16x32_bf16 v[92:95], a[216:219], a[224:227], v[92:95]\n\t"
    "v_mfma_f32_16x16x32_bf16 v[92:95], a[220:223], v[40:43], v[92:95]\n\t"
    "ds_read_b64_tr_b16 a[216:217], v27 offset:256\n\t"
    "s_waitcnt lgkmcnt(1)\n\t"
    "v_mfma_f32_16x16x32_bf16 v[64:67], a[192:195], a[248:251], v[64:67]\n\t"
    "ds_read_b64_tr_b16 a[218:219], v27 offset:1280\n\t"
    "v_mfma_f32_16x16x32_bf16 v[64:67], a[196:199], a[104:107], v[64:67]\n\t"
    "ds_read_b64_tr_b16 a[220:221], v27 offset:8448\n\t"
    "v_mfma_f32_16x16x32_bf16 v[96:99], a[196:199], a[108:111], v[96:99]\n\t"
    "ds_read_b64_tr_b16 a[222:223], v27 offset:9472\n\t"
    "v_mfma_f32_16x16x32_bf16 v[96:99], a[192:195], a[252:255], v[96:99]\n\t"
    "s_and_b32 s86, s84, 15\n\t"
    "s_lshr_b64 s[84:85], s[84:85], 4\n\t"
    "v_mfma_f32_16x16x32_bf16 v[128:131], a[192:195], v[0:3], v[128:131]\n\t"
    "s_lshl_b32 s61, s86, 19\n\t"
    "s_lshl_b32 s60, s86, 7\n\t"
    "s_add_u32 s62, s61, 0x40000\n\t"
    "v_mfma_f32_16x16x32_bf16 v[128:131], a[196:199], a[112:115], v[128:131]\n\t"
    "v_mfma_f32_16x16x32_bf16 v[160:163], a[196:199], a[116:119], v[160:163]\n\t"
    "v_mfma_f32_16x16x32_bf16 v[160:163], a[192:195], v[4:7], v[160:163]\n\t"
    "v_mfma_f32_16x16x32_bf16 v[192:195], a[192:195], v[8:11], v[192:195]\n\t"
    "s_waitcnt vmcnt(20) lgkmcnt(0)\n\t"
    "s_barrier\n\t"
    "v_mfma_f32_16x16x32_bf16 v[192:195], a[196:199], a[120:123], v[192:195]\n\t"
    "s_add_u32 m0, s63, 0x1a000\n\t"
    "v_mfma_f32_16x16x32_bf16 v[224:227], a[196:199], a[124:127], v[224:227]\n\t"
    "buffer_load_dwordx4 v19, s[36:39], s60 offen lds\n\t"
    "v_mfma_f32_16x16x32_bf16 v[224:227], a[192:195], v[12:15], v[224:227]\n\t"
    "ds_read_b64_tr_b16 a[192:193], v26 offset:16384\n\t"
    "v_mfma_f32_16x16x32_bf16 v[196:199], a[200:203], v[8:11], v[196:199]\n\t"
    "ds_read_b64_tr_b16 a[194:195], v26 offset:17408\n\t"
    "v_mfma_f32_16x16x32_bf16 v[196:199], a[204:207], a[120:123], v[196:199]\n\t"
    "ds_read_b64_tr_b16 a[196:197], v26 offset:24576\n\t"
    "v_mfma_f32_16x16x32_bf16 v[164:167], a[204:207], a[116:119], v[164:167]\n\t"
    "ds_read_b64_tr_b16 a[198:199], v26 offset:25600\n\t"
    "s_add_u32 m0, s65, 0x20000\n\t"
    "v_mfma_f32_16x16x32_bf16 v[164:167], a[200:203], v[4:7], v[164:167]\n\t"
    "buffer_load_dwordx4 v17, s[40:43], s61 offen lds\n\t"
    "v_mfma_f32_16x16x32_bf16 v[132:135], a[200:203], v[0:3], v[132:135]\n\t"
    "s_add_u32 m0, s63, 0x1b000\n\t"
    "v_mfma_f32_16x16x32_bf16 v[132:135], a[204:207], a[112:115], v[132:135]\n\t"
    "buffer_load_dwordx4 v20, s[36:39], s60 offen lds\n\t"
    "v_mfma_f32_16x16x32_bf16 v[100:103], a[204:207], a[108:111], v[100:103]\n\t"
    "v_mfma_f32_16x16x32_bf16 v[100:103], a[200:203], a[252:255], v[100:103]\n\t"
    "s_add_u32 m0, s63, 0x1c000\n\t"
    "v_mfma_f32_16x16x32_bf16 v[68:71], a[200:203], a[248:251], v[68:71]\n\t"
    "buffer_load_dwordx4 v21, s[36:39], s60 offen lds\n\t"
    "v_mfma_f32_16x16x32_bf16 v[68:71], a[204:207], a[104:107], v[68:71]\n\t"
    "ds_read_b64_tr_b16 a[200:201], v27 offset:16384\n\t"
    "v_mfma_f32_16x16x32_bf16 v[72:75], a[208:211], a[248:251], v[72:75]\n\t"
    "ds_read_b64_tr_b16 a[202:203], v27 offset:17408\n\t"
    "v_mfma_f32_16x16x32_bf16 v[72:75], a[212:215], a[104:107], v[72:75]\n\t"
    "ds_read_b64_tr_b16 a[204:205], v27 offset:24576\n\t"
    "v_mfma_f32_16x16x32_bf16 v[104:107], a[212:215], a[108:111], v[104:107]\n\t"
    "ds_read_b64_tr_b16 a[206:207], v27 offset:25600\n\t"
    "s_add_u32 m0, s65, 0x20400\n\t"
    "v_mfma_f32_16x16x32_bf16 v[104:107], a[208:211], a[252:255], v[104:107]\n\t"
    "buffer_load_dwordx4 v18, s[40:43], s61 offen lds\n\t"
    "v_mfma_f32_16x16x32_bf16 v[136:139], a[208:211], v[0:3], v[136:139]\n\t"
    "s_add_u32 m0, s63, 0x1d000\n\t"
    "v_mfma_f32_16x16x32_bf16 v[136:139], a[212:215], a[112:115], v[136:139]\n\t"
    "buffer_load_dwordx4 v32, s[36:39], s60 offen lds\n\t"
    "v_mfma_f32_16x16x32_bf16 v[168:171], a[212:215], a[116:119], v[168:171]\n\t"
    "v_mfma_f32_16x16x32_bf16 v[168:171], a[208:211], v[4:7], v[168:171]\n\t"
    "v_mfma_f32_16x16x32_bf16 v[200:203], a[208:211], v[8:11], v[200:203]\n\t"
    "s_add_u32 m0, s65, 0x22000\n\t"
    "v_mfma_f32_16x16x32_bf16 v[200:203], a[212:215], a[120:123], v[200:203]\n\t"
    "buffer_load_dwordx4 v17, s[40:43], s62 offen lds\n\t"
    "v_mfma_f32_16x16x32_bf16 v[232:235], a[212:215], a[124:127], v[232:235]\n\t"
    "v_mfma_f32_16x16x32_bf16 v[232:235], a[208:211], v[12:15], v[232:235]\n\t"
    "ds_read_b64_tr_b16 a[208:209], v26 offset:16640\n\t"
    "v_mfma_f32_16x16x32_bf16 v[204:207], a[216:219], v[8:11], v[204:207]\n\t"
    "ds_read_b64_tr_b16 a[210:211], v26 offset:17664\n\t"
    "v_mfma_f32_16x16x32_bf16 v[204:207], a[220:223], a[120:123], v[204:207]\n\t"
    "ds_read_b64_tr_b16 a[212:213], v26 offset:24832\n\t"
    "v_mfma_f32_16x16x32_bf16 v[172:175], a[220:223], a[116:119], v[172:175]\n\t"
    "ds_read_b64_tr_b16 a[214:215], v26 offset:25856\n\t"
    "s_add_u32 m0, s63, 0x1e000\n\t"
    "v_mfma_f32_16x16x32_bf16 v[172:175], a[216:219], v[4:7], v[172:175]\n\t"
    "buffer_load_dwordx4 v33, s[36:39], s60 offen lds\n\t"
    "v_mfma_f32_16x16x32_bf16 v[140:143], a[216:219], v[0:3], v[140:143]\n\t"
    "s_add_u32 m0, s63, 0x1f000\n\t"
    "v_mfma_f32_16x16x32_bf16 v[140:143], a[220:223], a[112:115], v[140:143]\n\t"
    "buffer_load_dwordx4 v34, s[36:39], s60 offen lds\n\t"
    "v_mfma_f32_16x16x32_bf16 v[108:111], a[220:223], a[108:111], v[108:111]\n\t"
    "s_add_u32 m0, s65, 0x22400\n\t"
    "v_mfma_f32_16x16x32_bf16 v[108:111], a[216:219], a[252:255], v[108:111]\n\t"
    "buffer_load_dwordx4 v18, s[40:43], s62 offen lds\n\t"
    "v_mfma_f32_16x16x32_bf16 v[76:79], a[216:219], a[248:251], v[76:79]\n\t"
    "v_mfma_f32_16x16x32_bf16 v[76:79], a[220:223], a[104:107], v[76:79]\n\t"
    "ds_read_b64_tr_b16 a[216:217], v27 offset:16640\n\t"
    "s_waitcnt lgkmcnt(5)\n\t"
    "v_mfma_f32_16x16x32_bf16 v[80:83], a[192:195], a[248:251], v[80:83]\n\t"
    "ds_read_b64_tr_b16 a[218:219], v27 offset:17664\n\t"
    "v_mfma_f32_16x16x32_bf16 v[80:83], a[196:199], a[104:107], v[80:83]\n\t"
    "ds_read_b64_tr_b16 a[220:221], v27 offset:24832\n\t"
    "v_mfma_f32_16x16x32_bf16 v[112:115], a[196:199], a[108:111], v[112:115]\n\t"
    "ds_read_b64_tr_b16 a[222:223], v27 offset:25856\n\t"
    "v_mfma_f32_16x16x32_bf16 v[112:115], a[192:195], a[252:255], v[112:115]\n\t"
    "s_add_u32 s61, s61, 0x80\n\t"
    "s_add_u32 s62, s62, 0x80\n\t"
    "v_mfma_f32_16x16x32_bf16 v[144:147], a[192:195], v[0:3], v[144:147]\n\t"
    "v_mfma_f32_16x16x32_bf16 v[144:147], a[196:199], a[112:115], v[144:147]\n\t"
    "v_mfma_f32_16x16x32_bf16 v[176:179], a[196:199], a[116:119], v[176:179]\n\t"
    "v_mfma_f32_16x16x32_bf16 v[176:179], a[192:195], v[4:7], v[176:179]\n\t"
    "v_mfma_f32_16x16x32_bf16 v[208:211], a[192:195], v[8:11], v[208:211]\n\t"
    "s_waitcnt vmcnt(18) lgkmcnt(0)\n\t"
    "s_barrier\n\t"
    "v_mfma_f32_16x16x32_bf16 v[208:211], a[196:199], a[120:123], v[208:211]\n\t"
    "ds_read_b128 a[224:227], v24 offset:0\n\t"
    "s_add_u32 m0, s65, 0x24000\n\t"
    "v_mfma_f32_16x16x32_bf16 v[240:243], a[196:199], a[124:127], v[240:243]\n\t"
    "buffer_load_dwordx4 v17, s[40:43], s61 offen lds\n\t"
    "v_mfma_f32_16x16x32_bf16 v[240:243], a[192:195], v[12:15], v[240:243]\n\t"
    "ds_read_b64_tr_b16 a[192:193], v22 offset:0\n\t"
    "v_mfma_f32_16x16x32_bf16 v[212:215], a[200:203], v[8:11], v[212:215]\n\t"
    "ds_read_b64_tr_b16 a[194:195], v22 offset:1024\n\t"
    "v_mfma_f32_16x16x32_bf16 v[212:215], a[204:207], a[120:123], v[212:215]\n\t"
    "ds_read_b64_tr_b16 a[196:197], v22 offset:8192\n\t"
    "v_mfma_f32_16x16x32_bf16 v[180:183], a[204:207], a[116:119], v[180:183]\n\t"
    "ds_read_b64_tr_b16 a[198:199], v22 offset:9216\n\t"
    "v_mfma_f32_16x16x32_bf16 v[180:183], a[200:203], v[4:7], v[180:183]\n\t"
    "ds_read_b128 a[228:231], v24 offset:2048\n\t"
    "v_mfma_f32_16x16x32_bf16 v[148:151], a[200:203], v[0:3], v[148:151]\n\t"
    "ds_read_b128 a[232:235], v24 offset:4096\n\t"
    "s_add_u32 m0, s65, 0x24400\n\t"
    "v_mfma_f32_16x16x32_bf16 v[148:151], a[204:207], a[112:115], v[148:151]\n\t"
    "buffer_load_dwordx4 v18, s[40:43], s61 offen lds\n\t"
    "v_mfma_f32_16x16x32_bf16 v[116:119], a[204:207], a[108:111], v[116:119]\n\t"
    "ds_read_b128 a[236:239], v24 offset:6144\n\t"
    "v_mfma_f32_16x16x32_bf16 v[116:119], a[200:203], a[252:255], v[116:119]\n\t"
    "ds_read_b128 a[240:243], v24 offset:8192\n\t"
    "v_mfma_f32_16x16x32_bf16 v[84:87], a[200:203], a[248:251], v[84:87]\n\t"
    "ds_read_b128 a[244:247], v25 offset:0\n\t"
    "v_mfma_f32_16x16x32_bf16 v[84:87], a[204:207], a[104:107], v[84:87]\n\t"
    "ds_read_b64_tr_b16 a[200:201], v23 offset:0\n\t"
    "v_mfma_f32_16x16x32_bf16 v[88:91], a[208:211], a[248:251], v[88:91]\n\t"
    "ds_read_b64_tr_b16 a[202:203], v23 offset:1024\n\t"
    "v_mfma_f32_16x16x32_bf16 v[88:91], a[212:215], a[104:107], v[88:91]\n\t"
    "ds_read_b64_tr_b16 a[204:205], v23 offset:8192\n\t"
    "v_mfma_f32_16x16x32_bf16 v[120:123], a[212:215], a[108:111], v[120:123]\n\t"
    "ds_read_b64_tr_b16 a[206:207], v23 offset:9216\n\t"
    "s_add_u32 m0, s65, 0x26000\n\t"
    "v_mfma_f32_16x16x32_bf16 v[120:123], a[208:211], a[252:255], v[120:123]\n\t"
    "buffer_load_dwordx4 v17, s[40:43], s62 offen lds\n\t"
    "v_mfma_f32_16x16x32_bf16 v[152:155], a[208:211], v[0:3], v[152:155]\n\t"
    "ds_read_b128 v[40:43], v24 offset:512\n\t"
    "v_mfma_f32_16x16x32_bf16 v[152:155], a[212:215], a[112:115], v[152:155]\n\t"
    "ds_read_b128 v[44:47], v24 offset:2560\n\t"
    "s_add_u32 m0, s65, 0x26400\n\t"
    "v_mfma_f32_16x16x32_bf16 v[184:187], a[212:215], a[116:119], v[184:187]\n\t"
    "buffer_load_dwordx4 v18, s[40:43], s62 offen lds\n\t"
    "v_mfma_f32_16x16x32_bf16 v[184:187], a[208:211], v[4:7], v[184:187]\n\t"
    "ds_read_b128 v[48:51], v24 offset:4608\n\t"
    "v_mfma_f32_16x16x32_bf16 v[216:219], a[208:211], v[8:11], v[216:219]\n\t"
    "ds_read_b128 v[52:55], v24 offset:6656\n\t"
    "v_mfma_f32_16x16x32_bf16 v[216:219], a[212:215], a[120:123], v[216:219]\n\t"
    "ds_read_b128 v[56:59], v24 offset:8704\n\t"
    "s_add_u32 m0, s65, 0xa000\n\t"
    "s_mov_b32 s68, 0x22000\n\t"
    "v_mfma_f32_16x16x32_bf16 v[248:251], a[212:215], a[124:127], v[248:251]\n\t"
    "buffer_load_dwordx4 v16, s[44:47], s68 offen nt lds\n\t"
    "v_mfma_f32_16x16x32_bf16 v[248:251], a[208:211], v[12:15], v[248:251]\n\t"
    "ds_read_b64_tr_b16 a[208:209], v22 offset:256\n\t"
    "v_mfma_f32_16x16x32_bf16 v[220:223], a[216:219], v[8:11], v[220:223]\n\t"
    "ds_read_b64_tr_b16 a[210:211], v22 offset:1280\n\t"
    "v_mfma_f32_16x16x32_bf16 v[220:223], a[220:223], a[120:123], v[220:223]\n\t"
    "ds_read_b64_tr_b16 a[212:213], v22 offset:8448\n\t"
    "v_mfma_f32_16x16x32_bf16 v[188:191], a[220:223], a[116:119], v[188:191]\n\t"
    "ds_read_b64_tr_b16 a[214:215], v22 offset:9472\n\t"
    "v_mfma_f32_16x16x32_bf16 v[188:191], a[216:219], v[4:7], v[188:191]\n\t"
    "ds_read_b128 v[60:63], v25 offset:512\n\t"
    "v_mfma_f32_16x16x32_bf16 v[156:159], a[216:219], v[0:3], v[156:159]\n\t"
    "v_mfma_f32_16x16x32_bf16 v[156:159], a[220:223], a[112:115], v[156:159]\n\t"
    "v_mfma_f32_16x16x32_bf16 v[124:127], a[220:223], a[108:111], v[124:127]\n\t"
    "buffer_load_dwordx4 v16, s[44:47], s68 offen offset:1024 nt lds\n\t"
    "v_mfma_f32_16x16x32_bf16 v[124:127], a[216:219], a[252:255], v[124:127]\n\t"
    "v_mfma_f32_16x16x32_bf16 v[92:95], a[216:219], a[248:251], v[92:95]\n\t"
    "v_mfma_f32_16x16x32_bf16 v[92:95], a[220:223], a[104:107], v[92:95]\n\t"
    "ds_read_b64_tr_b16 a[216:217], v23 offset:256\n\t"
    "s_waitcnt lgkmcnt(1)\n\t"
    "v_mfma_f32_16x16x32_bf16 v[64:67], a[192:195], a[224:227], v[64:67]\n\t"
    "ds_read_b64_tr_b16 a[218:219], v23 offset:1280\n\t"
    "v_mfma_f32_16x16x32_bf16 v[64:67], a[196:199], v[40:43], v[64:67]\n\t"
    "ds_read_b64_tr_b16 a[220:221], v23 offset:8448\n\t"
    "v_mfma_f32_16x16x32_bf16 v[96:99], a[196:199], v[44:47], v[96:99]\n\t"
    "ds_read_b64_tr_b16 a[222:223], v23 offset:9472\n\t"
    "v_mfma_f32_16x16x32_bf16 v[96:99], a[192:195], a[228:231], v[96:99]\n\t"
    "s_and_b32 s86, s84, 15\n\t"
    "s_lshr_b64 s[84:85], s[84:85], 4\n\t"
    "v_mfma_f32_16x16x32_bf16 v[128:131], a[192:195], a[232:235], v[128:131]\n\t"
    "s_lshl_b32 s61, s86, 19\n\t"
    "s_lshl_b32 s60, s86, 7\n\t"
    "s_add_u32 s62, s61, 0x40000\n\t"
    "v_mfma_f32_16x16x32_bf16 v[128:131], a[196:199], v[48:51], v[128:131]\n\t"
    "v_mfma_f32_16x16x32_bf16 v[160:163], a[196:199], v[52:55], v[160:163]\n\t"
    "v_mfma_f32_16x16x32_bf16 v[160:163], a[192:195], a[236:239], v[160:163]\n\t"
    "v_mfma_f32_16x16x32_bf16 v[192:195], a[192:195], a[240:243], v[192:195]\n\t"
    "s_waitcnt vmcnt(20) lgkmcnt(0)\n\t"
    "s_barrier\n\t"
    "v_mfma_f32_16x16x32_bf16 v[192:195], a[196:199], v[56:59], v[192:195]\n\t"
    "s_add_u32 m0, s63, 0xc000\n\t"
    "v_mfma_f32_16x16x32_bf16 v[224:227], a[196:199], v[60:63], v[224:227]\n\t"
    "buffer_load_dwordx4 v19, s[36:39], s60 offen lds\n\t"
    "v_mfma_f32_16x16x32_bf16 v[224:227], a[192:195], a[244:247], v[224:227]\n\t"
    "ds_read_b64_tr_b16 a[192:193], v22 offset:16384\n\t"
    "v_mfma_f32_16x16x32_bf16 v[196:199], a[200:203], a[240:243], v[196:199]\n\t"
    "ds_read_b64_tr_b16 a[194:195], v22 offset:17408\n\t"
    "v_mfma_f32_16x16x32_bf16 v[196:199], a[204:207], v[56:59], v[196:199]\n\t"
    "ds_read_b64_tr_b16 a[196:197], v22 offset:24576\n\t"
    "v_mfma_f32_16x16x32_bf16 v[164:167], a[204:207], v[52:55], v[164:167]\n\t"
    "ds_read_b64_tr_b16 a[198:199], v22 offset:25600\n\t"
    "s_add_u32 m0, s65, 0x12000\n\t"
    "v_mfma_f32_16x16x32_bf16 v[164:167], a[200:203], a[236:239], v[164:167]\n\t"
    "buffer_load_dwordx4 v17, s[40:43], s61 offen lds\n\t"
    "v_mfma_f32_16x16x32_bf16 v[132:135], a[200:203], a[232:235], v[132:135]\n\t"
    "s_add_u32 m0, s63, 0xd000\n\t"
    "v_mfma_f32_16x16x32_bf16 v[132:135], a[204:207], v[48:51], v[132:135]\n\t"
    "buffer_load_dwordx4 v20, s[36:39], s60 offen lds\n\t"
    "v_mfma_f32_16x16x32_bf16 v[100:103], a[204:207], v[44:47], v[100:103]\n\t"
    "v_mfma_f32_16x16x32_bf16 v[100:103], a[200:203], a[228:231], v[100:103]\n\t"
    "s_add_u32 m0, s63, 0xe000\n\t"
    "v_mfma_f32_16x16x32_bf16 v[68:71], a[200:203], a[224:227], v[68:71]\n\t"
    "buffer_load_dwordx4 v21, s[36:39], s60 offen lds\n\t"
    "v_mfma_f32_16x16x32_bf16 v[68:71], a[204:207], v[40:43], v[68:71]\n\t"
    "ds_read_b64_tr_b16 a[200:201], v23 offset:16384\n\t"
    "v_mfma_f32_16x16x32_bf16 v[72:75], a[208:211], a[224:227], v[72:75]\n\t"
    "ds_read_b64_tr_b16 a[202:203], v23 offset:17408\n\t"
    "v_mfma_f32_16x16x32_bf16 v[72:75], a[212:215], v[40:43], v[72:75]\n\t"
    "ds_read_b64_tr_b16 a[204:205], v23 offset:24576\n\t"
    "v_mfma_f32_16x16x32_bf16 v[104:107], a[212:215], v[44:47], v[104:107]\n\t"
    "ds_read_b64_tr_b16 a[206:207], v23 offset:25600\n\t"
    "s_add_u32 m0, s65, 0x12400\n\t"
    "v_mfma_f32_16x16x32_bf16 v[104:107], a[208:211], a[228:231], v[104:107]\n\t"
    "buffer_load_dwordx4 v18, s[40:43], s61 offen lds\n\t"
    "v_mfma_f32_16x16x32_bf16 v[136:139], a[208:211], a[232:235], v[136:139]\n\t"
    "s_add_u32 m0, s63, 0xf000\n\t"
    "v_mfma_f32_16x16x32_bf16 v[136:139], a[212:215], v[48:51], v[136:139]\n\t"
    "buffer_load_dwordx4 v32, s[36:39], s60 offen lds\n\t"
    "v_mfma_f32_16x16x32_bf16 v[168:171], a[212:215], v[52:55], v[168:171]\n\t"
    "v_mfma_f32_16x16x32_bf16 v[168:171], a[208:211], a[236:239], v[168:171]\n\t"
    "v_mfma_f32_16x16x32_bf16 v[200:203], a[208:211], a[240:243], v[200:203]\n\t"
    "s_add_u32 m0, s65, 0x14000\n\t"
    "v_mfma_f32_16x16x32_bf16 v[200:203], a[212:215], v[56:59], v[200:203]\n\t"
    "buffer_load_dwordx4 v17, s[40:43], s62 offen lds\n\t"
    "v_mfma_f32_16x16x32_bf16 v[232:235], a[212:215], v[60:63], v[232:235]\n\t"
    "v_mfma_f32_16x16x32_bf16 v[232:235], a[208:211], a[244:247], v[232:235]\n\t"
    "ds_read_b64_tr_b16 a[208:209], v22 offset:16640\n\t"
    "v_mfma_f32_16x16x32_bf16 v[204:207], a[216:219], a[240:243], v[204:207]\n\t"
    "ds_read_b64_tr_b16 a[210:211], v22 offset:17664\n\t"
    "v_mfma_f32_16x16x32_bf16 v[204:207], a[220:223], v[56:59], v[204:207]\n\t"
    "ds_read_b64_tr_b16 a[212:213], v22 offset:24832\n\t"
    "v_mfma_f32_16x16x32_bf16 v[172:175], a[220:223], v[52:55], v[172:175]\n\t"
    "ds_read_b64_tr_b16 a[214:215], v22 offset:25856\n\t"
    "s_add_u32 m0, s63, 0x10000\n\t"
    "v_mfma_f32_16x16x32_bf16 v[172:175], a[216:219], a[236:239], v[172:175]\n\t"
    "buffer_load_dwordx4 v33, s[36:39], s60 offen lds\n\t"
    "v_mfma_f32_16x16x32_bf16 v[140:143], a[216:219], a[232:235], v[140:143]\n\t"
    "s_add_u32 m0, s63, 0x11000\n\t"
    "v_mfma_f32_16x16x32_bf16 v[140:143], a[220:223], v[48:51], v[140:143]\n\t"
    "buffer_load_dwordx4 v34, s[36:39], s60 offen lds\n\t"
    "v_mfma_f32_16x16x32_bf16 v[108:111], a[220:223], v[44:47], v[108:111]\n\t"
    "s_add_u32 m0, s65, 0x14400\n\t"
    "v_mfma_f32_16x16x32_bf16 v[108:111], a[216:219], a[228:231], v[108:111]\n\t"
    "buffer_load_dwordx4 v18, s[40:43], s62 offen lds\n\t"
    "v_mfma_f32_16x16x32_bf16 v[76:79], a[216:219], a[224:227], v[76:79]\n\t"
    "v_mfma_f32_16x16x32_bf16 v[76:79], a[220:223], v[40:43], v[76:79]\n\t"
    "ds_read_b64_tr_b16 a[216:217], v23 offset:16640\n\t"
    "s_waitcnt lgkmcnt(5)\n\t"
    "v_mfma_f32_16x16x32_bf16 v[80:83], a[192:195], a[224:227], v[80:83]\n\t"
    "ds_read_b64_tr_b16 a[218:219], v23 offset:17664\n\t"
    "v_mfma_f32_16x16x32_bf16 v[80:83], a[196:199], v[40:43], v[80:83]\n\t"
    "ds_read_b64_tr_b16 a[220:221], v23 offset:24832\n\t"
    "v_mfma_f32_16x16x32_bf16 v[112:115], a[196:199], v[44:47], v[112:115]\n\t"
    "ds_read_b64_tr_b16 a[222:223], v23 offset:25856\n\t"
    "v_mfma_f32_16x16x32_bf16 v[112:115], a[192:195], a[228:231], v[112:115]\n\t"
    "s_add_u32 s61, s61, 0x80\n\t"
    "s_add_u32 s62, s62, 0x80\n\t"
    "v_mfma_f32_16x16x32_bf16 v[144:147], a[192:195], a[232:235], v[144:147]\n\t"
    "v_mfma_f32_16x16x32_bf16 v[144:147], a[196:199], v[48:51], v[144:147]\n\t"
    "v_mfma_f32_16x16x32_bf16 v[176:179], a[196:199], v[52:55], v[176:179]\n\t"
    "v_mfma_f32_16x16x32_bf16 v[176:179], a[192:195], a[236:239], v[176:179]\n\t"
    "v_mfma_f32_16x16x32_bf16 v[208:211], a[192:195], a[240:243], v[208:211]\n\t"
    "s_waitcnt vmcnt(16) lgkmcnt(0)\n\t"
    "s_barrier\n\t"
    "v_mfma_f32_16x16x32_bf16 v[208:211], a[196:199], v[56:59], v[208:211]\n\t"
    "ds_read_b128 a[248:251], v28 offset:0\n\t"
    "s_add_u32 m0, s65, 0x16000\n\t"
    "v_mfma_f32_16x16x32_bf16 v[240:243], a[196:199], v[60:63], v[240:243]\n\t"
    "buffer_load_dwordx4 v17, s[40:43], s61 offen lds\n\t"
    "v_mfma_f32_16x16x32_bf16 v[240:243], a[192:195], a[244:247], v[240:243]\n\t"
    "ds_read_b64_tr_b16 a[192:193], v26 offset:0\n\t"
    "v_mfma_f32_16x16x32_bf16 v[212:215], a[200:203], a[240:243], v[212:215]\n\t"
    "ds_read_b64_tr_b16 a[194:195], v26 offset:1024\n\t"
    "v_mfma_f32_16x16x32_bf16 v[212:215], a[204:207], v[56:59], v[212:215]\n\t"
    "ds_read_b64_tr_b16 a[196:197], v26 offset:8192\n\t"
    "v_mfma_f32_16x16x32_bf16 v[180:183], a[204:207], v[52:55], v[180:183]\n\t"
    "ds_read_b64_tr_b16 a[198:199], v26 offset:9216\n\t"
    "s_add_u32 m0, s65, 0x16400\n\t"
    "v_mfma_f32_16x16x32_bf16 v[180:183], a[200:203], a[236:239], v[180:183]\n\t"
    "buffer_load_dwordx4 v18, s[40:43], s61 offen lds\n\t"
    "v_mfma_f32_16x16x32_bf16 v[148:151], a[200:203], a[232:235], v[148:151]\n\t"
    "ds_read_b128 a[252:255], v28 offset:2048\n\t"
    "v_mfma_f32_16x16x32_bf16 v[148:151], a[204:207], v[48:51], v[148:151]\n\t"
    "ds_read_b128 v[0:3], v28 offset:4096\n\t"
    "v_mfma_f32_16x16x32_bf16 v[116:119], a[204:207], v[44:47], v[116:119]\n\t"
    "ds_read_b128 v[4:7], v28 offset:6144\n\t"
    "s_add_u32 m0, s65, 0x18000\n\t"
    "v_mfma_f32_16x16x32_bf16 v[116:119], a[200:203], a[228:231], v[116:119]\n\t"
    "buffer_load_dwordx4 v17, s[40:43], s62 offen lds\n\t"
    "v_mfma_f32_16x16x32_bf16 v[84:87], a[200:203], a[224:227], v[84:87]\n\t"
    "ds_read_b128 v[8:11], v28 offset:8192\n\t"
    "v_mfma_f32_16x16x32_bf16 v[84:87], a[204:207], v[40:43], v[84:87]\n\t"
    "ds_read_b64_tr_b16 a[200:201], v27 offset:0\n\t"
    "v_mfma_f32_16x16x32_bf16 v[88:91], a[208:211], a[224:227], v[88:91]\n\t"
    "ds_read_b64_tr_b16 a[202:203], v27 offset:1024\n\t"
    "v_mfma_f32_16x16x32_bf16 v[88:91], a[212:215], v[40:43], v[88:91]\n\t"
    "ds_read_b64_tr_b16 a[204:205], v27 offset:8192\n\t"
    "v_mfma_f32_16x16x32_bf16 v[120:123], a[212:215], v[44:47], v[120:123]\n\t"
    "ds_read_b64_tr_b16 a[206:207], v27 offset:9216\n\t"
    "s_add_u32 m0, s65, 0x18400\n\t"
    "v_mfma_f32_16x16x32_bf16 v[120:123], a[208:211], a[228:231], v[120:123]\n\t"
    "buffer_load_dwordx4 v18, s[40:43], s62 offen lds\n\t"
    "v_mfma_f32_16x16x32_bf16 v[152:155], a[208:211], a[232:235], v[152:155]\n\t"
    "ds_read_b128 v[12:15], v35 offset:0\n\t"
    "v_mfma_f32_16x16x32_bf16 v[152:155], a[212:215], v[48:51], v[152:155]\n\t"
    "ds_read_b128 a[104:107], v28 offset:512\n\t"
    "s_mov_b32 s68, 0x2000\n\t"
    "v_mfma_f32_16x16x32_bf16 v[184:187], a[212:215], v[52:55], v[184:187]\n\t"
    "buffer_load_dwordx4 a[0:3], v16, s[48:51], s68 offen nt\n\t"
    "v_mfma_f32_16x16x32_bf16 v[184:187], a[208:211], a[236:239], v[184:187]\n\t"
    "ds_read_b128 a[108:111], v28 offset:2560\n\t"
    "v_mfma_f32_16x16x32_bf16 v[216:219], a[208:211], a[240:243], v[216:219]\n\t"
    "ds_read_b128 a[112:115], v28 offset:4608\n\t"
    "v_mfma_f32_16x16x32_bf16 v[216:219], a[212:215], v[56:59], v[216:219]\n\t"
    "ds_read_b128 a[116:119], v28 offset:6656\n\t"
    "v_mfma_f32_16x16x32_bf16 v[248:251], a[212:215], v[60:63], v[248:251]\n\t"
    "buffer_load_dwordx4 a[4:7], v16, s[48:51], s68 offen offset:1024 nt\n\t"
    "v_mfma_f32_16x16x32_bf16 v[248:251], a[208:211], a[244:247], v[248:251]\n\t"
    "ds_read_b64_tr_b16 a[208:209], v26 offset:256\n\t"
    "v_mfma_f32_16x16x32_bf16 v[220:223], a[216:219], a[240:243], v[220:223]\n\t"
    "ds_read_b64_tr_b16 a[210:211], v26 offset:1280\n\t"
    "v_mfma_f32_16x16x32_bf16 v[220:223], a[220:223], v[56:59], v[220:223]\n\t"
    "ds_read_b64_tr_b16 a[212:213], v26 offset:8448\n\t"
    "v_mfma_f32_16x16x32_bf16 v[188:191], a[220:223], v[52:55], v[188:191]\n\t"
    "ds_read_b64_tr_b16 a[214:215], v26 offset:9472\n\t"
    "s_mov_b32 s68, 0x22000\n\t"
    "v_mfma_f32_16x16x32_bf16 v[188:191], a[216:219], a[236:239], v[188:191]\n\t"
    "buffer_load_dwordx4 a[8:11], v16, s[48:51], s68 offen nt\n\t"
    "v_mfma_f32_16x16x32_bf16 v[156:159], a[216:219], a[232:235], v[156:159]\n\t"
    "ds_read_b128 a[120:123], v28 offset:8704\n\t"
    "v_mfma_f32_16x16x32_bf16 v[156:159], a[220:223], v[48:51], v[156:159]\n\t"
    "ds_read_b128 a[124:127], v35 offset:512\n\t"
    "v_mfma_f32_16x16x32_bf16 v[124:127], a[220:223], v[44:47], v[124:127]\n\t"
    "buffer_load_dwordx4 a[12:15], v16, s[48:51], s68 offen offset:1024 nt\n\t"
    "v_mfma_f32_16x16x32_bf16 v[124:127], a[216:219], a[228:231], v[124:127]\n\t"
    "v_mfma_f32_16x16x32_bf16 v[92:95], a[216:219], a[224:227], v[92:95]\n\t"
    "v_mfma_f32_16x16x32_bf16 v[92:95], a[220:223], v[40:43], v[92:95]\n\t"
    "ds_read_b64_tr_b16 a[216:217], v27 offset:256\n\t"
    "s_waitcnt lgkmcnt(1)\n\t"
    "v_mfma_f32_16x16x32_bf16 v[64:67], a[192:195], a[248:251], v[64:67]\n\t"
    "ds_read_b64_tr_b16 a[218:219], v27 offset:1280\n\t"
    "v_mfma_f32_16x16x32_bf16 v[64:67], a[196:199], a[104:107], v[64:67]\n\t"
    "ds_read_b64_tr_b16 a[220:221], v27 offset:8448\n\t"
    "v_mfma_f32_16x16x32_bf16 v[96:99], a[196:199], a[108:111], v[96:99]\n\t"
    "ds_read_b64_tr_b16 a[222:223], v27 offset:9472\n\t"
    "v_mfma_f32_16x16x32_bf16 v[96:99], a[192:195], a[252:255], v[96:99]\n\t"
    "s_and_b32 s86, s84, 15\n\t"
    "s_lshr_b64 s[84:85], s[84:85], 4\n\t"
    "v_mfma_f32_16x16x32_bf16 v[128:131], a[192:195], v[0:3], v[128:131]\n\t"
    "s_lshl_b32 s61, s86, 19\n\t"
    "s_lshl_b32 s60, s86, 7\n\t"
    "s_add_u32 s62, s61, 0x40000\n\t"
    "v_mfma_f32_16x16x32_bf16 v[128:131], a[196:199], a[112:115], v[128:131]\n\t"
    "v_mfma_f32_16x16x32_bf16 v[160:163], a[196:199], a[116:119], v[160:163]\n\t"
    "v_mfma_f32_16x16x32_bf16 v[160:163], a[192:195], v[4:7], v[160:163]\n\t"
    "v_mfma_f32_16x16x32_bf16 v[192:195], a[192:195], v[8:11], v[192:195]\n\t"
    "s_waitcnt vmcnt(20) lgkmcnt(0)\n\t"
    "s_barrier\n\t"
    "v_mfma_f32_16x16x32_bf16 v[192:195], a[196:199], a[120:123], v[192:195]\n\t"
    "s_add_u32 m0, s63, 0x1a000\n\t"
    "v_mfma_f32_16x16x32_bf16 v[224:227], a[196:199], a[124:127], v[224:227]\n\t"
    "buffer_load_dwordx4 v19, s[36:39], s60 offen lds\n\t"
    "v_mfma_f32_16x16x32_bf16 v[224:227], a[192:195], v[12:15], v[224:227]\n\t"
    "ds_read_b64_tr_b16 a[192:193], v26 offset:16384\n\t"
    "v_mfma_f32_16x16x32_bf16 v[196:199], a[200:203], v[8:11], v[196:199]\n\t"
    "ds_read_b64_tr_b16 a[194:195], v26 offset:17408\n\t"
    "v_mfma_f32_16x16x32_bf16 v[196:199], a[204:207], a[120:123], v[196:199]\n\t"
    "ds_read_b64_tr_b16 a[196:197], v26 offset:24576\n\t"
    "v_mfma_f32_16x16x32_bf16 v[164:167], a[204:207], a[116:119], v[164:167]\n\t"
    "ds_read_b64_tr_b16 a[198:199], v26 offset:25600\n\t"
    "s_add_u32 m0, s65, 0x20000\n\t"
    "v_mfma_f32_16x16x32_bf16 v[164:167], a[200:203], v[4:7], v[164:167]\n\t"
    "buffer_load_dwordx4 v17, s[40:43], s61 offen lds\n\t"
    "v_mfma_f32_16x16x32_bf16 v[132:135], a[200:203], v[0:3], v[132:135]\n\t"
    "s_add_u32 m0, s63, 0x1b000\n\t"
    "v_mfma_f32_16x16x32_bf16 v[132:135], a[204:207], a[112:115], v[132:135]\n\t"
    "buffer_load_dwordx4 v20, s[36:39], s60 offen lds\n\t"
    "v_mfma_f32_16x16x32_bf16 v[100:103], a[204:207], a[108:111], v[100:103]\n\t"
    "v_mfma_f32_16x16x32_bf16 v[100:103], a[200:203], a[252:255], v[100:103]\n\t"
    "s_add_u32 m0, s63, 0x1c000\n\t"
    "v_mfma_f32_16x16x32_bf16 v[68:71], a[200:203], a[248:251], v[68:71]\n\t"
    "buffer_load_dwordx4 v21, s[36:39], s60 offen lds\n\t"
    "v_mfma_f32_16x16x32_bf16 v[68:71], a[204:207], a[104:107], v[68:71]\n\t"
    "ds_read_b64_tr_b16 a[200:201], v27 offset:16384\n\t"
    "v_mfma_f32_16x16x32_bf16 v[72:75], a[208:211], a[248:251], v[72:75]\n\t"
    "ds_read_b64_tr_b16 a[202:203], v27 offset:17408\n\t"
    "v_mfma_f32_16x16x32_bf16 v[72:75], a[212:215], a[104:107], v[72:75]\n\t"
    "ds_read_b64_tr_b16 a[204:205], v27 offset:24576\n\t"
    "v_mfma_f32_16x16x32_bf16 v[104:107], a[212:215], a[108:111], v[104:107]\n\t"
    "ds_read_b64_tr_b16 a[206:207], v27 offset:25600\n\t"
    "s_add_u32 m0, s65, 0x20400\n\t"
    "v_mfma_f32_16x16x32_bf16 v[104:107], a[208:211], a[252:255], v[104:107]\n\t"
    "buffer_load_dwordx4 v18, s[40:43], s61 offen lds\n\t"
    "v_mfma_f32_16x16x32_bf16 v[136:139], a[208:211], v[0:3], v[136:139]\n\t"
    "s_add_u32 m0, s63, 0x1d000\n\t"
    "v_mfma_f32_16x16x32_bf16 v[136:139], a[212:215], a[112:115], v[136:139]\n\t"
    "buffer_load_dwordx4 v32, s[36:39], s60 offen lds\n\t"
    "v_mfma_f32_16x16x32_bf16 v[168:171], a[212:215], a[116:119], v[168:171]\n\t"
    "v_mfma_f32_16x16x32_bf16 v[168:171], a[208:211], v[4:7], v[168:171]\n\t"
    "v_mfma_f32_16x16x32_bf16 v[200:203], a[208:211], v[8:11], v[200:203]\n\t"
    "s_add_u32 m0, s65, 0x22000\n\t"
    "v_mfma_f32_16x16x32_bf16 v[200:203], a[212:215], a[120:123], v[200:203]\n\t"
    "buffer_load_dwordx4 v17, s[40:43], s62 offen lds\n\t"
    "v_mfma_f32_16x16x32_bf16 v[232:235], a[212:215], a[124:127], v[232:235]\n\t"
    "v_mfma_f32_16x16x32_bf16 v[232:235], a[208:211], v[12:15], v[232:235]\n\t"
    "ds_read_b64_tr_b16 a[208:209], v26 offset:16640\n\t"
    "v_mfma_f32_16x16x32_bf16 v[204:207], a[216:219], v[8:11], v[204:207]\n\t"
    "ds_read_b64_tr_b16 a[210:211], v26 offset:17664\n\t"
    "v_mfma_f32_16x16x32_bf16 v[204:207], a[220:223], a[120:123], v[204:207]\n\t"
    "ds_read_b64_tr_b16 a[212:213], v26 offset:24832\n\t"
    "v_mfma_f32_16x16x32_bf16 v[172:175], a[220:223], a[116:119], v[172:175]\n\t"
    "ds_read_b64_tr_b16 a[214:215], v26 offset:25856\n\t"
    "s_add_u32 m0, s63, 0x1e000\n\t"
    "v_mfma_f32_16x16x32_bf16 v[172:175], a[216:219], v[4:7], v[172:175]\n\t"
    "buffer_load_dwordx4 v33, s[36:39], s60 offen lds\n\t"
    "v_mfma_f32_16x16x32_bf16 v[140:143], a[216:219], v[0:3], v[140:143]\n\t"
    "s_add_u32 m0, s63, 0x1f000\n\t"
    "v_mfma_f32_16x16x32_bf16 v[140:143], a[220:223], a[112:115], v[140:143]\n\t"
    "buffer_load_dwordx4 v34, s[36:39], s60 offen lds\n\t"
    "v_mfma_f32_16x16x32_bf16 v[108:111], a[220:223], a[108:111], v[108:111]\n\t"
    "s_add_u32 m0, s65, 0x22400\n\t"
    "v_mfma_f32_16x16x32_bf16 v[108:111], a[216:219], a[252:255], v[108:111]\n\t"
    "buffer_load_dwordx4 v18, s[40:43], s62 offen lds\n\t"
    "v_mfma_f32_16x16x32_bf16 v[76:79], a[216:219], a[248:251], v[76:79]\n\t"
    "v_mfma_f32_16x16x32_bf16 v[76:79], a[220:223], a[104:107], v[76:79]\n\t"
    "ds_read_b64_tr_b16 a[216:217], v27 offset:16640\n\t"
    "s_waitcnt lgkmcnt(5)\n\t"
    "v_mfma_f32_16x16x32_bf16 v[80:83], a[192:195], a[248:251], v[80:83]\n\t"
    "ds_read_b64_tr_b16 a[218:219], v27 offset:17664\n\t"
    "v_mfma_f32_16x16x32_bf16 v[80:83], a[196:199], a[104:107], v[80:83]\n\t"
    "ds_read_b64_tr_b16 a[220:221], v27 offset:24832\n\t"
    "v_mfma_f32_16x16x32_bf16 v[112:115], a[196:199], a[108:111], v[112:115]\n\t"
    "ds_read_b64_tr_b16 a[222:223], v27 offset:25856\n\t"
    "v_mfma_f32_16x16x32_bf16 v[112:115], a[192:195], a[252:255], v[112:115]\n\t"
    "s_add_u32 s61, s61, 0x80\n\t"
    "s_add_u32 s62, s62, 0x80\n\t"
    "v_mfma_f32_16x16x32_bf16 v[144:147], a[192:195], v[0:3], v[144:147]\n\t"
    "v_mfma_f32_16x16x32_bf16 v[144:147], a[196:199], a[112:115], v[144:147]\n\t"
    "v_mfma_f32_16x16x32_bf16 v[176:179], a[196:199], a[116:119], v[176:179]\n\t"
    "v_mfma_f32_16x16x32_bf16 v[176:179], a[192:195], v[4:7], v[176:179]\n\t"
    "v_mfma_f32_16x16x32_bf16 v[208:211], a[192:195], v[8:11], v[208:211]\n\t"
    "s_waitcnt vmcnt(18) lgkmcnt(0)\n\t"
    "s_barrier\n\t"
    "v_mfma_f32_16x16x32_bf16 v[208:211], a[196:199], a[120:123], v[208:211]\n\t"
    "ds_read_b128 a[224:227], v24 offset:0\n\t"
    "s_add_u32 m0, s65, 0x24000\n\t"
    "v_mfma_f32_16x16x32_bf16 v[240:243], a[196:199], a[124:127], v[240:243]\n\t"
    "buffer_load_dwordx4 v17, s[40:43], s61 offen lds\n\t"
    "v_mfma_f32_16x16x32_bf16 v[240:243], a[192:195], v[12:15], v[240:243]\n\t"
    "ds_read_b64_tr_b16 a[192:193], v22 offset:0\n\t"
    "v_mfma_f32_16x16x32_bf16 v[212:215], a[200:203], v[8:11], v[212:215]\n\t"
    "ds_read_b64_tr_b16 a[194:195], v22 offset:1024\n\t"
    "v_mfma_f32_16x16x32_bf16 v[212:215], a[204:207], a[120:123], v[212:215]\n\t"
    "ds_read_b64_tr_b16 a[196:197], v22 offset:8192\n\t"
    "v_mfma_f32_16x16x32_bf16 v[180:183], a[204:207], a[116:119], v[180:183]\n\t"
    "ds_read_b64_tr_b16 a[198:199], v22 offset:9216\n\t"
    "v_mfma_f32_16x16x32_bf16 v[180:183], a[200:203], v[4:7], v[180:183]\n\t"
    "ds_read_b128 a[228:231], v24 offset:2048\n\t"
    "v_mfma_f32_16x16x32_bf16 v[148:151], a[200:203], v[0:3], v[148:151]\n\t"
    "ds_read_b128 a[232:235], v24 offset:4096\n\t"
    "s_add_u32 m0, s65, 0x24400\n\t"
    "v_mfma_f32_16x16x32_bf16 v[148:151], a[204:207], a[112:115], v[148:151]\n\t"
    "buffer_load_dwordx4 v18, s[40:43], s61 offen lds\n\t"
    "v_mfma_f32_16x16x32_bf16 v[116:119], a[204:207], a[108:111], v[116:119]\n\t"
    "ds_read_b128 a[236:239], v24 offset:6144\n\t"
    "v_mfma_f32_16x16x32_bf16 v[116:119], a[200:203], a[252:255], v[116:119]\n\t"
    "ds_read_b128 a[240:243], v24 offset:8192\n\t"
    "v_mfma_f32_16x16x32_bf16 v[84:87], a[200:203], a[248:251], v[84:87]\n\t"
    "ds_read_b128 a[244:247], v25 offset:0\n\t"
    "v_mfma_f32_16x16x32_bf16 v[84:87], a[204:207], a[104:107], v[84:87]\n\t"
    "ds_read_b64_tr_b16 a[200:201], v23 offset:0\n\t"
    "v_mfma_f32_16x16x32_bf16 v[88:91], a[208:211], a[248:251], v[88:91]\n\t"
    "ds_read_b64_tr_b16 a[202:203], v23 offset:1024\n\t"
    "v_mfma_f32_16x16x32_bf16 v[88:91], a[212:215], a[104:107], v[88:91]\n\t"
    "ds_read_b64_tr_b16 a[204:205], v23 offset:8192\n\t"
    "v_mfma_f32_16x16x32_bf16 v[120:123], a[212:215], a[108:111], v[120:123]\n\t"
    "ds_read_b64_tr_b16 a[206:207], v23 offset:9216\n\t"
    "s_add_u32 m0, s65, 0x26000\n\t"
    "v_mfma_f32_16x16x32_bf16 v[120:123], a[208:211], a[252:255], v[120:123]\n\t"
    "buffer_load_dwordx4 v17, s[40:43], s62 offen lds\n\t"
    "v_mfma_f32_16x16x32_bf16 v[152:155], a[208:211], v[0:3], v[152:155]\n\t"
    "ds_read_b128 v[40:43], v24 offset:512\n\t"
    "v_mfma_f32_16x16x32_bf16 v[152:155], a[212:215], a[112:115], v[152:155]\n\t"
    "ds_read_b128 v[44:47], v24 offset:2560\n\t"
    "s_add_u32 m0, s65, 0x26400\n\t"
    "v_mfma_f32_16x16x32_bf16 v[184:187], a[212:215], a[116:119], v[184:187]\n\t"
    "buffer_load_dwordx4 v18, s[40:43], s62 offen lds\n\t"
    "v_mfma_f32_16x16x32_bf16 v[184:187], a[208:211], v[4:7], v[184:187]\n\t"
    "ds_read_b128 v[48:51], v24 offset:4608\n\t"
    "v_mfma_f32_16x16x32_bf16 v[216:219], a[208:211], v[8:11], v[216:219]\n\t"
    "ds_read_b128 v[52:55], v24 offset:6656\n\t"
    "v_mfma_f32_16x16x32_bf16 v[216:219], a[212:215], a[120:123], v[216:219]\n\t"
    "ds_read_b128 v[56:59], v24 offset:8704\n\t"
    "s_mov_b32 s68, 0x4000\n\t"
    "v_mfma_f32_16x16x32_bf16 v[248:251], a[212:215], a[124:127], v[248:251]\n\t"
    "buffer_load_dwordx4 a[16:19], v16, s[44:47], s68 offen nt\n\t"
    "v_mfma_f32_16x16x32_bf16 v[248:251], a[208:211], v[12:15], v[248:251]\n\t"
    "ds_read_b64_tr_b16 a[208:209], v22 offset:256\n\t"
    "v_mfma_f32_16x16x32_bf16 v[220:223], a[216:219], v[8:11], v[220:223]\n\t"
    "ds_read_b64_tr_b16 a[210:211], v22 offset:1280\n\t"
    "v_mfma_f32_16x16x32_bf16 v[220:223], a[220:223], a[120:123], v[220:223]\n\t"
    "ds_read_b64_tr_b16 a[212:213], v22 offset:8448\n\t"
    "v_mfma_f32_16x16x32_bf16 v[188:191], a[220:223], a[116:119], v[188:191]\n\t"
    "ds_read_b64_tr_b16 a[214:215], v22 offset:9472\n\t"
    "v_mfma_f32_16x16x32_bf16 v[188:191], a[216:219], v[4:7], v[188:191]\n\t"
    "ds_read_b128 v[60:63], v25 offset:512\n\t"
    "v_mfma_f32_16x16x32_bf16 v[156:159], a[216:219], v[0:3], v[156:159]\n\t"
    "v_mfma_f32_16x16x32_bf16 v[156:159], a[220:223], a[112:115], v[156:159]\n\t"
    "v_mfma_f32_16x16x32_bf16 v[124:127], a[220:223], a[108:111], v[124:127]\n\t"
    "buffer_load_dwordx4 a[20:23], v16, s[44:47], s68 offen offset:1024 nt\n\t"
    "v_mfma_f32_16x16x32_bf16 v[124:127], a[216:219], a[252:255], v[124:127]\n\t"
    "v_mfma_f32_16x16x32_bf16 v[92:95], a[216:219], a[248:251], v[92:95]\n\t"
    "v_mfma_f32_16x16x32_bf16 v[92:95], a[220:223], a[104:107], v[92:95]\n\t"
    "ds_read_b64_tr_b16 a[216:217], v23 offset:256\n\t"
    "s_waitcnt lgkmcnt(1)\n\t"
    "v_mfma_f32_16x16x32_bf16 v[64:67], a[192:195], a[224:227], v[64:67]\n\t"
    "ds_read_b64_tr_b16 a[218:219], v23 offset:1280\n\t"
    "v_mfma_f32_16x16x32_bf16 v[64:67], a[196:199], v[40:43], v[64:67]\n\t"
    "ds_read_b64_tr_b16 a[220:221], v23 offset:8448\n\t"
    "v_mfma_f32_16x16x32_bf16 v[96:99], a[196:199], v[44:47], v[96:99]\n\t"
    "ds_read_b64_tr_b16 a[222:223], v23 offset:9472\n\t"
    "v_mfma_f32_16x16x32_bf16 v[96:99], a[192:195], a[228:231], v[96:99]\n\t"
    "s_and_b32 s86, s84, 15\n\t"
    "s_lshr_b64 s[84:85], s[84:85], 4\n\t"
    "v_mfma_f32_16x16x32_bf16 v[128:131], a[192:195], a[232:235], v[128:131]\n\t"
    "s_lshl_b32 s61, s86, 19\n\t"
    "s_lshl_b32 s60, s86, 7\n\t"
    "s_add_u32 s62, s61, 0x40000\n\t"
    "v_mfma_f32_16x16x32_bf16 v[128:131], a[196:199], v[48:51], v[128:131]\n\t"
    "v_mfma_f32_16x16x32_bf16 v[160:163], a[196:199], v[52:55], v[160:163]\n\t"
    "v_mfma_f32_16x16x32_bf16 v[160:163], a[192:195], a[236:239], v[160:163]\n\t"
    "v_mfma_f32_16x16x32_bf16 v[192:195], a[192:195], a[240:243], v[192:195]\n\t"
    "s_waitcnt vmcnt(20) lgkmcnt(0)\n\t"
    "s_barrier\n\t"
    "v_mfma_f32_16x16x32_bf16 v[192:195], a[196:199], v[56:59], v[192:195]\n\t"
    "s_add_u32 m0, s63, 0xc000\n\t"
    "v_mfma_f32_16x16x32_bf16 v[224:227], a[196:199], v[60:63], v[224:227]\n\t"
    "buffer_load_dwordx4 v19, s[36:39], s60 offen lds\n\t"
    "v_mfma_f32_16x16x32_bf16 v[224:227], a[192:195], a[244:247], v[224:227]\n\t"
    "ds_read_b64_tr_b16 a[192:193], v22 offset:16384\n\t"
    "v_mfma_f32_16x16x32_bf16 v[196:199], a[200:203], a[240:243], v[196:199]\n\t"
    "ds_read_b64_tr_b16 a[194:195], v22 offset:17408\n\t"
    "v_mfma_f32_16x16x32_bf16 v[196:199], a[204:207], v[56:59], v[196:199]\n\t"
    "ds_read_b64_tr_b16 a[196:197], v22 offset:24576\n\t"
    "v_mfma_f32_16x16x32_bf16 v[164:167], a[204:207], v[52:55], v[164:167]\n\t"
    "ds_read_b64_tr_b16 a[198:199], v22 offset:25600\n\t"
    "s_add_u32 m0, s65, 0x12000\n\t"
    "v_mfma_f32_16x16x32_bf16 v[164:167], a[200:203], a[236:239], v[164:167]\n\t"
    "buffer_load_dwordx4 v17, s[40:43], s61 offen lds\n\t"
    "v_mfma_f32_16x16x32_bf16 v[132:135], a[200:203], a[232:235], v[132:135]\n\t"
    "s_add_u32 m0, s63, 0xd000\n\t"
    "v_mfma_f32_16x16x32_bf16 v[132:135], a[204:207], v[48:51], v[132:135]\n\t"
    "buffer_load_dwordx4 v20, s[36:39], s60 offen lds\n\t"
    "v_mfma_f32_16x16x32_bf16 v[100:103], a[204:207], v[44:47], v[100:103]\n\t"
    "v_mfma_f32_16x16x32_bf16 v[100:103], a[200:203], a[228:231], v[100:103]\n\t"
    "s_add_u32 m0, s63, 0xe000\n\t"
    "v_mfma_f32_16x16x32_bf16 v[68:71], a[200:203], a[224:227], v[68:71]\n\t"
    "buffer_load_dwordx4 v21, s[36:39], s60 offen lds\n\t"
    "v_mfma_f32_16x16x32_bf16 v[68:71], a[204:207], v[40:43], v[68:71]\n\t"
    "ds_read_b64_tr_b16 a[200:201], v23 offset:16384\n\t"
    "v_mfma_f32_16x16x32_bf16 v[72:75], a[208:211], a[224:227], v[72:75]\n\t"
    "ds_read_b64_tr_b16 a[202:203], v23 offset:17408\n\t"
    "v_mfma_f32_16x16x32_bf16 v[72:75], a[212:215], v[40:43], v[72:75]\n\t"
    "ds_read_b64_tr_b16 a[204:205], v23 offset:24576\n\t"
    "v_mfma_f32_16x16x32_bf16 v[104:107], a[212:215], v[44:47], v[104:107]\n\t"
    "ds_read_b64_tr_b16 a[206:207], v23 offset:25600\n\t"
    "s_add_u32 m0, s65, 0x12400\n\t"
    "v_mfma_f32_16x16x32_bf16 v[104:107], a[208:211], a[228:231], v[104:107]\n\t"
    "buffer_load_dwordx4 v18, s[40:43], s61 offen lds\n\t"
    "v_mfma_f32_16x16x32_bf16 v[136:139], a[208:211], a[232:235], v[136:139]\n\t"
    "s_add_u32 m0, s63, 0xf000\n\t"
    "v_mfma_f32_16x16x32_bf16 v[136:139], a[212:215], v[48:51], v[136:139]\n\t"
    "buffer_load_dwordx4 v32, s[36:39], s60 offen lds\n\t"
    "v_mfma_f32_16x16x32_bf16 v[168:171], a[212:215], v[52:55], v[168:171]\n\t"
    "v_mfma_f32_16x16x32_bf16 v[168:171], a[208:211], a[236:239], v[168:171]\n\t"
    "v_mfma_f32_16x16x32_bf16 v[200:203], a[208:211], a[240:243], v[200:203]\n\t"
    "s_add_u32 m0, s65, 0x14000\n\t"
    "v_mfma_f32_16x16x32_bf16 v[200:203], a[212:215], v[56:59], v[200:203]\n\t"
    "buffer_load_dwordx4 v17, s[40:43], s62 offen lds\n\t"
    "v_mfma_f32_16x16x32_bf16 v[232:235], a[212:215], v[60:63], v[232:235]\n\t"
    "v_mfma_f32_16x16x32_bf16 v[232:235], a[208:211], a[244:247], v[232:235]\n\t"
    "ds_read_b64_tr_b16 a[208:209], v22 offset:16640\n\t"
    "v_mfma_f32_16x16x32_bf16 v[204:207], a[216:219], a[240:243], v[204:207]\n\t"
    "ds_read_b64_tr_b16 a[210:211], v22 offset:17664\n\t"
    "v_mfma_f32_16x16x32_bf16 v[204:207], a[220:223], v[56:59], v[204:207]\n\t"
    "ds_read_b64_tr_b16 a[212:213], v22 offset:24832\n\t"
    "v_mfma_f32_16x16x32_bf16 v[172:175], a[220:223], v[52:55], v[172:175]\n\t"
    "ds_read_b64_tr_b16 a[214:215], v22 offset:25856\n\t"
    "s_add_u32 m0, s63, 0x10000\n\t"
    "v_mfma_f32_16x16x32_bf16 v[172:175], a[216:219], a[236:239], v[172:175]\n\t"
    "buffer_load_dwordx4 v33, s[36:39], s60 offen lds\n\t"
    "v_mfma_f32_16x16x32_bf16 v[140:143], a[216:219], a[232:235], v[140:143]\n\t"
    "s_add_u32 m0, s63, 0x11000\n\t"
    "v_mfma_f32_16x16x32_bf16 v[140:143], a[220:223], v[48:51], v[140:143]\n\t"
    "buffer_load_dwordx4 v34, s[36:39], s60 offen lds\n\t"
    "v_mfma_f32_16x16x32_bf16 v[108:111], a[220:223], v[44:47], v[108:111]\n\t"
    "s_add_u32 m0, s65, 0x14400\n\t"
    "v_mfma_f32_16x16x32_bf16 v[108:111], a[216:219], a[228:231], v[108:111]\n\t"
    "buffer_load_dwordx4 v18, s[40:43], s62 offen lds\n\t"
    "v_mfma_f32_16x16x32_bf16 v[76:79], a[216:219], a[224:227], v[76:79]\n\t"
    "v_mfma_f32_16x16x32_bf16 v[76:79], a[220:223], v[40:43], v[76:79]\n\t"
    "ds_read_b64_tr_b16 a[216:217], v23 offset:16640\n\t"
    "s_waitcnt lgkmcnt(5)\n\t"
    "v_mfma_f32_16x16x32_bf16 v[80:83], a[192:195], a[224:227], v[80:83]\n\t"
    "ds_read_b64_tr_b16 a[218:219], v23 offset:17664\n\t"
    "v_mfma_f32_16x16x32_bf16 v[80:83], a[196:199], v[40:43], v[80:83]\n\t"
    "ds_read_b64_tr_b16 a[220:221], v23 offset:24832\n\t"
    "v_mfma_f32_16x16x32_bf16 v[112:115], a[196:199], v[44:47], v[112:115]\n\t"
    "ds_read_b64_tr_b16 a[222:223], v23 offset:25856\n\t"
    "v_mfma_f32_16x16x32_bf16 v[112:115], a[192:195], a[228:231], v[112:115]\n\t"
    "s_add_u32 s61, s61, 0x80\n\t"
    "s_add_u32 s62, s62, 0x80\n\t"
    "v_mfma_f32_16x16x32_bf16 v[144:147], a[192:195], a[232:235], v[144:147]\n\t"
    "v_mfma_f32_16x16x32_bf16 v[144:147], a[196:199], v[48:51], v[144:147]\n\t"
    "v_mfma_f32_16x16x32_bf16 v[176:179], a[196:199], v[52:55], v[176:179]\n\t"
    "v_mfma_f32_16x16x32_bf16 v[176:179], a[192:195], a[236:239], v[176:179]\n\t"
    "v_mfma_f32_16x16x32_bf16 v[208:211], a[192:195], a[240:243], v[208:211]\n\t"
    "s_waitcnt vmcnt(16) lgkmcnt(0)\n\t"
    "s_barrier\n\t"
    "v_mfma_f32_16x16x32_bf16 v[208:211], a[196:199], v[56:59], v[208:211]\n\t"
    "ds_read_b128 a[248:251], v28 offset:0\n\t"
    "s_add_u32 m0, s65, 0x16000\n\t"
    "v_mfma_f32_16x16x32_bf16 v[240:243], a[196:199], v[60:63], v[240:243]\n\t"
    "buffer_load_dwordx4 v17, s[40:43], s61 offen lds\n\t"
    "v_mfma_f32_16x16x32_bf16 v[240:243], a[192:195], a[244:247], v[240:243]\n\t"
    "ds_read_b64_tr_b16 a[192:193], v26 offset:0\n\t"
    "v_mfma_f32_16x16x32_bf16 v[212:215], a[200:203], a[240:243], v[212:215]\n\t"
    "ds_read_b64_tr_b16 a[194:195], v26 offset:1024\n\t"
    "v_mfma_f32_16x16x32_bf16 v[212:215], a[204:207], v[56:59], v[212:215]\n\t"
    "ds_read_b64_tr_b16 a[196:197], v26 offset:8192\n\t"
    "v_mfma_f32_16x16x32_bf16 v[180:183], a[204:207], v[52:55], v[180:183]\n\t"
    "ds_read_b64_tr_b16 a[198:199], v26 offset:9216\n\t"
    "s_add_u32 m0, s65, 0x16400\n\t"
    "v_mfma_f32_16x16x32_bf16 v[180:183], a[200:203], a[236:239], v[180:183]\n\t"
    "buffer_load_dwordx4 v18, s[40:43], s61 offen lds\n\t"
    "v_mfma_f32_16x16x32_bf16 v[148:151], a[200:203], a[232:235], v[148:151]\n\t"
    "ds_read_b128 a[252:255], v28 offset:2048\n\t"
    "v_mfma_f32_16x16x32_bf16 v[148:151], a[204:207], v[48:51], v[148:151]\n\t"
    "ds_read_b128 v[0:3], v28 offset:4096\n\t"
    "v_mfma_f32_16x16x32_bf16 v[116:119], a[204:207], v[44:47], v[116:119]\n\t"
    "ds_read_b128 v[4:7], v28 offset:6144\n\t"
    "s_add_u32 m0, s65, 0x18000\n\t"
    "v_mfma_f32_16x16x32_bf16 v[116:119], a[200:203], a[228:231], v[116:119]\n\t"
    "buffer_load_dwordx4 v17, s[40:43], s62 offen lds\n\t"
    "v_mfma_f32_16x16x32_bf16 v[84:87], a[200:203], a[224:227], v[84:87]\n\t"
    "ds_read_b128 v[8:11], v28 offset:8192\n\t"
    "v_mfma_f32_16x16x32_bf16 v[84:87], a[204:207], v[40:43], v[84:87]\n\t"
    "ds_read_b64_tr_b16 a[200:201], v27 offset:0\n\t"
    "v_mfma_f32_16x16x32_bf16 v[88:91], a[208:211], a[224:227], v[88:91]\n\t"
    "ds_read_b64_tr_b16 a[202:203], v27 offset:1024\n\t"
    "v_mfma_f32_16x16x32_bf16 v[88:91], a[212:215], v[40:43], v[88:91]\n\t"
    "ds_read_b64_tr_b16 a[204:205], v27 offset:8192\n\t"
    "v_mfma_f32_16x16x32_bf16 v[120:123], a[212:215], v[44:47], v[120:123]\n\t"
    "ds_read_b64_tr_b16 a[206:207], v27 offset:9216\n\t"
    "s_add_u32 m0, s65, 0x18400\n\t"
    "v_mfma_f32_16x16x32_bf16 v[120:123], a[208:211], a[228:231], v[120:123]\n\t"
    "buffer_load_dwordx4 v18, s[40:43], s62 offen lds\n\t"
    "v_mfma_f32_16x16x32_bf16 v[152:155], a[208:211], a[232:235], v[152:155]\n\t"
    "ds_read_b128 v[12:15], v35 offset:0\n\t"
    "v_mfma_f32_16x16x32_bf16 v[152:155], a[212:215], v[48:51], v[152:155]\n\t"
    "ds_read_b128 a[104:107], v28 offset:512\n\t"
    "s_mov_b32 s68, 0x24000\n\t"
    "v_mfma_f32_16x16x32_bf16 v[184:187], a[212:215], v[52:55], v[184:187]\n\t"
    "buffer_load_dwordx4 a[24:27], v16, s[44:47], s68 offen nt\n\t"
    "v_mfma_f32_16x16x32_bf16 v[184:187], a[208:211], a[236:239], v[184:187]\n\t"
    "ds_read_b128 a[108:111], v28 offset:2560\n\t"
    "v_mfma_f32_16x16x32_bf16 v[216:219], a[208:211], a[240:243], v[216:219]\n\t"
    "ds_read_b128 a[112:115], v28 offset:4608\n\t"
    "v_mfma_f32_16x16x32_bf16 v[216:219], a[212:215], v[56:59], v[216:219]\n\t"
    "ds_read_b128 a[116:119], v28 offset:6656\n\t"
    "v_mfma_f32_16x16x32_bf16 v[248:251], a[212:215], v[60:63], v[248:251]\n\t"
    "buffer_load_dwordx4 a[28:31], v16, s[44:47], s68 offen offset:1024 nt\n\t"
    "v_mfma_f32_16x16x32_bf16 v[248:251], a[208:211], a[244:247], v[248:251]\n\t"
    "ds_read_b64_tr_b16 a[208:209], v26 offset:256\n\t"
    "v_mfma_f32_16x16x32_bf16 v[220:223], a[216:219], a[240:243], v[220:223]\n\t"
    "ds_read_b64_tr_b16 a[210:211], v26 offset:1280\n\t"
    "v_mfma_f32_16x16x32_bf16 v[220:223], a[220:223], v[56:59], v[220:223]\n\t"
    "ds_read_b64_tr_b16 a[212:213], v26 offset:8448\n\t"
    "v_mfma_f32_16x16x32_bf16 v[188:191], a[220:223], v[52:55], v[188:191]\n\t"
    "ds_read_b64_tr_b16 a[214:215], v26 offset:9472\n\t"
    "s_mov_b32 s68, 0x4000\n\t"
    "v_mfma_f32_16x16x32_bf16 v[188:191], a[216:219], a[236:239], v[188:191]\n\t"
    "buffer_load_dwordx4 a[32:35], v16, s[48:51], s68 offen nt\n\t"
    "v_mfma_f32_16x16x32_bf16 v[156:159], a[216:219], a[232:235], v[156:159]\n\t"
    "ds_read_b128 a[120:123], v28 offset:8704\n\t"
    "v_mfma_f32_16x16x32_bf16 v[156:159], a[220:223], v[48:51], v[156:159]\n\t"
    "ds_read_b128 a[124:127], v35 offset:512\n\t"
    "v_mfma_f32_16x16x32_bf16 v[124:127], a[220:223], v[44:47], v[124:127]\n\t"
    "buffer_load_dwordx4 a[36:39], v16, s[48:51], s68 offen offset:1024 nt\n\t"
    "v_mfma_f32_16x16x32_bf16 v[124:127], a[216:219], a[228:231], v[124:127]\n\t"
    "v_mfma_f32_16x16x32_bf16 v[92:95], a[216:219], a[224:227], v[92:95]\n\t"
    "v_mfma_f32_16x16x32_bf16 v[92:95], a[220:223], v[40:43], v[92:95]\n\t"
    "ds_read_b64_tr_b16 a[216:217], v27 offset:256\n\t"
    "s_waitcnt lgkmcnt(1)\n\t"
    "v_mfma_f32_16x16x32_bf16 v[64:67], a[192:195], a[248:251], v[64:67]\n\t"
    "ds_read_b64_tr_b16 a[218:219], v27 offset:1280\n\t"
    "v_mfma_f32_16x16x32_bf16 v[64:67], a[196:199], a[104:107], v[64:67]\n\t"
    "ds_read_b64_tr_b16 a[220:221], v27 offset:8448\n\t"
    "v_mfma_f32_16x16x32_bf16 v[96:99], a[196:199], a[108:111], v[96:99]\n\t"
    "ds_read_b64_tr_b16 a[222:223], v27 offset:9472\n\t"
    "v_mfma_f32_16x16x32_bf16 v[96:99], a[192:195], a[252:255], v[96:99]\n\t"
    "s_and_b32 s86, s84, 15\n\t"
    "s_lshr_b64 s[84:85], s[84:85], 4\n\t"
    "v_mfma_f32_16x16x32_bf16 v[128:131], a[192:195], v[0:3], v[128:131]\n\t"
    "s_lshl_b32 s61, s86, 19\n\t"
    "s_lshl_b32 s60, s86, 7\n\t"
    "s_add_u32 s62, s61, 0x40000\n\t"
    "v_mfma_f32_16x16x32_bf16 v[128:131], a[196:199], a[112:115], v[128:131]\n\t"
    "v_mfma_f32_16x16x32_bf16 v[160:163], a[196:199], a[116:119], v[160:163]\n\t"
    "v_mfma_f32_16x16x32_bf16 v[160:163], a[192:195], v[4:7], v[160:163]\n\t"
    "v_mfma_f32_16x16x32_bf16 v[192:195], a[192:195], v[8:11], v[192:195]\n\t"
    "s_waitcnt vmcnt(20) lgkmcnt(0)\n\t"
    "s_barrier\n\t"
    "v_mfma_f32_16x16x32_bf16 v[192:195], a[196:199], a[120:123], v[192:195]\n\t"
    "s_add_u32 m0, s63, 0x1a000\n\t"
    "v_mfma_f32_16x16x32_bf16 v[224:227], a[196:199], a[124:127], v[224:227]\n\t"
    "buffer_load_dwordx4 v19, s[36:39], s60 offen lds\n\t"
    "v_mfma_f32_16x16x32_bf16 v[224:227], a[192:195], v[12:15], v[224:227]\n\t"
    "ds_read_b64_tr_b16 a[192:193], v26 offset:16384\n\t"
    "v_mfma_f32_16x16x32_bf16 v[196:199], a[200:203], v[8:11], v[196:199]\n\t"
    "ds_read_b64_tr_b16 a[194:195], v26 offset:17408\n\t"
    "v_mfma_f32_16x16x32_bf16 v[196:199], a[204:207], a[120:123], v[196:199]\n\t"
    "ds_read_b64_tr_b16 a[196:197], v26 offset:24576\n\t"
    "v_mfma_f32_16x16x32_bf16 v[164:167], a[204:207], a[116:119], v[164:167]\n\t"
    "ds_read_b64_tr_b16 a[198:199], v26 offset:25600\n\t"
    "s_add_u32 m0, s65, 0x20000\n\t"
    "v_mfma_f32_16x16x32_bf16 v[164:167], a[200:203], v[4:7], v[164:167]\n\t"
    "buffer_load_dwordx4 v17, s[40:43], s61 offen lds\n\t"
    "v_mfma_f32_16x16x32_bf16 v[132:135], a[200:203], v[0:3], v[132:135]\n\t"
    "s_add_u32 m0, s63, 0x1b000\n\t"
    "v_mfma_f32_16x16x32_bf16 v[132:135], a[204:207], a[112:115], v[132:135]\n\t"
    "buffer_load_dwordx4 v20, s[36:39], s60 offen lds\n\t"
    "v_mfma_f32_16x16x32_bf16 v[100:103], a[204:207], a[108:111], v[100:103]\n\t"
    "v_mfma_f32_16x16x32_bf16 v[100:103], a[200:203], a[252:255], v[100:103]\n\t"
    "s_add_u32 m0, s63, 0x1c000\n\t"
    "v_mfma_f32_16x16x32_bf16 v[68:71], a[200:203], a[248:251], v[68:71]\n\t"
    "buffer_load_dwordx4 v21, s[36:39], s60 offen lds\n\t"
    "v_mfma_f32_16x16x32_bf16 v[68:71], a[204:207], a[104:107], v[68:71]\n\t"
    "ds_read_b64_tr_b16 a[200:201], v27 offset:16384\n\t"
    "v_mfma_f32_16x16x32_bf16 v[72:75], a[208:211], a[248:251], v[72:75]\n\t"
    "ds_read_b64_tr_b16 a[202:203], v27 offset:17408\n\t"
    "v_mfma_f32_16x16x32_bf16 v[72:75], a[212:215], a[104:107], v[72:75]\n\t"
    "ds_read_b64_tr_b16 a[204:205], v27 offset:24576\n\t"
    "v_mfma_f32_16x16x32_bf16 v[104:107], a[212:215], a[108:111], v[104:107]\n\t"
    "ds_read_b64_tr_b16 a[206:207], v27 offset:25600\n\t"
    "s_add_u32 m0, s65, 0x20400\n\t"
    "v_mfma_f32_16x16x32_bf16 v[104:107], a[208:211], a[252:255], v[104:107]\n\t"
    "buffer_load_dwordx4 v18, s[40:43], s61 offen lds\n\t"
    "v_mfma_f32_16x16x32_bf16 v[136:139], a[208:211], v[0:3], v[136:139]\n\t"
    "s_add_u32 m0, s63, 0x1d000\n\t"
    "v_mfma_f32_16x16x32_bf16 v[136:139], a[212:215], a[112:115], v[136:139]\n\t"
    "buffer_load_dwordx4 v32, s[36:39], s60 offen lds\n\t"
    "v_mfma_f32_16x16x32_bf16 v[168:171], a[212:215], a[116:119], v[168:171]\n\t"
    "v_mfma_f32_16x16x32_bf16 v[168:171], a[208:211], v[4:7], v[168:171]\n\t"
    "v_mfma_f32_16x16x32_bf16 v[200:203], a[208:211], v[8:11], v[200:203]\n\t"
    "s_add_u32 m0, s65, 0x22000\n\t"
    "v_mfma_f32_16x16x32_bf16 v[200:203], a[212:215], a[120:123], v[200:203]\n\t"
    "buffer_load_dwordx4 v17, s[40:43], s62 offen lds\n\t"
    "v_mfma_f32_16x16x32_bf16 v[232:235], a[212:215], a[124:127], v[232:235]\n\t"
    "v_mfma_f32_16x16x32_bf16 v[232:235], a[208:211], v[12:15], v[232:235]\n\t"
    "ds_read_b64_tr_b16 a[208:209], v26 offset:16640\n\t"
    "v_mfma_f32_16x16x32_bf16 v[204:207], a[216:219], v[8:11], v[204:207]\n\t"
    "ds_read_b64_tr_b16 a[210:211], v26 offset:17664\n\t"
    "v_mfma_f32_16x16x32_bf16 v[204:207], a[220:223], a[120:123], v[204:207]\n\t"
    "ds_read_b64_tr_b16 a[212:213], v26 offset:24832\n\t"
    "v_mfma_f32_16x16x32_bf16 v[172:175], a[220:223], a[116:119], v[172:175]\n\t"
    "ds_read_b64_tr_b16 a[214:215], v26 offset:25856\n\t"
    "s_add_u32 m0, s63, 0x1e000\n\t"
    "v_mfma_f32_16x16x32_bf16 v[172:175], a[216:219], v[4:7], v[172:175]\n\t"
    "buffer_load_dwordx4 v33, s[36:39], s60 offen lds\n\t"
    "v_mfma_f32_16x16x32_bf16 v[140:143], a[216:219], v[0:3], v[140:143]\n\t"
    "s_add_u32 m0, s63, 0x1f000\n\t"
    "v_mfma_f32_16x16x32_bf16 v[140:143], a[220:223], a[112:115], v[140:143]\n\t"
    "buffer_load_dwordx4 v34, s[36:39], s60 offen lds\n\t"
    "v_mfma_f32_16x16x32_bf16 v[108:111], a[220:223], a[108:111], v[108:111]\n\t"
    "s_add_u32 m0, s65, 0x22400\n\t"
    "v_mfma_f32_16x16x32_bf16 v[108:111], a[216:219], a[252:255], v[108:111]\n\t"
    "buffer_load_dwordx4 v18, s[40:43], s62 offen lds\n\t"
    "v_mfma_f32_16x16x32_bf16 v[76:79], a[216:219], a[248:251], v[76:79]\n\t"
    "v_mfma_f32_16x16x32_bf16 v[76:79], a[220:223], a[104:107], v[76:79]\n\t"
    "ds_read_b64_tr_b16 a[216:217], v27 offset:16640\n\t"
    "s_waitcnt lgkmcnt(5)\n\t"
    "v_mfma_f32_16x16x32_bf16 v[80:83], a[192:195], a[248:251], v[80:83]\n\t"
    "ds_read_b64_tr_b16 a[218:219], v27 offset:17664\n\t"
    "v_mfma_f32_16x16x32_bf16 v[80:83], a[196:199], a[104:107], v[80:83]\n\t"
    "ds_read_b64_tr_b16 a[220:221], v27 offset:24832\n\t"
    "v_mfma_f32_16x16x32_bf16 v[112:115], a[196:199], a[108:111], v[112:115]\n\t"
    "ds_read_b64_tr_b16 a[222:223], v27 offset:25856\n\t"
    "v_mfma_f32_16x16x32_bf16 v[112:115], a[192:195], a[252:255], v[112:115]\n\t"
    "s_add_u32 s61, s61, 0x80\n\t"
    "s_add_u32 s62, s62, 0x80\n\t"
    "v_mfma_f32_16x16x32_bf16 v[144:147], a[192:195], v[0:3], v[144:147]\n\t"
    "v_mfma_f32_16x16x32_bf16 v[144:147], a[196:199], a[112:115], v[144:147]\n\t"
    "v_mfma_f32_16x16x32_bf16 v[176:179], a[196:199], a[116:119], v[176:179]\n\t"
    "v_mfma_f32_16x16x32_bf16 v[176:179], a[192:195], v[4:7], v[176:179]\n\t"
    "v_mfma_f32_16x16x32_bf16 v[208:211], a[192:195], v[8:11], v[208:211]\n\t"
    "s_waitcnt vmcnt(18) lgkmcnt(0)\n\t"
    "s_barrier\n\t"
    "v_mfma_f32_16x16x32_bf16 v[208:211], a[196:199], a[120:123], v[208:211]\n\t"
    "ds_read_b128 a[224:227], v24 offset:0\n\t"
    "s_add_u32 m0, s65, 0x24000\n\t"
    "v_mfma_f32_16x16x32_bf16 v[240:243], a[196:199], a[124:127], v[240:243]\n\t"
    "buffer_load_dwordx4 v17, s[40:43], s61 offen lds\n\t"
    "v_mfma_f32_16x16x32_bf16 v[240:243], a[192:195], v[12:15], v[240:243]\n\t"
    "ds_read_b64_tr_b16 a[192:193], v22 offset:0\n\t"
    "v_mfma_f32_16x16x32_bf16 v[212:215], a[200:203], v[8:11], v[212:215]\n\t"
    "ds_read_b64_tr_b16 a[194:195], v22 offset:1024\n\t"
    "v_mfma_f32_16x16x32_bf16 v[212:215], a[204:207], a[120:123], v[212:215]\n\t"
    "ds_read_b64_tr_b16 a[196:197], v22 offset:8192\n\t"
    "v_mfma_f32_16x16x32_bf16 v[180:183], a[204:207], a[116:119], v[180:183]\n\t"
    "ds_read_b64_tr_b16 a[198:199], v22 offset:9216\n\t"
    "v_mfma_f32_16x16x32_bf16 v[180:183], a[200:203], v[4:7], v[180:183]\n\t"
    "ds_read_b128 a[228:231], v24 offset:2048\n\t"
    "v_mfma_f32_16x16x32_bf16 v[148:151], a[200:203], v[0:3], v[148:151]\n\t"
    "ds_read_b128 a[232:235], v24 offset:4096\n\t"
    "s_add_u32 m0, s65, 0x24400\n\t"
    "v_mfma_f32_16x16x32_bf16 v[148:151], a[204:207], a[112:115], v[148:151]\n\t"
    "buffer_load_dwordx4 v18, s[40:43], s61 offen lds\n\t"
    "v_mfma_f32_16x16x32_bf16 v[116:119], a[204:207], a[108:111], v[116:119]\n\t"
    "ds_read_b128 a[236:239], v24 offset:6144\n\t"
    "v_mfma_f32_16x16x32_bf16 v[116:119], a[200:203], a[252:255], v[116:119]\n\t"
    "ds_read_b128 a[240:243], v24 offset:8192\n\t"
    "v_mfma_f32_16x16x32_bf16 v[84:87], a[200:203], a[248:251], v[84:87]\n\t"
    "ds_read_b128 a[244:247], v25 offset:0\n\t"
    "v_mfma_f32_16x16x32_bf16 v[84:87], a[204:207], a[104:107], v[84:87]\n\t"
    "ds_read_b64_tr_b16 a[200:201], v23 offset:0\n\t"
    "v_mfma_f32_16x16x32_bf16 v[88:91], a[208:211], a[248:251], v[88:91]\n\t"
    "ds_read_b64_tr_b16 a[202:203], v23 offset:1024\n\t"
    "v_mfma_f32_16x16x32_bf16 v[88:91], a[212:215], a[104:107], v[88:91]\n\t"
    "ds_read_b64_tr_b16 a[204:205], v23 offset:8192\n\t"
    "v_mfma_f32_16x16x32_bf16 v[120:123], a[212:215], a[108:111], v[120:123]\n\t"
    "ds_read_b64_tr_b16 a[206:207], v23 offset:9216\n\t"
    "s_add_u32 m0, s65, 0x26000\n\t"
    "v_mfma_f32_16x16x32_bf16 v[120:123], a[208:211], a[252:255], v[120:123]\n\t"
    "buffer_load_dwordx4 v17, s[40:43], s62 offen lds\n\t"
    "v_mfma_f32_16x16x32_bf16 v[152:155], a[208:211], v[0:3], v[152:155]\n\t"
    "ds_read_b128 v[40:43], v24 offset:512\n\t"
    "v_mfma_f32_16x16x32_bf16 v[152:155], a[212:215], a[112:115], v[152:155]\n\t"
    "ds_read_b128 v[44:47], v24 offset:2560\n\t"
    "s_add_u32 m0, s65, 0x26400\n\t"
    "v_mfma_f32_16x16x32_bf16 v[184:187], a[212:215], a[116:119], v[184:187]\n\t"
    "buffer_load_dwordx4 v18, s[40:43], s62 offen lds\n\t"
    "v_mfma_f32_16x16x32_bf16 v[184:187], a[208:211], v[4:7], v[184:187]\n\t"
    "ds_read_b128 v[48:51], v24 offset:4608\n\t"
    "v_mfma_f32_16x16x32_bf16 v[216:219], a[208:211], v[8:11], v[216:219]\n\t"
    "ds_read_b128 v[52:55], v24 offset:6656\n\t"
    "v_mfma_f32_16x16x32_bf16 v[216:219], a[212:215], a[120:123], v[216:219]\n\t"
    "ds_read_b128 v[56:59], v24 offset:8704\n\t"
    "s_mov_b32 s68, 0x24000\n\t"
    "v_mfma_f32_16x16x32_bf16 v[248:251], a[212:215], a[124:127], v[248:251]\n\t"
    "buffer_load_dwordx4 a[40:43], v16, s[48:51], s68 offen nt\n\t"
    "v_mfma_f32_16x16x32_bf16 v[248:251], a[208:211], v[12:15], v[248:251]\n\t"
    "ds_read_b64_tr_b16 a[208:209], v22 offset:256\n\t"
    "v_mfma_f32_16x16x32_bf16 v[220:223], a[216:219], v[8:11], v[220:223]\n\t"
    "ds_read_b64_tr_b16 a[210:211], v22 offset:1280\n\t"
    "v_mfma_f32_16x16x32_bf16 v[220:223], a[220:223], a[120:123], v[220:223]\n\t"
    "ds_read_b64_tr_b16 a[212:213], v22 offset:8448\n\t"
    "v_mfma_f32_16x16x32_bf16 v[188:191], a[220:223], a[116:119], v[188:191]\n\t"
    "ds_read_b64_tr_b16 a[214:215], v22 offset:9472\n\t"
    "v_mfma_f32_16x16x32_bf16 v[188:191], a[216:219], v[4:7], v[188:191]\n\t"
    "ds_read_b128 v[60:63], v25 offset:512\n\t"
    "v_mfma_f32_16x16x32_bf16 v[156:159], a[216:219], v[0:3], v[156:159]\n\t"
    "v_mfma_f32_16x16x32_bf16 v[156:159], a[220:223], a[112:115], v[156:159]\n\t"
    "v_mfma_f32_16x16x32_bf16 v[124:127], a[220:223], a[108:111], v[124:127]\n\t"
    "buffer_load_dwordx4 a[44:47], v16, s[48:51], s68 offen offset:1024 nt\n\t"
    "v_mfma_f32_16x16x32_bf16 v[124:127], a[216:219], a[252:255], v[124:127]\n\t"
    "v_mfma_f32_16x16x32_bf16 v[92:95], a[216:219], a[248:251], v[92:95]\n\t"
    "v_mfma_f32_16x16x32_bf16 v[92:95], a[220:223], a[104:107], v[92:95]\n\t"
    "ds_read_b64_tr_b16 a[216:217], v23 offset:256\n\t"
    "s_waitcnt lgkmcnt(1)\n\t"
    "v_mfma_f32_16x16x32_bf16 v[64:67], a[192:195], a[224:227], v[64:67]\n\t"
    "ds_read_b64_tr_b16 a[218:219], v23 offset:1280\n\t"
    "v_mfma_f32_16x16x32_bf16 v[64:67], a[196:199], v[40:43], v[64:67]\n\t"
    "ds_read_b64_tr_b16 a[220:221], v23 offset:8448\n\t"
    "v_mfma_f32_16x16x32_bf16 v[96:99], a[196:199], v[44:47], v[96:99]\n\t"
    "ds_read_b64_tr_b16 a[222:223], v23 offset:9472\n\t"
    "v_mfma_f32_16x16x32_bf16 v[96:99], a[192:195], a[228:231], v[96:99]\n\t"
    "s_and_b32 s86, s84, 15\n\t"
    "s_lshr_b64 s[84:85], s[84:85], 4\n\t"
    "v_mfma_f32_16x16x32_bf16 v[128:131], a[192:195], a[232:235], v[128:131]\n\t"
    "s_lshl_b32 s61, s86, 19\n\t"
    "s_lshl_b32 s60, s86, 7\n\t"
    "s_add_u32 s62, s61, 0x40000\n\t"
    "v_mfma_f32_16x16x32_bf16 v[128:131], a[196:199], v[48:51], v[128:131]\n\t"
    "v_mfma_f32_16x16x32_bf16 v[160:163], a[196:199], v[52:55], v[160:163]\n\t"
    "v_mfma_f32_16x16x32_bf16 v[160:163], a[192:195], a[236:239], v[160:163]\n\t"
    "v_mfma_f32_16x16x32_bf16 v[192:195], a[192:195], a[240:243], v[192:195]\n\t"
    "s_waitcnt vmcnt(20) lgkmcnt(0)\n\t"
    "s_barrier\n\t"
    "v_mfma_f32_16x16x32_bf16 v[192:195], a[196:199], v[56:59], v[192:195]\n\t"
    "s_add_u32 m0, s63, 0xc000\n\t"
    "v_mfma_f32_16x16x32_bf16 v[224:227], a[196:199], v[60:63], v[224:227]\n\t"
    "buffer_load_dwordx4 v19, s[36:39], s60 offen lds\n\t"
    "v_mfma_f32_16x16x32_bf16 v[224:227], a[192:195], a[244:247], v[224:227]\n\t"
    "ds_read_b64_tr_b16 a[192:193], v22 offset:16384\n\t"
    "v_mfma_f32_16x16x32_bf16 v[196:199], a[200:203], a[240:243], v[196:199]\n\t"
    "ds_read_b64_tr_b16 a[194:195], v22 offset:17408\n\t"
    "v_mfma_f32_16x16x32_bf16 v[196:199], a[204:207], v[56:59], v[196:199]\n\t"
    "ds_read_b64_tr_b16 a[196:197], v22 offset:24576\n\t"
    "v_mfma_f32_16x16x32_bf16 v[164:167], a[204:207], v[52:55], v[164:167]\n\t"
    "ds_read_b64_tr_b16 a[198:199], v22 offset:25600\n\t"
    "s_add_u32 m0, s65, 0x12000\n\t"
    "v_mfma_f32_16x16x32_bf16 v[164:167], a[200:203], a[236:239], v[164:167]\n\t"
    "buffer_load_dwordx4 v17, s[40:43], s61 offen lds\n\t"
    "v_mfma_f32_16x16x32_bf16 v[132:135], a[200:203], a[232:235], v[132:135]\n\t"
    "s_add_u32 m0, s63, 0xd000\n\t"
    "v_mfma_f32_16x16x32_bf16 v[132:135], a[204:207], v[48:51], v[132:135]\n\t"
    "buffer_load_dwordx4 v20, s[36:39], s60 offen lds\n\t"
    "v_mfma_f32_16x16x32_bf16 v[100:103], a[204:207], v[44:47], v[100:103]\n\t"
    "v_mfma_f32_16x16x32_bf16 v[100:103], a[200:203], a[228:231], v[100:103]\n\t"
    "s_add_u32 m0, s63, 0xe000\n\t"
    "v_mfma_f32_16x16x32_bf16 v[68:71], a[200:203], a[224:227], v[68:71]\n\t"
    "buffer_load_dwordx4 v21, s[36:39], s60 offen lds\n\t"
    "v_mfma_f32_16x16x32_bf16 v[68:71], a[204:207], v[40:43], v[68:71]\n\t"
    "ds_read_b64_tr_b16 a[200:201], v23 offset:16384\n\t"
    "v_mfma_f32_16x16x32_bf16 v[72:75], a[208:211], a[224:227], v[72:75]\n\t"
    "ds_read_b64_tr_b16 a[202:203], v23 offset:17408\n\t"
    "v_mfma_f32_16x16x32_bf16 v[72:75], a[212:215], v[40:43], v[72:75]\n\t"
    "ds_read_b64_tr_b16 a[204:205], v23 offset:24576\n\t"
    "v_mfma_f32_16x16x32_bf16 v[104:107], a[212:215], v[44:47], v[104:107]\n\t"
    "ds_read_b64_tr_b16 a[206:207], v23 offset:25600\n\t"
    "s_add_u32 m0, s65, 0x12400\n\t"
    "v_mfma_f32_16x16x32_bf16 v[104:107], a[208:211], a[228:231], v[104:107]\n\t"
    "buffer_load_dwordx4 v18, s[40:43], s61 offen lds\n\t"
    "v_mfma_f32_16x16x32_bf16 v[136:139], a[208:211], a[232:235], v[136:139]\n\t"
    "s_add_u32 m0, s63, 0xf000\n\t"
    "v_mfma_f32_16x16x32_bf16 v[136:139], a[212:215], v[48:51], v[136:139]\n\t"
    "buffer_load_dwordx4 v32, s[36:39], s60 offen lds\n\t"
    "v_mfma_f32_16x16x32_bf16 v[168:171], a[212:215], v[52:55], v[168:171]\n\t"
    "v_mfma_f32_16x16x32_bf16 v[168:171], a[208:211], a[236:239], v[168:171]\n\t"
    "v_mfma_f32_16x16x32_bf16 v[200:203], a[208:211], a[240:243], v[200:203]\n\t"
    "s_add_u32 m0, s65, 0x14000\n\t"
    "v_mfma_f32_16x16x32_bf16 v[200:203], a[212:215], v[56:59], v[200:203]\n\t"
    "buffer_load_dwordx4 v17, s[40:43], s62 offen lds\n\t"
    "v_mfma_f32_16x16x32_bf16 v[232:235], a[212:215], v[60:63], v[232:235]\n\t"
    "v_mfma_f32_16x16x32_bf16 v[232:235], a[208:211], a[244:247], v[232:235]\n\t"
    "ds_read_b64_tr_b16 a[208:209], v22 offset:16640\n\t"
    "v_mfma_f32_16x16x32_bf16 v[204:207], a[216:219], a[240:243], v[204:207]\n\t"
    "ds_read_b64_tr_b16 a[210:211], v22 offset:17664\n\t"
    "v_mfma_f32_16x16x32_bf16 v[204:207], a[220:223], v[56:59], v[204:207]\n\t"
    "ds_read_b64_tr_b16 a[212:213], v22 offset:24832\n\t"
    "v_mfma_f32_16x16x32_bf16 v[172:175], a[220:223], v[52:55], v[172:175]\n\t"
    "ds_read_b64_tr_b16 a[214:215], v22 offset:25856\n\t"
    "s_add_u32 m0, s63, 0x10000\n\t"
    "v_mfma_f32_16x16x32_bf16 v[172:175], a[216:219], a[236:239], v[172:175]\n\t"
    "buffer_load_dwordx4 v33, s[36:39], s60 offen lds\n\t"
    "v_mfma_f32_16x16x32_bf16 v[140:143], a[216:219], a[232:235], v[140:143]\n\t"
    "s_add_u32 m0, s63, 0x11000\n\t"
    "v_mfma_f32_16x16x32_bf16 v[140:143], a[220:223], v[48:51], v[140:143]\n\t"
    "buffer_load_dwordx4 v34, s[36:39], s60 offen lds\n\t"
    "v_mfma_f32_16x16x32_bf16 v[108:111], a[220:223], v[44:47], v[108:111]\n\t"
    "s_add_u32 m0, s65, 0x14400\n\t"
    "v_mfma_f32_16x16x32_bf16 v[108:111], a[216:219], a[228:231], v[108:111]\n\t"
    "buffer_load_dwordx4 v18, s[40:43], s62 offen lds\n\t"
    "v_mfma_f32_16x16x32_bf16 v[76:79], a[216:219], a[224:227], v[76:79]\n\t"
    "v_mfma_f32_16x16x32_bf16 v[76:79], a[220:223], v[40:43], v[76:79]\n\t"
    "ds_read_b64_tr_b16 a[216:217], v23 offset:16640\n\t"
    "s_waitcnt lgkmcnt(5)\n\t"
    "v_mfma_f32_16x16x32_bf16 v[80:83], a[192:195], a[224:227], v[80:83]\n\t"
    "ds_read_b64_tr_b16 a[218:219], v23 offset:17664\n\t"
    "v_mfma_f32_16x16x32_bf16 v[80:83], a[196:199], v[40:43], v[80:83]\n\t"
    "ds_read_b64_tr_b16 a[220:221], v23 offset:24832\n\t"
    "v_mfma_f32_16x16x32_bf16 v[112:115], a[196:199], v[44:47], v[112:115]\n\t"
    "ds_read_b64_tr_b16 a[222:223], v23 offset:25856\n\t"
    "v_mfma_f32_16x16x32_bf16 v[112:115], a[192:195], a[228:231], v[112:115]\n\t"
    "s_add_u32 s61, s61, 0x80\n\t"
    "s_add_u32 s62, s62, 0x80\n\t"
    "v_mfma_f32_16x16x32_bf16 v[144:147], a[192:195], a[232:235], v[144:147]\n\t"
    "v_mfma_f32_16x16x32_bf16 v[144:147], a[196:199], v[48:51], v[144:147]\n\t"
    "v_mfma_f32_16x16x32_bf16 v[176:179], a[196:199], v[52:55], v[176:179]\n\t"
    "v_mfma_f32_16x16x32_bf16 v[176:179], a[192:195], a[236:239], v[176:179]\n\t"
    "v_mfma_f32_16x16x32_bf16 v[208:211], a[192:195], a[240:243], v[208:211]\n\t"
    "s_waitcnt vmcnt(16) lgkmcnt(0)\n\t"
    "s_barrier\n\t"
    "v_mfma_f32_16x16x32_bf16 v[208:211], a[196:199], v[56:59], v[208:211]\n\t"
    "ds_read_b128 a[248:251], v28 offset:0\n\t"
    "s_add_u32 m0, s65, 0x16000\n\t"
    "v_mfma_f32_16x16x32_bf16 v[240:243], a[196:199], v[60:63], v[240:243]\n\t"
    "buffer_load_dwordx4 v17, s[40:43], s61 offen lds\n\t"
    "v_mfma_f32_16x16x32_bf16 v[240:243], a[192:195], a[244:247], v[240:243]\n\t"
    "ds_read_b64_tr_b16 a[192:193], v26 offset:0\n\t"
    "v_mfma_f32_16x16x32_bf16 v[212:215], a[200:203], a[240:243], v[212:215]\n\t"
    "ds_read_b64_tr_b16 a[194:195], v26 offset:1024\n\t"
    "v_mfma_f32_16x16x32_bf16 v[212:215], a[204:207], v[56:59], v[212:215]\n\t"
    "ds_read_b64_tr_b16 a[196:197], v26 offset:8192\n\t"
    "v_mfma_f32_16x16x32_bf16 v[180:183], a[204:207], v[52:55], v[180:183]\n\t"
    "ds_read_b64_tr_b16 a[198:199], v26 offset:9216\n\t"
    "s_add_u32 m0, s65, 0x16400\n\t"
    "v_mfma_f32_16x16x32_bf16 v[180:183], a[200:203], a[236:239], v[180:183]\n\t"
    "buffer_load_dwordx4 v18, s[40:43], s61 offen lds\n\t"
    "v_mfma_f32_16x16x32_bf16 v[148:151], a[200:203], a[232:235], v[148:151]\n\t"
    "ds_read_b128 a[252:255], v28 offset:2048\n\t"
    "v_mfma_f32_16x16x32_bf16 v[148:151], a[204:207], v[48:51], v[148:151]\n\t"
    "ds_read_b128 v[0:3], v28 offset:4096\n\t"
    "v_mfma_f32_16x16x32_bf16 v[116:119], a[204:207], v[44:47], v[116:119]\n\t"
    "ds_read_b128 v[4:7], v28 offset:6144\n\t"
    "s_add_u32 m0, s65, 0x18000\n\t"
    "v_mfma_f32_16x16x32_bf16 v[116:119], a[200:203], a[228:231], v[116:119]\n\t"
    "buffer_load_dwordx4 v17, s[40:43], s62 offen lds\n\t"
    "v_mfma_f32_16x16x32_bf16 v[84:87], a[200:203], a[224:227], v[84:87]\n\t"
    "ds_read_b128 v[8:11], v28 offset:8192\n\t"
    "v_mfma_f32_16x16x32_bf16 v[84:87], a[204:207], v[40:43], v[84:87]\n\t"
    "ds_read_b64_tr_b16 a[200:201], v27 offset:0\n\t"
    "v_mfma_f32_16x16x32_bf16 v[88:91], a[208:211], a[224:227], v[88:91]\n\t"
    "ds_read_b64_tr_b16 a[202:203], v27 offset:1024\n\t"
    "v_mfma_f32_16x16x32_bf16 v[88:91], a[212:215], v[40:43], v[88:91]\n\t"
    "ds_read_b64_tr_b16 a[204:205], v27 offset:8192\n\t"
    "v_mfma_f32_16x16x32_bf16 v[120:123], a[212:215], v[44:47], v[120:123]\n\t"
    "ds_read_b64_tr_b16 a[206:207], v27 offset:9216\n\t"
    "s_add_u32 m0, s65, 0x18400\n\t"
    "v_mfma_f32_16x16x32_bf16 v[120:123], a[208:211], a[228:231], v[120:123]\n\t"
    "buffer_load_dwordx4 v18, s[40:43], s62 offen lds\n\t"
    "v_mfma_f32_16x16x32_bf16 v[152:155], a[208:211], a[232:235], v[152:155]\n\t"
    "ds_read_b128 v[12:15], v35 offset:0\n\t"
    "v_mfma_f32_16x16x32_bf16 v[152:155], a[212:215], v[48:51], v[152:155]\n\t"
    "ds_read_b128 a[104:107], v28 offset:512\n\t"
    "s_mov_b32 s68, 0x6000\n\t"
    "v_mfma_f32_16x16x32_bf16 v[184:187], a[212:215], v[52:55], v[184:187]\n\t"
    "buffer_load_dwordx4 a[48:51], v16, s[44:47], s68 offen nt\n\t"
    "v_mfma_f32_16x16x32_bf16 v[184:187], a[208:211], a[236:239], v[184:187]\n\t"
    "ds_read_b128 a[108:111], v28 offset:2560\n\t"
    "v_mfma_f32_16x16x32_bf16 v[216:219], a[208:211], a[240:243], v[216:219]\n\t"
    "ds_read_b128 a[112:115], v28 offset:4608\n\t"
    "v_mfma_f32_16x16x32_bf16 v[216:219], a[212:215], v[56:59], v[216:219]\n\t"
    "ds_read_b128 a[116:119], v28 offset:6656\n\t"
    "v_mfma_f32_16x16x32_bf16 v[248:251], a[212:215], v[60:63], v[248:251]\n\t"
    "buffer_load_dwordx4 a[52:55], v16, s[44:47], s68 offen offset:1024 nt\n\t"
    "v_mfma_f32_16x16x32_bf16 v[248:251], a[208:211], a[244:247], v[248:251]\n\t"
    "ds_read_b64_tr_b16 a[208:209], v26 offset:256\n\t"
    "v_mfma_f32_16x16x32_bf16 v[220:223], a[216:219], a[240:243], v[220:223]\n\t"
    "ds_read_b64_tr_b16 a[210:211], v26 offset:1280\n\t"
    "v_mfma_f32_16x16x32_bf16 v[220:223], a[220:223], v[56:59], v[220:223]\n\t"
    "ds_read_b64_tr_b16 a[212:213], v26 offset:8448\n\t"
    "v_mfma_f32_16x16x32_bf16 v[188:191], a[220:223], v[52:55], v[188:191]\n\t"
    "ds_read_b64_tr_b16 a[214:215], v26 offset:9472\n\t"
    "s_mov_b32 s68, 0x26000\n\t"
    "v_mfma_f32_16x16x32_bf16 v[188:191], a[216:219], a[236:239], v[188:191]\n\t"
    "buffer_load_dwordx4 a[56:59], v16, s[44:47], s68 offen nt\n\t"
    "v_mfma_f32_16x16x32_bf16 v[156:159], a[216:219], a[232:235], v[156:159]\n\t"
    "ds_read_b128 a[120:123], v28 offset:8704\n\t"
    "v_mfma_f32_16x16x32_bf16 v[156:159], a[220:223], v[48:51], v[156:159]\n\t"
    "ds_read_b128 a[124:127], v35 offset:512\n\t"
    "v_mfma_f32_16x16x32_bf16 v[124:127], a[220:223], v[44:47], v[124:127]\n\t"
    "buffer_load_dwordx4 a[60:63], v16, s[44:47], s68 offen offset:1024 nt\n\t"
    "v_mfma_f32_16x16x32_bf16 v[124:127], a[216:219], a[228:231], v[124:127]\n\t"
    "v_mfma_f32_16x16x32_bf16 v[92:95], a[216:219], a[224:227], v[92:95]\n\t"
    "v_mfma_f32_16x16x32_bf16 v[92:95], a[220:223], v[40:43], v[92:95]\n\t"
    "ds_read_b64_tr_b16 a[216:217], v27 offset:256\n\t"
    "s_waitcnt lgkmcnt(1)\n\t"
    "v_mfma_f32_16x16x32_bf16 v[64:67], a[192:195], a[248:251], v[64:67]\n\t"
    "ds_read_b64_tr_b16 a[218:219], v27 offset:1280\n\t"
    "v_mfma_f32_16x16x32_bf16 v[64:67], a[196:199], a[104:107], v[64:67]\n\t"
    "ds_read_b64_tr_b16 a[220:221], v27 offset:8448\n\t"
    "v_mfma_f32_16x16x32_bf16 v[96:99], a[196:199], a[108:111], v[96:99]\n\t"
    "ds_read_b64_tr_b16 a[222:223], v27 offset:9472\n\t"
    "v_mfma_f32_16x16x32_bf16 v[96:99], a[192:195], a[252:255], v[96:99]\n\t"
    "s_and_b32 s86, s84, 15\n\t"
    "s_lshr_b64 s[84:85], s[84:85], 4\n\t"
    "v_mfma_f32_16x16x32_bf16 v[128:131], a[192:195], v[0:3], v[128:131]\n\t"
    "s_lshl_b32 s61, s86, 19\n\t"
    "s_lshl_b32 s60, s86, 7\n\t"
    "s_add_u32 s62, s61, 0x40000\n\t"
    "v_mfma_f32_16x16x32_bf16 v[128:131], a[196:199], a[112:115], v[128:131]\n\t"
    "v_mfma_f32_16x16x32_bf16 v[160:163], a[196:199], a[116:119], v[160:163]\n\t"
    "v_mfma_f32_16x16x32_bf16 v[160:163], a[192:195], v[4:7], v[160:163]\n\t"
    "v_mfma_f32_16x16x32_bf16 v[192:195], a[192:195], v[8:11], v[192:195]\n\t"
    "s_waitcnt vmcnt(20) lgkmcnt(0)\n\t"
    "s_barrier\n\t"
    "v_mfma_f32_16x16x32_bf16 v[192:195], a[196:199], a[120:123], v[192:195]\n\t"
    "s_add_u32 m0, s63, 0x1a000\n\t"
    "v_mfma_f32_16x16x32_bf16 v[224:227], a[196:199], a[124:127], v[224:227]\n\t"
    "buffer_load_dwordx4 v19, s[36:39], s60 offen lds\n\t"
    "v_mfma_f32_16x16x32_bf16 v[224:227], a[192:195], v[12:15], v[224:227]\n\t"
    "ds_read_b64_tr_b16 a[192:193], v26 offset:16384\n\t"
    "v_mfma_f32_16x16x32_bf16 v[196:199], a[200:203], v[8:11], v[196:199]\n\t"
    "ds_read_b64_tr_b16 a[194:195], v26 offset:17408\n\t"
    "v_mfma_f32_16x16x32_bf16 v[196:199], a[204:207], a[120:123], v[196:199]\n\t"
    "ds_read_b64_tr_b16 a[196:197], v26 offset:24576\n\t"
    "v_mfma_f32_16x16x32_bf16 v[164:167], a[204:207], a[116:119], v[164:167]\n\t"
    "ds_read_b64_tr_b16 a[198:199], v26 offset:25600\n\t"
    "s_add_u32 m0, s65, 0x20000\n\t"
    "v_mfma_f32_16x16x32_bf16 v[164:167], a[200:203], v[4:7], v[164:167]\n\t"
    "buffer_load_dwordx4 v17, s[40:43], s61 offen lds\n\t"
    "v_mfma_f32_16x16x32_bf16 v[132:135], a[200:203], v[0:3], v[132:135]\n\t"
    "s_add_u32 m0, s63, 0x1b000\n\t"
    "v_mfma_f32_16x16x32_bf16 v[132:135], a[204:207], a[112:115], v[132:135]\n\t"
    "buffer_load_dwordx4 v20, s[36:39], s60 offen lds\n\t"
    "v_mfma_f32_16x16x32_bf16 v[100:103], a[204:207], a[108:111], v[100:103]\n\t"
    "v_mfma_f32_16x16x32_bf16 v[100:103], a[200:203], a[252:255], v[100:103]\n\t"
    "s_add_u32 m0, s63, 0x1c000\n\t"
    "v_mfma_f32_16x16x32_bf16 v[68:71], a[200:203], a[248:251], v[68:71]\n\t"
    "buffer_load_dwordx4 v21, s[36:39], s60 offen lds\n\t"
    "v_mfma_f32_16x16x32_bf16 v[68:71], a[204:207], a[104:107], v[68:71]\n\t"
    "ds_read_b64_tr_b16 a[200:201], v27 offset:16384\n\t"
    "v_mfma_f32_16x16x32_bf16 v[72:75], a[208:211], a[248:251], v[72:75]\n\t"
    "ds_read_b64_tr_b16 a[202:203], v27 offset:17408\n\t"
    "v_mfma_f32_16x16x32_bf16 v[72:75], a[212:215], a[104:107], v[72:75]\n\t"
    "ds_read_b64_tr_b16 a[204:205], v27 offset:24576\n\t"
    "v_mfma_f32_16x16x32_bf16 v[104:107], a[212:215], a[108:111], v[104:107]\n\t"
    "ds_read_b64_tr_b16 a[206:207], v27 offset:25600\n\t"
    "s_add_u32 m0, s65, 0x20400\n\t"
    "v_mfma_f32_16x16x32_bf16 v[104:107], a[208:211], a[252:255], v[104:107]\n\t"
    "buffer_load_dwordx4 v18, s[40:43], s61 offen lds\n\t"
    "v_mfma_f32_16x16x32_bf16 v[136:139], a[208:211], v[0:3], v[136:139]\n\t"
    "s_add_u32 m0, s63, 0x1d000\n\t"
    "v_mfma_f32_16x16x32_bf16 v[136:139], a[212:215], a[112:115], v[136:139]\n\t"
    "buffer_load_dwordx4 v32, s[36:39], s60 offen lds\n\t"
    "v_mfma_f32_16x16x32_bf16 v[168:171], a[212:215], a[116:119], v[168:171]\n\t"
    "v_mfma_f32_16x16x32_bf16 v[168:171], a[208:211], v[4:7], v[168:171]\n\t"
    "v_mfma_f32_16x16x32_bf16 v[200:203], a[208:211], v[8:11], v[200:203]\n\t"
    "s_add_u32 m0, s65, 0x22000\n\t"
    "v_mfma_f32_16x16x32_bf16 v[200:203], a[212:215], a[120:123], v[200:203]\n\t"
    "buffer_load_dwordx4 v17, s[40:43], s62 offen lds\n\t"
    "v_mfma_f32_16x16x32_bf16 v[232:235], a[212:215], a[124:127], v[232:235]\n\t"
    "v_mfma_f32_16x16x32_bf16 v[232:235], a[208:211], v[12:15], v[232:235]\n\t"
    "ds_read_b64_tr_b16 a[208:209], v26 offset:16640\n\t"
    "v_mfma_f32_16x16x32_bf16 v[204:207], a[216:219], v[8:11], v[204:207]\n\t"
    "ds_read_b64_tr_b16 a[210:211], v26 offset:17664\n\t"
    "v_mfma_f32_16x16x32_bf16 v[204:207], a[220:223], a[120:123], v[204:207]\n\t"
    "ds_read_b64_tr_b16 a[212:213], v26 offset:24832\n\t"
    "v_mfma_f32_16x16x32_bf16 v[172:175], a[220:223], a[116:119], v[172:175]\n\t"
    "ds_read_b64_tr_b16 a[214:215], v26 offset:25856\n\t"
    "s_add_u32 m0, s63, 0x1e000\n\t"
    "v_mfma_f32_16x16x32_bf16 v[172:175], a[216:219], v[4:7], v[172:175]\n\t"
    "buffer_load_dwordx4 v33, s[36:39], s60 offen lds\n\t"
    "v_mfma_f32_16x16x32_bf16 v[140:143], a[216:219], v[0:3], v[140:143]\n\t"
    "s_add_u32 m0, s63, 0x1f000\n\t"
    "v_mfma_f32_16x16x32_bf16 v[140:143], a[220:223], a[112:115], v[140:143]\n\t"
    "buffer_load_dwordx4 v34, s[36:39], s60 offen lds\n\t"
    "v_mfma_f32_16x16x32_bf16 v[108:111], a[220:223], a[108:111], v[108:111]\n\t"
    "s_add_u32 m0, s65, 0x22400\n\t"
    "v_mfma_f32_16x16x32_bf16 v[108:111], a[216:219], a[252:255], v[108:111]\n\t"
    "buffer_load_dwordx4 v18, s[40:43], s62 offen lds\n\t"
    "v_mfma_f32_16x16x32_bf16 v[76:79], a[216:219], a[248:251], v[76:79]\n\t"
    "v_mfma_f32_16x16x32_bf16 v[76:79], a[220:223], a[104:107], v[76:79]\n\t"
    "ds_read_b64_tr_b16 a[216:217], v27 offset:16640\n\t"
    "s_waitcnt lgkmcnt(5)\n\t"
    "v_mfma_f32_16x16x32_bf16 v[80:83], a[192:195], a[248:251], v[80:83]\n\t"
    "ds_read_b64_tr_b16 a[218:219], v27 offset:17664\n\t"
    "v_mfma_f32_16x16x32_bf16 v[80:83], a[196:199], a[104:107], v[80:83]\n\t"
    "ds_read_b64_tr_b16 a[220:221], v27 offset:24832\n\t"
    "v_mfma_f32_16x16x32_bf16 v[112:115], a[196:199], a[108:111], v[112:115]\n\t"
    "ds_read_b64_tr_b16 a[222:223], v27 offset:25856\n\t"
    "v_mfma_f32_16x16x32_bf16 v[112:115], a[192:195], a[252:255], v[112:115]\n\t"
    "s_add_u32 s61, s61, 0x80\n\t"
    "s_add_u32 s62, s62, 0x80\n\t"
    "v_mfma_f32_16x16x32_bf16 v[144:147], a[192:195], v[0:3], v[144:147]\n\t"
    "v_mfma_f32_16x16x32_bf16 v[144:147], a[196:199], a[112:115], v[144:147]\n\t"
    "v_mfma_f32_16x16x32_bf16 v[176:179], a[196:199], a[116:119], v[176:179]\n\t"
    "v_mfma_f32_16x16x32_bf16 v[176:179], a[192:195], v[4:7], v[176:179]\n\t"
    "v_mfma_f32_16x16x32_bf16 v[208:211], a[192:195], v[8:11], v[208:211]\n\t"
    "s_waitcnt vmcnt(18) lgkmcnt(0)\n\t"
    "s_barrier\n\t"
    "v_mfma_f32_16x16x32_bf16 v[208:211], a[196:199], a[120:123], v[208:211]\n\t"
    "ds_read_b128 a[224:227], v24 offset:0\n\t"
    "s_add_u32 m0, s65, 0x24000\n\t"
    "v_mfma_f32_16x16x32_bf16 v[240:243], a[196:199], a[124:127], v[240:243]\n\t"
    "buffer_load_dwordx4 v17, s[40:43], s61 offen lds\n\t"
    "v_mfma_f32_16x16x32_bf16 v[240:243], a[192:195], v[12:15], v[240:243]\n\t"
    "ds_read_b64_tr_b16 a[192:193], v22 offset:0\n\t"
    "v_mfma_f32_16x16x32_bf16 v[212:215], a[200:203], v[8:11], v[212:215]\n\t"
    "ds_read_b64_tr_b16 a[194:195], v22 offset:1024\n\t"
    "v_mfma_f32_16x16x32_bf16 v[212:215], a[204:207], a[120:123], v[212:215]\n\t"
    "ds_read_b64_tr_b16 a[196:197], v22 offset:8192\n\t"
    "v_mfma_f32_16x16x32_bf16 v[180:183], a[204:207], a[116:119], v[180:183]\n\t"
    "ds_read_b64_tr_b16 a[198:199], v22 offset:9216\n\t"
    "v_mfma_f32_16x16x32_bf16 v[180:183], a[200:203], v[4:7], v[180:183]\n\t"
    "ds_read_b128 a[228:231], v24 offset:2048\n\t"
    "v_mfma_f32_16x16x32_bf16 v[148:151], a[200:203], v[0:3], v[148:151]\n\t"
    "ds_read_b128 a[232:235], v24 offset:4096\n\t"
    "s_add_u32 m0, s65, 0x24400\n\t"
    "v_mfma_f32_16x16x32_bf16 v[148:151], a[204:207], a[112:115], v[148:151]\n\t"
    "buffer_load_dwordx4 v18, s[40:43], s61 offen lds\n\t"
    "v_mfma_f32_16x16x32_bf16 v[116:119], a[204:207], a[108:111], v[116:119]\n\t"
    "ds_read_b128 a[236:239], v24 offset:6144\n\t"
    "v_mfma_f32_16x16x32_bf16 v[116:119], a[200:203], a[252:255], v[116:119]\n\t"
    "ds_read_b128 a[240:243], v24 offset:8192\n\t"
    "v_mfma_f32_16x16x32_bf16 v[84:87], a[200:203], a[248:251], v[84:87]\n\t"
    "ds_read_b128 a[244:247], v25 offset:0\n\t"
    "v_mfma_f32_16x16x32_bf16 v[84:87], a[204:207], a[104:107], v[84:87]\n\t"
    "ds_read_b64_tr_b16 a[200:201], v23 offset:0\n\t"
    "v_mfma_f32_16x16x32_bf16 v[88:91], a[208:211], a[248:251], v[88:91]\n\t"
    "ds_read_b64_tr_b16 a[202:203], v23 offset:1024\n\t"
    "v_mfma_f32_16x16x32_bf16 v[88:91], a[212:215], a[104:107], v[88:91]\n\t"
    "ds_read_b64_tr_b16 a[204:205], v23 offset:8192\n\t"
    "v_mfma_f32_16x16x32_bf16 v[120:123], a[212:215], a[108:111], v[120:123]\n\t"
    "ds_read_b64_tr_b16 a[206:207], v23 offset:9216\n\t"
    "s_add_u32 m0, s65, 0x26000\n\t"
    "v_mfma_f32_16x16x32_bf16 v[120:123], a[208:211], a[252:255], v[120:123]\n\t"
    "buffer_load_dwordx4 v17, s[40:43], s62 offen lds\n\t"
    "v_mfma_f32_16x16x32_bf16 v[152:155], a[208:211], v[0:3], v[152:155]\n\t"
    "ds_read_b128 v[40:43], v24 offset:512\n\t"
    "v_mfma_f32_16x16x32_bf16 v[152:155], a[212:215], a[112:115], v[152:155]\n\t"
    "ds_read_b128 v[44:47], v24 offset:2560\n\t"
    "s_add_u32 m0, s65, 0x26400\n\t"
    "v_mfma_f32_16x16x32_bf16 v[184:187], a[212:215], a[116:119], v[184:187]\n\t"
    "buffer_load_dwordx4 v18, s[40:43], s62 offen lds\n\t"
    "v_mfma_f32_16x16x32_bf16 v[184:187], a[208:211], v[4:7], v[184:187]\n\t"
    "ds_read_b128 v[48:51], v24 offset:4608\n\t"
    "v_mfma_f32_16x16x32_bf16 v[216:219], a[208:211], v[8:11], v[216:219]\n\t"
    "ds_read_b128 v[52:55], v24 offset:6656\n\t"
    "v_mfma_f32_16x16x32_bf16 v[216:219], a[212:215], a[120:123], v[216:219]\n\t"
    "ds_read_b128 v[56:59], v24 offset:8704\n\t"
    "s_mov_b32 s68, 0x6000\n\t"
    "v_mfma_f32_16x16x32_bf16 v[248:251], a[212:215], a[124:127], v[248:251]\n\t"
    "buffer_load_dwordx4 a[64:67], v16, s[48:51], s68 offen nt\n\t"
    "v_mfma_f32_16x16x32_bf16 v[248:251], a[208:211], v[12:15], v[248:251]\n\t"
    "ds_read_b64_tr_b16 a[208:209], v22 offset:256\n\t"
    "v_mfma_f32_16x16x32_bf16 v[220:223], a[216:219], v[8:11], v[220:223]\n\t"
    "ds_read_b64_tr_b16 a[210:211], v22 offset:1280\n\t"
    "v_mfma_f32_16x16x32_bf16 v[220:223], a[220:223], a[120:123], v[220:223]\n\t"
    "ds_read_b64_tr_b16 a[212:213], v22 offset:8448\n\t"
    "v_mfma_f32_16x16x32_bf16 v[188:191], a[220:223], a[116:119], v[188:191]\n\t"
    "ds_read_b64_tr_b16 a[214:215], v22 offset:9472\n\t"
    "v_mfma_f32_16x16x32_bf16 v[188:191], a[216:219], v[4:7], v[188:191]\n\t"
    "ds_read_b128 v[60:63], v25 offset:512\n\t"
    "v_mfma_f32_16x16x32_bf16 v[156:159], a[216:219], v[0:3], v[156:159]\n\t"
    "v_mfma_f32_16x16x32_bf16 v[156:159], a[220:223], a[112:115], v[156:159]\n\t"
    "v_mfma_f32_16x16x32_bf16 v[124:127], a[220:223], a[108:111], v[124:127]\n\t"
    "buffer_load_dwordx4 a[68:71], v16, s[48:51], s68 offen offset:1024 nt\n\t"
    "v_mfma_f32_16x16x32_bf16 v[124:127], a[216:219], a[252:255], v[124:127]\n\t"
    "v_mfma_f32_16x16x32_bf16 v[92:95], a[216:219], a[248:251], v[92:95]\n\t"
    "v_mfma_f32_16x16x32_bf16 v[92:95], a[220:223], a[104:107], v[92:95]\n\t"
    "ds_read_b64_tr_b16 a[216:217], v23 offset:256\n\t"
    "s_waitcnt lgkmcnt(1)\n\t"
    "v_mfma_f32_16x16x32_bf16 v[64:67], a[192:195], a[224:227], v[64:67]\n\t"
    "ds_read_b64_tr_b16 a[218:219], v23 offset:1280\n\t"
    "v_mfma_f32_16x16x32_bf16 v[64:67], a[196:199], v[40:43], v[64:67]\n\t"
    "ds_read_b64_tr_b16 a[220:221], v23 offset:8448\n\t"
    "v_mfma_f32_16x16x32_bf16 v[96:99], a[196:199], v[44:47], v[96:99]\n\t"
    "ds_read_b64_tr_b16 a[222:223], v23 offset:9472\n\t"
    "v_mfma_f32_16x16x32_bf16 v[96:99], a[192:195], a[228:231], v[96:99]\n\t"
    "s_and_b32 s86, s84, 15\n\t"
    "s_lshr_b64 s[84:85], s[84:85], 4\n\t"
    "v_mfma_f32_16x16x32_bf16 v[128:131], a[192:195], a[232:235], v[128:131]\n\t"
    "s_lshl_b32 s61, s86, 19\n\t"
    "s_lshl_b32 s60, s86, 7\n\t"
    "s_add_u32 s62, s61, 0x40000\n\t"
    "v_mfma_f32_16x16x32_bf16 v[128:131], a[196:199], v[48:51], v[128:131]\n\t"
    "v_mfma_f32_16x16x32_bf16 v[160:163], a[196:199], v[52:55], v[160:163]\n\t"
    "v_mfma_f32_16x16x32_bf16 v[160:163], a[192:195], a[236:239], v[160:163]\n\t"
    "v_mfma_f32_16x16x32_bf16 v[192:195], a[192:195], a[240:243], v[192:195]\n\t"
    "s_waitcnt vmcnt(20) lgkmcnt(0)\n\t"
    "s_barrier\n\t"
    "v_mfma_f32_16x16x32_bf16 v[192:195], a[196:199], v[56:59], v[192:195]\n\t"
    "s_add_u32 m0, s63, 0xc000\n\t"
    "v_mfma_f32_16x16x32_bf16 v[224:227], a[196:199], v[60:63], v[224:227]\n\t"
    "buffer_load_dwordx4 v19, s[36:39], s60 offen lds\n\t"
    "v_mfma_f32_16x16x32_bf16 v[224:227], a[192:195], a[244:247], v[224:227]\n\t"
    "ds_read_b64_tr_b16 a[192:193], v22 offset:16384\n\t"
    "v_mfma_f32_16x16x32_bf16 v[196:199], a[200:203], a[240:243], v[196:199]\n\t"
    "ds_read_b64_tr_b16 a[194:195], v22 offset:17408\n\t"
    "v_mfma_f32_16x16x32_bf16 v[196:199], a[204:207], v[56:59], v[196:199]\n\t"
    "ds_read_b64_tr_b16 a[196:197], v22 offset:24576\n\t"
    "v_mfma_f32_16x16x32_bf16 v[164:167], a[204:207], v[52:55], v[164:167]\n\t"
    "ds_read_b64_tr_b16 a[198:199], v22 offset:25600\n\t"
    "s_add_u32 m0, s65, 0x12000\n\t"
    "v_mfma_f32_16x16x32_bf16 v[164:167], a[200:203], a[236:239], v[164:167]\n\t"
    "buffer_load_dwordx4 v17, s[40:43], s61 offen lds\n\t"
    "v_mfma_f32_16x16x32_bf16 v[132:135], a[200:203], a[232:235], v[132:135]\n\t"
    "s_add_u32 m0, s63, 0xd000\n\t"
    "v_mfma_f32_16x16x32_bf16 v[132:135], a[204:207], v[48:51], v[132:135]\n\t"
    "buffer_load_dwordx4 v20, s[36:39], s60 offen lds\n\t"
    "v_mfma_f32_16x16x32_bf16 v[100:103], a[204:207], v[44:47], v[100:103]\n\t"
    "v_mfma_f32_16x16x32_bf16 v[100:103], a[200:203], a[228:231], v[100:103]\n\t"
    "s_add_u32 m0, s63, 0xe000\n\t"
    "v_mfma_f32_16x16x32_bf16 v[68:71], a[200:203], a[224:227], v[68:71]\n\t"
    "buffer_load_dwordx4 v21, s[36:39], s60 offen lds\n\t"
    "v_mfma_f32_16x16x32_bf16 v[68:71], a[204:207], v[40:43], v[68:71]\n\t"
    "ds_read_b64_tr_b16 a[200:201], v23 offset:16384\n\t"
    "v_mfma_f32_16x16x32_bf16 v[72:75], a[208:211], a[224:227], v[72:75]\n\t"
    "ds_read_b64_tr_b16 a[202:203], v23 offset:17408\n\t"
    "v_mfma_f32_16x16x32_bf16 v[72:75], a[212:215], v[40:43], v[72:75]\n\t"
    "ds_read_b64_tr_b16 a[204:205], v23 offset:24576\n\t"
    "v_mfma_f32_16x16x32_bf16 v[104:107], a[212:215], v[44:47], v[104:107]\n\t"
    "ds_read_b64_tr_b16 a[206:207], v23 offset:25600\n\t"
    "s_add_u32 m0, s65, 0x12400\n\t"
    "v_mfma_f32_16x16x32_bf16 v[104:107], a[208:211], a[228:231], v[104:107]\n\t"
    "buffer_load_dwordx4 v18, s[40:43], s61 offen lds\n\t"
    "v_mfma_f32_16x16x32_bf16 v[136:139], a[208:211], a[232:235], v[136:139]\n\t"
    "s_add_u32 m0, s63, 0xf000\n\t"
    "v_mfma_f32_16x16x32_bf16 v[136:139], a[212:215], v[48:51], v[136:139]\n\t"
    "buffer_load_dwordx4 v32, s[36:39], s60 offen lds\n\t"
    "v_mfma_f32_16x16x32_bf16 v[168:171], a[212:215], v[52:55], v[168:171]\n\t"
    "v_mfma_f32_16x16x32_bf16 v[168:171], a[208:211], a[236:239], v[168:171]\n\t"
    "v_mfma_f32_16x16x32_bf16 v[200:203], a[208:211], a[240:243], v[200:203]\n\t"
    "s_add_u32 m0, s65, 0x14000\n\t"
    "v_mfma_f32_16x16x32_bf16 v[200:203], a[212:215], v[56:59], v[200:203]\n\t"
    "buffer_load_dwordx4 v17, s[40:43], s62 offen lds\n\t"
    "v_mfma_f32_16x16x32_bf16 v[232:235], a[212:215], v[60:63], v[232:235]\n\t"
    "v_mfma_f32_16x16x32_bf16 v[232:235], a[208:211], a[244:247], v[232:235]\n\t"
    "ds_read_b64_tr_b16 a[208:209], v22 offset:16640\n\t"
    "v_mfma_f32_16x16x32_bf16 v[204:207], a[216:219], a[240:243], v[204:207]\n\t"
    "ds_read_b64_tr_b16 a[210:211], v22 offset:17664\n\t"
    "v_mfma_f32_16x16x32_bf16 v[204:207], a[220:223], v[56:59], v[204:207]\n\t"
    "ds_read_b64_tr_b16 a[212:213], v22 offset:24832\n\t"
    "v_mfma_f32_16x16x32_bf16 v[172:175], a[220:223], v[52:55], v[172:175]\n\t"
    "ds_read_b64_tr_b16 a[214:215], v22 offset:25856\n\t"
    "s_add_u32 m0, s63, 0x10000\n\t"
    "v_mfma_f32_16x16x32_bf16 v[172:175], a[216:219], a[236:239], v[172:175]\n\t"
    "buffer_load_dwordx4 v33, s[36:39], s60 offen lds\n\t"
    "v_mfma_f32_16x16x32_bf16 v[140:143], a[216:219], a[232:235], v[140:143]\n\t"
    "s_add_u32 m0, s63, 0x11000\n\t"
    "v_mfma_f32_16x16x32_bf16 v[140:143], a[220:223], v[48:51], v[140:143]\n\t"
    "buffer_load_dwordx4 v34, s[36:39], s60 offen lds\n\t"
    "v_mfma_f32_16x16x32_bf16 v[108:111], a[220:223], v[44:47], v[108:111]\n\t"
    "s_add_u32 m0, s65, 0x14400\n\t"
    "v_mfma_f32_16x16x32_bf16 v[108:111], a[216:219], a[228:231], v[108:111]\n\t"
    "buffer_load_dwordx4 v18, s[40:43], s62 offen lds\n\t"
    "v_mfma_f32_16x16x32_bf16 v[76:79], a[216:219], a[224:227], v[76:79]\n\t"
    "v_mfma_f32_16x16x32_bf16 v[76:79], a[220:223], v[40:43], v[76:79]\n\t"
    "ds_read_b64_tr_b16 a[216:217], v23 offset:16640\n\t"
    "s_waitcnt lgkmcnt(5)\n\t"
    "v_mfma_f32_16x16x32_bf16 v[80:83], a[192:195], a[224:227], v[80:83]\n\t"
    "ds_read_b64_tr_b16 a[218:219], v23 offset:17664\n\t"
    "v_mfma_f32_16x16x32_bf16 v[80:83], a[196:199], v[40:43], v[80:83]\n\t"
    "ds_read_b64_tr_b16 a[220:221], v23 offset:24832\n\t"
    "v_mfma_f32_16x16x32_bf16 v[112:115], a[196:199], v[44:47], v[112:115]\n\t"
    "ds_read_b64_tr_b16 a[222:223], v23 offset:25856\n\t"
    "v_mfma_f32_16x16x32_bf16 v[112:115], a[192:195], a[228:231], v[112:115]\n\t"
    "s_add_u32 s61, s61, 0x80\n\t"
    "s_add_u32 s62, s62, 0x80\n\t"
    "v_mfma_f32_16x16x32_bf16 v[144:147], a[192:195], a[232:235], v[144:147]\n\t"
    "v_mfma_f32_16x16x32_bf16 v[144:147], a[196:199], v[48:51], v[144:147]\n\t"
    "v_mfma_f32_16x16x32_bf16 v[176:179], a[196:199], v[52:55], v[176:179]\n\t"
    "v_mfma_f32_16x16x32_bf16 v[176:179], a[192:195], a[236:239], v[176:179]\n\t"
    "v_mfma_f32_16x16x32_bf16 v[208:211], a[192:195], a[240:243], v[208:211]\n\t"
    "s_waitcnt vmcnt(16) lgkmcnt(0)\n\t"
    "s_barrier\n\t"
    "v_mfma_f32_16x16x32_bf16 v[208:211], a[196:199], v[56:59], v[208:211]\n\t"
    "ds_read_b128 a[248:251], v28 offset:0\n\t"
    "s_add_u32 m0, s65, 0x16000\n\t"
    "v_mfma_f32_16x16x32_bf16 v[240:243], a[196:199], v[60:63], v[240:243]\n\t"
    "buffer_load_dwordx4 v17, s[40:43], s61 offen lds\n\t"
    "v_mfma_f32_16x16x32_bf16 v[240:243], a[192:195], a[244:247], v[240:243]\n\t"
    "ds_read_b64_tr_b16 a[192:193], v26 offset:0\n\t"
    "v_mfma_f32_16x16x32_bf16 v[212:215], a[200:203], a[240:243], v[212:215]\n\t"
    "ds_read_b64_tr_b16 a[194:195], v26 offset:1024\n\t"
    "v_mfma_f32_16x16x32_bf16 v[212:215], a[204:207], v[56:59], v[212:215]\n\t"
    "ds_read_b64_tr_b16 a[196:197], v26 offset:8192\n\t"
    "v_mfma_f32_16x16x32_bf16 v[180:183], a[204:207], v[52:55], v[180:183]\n\t"
    "ds_read_b64_tr_b16 a[198:199], v26 offset:9216\n\t"
    "s_add_u32 m0, s65, 0x16400\n\t"
    "v_mfma_f32_16x16x32_bf16 v[180:183], a[200:203], a[236:239], v[180:183]\n\t"
    "buffer_load_dwordx4 v18, s[40:43], s61 offen lds\n\t"
    "v_mfma_f32_16x16x32_bf16 v[148:151], a[200:203], a[232:235], v[148:151]\n\t"
    "ds_read_b128 a[252:255], v28 offset:2048\n\t"
    "v_mfma_f32_16x16x32_bf16 v[148:151], a[204:207], v[48:51], v[148:151]\n\t"
    "ds_read_b128 v[0:3], v28 offset:4096\n\t"
    "v_mfma_f32_16x16x32_bf16 v[116:119], a[204:207], v[44:47], v[116:119]\n\t"
    "ds_read_b128 v[4:7], v28 offset:6144\n\t"
    "s_add_u32 m0, s65, 0x18000\n\t"
    "v_mfma_f32_16x16x32_bf16 v[116:119], a[200:203], a[228:231], v[116:119]\n\t"
    "buffer_load_dwordx4 v17, s[40:43], s62 offen lds\n\t"
    "v_mfma_f32_16x16x32_bf16 v[84:87], a[200:203], a[224:227], v[84:87]\n\t"
    "ds_read_b128 v[8:11], v28 offset:8192\n\t"
    "v_mfma_f32_16x16x32_bf16 v[84:87], a[204:207], v[40:43], v[84:87]\n\t"
    "ds_read_b64_tr_b16 a[200:201], v27 offset:0\n\t"
    "v_mfma_f32_16x16x32_bf16 v[88:91], a[208:211], a[224:227], v[88:91]\n\t"
    "ds_read_b64_tr_b16 a[202:203], v27 offset:1024\n\t"
    "v_mfma_f32_16x16x32_bf16 v[88:91], a[212:215], v[40:43], v[88:91]\n\t"
    "ds_read_b64_tr_b16 a[204:205], v27 offset:8192\n\t"
    "v_mfma_f32_16x16x32_bf16 v[120:123], a[212:215], v[44:47], v[120:123]\n\t"
    "ds_read_b64_tr_b16 a[206:207], v27 offset:9216\n\t"
    "s_add_u32 m0, s65, 0x18400\n\t"
    "v_mfma_f32_16x16x32_bf16 v[120:123], a[208:211], a[228:231], v[120:123]\n\t"
    "buffer_load_dwordx4 v18, s[40:43], s62 offen lds\n\t"
    "v_mfma_f32_16x16x32_bf16 v[152:155], a[208:211], a[232:235], v[152:155]\n\t"
    "ds_read_b128 v[12:15], v35 offset:0\n\t"
    "v_mfma_f32_16x16x32_bf16 v[152:155], a[212:215], v[48:51], v[152:155]\n\t"
    "ds_read_b128 a[104:107], v28 offset:512\n\t"
    "s_mov_b32 s68, 0x26000\n\t"
    "v_mfma_f32_16x16x32_bf16 v[184:187], a[212:215], v[52:55], v[184:187]\n\t"
    "buffer_load_dwordx4 a[72:75], v16, s[48:51], s68 offen nt\n\t"
    "v_mfma_f32_16x16x32_bf16 v[184:187], a[208:211], a[236:239], v[184:187]\n\t"
    "ds_read_b128 a[108:111], v28 offset:2560\n\t"
    "v_mfma_f32_16x16x32_bf16 v[216:219], a[208:211], a[240:243], v[216:219]\n\t"
    "ds_read_b128 a[112:115], v28 offset:4608\n\t"
    "v_mfma_f32_16x16x32_bf16 v[216:219], a[212:215], v[56:59], v[216:219]\n\t"
    "ds_read_b128 a[116:119], v28 offset:6656\n\t"
    "v_mfma_f32_16x16x32_bf16 v[248:251], a[212:215], v[60:63], v[248:251]\n\t"
    "buffer_load_dwordx4 a[76:79], v16, s[48:51], s68 offen offset:1024 nt\n\t"
    "v_mfma_f32_16x16x32_bf16 v[248:251], a[208:211], a[244:247], v[248:251]\n\t"
    "ds_read_b64_tr_b16 a[208:209], v26 offset:256\n\t"
    "v_mfma_f32_16x16x32_bf16 v[220:223], a[216:219], a[240:243], v[220:223]\n\t"
    "ds_read_b64_tr_b16 a[210:211], v26 offset:1280\n\t"
    "v_mfma_f32_16x16x32_bf16 v[220:223], a[220:223], v[56:59], v[220:223]\n\t"
    "ds_read_b64_tr_b16 a[212:213], v26 offset:8448\n\t"
    "v_mfma_f32_16x16x32_bf16 v[188:191], a[220:223], v[52:55], v[188:191]\n\t"
    "ds_read_b64_tr_b16 a[214:215], v26 offset:9472\n\t"
    "s_mov_b32 s68, 0x8000\n\t"
    "v_mfma_f32_16x16x32_bf16 v[188:191], a[216:219], a[236:239], v[188:191]\n\t"
    "buffer_load_dwordx4 a[80:83], v16, s[44:47], s68 offen nt\n\t"
    "v_mfma_f32_16x16x32_bf16 v[156:159], a[216:219], a[232:235], v[156:159]\n\t"
    "ds_read_b128 a[120:123], v28 offset:8704\n\t"
    "v_mfma_f32_16x16x32_bf16 v[156:159], a[220:223], v[48:51], v[156:159]\n\t"
    "ds_read_b128 a[124:127], v35 offset:512\n\t"
    "v_mfma_f32_16x16x32_bf16 v[124:127], a[220:223], v[44:47], v[124:127]\n\t"
    "buffer_load_dwordx4 a[84:87], v16, s[44:47], s68 offen offset:1024 nt\n\t"
    "v_mfma_f32_16x16x32_bf16 v[124:127], a[216:219], a[228:231], v[124:127]\n\t"
    "v_mfma_f32_16x16x32_bf16 v[92:95], a[216:219], a[224:227], v[92:95]\n\t"
    "v_mfma_f32_16x16x32_bf16 v[92:95], a[220:223], v[40:43], v[92:95]\n\t"
    "ds_read_b64_tr_b16 a[216:217], v27 offset:256\n\t"
    "s_waitcnt lgkmcnt(1)\n\t"
    "v_mfma_f32_16x16x32_bf16 v[64:67], a[192:195], a[248:251], v[64:67]\n\t"
    "ds_read_b64_tr_b16 a[218:219], v27 offset:1280\n\t"
    "v_mfma_f32_16x16x32_bf16 v[64:67], a[196:199], a[104:107], v[64:67]\n\t"
    "ds_read_b64_tr_b16 a[220:221], v27 offset:8448\n\t"
    "v_mfma_f32_16x16x32_bf16 v[96:99], a[196:199], a[108:111], v[96:99]\n\t"
    "ds_read_b64_tr_b16 a[222:223], v27 offset:9472\n\t"
    "v_mfma_f32_16x16x32_bf16 v[96:99], a[192:195], a[252:255], v[96:99]\n\t"
    "s_and_b32 s86, s84, 15\n\t"
    "s_lshr_b64 s[84:85], s[84:85], 4\n\t"
    "v_mfma_f32_16x16x32_bf16 v[128:131], a[192:195], v[0:3], v[128:131]\n\t"
    "s_lshl_b32 s61, s86, 19\n\t"
    "s_lshl_b32 s60, s86, 7\n\t"
    "s_add_u32 s62, s61, 0x40000\n\t"
    "v_mfma_f32_16x16x32_bf16 v[128:131], a[196:199], a[112:115], v[128:131]\n\t"
    "v_mfma_f32_16x16x32_bf16 v[160:163], a[196:199], a[116:119], v[160:163]\n\t"
    "v_mfma_f32_16x16x32_bf16 v[160:163], a[192:195], v[4:7], v[160:163]\n\t"
    "v_mfma_f32_16x16x32_bf16 v[192:195], a[192:195], v[8:11], v[192:195]\n\t"
    "s_waitcnt vmcnt(20) lgkmcnt(0)\n\t"
    "s_barrier\n\t"
    "v_mfma_f32_16x16x32_bf16 v[192:195], a[196:199], a[120:123], v[192:195]\n\t"
    "s_add_u32 m0, s63, 0x1a000\n\t"
    "v_mfma_f32_16x16x32_bf16 v[224:227], a[196:199], a[124:127], v[224:227]\n\t"
    "buffer_load_dwordx4 v19, s[36:39], s60 offen lds\n\t"
    "v_mfma_f32_16x16x32_bf16 v[224:227], a[192:195], v[12:15], v[224:227]\n\t"
    "ds_read_b64_tr_b16 a[192:193], v26 offset:16384\n\t"
    "v_mfma_f32_16x16x32_bf16 v[196:199], a[200:203], v[8:11], v[196:199]\n\t"
    "ds_read_b64_tr_b16 a[194:195], v26 offset:17408\n\t"
    "v_mfma_f32_16x16x32_bf16 v[196:199], a[204:207], a[120:123], v[196:199]\n\t"
    "ds_read_b64_tr_b16 a[196:197], v26 offset:24576\n\t"
    "v_mfma_f32_16x16x32_bf16 v[164:167], a[204:207], a[116:119], v[164:167]\n\t"
    "ds_read_b64_tr_b16 a[198:199], v26 offset:25600\n\t"
    "s_add_u32 m0, s65, 0x20000\n\t"
    "v_mfma_f32_16x16x32_bf16 v[164:167], a[200:203], v[4:7], v[164:167]\n\t"
    "buffer_load_dwordx4 v17, s[40:43], s61 offen lds\n\t"
    "v_mfma_f32_16x16x32_bf16 v[132:135], a[200:203], v[0:3], v[132:135]\n\t"
    "s_add_u32 m0, s63, 0x1b000\n\t"
    "v_mfma_f32_16x16x32_bf16 v[132:135], a[204:207], a[112:115], v[132:135]\n\t"
    "buffer_load_dwordx4 v20, s[36:39], s60 offen lds\n\t"
    "v_mfma_f32_16x16x32_bf16 v[100:103], a[204:207], a[108:111], v[100:103]\n\t"
    "v_mfma_f32_16x16x32_bf16 v[100:103], a[200:203], a[252:255], v[100:103]\n\t"
    "s_add_u32 m0, s63, 0x1c000\n\t"
    "v_mfma_f32_16x16x32_bf16 v[68:71], a[200:203], a[248:251], v[68:71]\n\t"
    "buffer_load_dwordx4 v21, s[36:39], s60 offen lds\n\t"
    "v_mfma_f32_16x16x32_bf16 v[68:71], a[204:207], a[104:107], v[68:71]\n\t"
    "ds_read_b64_tr_b16 a[200:201], v27 offset:16384\n\t"
    "v_mfma_f32_16x16x32_bf16 v[72:75], a[208:211], a[248:251], v[72:75]\n\t"
    "ds_read_b64_tr_b16 a[202:203], v27 offset:17408\n\t"
    "v_mfma_f32_16x16x32_bf16 v[72:75], a[212:215], a[104:107], v[72:75]\n\t"
    "ds_read_b64_tr_b16 a[204:205], v27 offset:24576\n\t"
    "v_mfma_f32_16x16x32_bf16 v[104:107], a[212:215], a[108:111], v[104:107]\n\t"
    "ds_read_b64_tr_b16 a[206:207], v27 offset:25600\n\t"
    "s_add_u32 m0, s65, 0x20400\n\t"
    "v_mfma_f32_16x16x32_bf16 v[104:107], a[208:211], a[252:255], v[104:107]\n\t"
    "buffer_load_dwordx4 v18, s[40:43], s61 offen lds\n\t"
    "v_mfma_f32_16x16x32_bf16 v[136:139], a[208:211], v[0:3], v[136:139]\n\t"
    "s_add_u32 m0, s63, 0x1d000\n\t"
    "v_mfma_f32_16x16x32_bf16 v[136:139], a[212:215], a[112:115], v[136:139]\n\t"
    "buffer_load_dwordx4 v32, s[36:39], s60 offen lds\n\t"
    "v_mfma_f32_16x16x32_bf16 v[168:171], a[212:215], a[116:119], v[168:171]\n\t"
    "v_mfma_f32_16x16x32_bf16 v[168:171], a[208:211], v[4:7], v[168:171]\n\t"
    "v_mfma_f32_16x16x32_bf16 v[200:203], a[208:211], v[8:11], v[200:203]\n\t"
    "s_add_u32 m0, s65, 0x22000\n\t"
    "v_mfma_f32_16x16x32_bf16 v[200:203], a[212:215], a[120:123], v[200:203]\n\t"
    "buffer_load_dwordx4 v17, s[40:43], s62 offen lds\n\t"
    "v_mfma_f32_16x16x32_bf16 v[232:235], a[212:215], a[124:127], v[232:235]\n\t"
    "v_mfma_f32_16x16x32_bf16 v[232:235], a[208:211], v[12:15], v[232:235]\n\t"
    "ds_read_b64_tr_b16 a[208:209], v26 offset:16640\n\t"
    "v_mfma_f32_16x16x32_bf16 v[204:207], a[216:219], v[8:11], v[204:207]\n\t"
    "ds_read_b64_tr_b16 a[210:211], v26 offset:17664\n\t"
    "v_mfma_f32_16x16x32_bf16 v[204:207], a[220:223], a[120:123], v[204:207]\n\t"
    "ds_read_b64_tr_b16 a[212:213], v26 offset:24832\n\t"
    "v_mfma_f32_16x16x32_bf16 v[172:175], a[220:223], a[116:119], v[172:175]\n\t"
    "ds_read_b64_tr_b16 a[214:215], v26 offset:25856\n\t"
    "s_add_u32 m0, s63, 0x1e000\n\t"
    "v_mfma_f32_16x16x32_bf16 v[172:175], a[216:219], v[4:7], v[172:175]\n\t"
    "buffer_load_dwordx4 v33, s[36:39], s60 offen lds\n\t"
    "v_mfma_f32_16x16x32_bf16 v[140:143], a[216:219], v[0:3], v[140:143]\n\t"
    "s_add_u32 m0, s63, 0x1f000\n\t"
    "v_mfma_f32_16x16x32_bf16 v[140:143], a[220:223], a[112:115], v[140:143]\n\t"
    "buffer_load_dwordx4 v34, s[36:39], s60 offen lds\n\t"
    "v_mfma_f32_16x16x32_bf16 v[108:111], a[220:223], a[108:111], v[108:111]\n\t"
    "s_add_u32 m0, s65, 0x22400\n\t"
    "v_mfma_f32_16x16x32_bf16 v[108:111], a[216:219], a[252:255], v[108:111]\n\t"
    "buffer_load_dwordx4 v18, s[40:43], s62 offen lds\n\t"
    "v_mfma_f32_16x16x32_bf16 v[76:79], a[216:219], a[248:251], v[76:79]\n\t"
    "v_mfma_f32_16x16x32_bf16 v[76:79], a[220:223], a[104:107], v[76:79]\n\t"
    "ds_read_b64_tr_b16 a[216:217], v27 offset:16640\n\t"
    "s_waitcnt lgkmcnt(5)\n\t"
    "v_mfma_f32_16x16x32_bf16 v[80:83], a[192:195], a[248:251], v[80:83]\n\t"
    "ds_read_b64_tr_b16 a[218:219], v27 offset:17664\n\t"
    "v_mfma_f32_16x16x32_bf16 v[80:83], a[196:199], a[104:107], v[80:83]\n\t"
    "ds_read_b64_tr_b16 a[220:221], v27 offset:24832\n\t"
    "v_mfma_f32_16x16x32_bf16 v[112:115], a[196:199], a[108:111], v[112:115]\n\t"
    "ds_read_b64_tr_b16 a[222:223], v27 offset:25856\n\t"
    "v_mfma_f32_16x16x32_bf16 v[112:115], a[192:195], a[252:255], v[112:115]\n\t"
    "s_add_u32 s61, s61, 0x80\n\t"
    "s_add_u32 s62, s62, 0x80\n\t"
    "v_mfma_f32_16x16x32_bf16 v[144:147], a[192:195], v[0:3], v[144:147]\n\t"
    "v_mfma_f32_16x16x32_bf16 v[144:147], a[196:199], a[112:115], v[144:147]\n\t"
    "v_mfma_f32_16x16x32_bf16 v[176:179], a[196:199], a[116:119], v[176:179]\n\t"
    "v_mfma_f32_16x16x32_bf16 v[176:179], a[192:195], v[4:7], v[176:179]\n\t"
    "v_mfma_f32_16x16x32_bf16 v[208:211], a[192:195], v[8:11], v[208:211]\n\t"
    "s_waitcnt vmcnt(18) lgkmcnt(0)\n\t"
    "s_barrier\n\t"
    "v_mfma_f32_16x16x32_bf16 v[208:211], a[196:199], a[120:123], v[208:211]\n\t"
    "ds_read_b128 a[224:227], v24 offset:0\n\t"
    "s_add_u32 m0, s65, 0x24000\n\t"
    "v_mfma_f32_16x16x32_bf16 v[240:243], a[196:199], a[124:127], v[240:243]\n\t"
    "buffer_load_dwordx4 v17, s[40:43], s61 offen lds\n\t"
    "v_mfma_f32_16x16x32_bf16 v[240:243], a[192:195], v[12:15], v[240:243]\n\t"
    "ds_read_b64_tr_b16 a[192:193], v22 offset:0\n\t"
    "v_mfma_f32_16x16x32_bf16 v[212:215], a[200:203], v[8:11], v[212:215]\n\t"
    "ds_read_b64_tr_b16 a[194:195], v22 offset:1024\n\t"
    "v_mfma_f32_16x16x32_bf16 v[212:215], a[204:207], a[120:123], v[212:215]\n\t"
    "ds_read_b64_tr_b16 a[196:197], v22 offset:8192\n\t"
    "v_mfma_f32_16x16x32_bf16 v[180:183], a[204:207], a[116:119], v[180:183]\n\t"
    "ds_read_b64_tr_b16 a[198:199], v22 offset:9216\n\t"
    "v_mfma_f32_16x16x32_bf16 v[180:183], a[200:203], v[4:7], v[180:183]\n\t"
    "ds_read_b128 a[228:231], v24 offset:2048\n\t"
    "v_mfma_f32_16x16x32_bf16 v[148:151], a[200:203], v[0:3], v[148:151]\n\t"
    "ds_read_b128 a[232:235], v24 offset:4096\n\t"
    "s_add_u32 m0, s65, 0x24400\n\t"
    "v_mfma_f32_16x16x32_bf16 v[148:151], a[204:207], a[112:115], v[148:151]\n\t"
    "buffer_load_dwordx4 v18, s[40:43], s61 offen lds\n\t"
    "v_mfma_f32_16x16x32_bf16 v[116:119], a[204:207], a[108:111], v[116:119]\n\t"
    "ds_read_b128 a[236:239], v24 offset:6144\n\t"
    "v_mfma_f32_16x16x32_bf16 v[116:119], a[200:203], a[252:255], v[116:119]\n\t"
    "ds_read_b128 a[240:243], v24 offset:8192\n\t"
    "v_mfma_f32_16x16x32_bf16 v[84:87], a[200:203], a[248:251], v[84:87]\n\t"
    "ds_read_b128 a[244:247], v25 offset:0\n\t"
    "v_mfma_f32_16x16x32_bf16 v[84:87], a[204:207], a[104:107], v[84:87]\n\t"
    "ds_read_b64_tr_b16 a[200:201], v23 offset:0\n\t"
    "v_mfma_f32_16x16x32_bf16 v[88:91], a[208:211], a[248:251], v[88:91]\n\t"
    "ds_read_b64_tr_b16 a[202:203], v23 offset:1024\n\t"
    "v_mfma_f32_16x16x32_bf16 v[88:91], a[212:215], a[104:107], v[88:91]\n\t"
    "ds_read_b64_tr_b16 a[204:205], v23 offset:8192\n\t"
    "v_mfma_f32_16x16x32_bf16 v[120:123], a[212:215], a[108:111], v[120:123]\n\t"
    "ds_read_b64_tr_b16 a[206:207], v23 offset:9216\n\t"
    "s_add_u32 m0, s65, 0x26000\n\t"
    "v_mfma_f32_16x16x32_bf16 v[120:123], a[208:211], a[252:255], v[120:123]\n\t"
    "buffer_load_dwordx4 v17, s[40:43], s62 offen lds\n\t"
    "v_mfma_f32_16x16x32_bf16 v[152:155], a[208:211], v[0:3], v[152:155]\n\t"
    "ds_read_b128 v[40:43], v24 offset:512\n\t"
    "v_mfma_f32_16x16x32_bf16 v[152:155], a[212:215], a[112:115], v[152:155]\n\t"
    "ds_read_b128 v[44:47], v24 offset:2560\n\t"
    "s_add_u32 m0, s65, 0x26400\n\t"
    "v_mfma_f32_16x16x32_bf16 v[184:187], a[212:215], a[116:119], v[184:187]\n\t"
    "buffer_load_dwordx4 v18, s[40:43], s62 offen lds\n\t"
    "v_mfma_f32_16x16x32_bf16 v[184:187], a[208:211], v[4:7], v[184:187]\n\t"
    "ds_read_b128 v[48:51], v24 offset:4608\n\t"
    "v_mfma_f32_16x16x32_bf16 v[216:219], a[208:211], v[8:11], v[216:219]\n\t"
    "ds_read_b128 v[52:55], v24 offset:6656\n\t"
    "v_mfma_f32_16x16x32_bf16 v[216:219], a[212:215], a[120:123], v[216:219]\n\t"
    "ds_read_b128 v[56:59], v24 offset:8704\n\t"
    "s_mov_b32 s68, 0x28000\n\t"
    "v_mfma_f32_16x16x32_bf16 v[248:251], a[212:215], a[124:127], v[248:251]\n\t"
    "buffer_load_dwordx4 a[88:91], v16, s[44:47], s68 offen nt\n\t"
    "v_mfma_f32_16x16x32_bf16 v[248:251], a[208:211], v[12:15], v[248:251]\n\t"
    "ds_read_b64_tr_b16 a[208:209], v22 offset:256\n\t"
    "v_mfma_f32_16x16x32_bf16 v[220:223], a[216:219], v[8:11], v[220:223]\n\t"
    "ds_read_b64_tr_b16 a[210:211], v22 offset:1280\n\t"
    "v_mfma_f32_16x16x32_bf16 v[220:223], a[220:223], a[120:123], v[220:223]\n\t"
    "ds_read_b64_tr_b16 a[212:213], v22 offset:8448\n\t"
    "v_mfma_f32_16x16x32_bf16 v[188:191], a[220:223], a[116:119], v[188:191]\n\t"
    "ds_read_b64_tr_b16 a[214:215], v22 offset:9472\n\t"
    "v_mfma_f32_16x16x32_bf16 v[188:191], a[216:219], v[4:7], v[188:191]\n\t"
    "ds_read_b128 v[60:63], v25 offset:512\n\t"
    "v_mfma_f32_16x16x32_bf16 v[156:159], a[216:219], v[0:3], v[156:159]\n\t"
    "v_mfma_f32_16x16x32_bf16 v[156:159], a[220:223], a[112:115], v[156:159]\n\t"
    "v_mfma_f32_16x16x32_bf16 v[124:127], a[220:223], a[108:111], v[124:127]\n\t"
    "buffer_load_dwordx4 a[92:95], v16, s[44:47], s68 offen offset:1024 nt\n\t"
    "v_mfma_f32_16x16x32_bf16 v[124:127], a[216:219], a[252:255], v[124:127]\n\t"
    "v_mfma_f32_16x16x32_bf16 v[92:95], a[216:219], a[248:251], v[92:95]\n\t"
    "v_mfma_f32_16x16x32_bf16 v[92:95], a[220:223], a[104:107], v[92:95]\n\t"
    "ds_read_b64_tr_b16 a[216:217], v23 offset:256\n\t"
    "s_waitcnt lgkmcnt(1)\n\t"
    "v_mfma_f32_16x16x32_bf16 v[64:67], a[192:195], a[224:227], v[64:67]\n\t"
    "ds_read_b64_tr_b16 a[218:219], v23 offset:1280\n\t"
    "v_mfma_f32_16x16x32_bf16 v[64:67], a[196:199], v[40:43], v[64:67]\n\t"
    "ds_read_b64_tr_b16 a[220:221], v23 offset:8448\n\t"
    "v_mfma_f32_16x16x32_bf16 v[96:99], a[196:199], v[44:47], v[96:99]\n\t"
    "ds_read_b64_tr_b16 a[222:223], v23 offset:9472\n\t"
    "v_mfma_f32_16x16x32_bf16 v[96:99], a[192:195], a[228:231], v[96:99]\n\t"
    "s_and_b32 s86, s84, 15\n\t"
    "s_lshr_b64 s[84:85], s[84:85], 4\n\t"
    "v_mfma_f32_16x16x32_bf16 v[128:131], a[192:195], a[232:235], v[128:131]\n\t"
    "s_lshl_b32 s61, s86, 19\n\t"
    "s_lshl_b32 s60, s86, 7\n\t"
    "s_add_u32 s62, s61, 0x40000\n\t"
    "v_mfma_f32_16x16x32_bf16 v[128:131], a[196:199], v[48:51], v[128:131]\n\t"
    "v_mfma_f32_16x16x32_bf16 v[160:163], a[196:199], v[52:55], v[160:163]\n\t"
    "v_mfma_f32_16x16x32_bf16 v[160:163], a[192:195], a[236:239], v[160:163]\n\t"
    "v_mfma_f32_16x16x32_bf16 v[192:195], a[192:195], a[240:243], v[192:195]\n\t"
    "s_waitcnt vmcnt(20) lgkmcnt(0)\n\t"
    "s_barrier\n\t"
    "v_mfma_f32_16x16x32_bf16 v[192:195], a[196:199], v[56:59], v[192:195]\n\t"
    "s_add_u32 m0, s63, 0xc000\n\t"
    "v_mfma_f32_16x16x32_bf16 v[224:227], a[196:199], v[60:63], v[224:227]\n\t"
    "buffer_load_dwordx4 v19, s[36:39], s60 offen lds\n\t"
    "v_mfma_f32_16x16x32_bf16 v[224:227], a[192:195], a[244:247], v[224:227]\n\t"
    "ds_read_b64_tr_b16 a[192:193], v22 offset:16384\n\t"
    "v_mfma_f32_16x16x32_bf16 v[196:199], a[200:203], a[240:243], v[196:199]\n\t"
    "ds_read_b64_tr_b16 a[194:195], v22 offset:17408\n\t"
    "v_mfma_f32_16x16x32_bf16 v[196:199], a[204:207], v[56:59], v[196:199]\n\t"
    "ds_read_b64_tr_b16 a[196:197], v22 offset:24576\n\t"
    "v_mfma_f32_16x16x32_bf16 v[164:167], a[204:207], v[52:55], v[164:167]\n\t"
    "ds_read_b64_tr_b16 a[198:199], v22 offset:25600\n\t"
    "s_add_u32 m0, s65, 0x12000\n\t"
    "v_mfma_f32_16x16x32_bf16 v[164:167], a[200:203], a[236:239], v[164:167]\n\t"
    "buffer_load_dwordx4 v17, s[40:43], s61 offen lds\n\t"
    "v_mfma_f32_16x16x32_bf16 v[132:135], a[200:203], a[232:235], v[132:135]\n\t"
    "s_add_u32 m0, s63, 0xd000\n\t"
    "v_mfma_f32_16x16x32_bf16 v[132:135], a[204:207], v[48:51], v[132:135]\n\t"
    "buffer_load_dwordx4 v20, s[36:39], s60 offen lds\n\t"
    "v_mfma_f32_16x16x32_bf16 v[100:103], a[204:207], v[44:47], v[100:103]\n\t"
    "v_mfma_f32_16x16x32_bf16 v[100:103], a[200:203], a[228:231], v[100:103]\n\t"
    "s_add_u32 m0, s63, 0xe000\n\t"
    "v_mfma_f32_16x16x32_bf16 v[68:71], a[200:203], a[224:227], v[68:71]\n\t"
    "buffer_load_dwordx4 v21, s[36:39], s60 offen lds\n\t"
    "v_mfma_f32_16x16x32_bf16 v[68:71], a[204:207], v[40:43], v[68:71]\n\t"
    "ds_read_b64_tr_b16 a[200:201], v23 offset:16384\n\t"
    "v_mfma_f32_16x16x32_bf16 v[72:75], a[208:211], a[224:227], v[72:75]\n\t"
    "ds_read_b64_tr_b16 a[202:203], v23 offset:17408\n\t"
    "v_mfma_f32_16x16x32_bf16 v[72:75], a[212:215], v[40:43], v[72:75]\n\t"
    "ds_read_b64_tr_b16 a[204:205], v23 offset:24576\n\t"
    "v_mfma_f32_16x16x32_bf16 v[104:107], a[212:215], v[44:47], v[104:107]\n\t"
    "ds_read_b64_tr_b16 a[206:207], v23 offset:25600\n\t"
    "s_add_u32 m0, s65, 0x12400\n\t"
    "v_mfma_f32_16x16x32_bf16 v[104:107], a[208:211], a[228:231], v[104:107]\n\t"
    "buffer_load_dwordx4 v18, s[40:43], s61 offen lds\n\t"
    "v_mfma_f32_16x16x32_bf16 v[136:139], a[208:211], a[232:235], v[136:139]\n\t"
    "s_add_u32 m0, s63, 0xf000\n\t"
    "v_mfma_f32_16x16x32_bf16 v[136:139], a[212:215], v[48:51], v[136:139]\n\t"
    "buffer_load_dwordx4 v32, s[36:39], s60 offen lds\n\t"
    "v_mfma_f32_16x16x32_bf16 v[168:171], a[212:215], v[52:55], v[168:171]\n\t"
    "v_mfma_f32_16x16x32_bf16 v[168:171], a[208:211], a[236:239], v[168:171]\n\t"
    "v_mfma_f32_16x16x32_bf16 v[200:203], a[208:211], a[240:243], v[200:203]\n\t"
    "s_add_u32 m0, s65, 0x14000\n\t"
    "v_mfma_f32_16x16x32_bf16 v[200:203], a[212:215], v[56:59], v[200:203]\n\t"
    "buffer_load_dwordx4 v17, s[40:43], s62 offen lds\n\t"
    "v_mfma_f32_16x16x32_bf16 v[232:235], a[212:215], v[60:63], v[232:235]\n\t"
    "v_mfma_f32_16x16x32_bf16 v[232:235], a[208:211], a[244:247], v[232:235]\n\t"
    "ds_read_b64_tr_b16 a[208:209], v22 offset:16640\n\t"
    "v_mfma_f32_16x16x32_bf16 v[204:207], a[216:219], a[240:243], v[204:207]\n\t"
    "ds_read_b64_tr_b16 a[210:211], v22 offset:17664\n\t"
    "v_mfma_f32_16x16x32_bf16 v[204:207], a[220:223], v[56:59], v[204:207]\n\t"
    "ds_read_b64_tr_b16 a[212:213], v22 offset:24832\n\t"
    "v_mfma_f32_16x16x32_bf16 v[172:175], a[220:223], v[52:55], v[172:175]\n\t"
    "ds_read_b64_tr_b16 a[214:215], v22 offset:25856\n\t"
    "s_add_u32 m0, s63, 0x10000\n\t"
    "v_mfma_f32_16x16x32_bf16 v[172:175], a[216:219], a[236:239], v[172:175]\n\t"
    "buffer_load_dwordx4 v33, s[36:39], s60 offen lds\n\t"
    "v_mfma_f32_16x16x32_bf16 v[140:143], a[216:219], a[232:235], v[140:143]\n\t"
    "s_add_u32 m0, s63, 0x11000\n\t"
    "v_mfma_f32_16x16x32_bf16 v[140:143], a[220:223], v[48:51], v[140:143]\n\t"
    "buffer_load_dwordx4 v34, s[36:39], s60 offen lds\n\t"
    "v_mfma_f32_16x16x32_bf16 v[108:111], a[220:223], v[44:47], v[108:111]\n\t"
    "s_add_u32 m0, s65, 0x14400\n\t"
    "v_mfma_f32_16x16x32_bf16 v[108:111], a[216:219], a[228:231], v[108:111]\n\t"
    "buffer_load_dwordx4 v18, s[40:43], s62 offen lds\n\t"
    "v_mfma_f32_16x16x32_bf16 v[76:79], a[216:219], a[224:227], v[76:79]\n\t"
    "v_mfma_f32_16x16x32_bf16 v[76:79], a[220:223], v[40:43], v[76:79]\n\t"
    "ds_read_b64_tr_b16 a[216:217], v23 offset:16640\n\t"
    "s_waitcnt lgkmcnt(5)\n\t"
    "v_mfma_f32_16x16x32_bf16 v[80:83], a[192:195], a[224:227], v[80:83]\n\t"
    "ds_read_b64_tr_b16 a[218:219], v23 offset:17664\n\t"
    "v_mfma_f32_16x16x32_bf16 v[80:83], a[196:199], v[40:43], v[80:83]\n\t"
    "ds_read_b64_tr_b16 a[220:221], v23 offset:24832\n\t"
    "v_mfma_f32_16x16x32_bf16 v[112:115], a[196:199], v[44:47], v[112:115]\n\t"
    "ds_read_b64_tr_b16 a[222:223], v23 offset:25856\n\t"
    "v_mfma_f32_16x16x32_bf16 v[112:115], a[192:195], a[228:231], v[112:115]\n\t"
    "s_add_u32 s61, s61, 0x80\n\t"
    "s_add_u32 s62, s62, 0x80\n\t"
    "v_mfma_f32_16x16x32_bf16 v[144:147], a[192:195], a[232:235], v[144:147]\n\t"
    "v_mfma_f32_16x16x32_bf16 v[144:147], a[196:199], v[48:51], v[144:147]\n\t"
    "v_mfma_f32_16x16x32_bf16 v[176:179], a[196:199], v[52:55], v[176:179]\n\t"
    "v_mfma_f32_16x16x32_bf16 v[176:179], a[192:195], a[236:239], v[176:179]\n\t"
    "v_mfma_f32_16x16x32_bf16 v[208:211], a[192:195], a[240:243], v[208:211]\n\t"
    "s_waitcnt vmcnt(16) lgkmcnt(0)\n\t"
    "s_barrier\n\t"
    "v_mfma_f32_16x16x32_bf16 v[208:211], a[196:199], v[56:59], v[208:211]\n\t"
    "ds_read_b128 a[248:251], v28 offset:0\n\t"
    "s_add_u32 m0, s65, 0x16000\n\t"
    "v_mfma_f32_16x16x32_bf16 v[240:243], a[196:199], v[60:63], v[240:243]\n\t"
    "buffer_load_dwordx4 v17, s[40:43], s61 offen lds\n\t"
    "v_mfma_f32_16x16x32_bf16 v[240:243], a[192:195], a[244:247], v[240:243]\n\t"
    "ds_read_b64_tr_b16 a[192:193], v26 offset:0\n\t"
    "v_mfma_f32_16x16x32_bf16 v[212:215], a[200:203], a[240:243], v[212:215]\n\t"
    "ds_read_b64_tr_b16 a[194:195], v26 offset:1024\n\t"
    "v_mfma_f32_16x16x32_bf16 v[212:215], a[204:207], v[56:59], v[212:215]\n\t"
    "ds_read_b64_tr_b16 a[196:197], v26 offset:8192\n\t"
    "v_mfma_f32_16x16x32_bf16 v[180:183], a[204:207], v[52:55], v[180:183]\n\t"
    "ds_read_b64_tr_b16 a[198:199], v26 offset:9216\n\t"
    "v_mfma_f32_16x16x32_bf16 v[180:183], a[200:203], a[236:239], v[180:183]\n\t"
    "ds_read_b128 a[252:255], v28 offset:2048\n\t"
    "v_mfma_f32_16x16x32_bf16 v[148:151], a[200:203], a[232:235], v[148:151]\n\t"
    "ds_read_b128 v[0:3], v28 offset:4096\n\t"
    "s_add_u32 m0, s65, 0x16400\n\t"
    "v_mfma_f32_16x16x32_bf16 v[148:151], a[204:207], v[48:51], v[148:151]\n\t"
    "buffer_load_dwordx4 v18, s[40:43], s61 offen lds\n\t"
    "v_mfma_f32_16x16x32_bf16 v[116:119], a[204:207], v[44:47], v[116:119]\n\t"
    "ds_read_b128 v[4:7], v28 offset:6144\n\t"
    "v_mfma_f32_16x16x32_bf16 v[116:119], a[200:203], a[228:231], v[116:119]\n\t"
    "ds_read_b128 v[8:11], v28 offset:8192\n\t"
    "v_mfma_f32_16x16x32_bf16 v[84:87], a[200:203], a[224:227], v[84:87]\n\t"
    "ds_read_b128 v[12:15], v35 offset:0\n\t"
    "v_mfma_f32_16x16x32_bf16 v[84:87], a[204:207], v[40:43], v[84:87]\n\t"
    "ds_read_b64_tr_b16 a[200:201], v27 offset:0\n\t"
    "v_mfma_f32_16x16x32_bf16 v[88:91], a[208:211], a[224:227], v[88:91]\n\t"
    "ds_read_b64_tr_b16 a[202:203], v27 offset:1024\n\t"
    "v_mfma_f32_16x16x32_bf16 v[88:91], a[212:215], v[40:43], v[88:91]\n\t"
    "ds_read_b64_tr_b16 a[204:205], v27 offset:8192\n\t"
    "v_mfma_f32_16x16x32_bf16 v[120:123], a[212:215], v[44:47], v[120:123]\n\t"
    "ds_read_b64_tr_b16 a[206:207], v27 offset:9216\n\t"
    "s_add_u32 m0, s65, 0x18000\n\t"
    "v_mfma_f32_16x16x32_bf16 v[120:123], a[208:211], a[228:231], v[120:123]\n\t"
    "buffer_load_dwordx4 v17, s[40:43], s62 offen lds\n\t"
    "v_mfma_f32_16x16x32_bf16 v[152:155], a[208:211], a[232:235], v[152:155]\n\t"
    "ds_read_b128 a[104:107], v28 offset:512\n\t"
    "v_mfma_f32_16x16x32_bf16 v[152:155], a[212:215], v[48:51], v[152:155]\n\t"
    "ds_read_b128 a[108:111], v28 offset:2560\n\t"
    "s_add_u32 m0, s65, 0x18400\n\t"
    "v_mfma_f32_16x16x32_bf16 v[184:187], a[212:215], v[52:55], v[184:187]\n\t"
    "buffer_load_dwordx4 v18, s[40:43], s62 offen lds\n\t"
    "v_mfma_f32_16x16x32_bf16 v[184:187], a[208:211], a[236:239], v[184:187]\n\t"
    "ds_read_b128 a[112:115], v28 offset:4608\n\t"
    "v_mfma_f32_16x16x32_bf16 v[216:219], a[208:211], a[240:243], v[216:219]\n\t"
    "ds_read_b128 a[116:119], v28 offset:6656\n\t"
    "v_mfma_f32_16x16x32_bf16 v[216:219], a[212:215], v[56:59], v[216:219]\n\t"
    "ds_read_b128 a[120:123], v28 offset:8704\n\t"
    "s_mov_b32 s68, 0x8000\n\t"
    "v_mfma_f32_16x16x32_bf16 v[248:251], a[212:215], v[60:63], v[248:251]\n\t"
    "buffer_load_dwordx4 a[96:99], v16, s[48:51], s68 offen nt\n\t"
    "v_mfma_f32_16x16x32_bf16 v[248:251], a[208:211], a[244:247], v[248:251]\n\t"
    "ds_read_b64_tr_b16 a[208:209], v26 offset:256\n\t"
    "v_mfma_f32_16x16x32_bf16 v[220:223], a[216:219], a[240:243], v[220:223]\n\t"
    "ds_read_b64_tr_b16 a[210:211], v26 offset:1280\n\t"
    "v_mfma_f32_16x16x32_bf16 v[220:223], a[220:223], v[56:59], v[220:223]\n\t"
    "ds_read_b64_tr_b16 a[212:213], v26 offset:8448\n\t"
    "v_mfma_f32_16x16x32_bf16 v[188:191], a[220:223], v[52:55], v[188:191]\n\t"
    "ds_read_b64_tr_b16 a[214:215], v26 offset:9472\n\t"
    "v_mfma_f32_16x16x32_bf16 v[188:191], a[216:219], a[236:239], v[188:191]\n\t"
    "ds_read_b128 a[124:127], v35 offset:512\n\t"
    "v_mfma_f32_16x16x32_bf16 v[156:159], a[216:219], a[232:235], v[156:159]\n\t"
    "v_mfma_f32_16x16x32_bf16 v[156:159], a[220:223], v[48:51], v[156:159]\n\t"
    "v_mfma_f32_16x16x32_bf16 v[124:127], a[220:223], v[44:47], v[124:127]\n\t"
    "buffer_load_dwordx4 a[100:103], v16, s[48:51], s68 offen offset:1024 nt\n\t"
    "v_mfma_f32_16x16x32_bf16 v[124:127], a[216:219], a[228:231], v[124:127]\n\t"
    "v_mfma_f32_16x16x32_bf16 v[92:95], a[216:219], a[224:227], v[92:95]\n\t"
    "v_mfma_f32_16x16x32_bf16 v[92:95], a[220:223], v[40:43], v[92:95]\n\t"
    "ds_read_b64_tr_b16 a[216:217], v27 offset:256\n\t"
    "s_waitcnt lgkmcnt(1)\n\t"
    "v_mfma_f32_16x16x32_bf16 v[64:67], a[192:195], a[248:251], v[64:67]\n\t"
    "ds_read_b64_tr_b16 a[218:219], v27 offset:1280\n\t"
    "v_mfma_f32_16x16x32_bf16 v[64:67], a[196:199], a[104:107], v[64:67]\n\t"
    "ds_read_b64_tr_b16 a[220:221], v27 offset:8448\n\t"
    "v_mfma_f32_16x16x32_bf16 v[96:99], a[196:199], a[108:111], v[96:99]\n\t"
    "ds_read_b64_tr_b16 a[222:223], v27 offset:9472\n\t"
    "v_mfma_f32_16x16x32_bf16 v[96:99], a[192:195], a[252:255], v[96:99]\n\t"
    "s_and_b32 s86, s84, 15\n\t"
    "s_lshr_b64 s[84:85], s[84:85], 4\n\t"
    "v_mfma_f32_16x16x32_bf16 v[128:131], a[192:195], v[0:3], v[128:131]\n\t"
    "s_lshl_b32 s61, s86, 19\n\t"
    "s_lshl_b32 s60, s86, 7\n\t"
    "s_add_u32 s62, s61, 0x40000\n\t"
    "v_mfma_f32_16x16x32_bf16 v[128:131], a[196:199], a[112:115], v[128:131]\n\t"
    "v_mfma_f32_16x16x32_bf16 v[160:163], a[196:199], a[116:119], v[160:163]\n\t"
    "v_mfma_f32_16x16x32_bf16 v[160:163], a[192:195], v[4:7], v[160:163]\n\t"
    "v_mfma_f32_16x16x32_bf16 v[192:195], a[192:195], v[8:11], v[192:195]\n\t"
    "s_waitcnt vmcnt(18) lgkmcnt(0)\n\t"
    "s_barrier\n\t"
    "v_mfma_f32_16x16x32_bf16 v[192:195], a[196:199], a[120:123], v[192:195]\n\t"
    "s_add_u32 m0, s63, 0x1a000\n\t"
    "v_mfma_f32_16x16x32_bf16 v[224:227], a[196:199], a[124:127], v[224:227]\n\t"
    "buffer_load_dwordx4 v19, s[36:39], s60 offen lds\n\t"
    "v_mfma_f32_16x16x32_bf16 v[224:227], a[192:195], v[12:15], v[224:227]\n\t"
    "ds_read_b64_tr_b16 a[192:193], v26 offset:16384\n\t"
    "v_mfma_f32_16x16x32_bf16 v[196:199], a[200:203], v[8:11], v[196:199]\n\t"
    "ds_read_b64_tr_b16 a[194:195], v26 offset:17408\n\t"
    "v_mfma_f32_16x16x32_bf16 v[196:199], a[204:207], a[120:123], v[196:199]\n\t"
    "ds_read_b64_tr_b16 a[196:197], v26 offset:24576\n\t"
    "v_mfma_f32_16x16x32_bf16 v[164:167], a[204:207], a[116:119], v[164:167]\n\t"
    "ds_read_b64_tr_b16 a[198:199], v26 offset:25600\n\t"
    "s_add_u32 m0, s65, 0x20000\n\t"
    "v_mfma_f32_16x16x32_bf16 v[164:167], a[200:203], v[4:7], v[164:167]\n\t"
    "buffer_load_dwordx4 v17, s[40:43], s61 offen lds\n\t"
    "v_mfma_f32_16x16x32_bf16 v[132:135], a[200:203], v[0:3], v[132:135]\n\t"
    "s_add_u32 m0, s63, 0x1b000\n\t"
    "v_mfma_f32_16x16x32_bf16 v[132:135], a[204:207], a[112:115], v[132:135]\n\t"
    "buffer_load_dwordx4 v20, s[36:39], s60 offen lds\n\t"
    "v_mfma_f32_16x16x32_bf16 v[100:103], a[204:207], a[108:111], v[100:103]\n\t"
    "v_mfma_f32_16x16x32_bf16 v[100:103], a[200:203], a[252:255], v[100:103]\n\t"
    "s_add_u32 m0, s63, 0x1c000\n\t"
    "v_mfma_f32_16x16x32_bf16 v[68:71], a[200:203], a[248:251], v[68:71]\n\t"
    "buffer_load_dwordx4 v21, s[36:39], s60 offen lds\n\t"
    "v_mfma_f32_16x16x32_bf16 v[68:71], a[204:207], a[104:107], v[68:71]\n\t"
    "ds_read_b64_tr_b16 a[200:201], v27 offset:16384\n\t"
    "v_mfma_f32_16x16x32_bf16 v[72:75], a[208:211], a[248:251], v[72:75]\n\t"
    "ds_read_b64_tr_b16 a[202:203], v27 offset:17408\n\t"
    "v_mfma_f32_16x16x32_bf16 v[72:75], a[212:215], a[104:107], v[72:75]\n\t"
    "ds_read_b64_tr_b16 a[204:205], v27 offset:24576\n\t"
    "v_mfma_f32_16x16x32_bf16 v[104:107], a[212:215], a[108:111], v[104:107]\n\t"
    "ds_read_b64_tr_b16 a[206:207], v27 offset:25600\n\t"
    "s_add_u32 m0, s65, 0x20400\n\t"
    "v_mfma_f32_16x16x32_bf16 v[104:107], a[208:211], a[252:255], v[104:107]\n\t"
    "buffer_load_dwordx4 v18, s[40:43], s61 offen lds\n\t"
    "v_mfma_f32_16x16x32_bf16 v[136:139], a[208:211], v[0:3], v[136:139]\n\t"
    "s_add_u32 m0, s63, 0x1d000\n\t"
    "v_mfma_f32_16x16x32_bf16 v[136:139], a[212:215], a[112:115], v[136:139]\n\t"
    "buffer_load_dwordx4 v32, s[36:39], s60 offen lds\n\t"
    "v_mfma_f32_16x16x32_bf16 v[168:171], a[212:215], a[116:119], v[168:171]\n\t"
    "v_mfma_f32_16x16x32_bf16 v[168:171], a[208:211], v[4:7], v[168:171]\n\t"
    "v_mfma_f32_16x16x32_bf16 v[200:203], a[208:211], v[8:11], v[200:203]\n\t"
    "s_add_u32 m0, s65, 0x22000\n\t"
    "v_mfma_f32_16x16x32_bf16 v[200:203], a[212:215], a[120:123], v[200:203]\n\t"
    "buffer_load_dwordx4 v17, s[40:43], s62 offen lds\n\t"
    "v_mfma_f32_16x16x32_bf16 v[232:235], a[212:215], a[124:127], v[232:235]\n\t"
    "v_mfma_f32_16x16x32_bf16 v[232:235], a[208:211], v[12:15], v[232:235]\n\t"
    "ds_read_b64_tr_b16 a[208:209], v26 offset:16640\n\t"
    "v_mfma_f32_16x16x32_bf16 v[204:207], a[216:219], v[8:11], v[204:207]\n\t"
    "ds_read_b64_tr_b16 a[210:211], v26 offset:17664\n\t"
    "v_mfma_f32_16x16x32_bf16 v[204:207], a[220:223], a[120:123], v[204:207]\n\t"
    "ds_read_b64_tr_b16 a[212:213], v26 offset:24832\n\t"
    "v_mfma_f32_16x16x32_bf16 v[172:175], a[220:223], a[116:119], v[172:175]\n\t"
    "ds_read_b64_tr_b16 a[214:215], v26 offset:25856\n\t"
    "s_add_u32 m0, s63, 0x1e000\n\t"
    "v_mfma_f32_16x16x32_bf16 v[172:175], a[216:219], v[4:7], v[172:175]\n\t"
    "buffer_load_dwordx4 v33, s[36:39], s60 offen lds\n\t"
    "v_mfma_f32_16x16x32_bf16 v[140:143], a[216:219], v[0:3], v[140:143]\n\t"
    "s_add_u32 m0, s63, 0x1f000\n\t"
    "v_mfma_f32_16x16x32_bf16 v[140:143], a[220:223], a[112:115], v[140:143]\n\t"
    "buffer_load_dwordx4 v34, s[36:39], s60 offen lds\n\t"
    "v_mfma_f32_16x16x32_bf16 v[108:111], a[220:223], a[108:111], v[108:111]\n\t"
    "s_add_u32 m0, s65, 0x22400\n\t"
    "v_mfma_f32_16x16x32_bf16 v[108:111], a[216:219], a[252:255], v[108:111]\n\t"
    "buffer_load_dwordx4 v18, s[40:43], s62 offen lds\n\t"
    "v_mfma_f32_16x16x32_bf16 v[76:79], a[216:219], a[248:251], v[76:79]\n\t"
    "v_mfma_f32_16x16x32_bf16 v[76:79], a[220:223], a[104:107], v[76:79]\n\t"
    "ds_read_b64_tr_b16 a[216:217], v27 offset:16640\n\t"
    "s_waitcnt lgkmcnt(5)\n\t"
    "v_mfma_f32_16x16x32_bf16 v[80:83], a[192:195], a[248:251], v[80:83]\n\t"
    "ds_read_b64_tr_b16 a[218:219], v27 offset:17664\n\t"
    "v_mfma_f32_16x16x32_bf16 v[80:83], a[196:199], a[104:107], v[80:83]\n\t"
    "ds_read_b64_tr_b16 a[220:221], v27 offset:24832\n\t"
    "v_mfma_f32_16x16x32_bf16 v[112:115], a[196:199], a[108:111], v[112:115]\n\t"
    "ds_read_b64_tr_b16 a[222:223], v27 offset:25856\n\t"
    "v_mfma_f32_16x16x32_bf16 v[112:115], a[192:195], a[252:255], v[112:115]\n\t"
    "s_add_u32 s61, s61, 0x80\n\t"
    "s_add_u32 s62, s62, 0x80\n\t"
    "v_mfma_f32_16x16x32_bf16 v[144:147], a[192:195], v[0:3], v[144:147]\n\t"
    "v_mfma_f32_16x16x32_bf16 v[144:147], a[196:199], a[112:115], v[144:147]\n\t"
    "v_add_u32_e32 v33, s65, v31\n\t"
    "v_lshlrev_b32_e32 v32, 3, v31\n\t"
    "v_mfma_f32_16x16x32_bf16 v[176:179], a[196:199], a[116:119], v[176:179]\n\t"
    "v_add_u32_e32 v34, 0x10000, v33\n\t"
    "v_add_u32_e32 v36, 0x20000, v33\n\t"
    "v_mfma_f32_16x16x32_bf16 v[176:179], a[192:195], v[4:7], v[176:179]\n\t"
    "v_mfma_f32_16x16x32_bf16 v[208:211], a[192:195], v[8:11], v[208:211]\n\t"
    "s_waitcnt vmcnt(16) lgkmcnt(0)\n\t"
    "s_barrier\n\t"
    "v_mfma_f32_16x16x32_bf16 v[208:211], a[196:199], a[120:123], v[208:211]\n\t"
    "ds_read_b128 a[224:227], v24 offset:0\n\t"
    "v_mfma_f32_16x16x32_bf16 v[240:243], a[196:199], a[124:127], v[240:243]\n\t"
    "ds_read_b128 a[228:231], v24 offset:2048\n\t"
    "v_mfma_f32_16x16x32_bf16 v[240:243], a[192:195], v[12:15], v[240:243]\n\t"
    "ds_read_b64_tr_b16 a[192:193], v22 offset:0\n\t"
    "v_mfma_f32_16x16x32_bf16 v[212:215], a[200:203], v[8:11], v[212:215]\n\t"
    "ds_read_b64_tr_b16 a[194:195], v22 offset:1024\n\t"
    "v_mfma_f32_16x16x32_bf16 v[212:215], a[204:207], a[120:123], v[212:215]\n\t"
    "ds_read_b64_tr_b16 a[196:197], v22 offset:8192\n\t"
    "v_mfma_f32_16x16x32_bf16 v[180:183], a[204:207], a[116:119], v[180:183]\n\t"
    "ds_read_b64_tr_b16 a[198:199], v22 offset:9216\n\t"
    "s_add_u32 m0, s65, 0x24000\n\t"
    "v_mfma_f32_16x16x32_bf16 v[180:183], a[200:203], v[4:7], v[180:183]\n\t"
    "buffer_load_dwordx4 v17, s[40:43], s61 offen lds\n\t"
    "v_mfma_f32_16x16x32_bf16 v[148:151], a[200:203], v[0:3], v[148:151]\n\t"
    "ds_read_b128 a[232:235], v24 offset:4096\n\t"
    "v_mfma_f32_16x16x32_bf16 v[148:151], a[204:207], a[112:115], v[148:151]\n\t"
    "ds_read_b128 a[236:239], v24 offset:6144\n\t"
    "v_mfma_f32_16x16x32_bf16 v[116:119], a[204:207], a[108:111], v[116:119]\n\t"
    "ds_read_b128 a[240:243], v24 offset:8192\n\t"
    "v_mfma_f32_16x16x32_bf16 v[116:119], a[200:203], a[252:255], v[116:119]\n\t"
    "ds_read_b128 a[244:247], v25 offset:0\n\t"
    "s_add_u32 m0, s65, 0x24400\n\t"
    "v_mfma_f32_16x16x32_bf16 v[84:87], a[200:203], a[248:251], v[84:87]\n\t"
    "buffer_load_dwordx4 v18, s[40:43], s61 offen lds\n\t"
    "v_mfma_f32_16x16x32_bf16 v[84:87], a[204:207], a[104:107], v[84:87]\n\t"
    "ds_read_b64_tr_b16 a[200:201], v23 offset:0\n\t"
    "v_mfma_f32_16x16x32_bf16 v[88:91], a[208:211], a[248:251], v[88:91]\n\t"
    "ds_read_b64_tr_b16 a[202:203], v23 offset:1024\n\t"
    "v_mfma_f32_16x16x32_bf16 v[88:91], a[212:215], a[104:107], v[88:91]\n\t"
    "ds_read_b64_tr_b16 a[204:205], v23 offset:8192\n\t"
    "v_mfma_f32_16x16x32_bf16 v[120:123], a[212:215], a[108:111], v[120:123]\n\t"
    "ds_read_b64_tr_b16 a[206:207], v23 offset:9216\n\t"
    "v_mfma_f32_16x16x32_bf16 v[120:123], a[208:211], a[252:255], v[120:123]\n\t"
    "ds_read_b128 v[40:43], v24 offset:512\n\t"
    "v_mfma_f32_16x16x32_bf16 v[152:155], a[208:211], v[0:3], v[152:155]\n\t"
    "ds_read_b128 v[44:47], v24 offset:2560\n\t"
    "v_mfma_f32_16x16x32_bf16 v[152:155], a[212:215], a[112:115], v[152:155]\n\t"
    "ds_read_b128 v[48:51], v24 offset:4608\n\t"
    "v_mfma_f32_16x16x32_bf16 v[184:187], a[212:215], a[116:119], v[184:187]\n\t"
    "ds_read_b128 v[52:55], v24 offset:6656\n\t"
    "v_mfma_f32_16x16x32_bf16 v[184:187], a[208:211], v[4:7], v[184:187]\n\t"
    "ds_read_b128 v[56:59], v24 offset:8704\n\t"
    "s_add_u32 m0, s65, 0x26000\n\t"
    "v_mfma_f32_16x16x32_bf16 v[216:219], a[208:211], v[8:11], v[216:219]\n\t"
    "buffer_load_dwordx4 v17, s[40:43], s62 offen lds\n\t"
    "v_mfma_f32_16x16x32_bf16 v[216:219], a[212:215], a[120:123], v[216:219]\n\t"
    "ds_read_b128 v[60:63], v25 offset:512\n\t"
    "v_mfma_f32_16x16x32_bf16 v[248:251], a[212:215], a[124:127], v[248:251]\n\t"
    "v_mfma_f32_16x16x32_bf16 v[248:251], a[208:211], v[12:15], v[248:251]\n\t"
    "ds_read_b64_tr_b16 a[208:209], v22 offset:256\n\t"
    "v_mfma_f32_16x16x32_bf16 v[220:223], a[216:219], v[8:11], v[220:223]\n\t"
    "ds_read_b64_tr_b16 a[210:211], v22 offset:1280\n\t"
    "v_mfma_f32_16x16x32_bf16 v[220:223], a[220:223], a[120:123], v[220:223]\n\t"
    "ds_read_b64_tr_b16 a[212:213], v22 offset:8448\n\t"
    "v_mfma_f32_16x16x32_bf16 v[188:191], a[220:223], a[116:119], v[188:191]\n\t"
    "ds_read_b64_tr_b16 a[214:215], v22 offset:9472\n\t"
    "v_mfma_f32_16x16x32_bf16 v[188:191], a[216:219], v[4:7], v[188:191]\n\t"
    "s_add_u32 m0, s65, 0x26400\n\t"
    "v_mfma_f32_16x16x32_bf16 v[156:159], a[216:219], v[0:3], v[156:159]\n\t"
    "buffer_load_dwordx4 v18, s[40:43], s62 offen lds\n\t"
    "v_mfma_f32_16x16x32_bf16 v[156:159], a[220:223], a[112:115], v[156:159]\n\t"
    "v_mfma_f32_16x16x32_bf16 v[124:127], a[220:223], a[108:111], v[124:127]\n\t"
    "v_mfma_f32_16x16x32_bf16 v[124:127], a[216:219], a[252:255], v[124:127]\n\t"
    "v_mfma_f32_16x16x32_bf16 v[92:95], a[216:219], a[248:251], v[92:95]\n\t"
    "v_mfma_f32_16x16x32_bf16 v[92:95], a[220:223], a[104:107], v[92:95]\n\t"
    "ds_read_b64_tr_b16 a[216:217], v23 offset:256\n\t"
    "s_waitcnt lgkmcnt(5)\n\t"
    "v_mfma_f32_16x16x32_bf16 v[64:67], a[192:195], a[224:227], v[64:67]\n\t"
    "ds_read_b64_tr_b16 a[218:219], v23 offset:1280\n\t"
    "v_mfma_f32_16x16x32_bf16 v[64:67], a[196:199], v[40:43], v[64:67]\n\t"
    "ds_read_b64_tr_b16 a[220:221], v23 offset:8448\n\t"
    "v_mfma_f32_16x16x32_bf16 v[96:99], a[196:199], v[44:47], v[96:99]\n\t"
    "ds_read_b64_tr_b16 a[222:223], v23 offset:9472\n\t"
    "v_mfma_f32_16x16x32_bf16 v[96:99], a[192:195], a[228:231], v[96:99]\n\t"
    "v_mfma_f32_16x16x32_bf16 v[128:131], a[192:195], a[232:235], v[128:131]\n\t"
    "buffer_load_dword v255, v32, s[44:47], s89 offen\n\t"
    "s_add_u32 s89, s89, 0x8000\n\t"
    "v_mfma_f32_16x16x32_bf16 v[128:131], a[196:199], v[48:51], v[128:131]\n\t"
    "v_mfma_f32_16x16x32_bf16 v[160:163], a[196:199], v[52:55], v[160:163]\n\t"
    "s_mov_b64 exec, 0xff\n\t"
    "global_load_dword v254, v37, s[90:91]\n\t"
    "s_mov_b64 exec, -1\n\t"
    "v_mfma_f32_16x16x32_bf16 v[160:163], a[192:195], a[236:239], v[160:163]\n\t"
    "v_mfma_f32_16x16x32_bf16 v[192:195], a[192:195], a[240:243], v[192:195]\n\t"
    "s_waitcnt vmcnt(18) lgkmcnt(0)\n\t"
    "s_barrier\n\t"
    "v_mfma_f32_16x16x32_bf16 v[192:195], a[196:199], v[56:59], v[192:195]\n\t"
    "v_mfma_f32_16x16x32_bf16 v[224:227], a[196:199], v[60:63], v[224:227]\n\t"
    "ds_write_b128 v33, a[0:3] offset:49152\n\t"
    "v_mfma_f32_16x16x32_bf16 v[224:227], a[192:195], a[244:247], v[224:227]\n\t"
    "ds_read_b64_tr_b16 a[192:193], v22 offset:16384\n\t"
    "v_mfma_f32_16x16x32_bf16 v[196:199], a[200:203], a[240:243], v[196:199]\n\t"
    "ds_read_b64_tr_b16 a[194:195], v22 offset:17408\n\t"
    "v_mfma_f32_16x16x32_bf16 v[196:199], a[204:207], v[56:59], v[196:199]\n\t"
    "ds_read_b64_tr_b16 a[196:197], v22 offset:24576\n\t"
    "v_mfma_f32_16x16x32_bf16 v[164:167], a[204:207], v[52:55], v[164:167]\n\t"
    "ds_read_b64_tr_b16 a[198:199], v22 offset:25600\n\t"
    "v_mfma_f32_16x16x32_bf16 v[164:167], a[200:203], a[236:239], v[164:167]\n\t"
    "ds_write_b128 v33, a[4:7] offset:50176\n\t"
    "v_mfma_f32_16x16x32_bf16 v[132:135], a[200:203], a[232:235], v[132:135]\n\t"
    "v_mfma_f32_16x16x32_bf16 v[132:135], a[204:207], v[48:51], v[132:135]\n\t"
    "ds_write_b128 v33, a[8:11] offset:57344\n\t"
    "v_mfma_f32_16x16x32_bf16 v[100:103], a[204:207], v[44:47], v[100:103]\n\t"
    "v_mfma_f32_16x16x32_bf16 v[100:103], a[200:203], a[228:231], v[100:103]\n\t"
    "v_mfma_f32_16x16x32_bf16 v[68:71], a[200:203], a[224:227], v[68:71]\n\t"
    "ds_write_b128 v33, a[12:15] offset:58368\n\t"
    "v_mfma_f32_16x16x32_bf16 v[68:71], a[204:207], v[40:43], v[68:71]\n\t"
    "ds_read_b64_tr_b16 a[200:201], v23 offset:16384\n\t"
    "v_mfma_f32_16x16x32_bf16 v[72:75], a[208:211], a[224:227], v[72:75]\n\t"
    "ds_read_b64_tr_b16 a[202:203], v23 offset:17408\n\t"
    "v_mfma_f32_16x16x32_bf16 v[72:75], a[212:215], v[40:43], v[72:75]\n\t"
    "ds_read_b64_tr_b16 a[204:205], v23 offset:24576\n\t"
    "v_mfma_f32_16x16x32_bf16 v[104:107], a[212:215], v[44:47], v[104:107]\n\t"
    "ds_read_b64_tr_b16 a[206:207], v23 offset:25600\n\t"
    "v_mfma_f32_16x16x32_bf16 v[104:107], a[208:211], a[228:231], v[104:107]\n\t"
    "ds_write_b128 v34, a[16:19] offset:0\n\t"
    "v_mfma_f32_16x16x32_bf16 v[136:139], a[208:211], a[232:235], v[136:139]\n\t"
    "v_mfma_f32_16x16x32_bf16 v[136:139], a[212:215], v[48:51], v[136:139]\n\t"
    "ds_write_b128 v34, a[20:23] offset:1024\n\t"
    "v_mfma_f32_16x16x32_bf16 v[168:171], a[212:215], v[52:55], v[168:171]\n\t"
    "v_mfma_f32_16x16x32_bf16 v[168:171], a[208:211], a[236:239], v[168:171]\n\t"
    "v_mfma_f32_16x16x32_bf16 v[200:203], a[208:211], a[240:243], v[200:203]\n\t"
    "v_mfma_f32_16x16x32_bf16 v[200:203], a[212:215], v[56:59], v[200:203]\n\t"
    "ds_write_b128 v34, a[24:27] offset:8192\n\t"
    "v_mfma_f32_16x16x32_bf16 v[232:235], a[212:215], v[60:63], v[232:235]\n\t"
    "v_mfma_f32_16x16x32_bf16 v[232:235], a[208:211], a[244:247], v[232:235]\n\t"
    "ds_read_b64_tr_b16 a[208:209], v22 offset:16640\n\t"
    "v_mfma_f32_16x16x32_bf16 v[204:207], a[216:219], a[240:243], v[204:207]\n\t"
    "ds_read_b64_tr_b16 a[210:211], v22 offset:17664\n\t"
    "v_mfma_f32_16x16x32_bf16 v[204:207], a[220:223], v[56:59], v[204:207]\n\t"
    "ds_read_b64_tr_b16 a[212:213], v22 offset:24832\n\t"
    "v_mfma_f32_16x16x32_bf16 v[172:175], a[220:223], v[52:55], v[172:175]\n\t"
    "ds_read_b64_tr_b16 a[214:215], v22 offset:25856\n\t"
    "v_mfma_f32_16x16x32_bf16 v[172:175], a[216:219], a[236:239], v[172:175]\n\t"
    "ds_write_b128 v34, a[28:31] offset:9216\n\t"
    "v_mfma_f32_16x16x32_bf16 v[140:143], a[216:219], a[232:235], v[140:143]\n\t"
    "v_mfma_f32_16x16x32_bf16 v[140:143], a[220:223], v[48:51], v[140:143]\n\t"
    "ds_write_b128 v34, a[32:35] offset:16384\n\t"
    "v_mfma_f32_16x16x32_bf16 v[108:111], a[220:223], v[44:47], v[108:111]\n\t"
    "v_mfma_f32_16x16x32_bf16 v[108:111], a[216:219], a[228:231], v[108:111]\n\t"
    "ds_write_b128 v34, a[36:39] offset:17408\n\t"
    "v_mfma_f32_16x16x32_bf16 v[76:79], a[216:219], a[224:227], v[76:79]\n\t"
    "v_mfma_f32_16x16x32_bf16 v[76:79], a[220:223], v[40:43], v[76:79]\n\t"
    "ds_read_b64_tr_b16 a[216:217], v23 offset:16640\n\t"
    "s_waitcnt lgkmcnt(9)\n\t"
    "v_mfma_f32_16x16x32_bf16 v[80:83], a[192:195], a[224:227], v[80:83]\n\t"
    "ds_read_b64_tr_b16 a[218:219], v23 offset:17664\n\t"
    "v_mfma_f32_16x16x32_bf16 v[80:83], a[196:199], v[40:43], v[80:83]\n\t"
    "ds_read_b64_tr_b16 a[220:221], v23 offset:24832\n\t"
    "v_mfma_f32_16x16x32_bf16 v[112:115], a[196:199], v[44:47], v[112:115]\n\t"
    "ds_read_b64_tr_b16 a[222:223], v23 offset:25856\n\t"
    "v_mfma_f32_16x16x32_bf16 v[112:115], a[192:195], a[228:231], v[112:115]\n\t"
    "v_mfma_f32_16x16x32_bf16 v[144:147], a[192:195], a[232:235], v[144:147]\n\t"
    "v_mfma_f32_16x16x32_bf16 v[144:147], a[196:199], v[48:51], v[144:147]\n\t"
    "v_mfma_f32_16x16x32_bf16 v[176:179], a[196:199], v[52:55], v[176:179]\n\t"
    "v_mfma_f32_16x16x32_bf16 v[176:179], a[192:195], a[236:239], v[176:179]\n\t"
    "v_mfma_f32_16x16x32_bf16 v[208:211], a[192:195], a[240:243], v[208:211]\n\t"
    "s_waitcnt vmcnt(6) lgkmcnt(0)\n\t"
    "s_barrier\n\t"
    "v_mfma_f32_16x16x32_bf16 v[208:211], a[196:199], v[56:59], v[208:211]\n\t"
    "ds_read_b128 a[248:251], v28 offset:0\n\t"
    "v_mfma_f32_16x16x32_bf16 v[240:243], a[196:199], v[60:63], v[240:243]\n\t"
    "ds_read_b128 a[252:255], v28 offset:2048\n\t"
    "v_mfma_f32_16x16x32_bf16 v[240:243], a[192:195], a[244:247], v[240:243]\n\t"
    "ds_read_b64_tr_b16 a[192:193], v26 offset:0\n\t"
    "v_mfma_f32_16x16x32_bf16 v[212:215], a[200:203], a[240:243], v[212:215]\n\t"
    "ds_read_b64_tr_b16 a[194:195], v26 offset:1024\n\t"
    "v_mfma_f32_16x16x32_bf16 v[212:215], a[204:207], v[56:59], v[212:215]\n\t"
    "ds_read_b64_tr_b16 a[196:197], v26 offset:8192\n\t"
    "v_mfma_f32_16x16x32_bf16 v[180:183], a[204:207], v[52:55], v[180:183]\n\t"
    "ds_read_b64_tr_b16 a[198:199], v26 offset:9216\n\t"
    "v_mfma_f32_16x16x32_bf16 v[180:183], a[200:203], a[236:239], v[180:183]\n\t"
    "ds_write_b128 v34, a[40:43] offset:24576\n\t"
    "v_mfma_f32_16x16x32_bf16 v[148:151], a[200:203], a[232:235], v[148:151]\n\t"
    "ds_read_b128 v[0:3], v28 offset:4096\n\t"
    "v_mfma_f32_16x16x32_bf16 v[148:151], a[204:207], v[48:51], v[148:151]\n\t"
    "ds_read_b128 v[4:7], v28 offset:6144\n\t"
    "v_mfma_f32_16x16x32_bf16 v[116:119], a[204:207], v[44:47], v[116:119]\n\t"
    "ds_read_b128 v[8:11], v28 offset:8192\n\t"
    "v_mfma_f32_16x16x32_bf16 v[116:119], a[200:203], a[228:231], v[116:119]\n\t"
    "ds_read_b128 v[12:15], v35 offset:0\n\t"
    "v_mfma_f32_16x16x32_bf16 v[84:87], a[200:203], a[224:227], v[84:87]\n\t"
    "ds_write_b128 v34, a[44:47] offset:25600\n\t"
    "v_mfma_f32_16x16x32_bf16 v[84:87], a[204:207], v[40:43], v[84:87]\n\t"
    "ds_read_b64_tr_b16 a[200:201], v27 offset:0\n\t"
    "v_mfma_f32_16x16x32_bf16 v[88:91], a[208:211], a[224:227], v[88:91]\n\t"
    "ds_read_b64_tr_b16 a[202:203], v27 offset:1024\n\t"
    "v_mfma_f32_16x16x32_bf16 v[88:91], a[212:215], v[40:43], v[88:91]\n\t"
    "ds_read_b64_tr_b16 a[204:205], v27 offset:8192\n\t"
    "v_mfma_f32_16x16x32_bf16 v[120:123], a[212:215], v[44:47], v[120:123]\n\t"
    "ds_read_b64_tr_b16 a[206:207], v27 offset:9216\n\t"
    "v_mfma_f32_16x16x32_bf16 v[120:123], a[208:211], a[228:231], v[120:123]\n\t"
    "ds_read_b128 a[104:107], v28 offset:512\n\t"
    "v_mfma_f32_16x16x32_bf16 v[152:155], a[208:211], a[232:235], v[152:155]\n\t"
    "ds_read_b128 a[108:111], v28 offset:2560\n\t"
    "v_mfma_f32_16x16x32_bf16 v[152:155], a[212:215], v[48:51], v[152:155]\n\t"
    "ds_read_b128 a[112:115], v28 offset:4608\n\t"
    "v_mfma_f32_16x16x32_bf16 v[184:187], a[212:215], v[52:55], v[184:187]\n\t"
    "ds_read_b128 a[116:119], v28 offset:6656\n\t"
    "v_mfma_f32_16x16x32_bf16 v[184:187], a[208:211], a[236:239], v[184:187]\n\t"
    "ds_read_b128 a[120:123], v28 offset:8704\n\t"
    "v_mfma_f32_16x16x32_bf16 v[216:219], a[208:211], a[240:243], v[216:219]\n\t"
    "ds_write_b128 v34, a[48:51] offset:32768\n\t"
    "v_mfma_f32_16x16x32_bf16 v[216:219], a[212:215], v[56:59], v[216:219]\n\t"
    "ds_read_b128 a[124:127], v35 offset:512\n\t"
    "v_mfma_f32_16x16x32_bf16 v[248:251], a[212:215], v[60:63], v[248:251]\n\t"
    "v_mfma_f32_16x16x32_bf16 v[248:251], a[208:211], a[244:247], v[248:251]\n\t"
    "ds_read_b64_tr_b16 a[208:209], v26 offset:256\n\t"
    "v_mfma_f32_16x16x32_bf16 v[220:223], a[216:219], a[240:243], v[220:223]\n\t"
    "ds_read_b64_tr_b16 a[210:211], v26 offset:1280\n\t"
    "v_mfma_f32_16x16x32_bf16 v[220:223], a[220:223], v[56:59], v[220:223]\n\t"
    "ds_read_b64_tr_b16 a[212:213], v26 offset:8448\n\t"
    "v_mfma_f32_16x16x32_bf16 v[188:191], a[220:223], v[52:55], v[188:191]\n\t"
    "ds_read_b64_tr_b16 a[214:215], v26 offset:9472\n\t"
    "v_mfma_f32_16x16x32_bf16 v[188:191], a[216:219], a[236:239], v[188:191]\n\t"
    "v_mfma_f32_16x16x32_bf16 v[156:159], a[216:219], a[232:235], v[156:159]\n\t"
    "ds_write_b128 v34, a[52:55] offset:33792\n\t"
    "v_mfma_f32_16x16x32_bf16 v[156:159], a[220:223], v[48:51], v[156:159]\n\t"
    "v_mfma_f32_16x16x32_bf16 v[124:127], a[220:223], v[44:47], v[124:127]\n\t"
    "v_mfma_f32_16x16x32_bf16 v[124:127], a[216:219], a[228:231], v[124:127]\n\t"
    "v_mfma_f32_16x16x32_bf16 v[92:95], a[216:219], a[224:227], v[92:95]\n\t"
    "v_mfma_f32_16x16x32_bf16 v[92:95], a[220:223], v[40:43], v[92:95]\n\t"
    "ds_read_b64_tr_b16 a[216:217], v27 offset:256\n\t"
    "s_waitcnt lgkmcnt(6)\n\t"
    "v_mfma_f32_16x16x32_bf16 v[64:67], a[192:195], a[248:251], v[64:67]\n\t"
    "ds_read_b64_tr_b16 a[218:219], v27 offset:1280\n\t"
    "v_mfma_f32_16x16x32_bf16 v[64:67], a[196:199], a[104:107], v[64:67]\n\t"
    "ds_read_b64_tr_b16 a[220:221], v27 offset:8448\n\t"
    "v_mfma_f32_16x16x32_bf16 v[96:99], a[196:199], a[108:111], v[96:99]\n\t"
    "ds_read_b64_tr_b16 a[222:223], v27 offset:9472\n\t"
    "v_mfma_f32_16x16x32_bf16 v[96:99], a[192:195], a[252:255], v[96:99]\n\t"
    "s_lshl_b32 s75, s77, 5\n\t"
    "v_xor_b32_e32 v22, s75, v22\n\t"
    "v_xor_b32_e32 v23, s75, v23\n\t"
    "v_mfma_f32_16x16x32_bf16 v[128:131], a[192:195], v[0:3], v[128:131]\n\t"
    "s_lshr_b32 s75, s8, 1\n\t"
    "s_mul_i32 s75, s75, 0x1e00\n\t"
    "s_add_u32 s75, s75, 0xffff2000\n\t"
    "v_mfma_f32_16x16x32_bf16 v[128:131], a[196:199], a[112:115], v[128:131]\n\t"
    "v_add_u32_e32 v22, s75, v22\n\t"
    "v_add_u32_e32 v23, s75, v23\n\t"
    "v_mfma_f32_16x16x32_bf16 v[160:163], a[196:199], a[116:119], v[160:163]\n\t"
    "v_add_u32_e32 v17, 0x10000, v30\n\t"
    "v_add_u32_e32 v19, 0x10000, v22\n\t"
    "v_mfma_f32_16x16x32_bf16 v[160:163], a[192:195], v[4:7], v[160:163]\n\t"
    "v_add_u32_e32 v21, 0x10000, v23\n\t"
    "v_add_u32_e32 v18, 0x20000, v30\n\t"
    "v_mfma_f32_16x16x32_bf16 v[192:195], a[192:195], v[8:11], v[192:195]\n\t"
    "s_waitcnt vmcnt(2) lgkmcnt(0)\n\t"
    "s_barrier\n\t"
    "v_mfma_f32_16x16x32_bf16 v[192:195], a[196:199], a[120:123], v[192:195]\n\t"
    "v_add_u32_e32 v20, 0x20000, v22\n\t"
    "v_add_u32_e32 v24, 0x20000, v23\n\t"
    "v_mfma_f32_16x16x32_bf16 v[224:227], a[196:199], a[124:127], v[224:227]\n\t"
    "ds_write_b128 v34, a[56:59] offset:40960\n\t"
    "v_lshrrev_b32_e32 v35, 1, v31\n\t"
    "v_mfma_f32_16x16x32_bf16 v[224:227], a[192:195], v[12:15], v[224:227]\n\t"
    "ds_read_b64_tr_b16 a[192:193], v26 offset:16384\n\t"
    "s_add_u32 s75, s65, 0x26000\n\t"
    "v_mfma_f32_16x16x32_bf16 v[196:199], a[200:203], v[8:11], v[196:199]\n\t"
    "ds_read_b64_tr_b16 a[194:195], v26 offset:17408\n\t"
    "v_add_u32_e32 v35, s75, v35\n\t"
    "v_mfma_f32_16x16x32_bf16 v[196:199], a[204:207], a[120:123], v[196:199]\n\t"
    "ds_read_b64_tr_b16 a[196:197], v26 offset:24576\n\t"
    "v_xor_b32_e32 v28, 0x800, v35\n\t"
    "v_mfma_f32_16x16x32_bf16 v[164:167], a[204:207], a[116:119], v[164:167]\n\t"
    "ds_read_b64_tr_b16 a[198:199], v26 offset:25600\n\t"
    "v_mfma_f32_16x16x32_bf16 v[164:167], a[200:203], v[4:7], v[164:167]\n\t"
    "ds_write_b128 v34, a[60:63] offset:41984\n\t"
    "v_mfma_f32_16x16x32_bf16 v[132:135], a[200:203], v[0:3], v[132:135]\n\t"
    "v_mfma_f32_16x16x32_bf16 v[132:135], a[204:207], a[112:115], v[132:135]\n\t"
    "ds_write_b128 v34, a[64:67] offset:49152\n\t"
    "v_mfma_f32_16x16x32_bf16 v[100:103], a[204:207], a[108:111], v[100:103]\n\t"
    "v_mfma_f32_16x16x32_bf16 v[100:103], a[200:203], a[252:255], v[100:103]\n\t"
    "v_mfma_f32_16x16x32_bf16 v[68:71], a[200:203], a[248:251], v[68:71]\n\t"
    "ds_write_b128 v34, a[68:71] offset:50176\n\t"
    "v_mfma_f32_16x16x32_bf16 v[68:71], a[204:207], a[104:107], v[68:71]\n\t"
    "ds_read_b64_tr_b16 a[200:201], v27 offset:16384\n\t"
    "v_mfma_f32_16x16x32_bf16 v[72:75], a[208:211], a[248:251], v[72:75]\n\t"
    "ds_read_b64_tr_b16 a[202:203], v27 offset:17408\n\t"
    "v_mfma_f32_16x16x32_bf16 v[72:75], a[212:215], a[104:107], v[72:75]\n\t"
    "ds_read_b64_tr_b16 a[204:205], v27 offset:24576\n\t"
    "v_mfma_f32_16x16x32_bf16 v[104:107], a[212:215], a[108:111], v[104:107]\n\t"
    "ds_read_b64_tr_b16 a[206:207], v27 offset:25600\n\t"
    "v_mfma_f32_16x16x32_bf16 v[104:107], a[208:211], a[252:255], v[104:107]\n\t"
    "ds_write_b128 v34, a[72:75] offset:57344\n\t"
    "v_mfma_f32_16x16x32_bf16 v[136:139], a[208:211], v[0:3], v[136:139]\n\t"
    "v_mfma_f32_16x16x32_bf16 v[136:139], a[212:215], a[112:115], v[136:139]\n\t"
    "ds_write_b128 v34, a[76:79] offset:58368\n\t"
    "v_mfma_f32_16x16x32_bf16 v[168:171], a[212:215], a[116:119], v[168:171]\n\t"
    "v_mfma_f32_16x16x32_bf16 v[168:171], a[208:211], v[4:7], v[168:171]\n\t"
    "v_mfma_f32_16x16x32_bf16 v[200:203], a[208:211], v[8:11], v[200:203]\n\t"
    "v_mfma_f32_16x16x32_bf16 v[200:203], a[212:215], a[120:123], v[200:203]\n\t"
    "ds_write_b128 v36, a[80:83] offset:0\n\t"
    "v_mfma_f32_16x16x32_bf16 v[232:235], a[212:215], a[124:127], v[232:235]\n\t"
    "v_mfma_f32_16x16x32_bf16 v[232:235], a[208:211], v[12:15], v[232:235]\n\t"
    "ds_read_b64_tr_b16 a[208:209], v26 offset:16640\n\t"
    "v_mfma_f32_16x16x32_bf16 v[204:207], a[216:219], v[8:11], v[204:207]\n\t"
    "ds_read_b64_tr_b16 a[210:211], v26 offset:17664\n\t"
    "v_mfma_f32_16x16x32_bf16 v[204:207], a[220:223], a[120:123], v[204:207]\n\t"
    "ds_read_b64_tr_b16 a[212:213], v26 offset:24832\n\t"
    "v_mfma_f32_16x16x32_bf16 v[172:175], a[220:223], a[116:119], v[172:175]\n\t"
    "ds_read_b64_tr_b16 a[214:215], v26 offset:25856\n\t"
    "v_mfma_f32_16x16x32_bf16 v[172:175], a[216:219], v[4:7], v[172:175]\n\t"
    "ds_write_b128 v36, a[84:87] offset:1024\n\t"
    "v_mfma_f32_16x16x32_bf16 v[140:143], a[216:219], v[0:3], v[140:143]\n\t"
    "v_mfma_f32_16x16x32_bf16 v[140:143], a[220:223], a[112:115], v[140:143]\n\t"
    "ds_write_b128 v36, a[88:91] offset:8192\n\t"
    "v_mfma_f32_16x16x32_bf16 v[108:111], a[220:223], a[108:111], v[108:111]\n\t"
    "v_mfma_f32_16x16x32_bf16 v[108:111], a[216:219], a[252:255], v[108:111]\n\t"
    "ds_write_b128 v36, a[92:95] offset:9216\n\t"
    "v_mfma_f32_16x16x32_bf16 v[76:79], a[216:219], a[248:251], v[76:79]\n\t"
    "v_mfma_f32_16x16x32_bf16 v[76:79], a[220:223], a[104:107], v[76:79]\n\t"
    "ds_read_b64_tr_b16 a[216:217], v27 offset:16640\n\t"
    "s_waitcnt lgkmcnt(9)\n\t"
    "v_mfma_f32_16x16x32_bf16 v[80:83], a[192:195], a[248:251], v[80:83]\n\t"
    "ds_read_b64_tr_b16 a[218:219], v27 offset:17664\n\t"
    "v_mfma_f32_16x16x32_bf16 v[80:83], a[196:199], a[104:107], v[80:83]\n\t"
    "ds_read_b64_tr_b16 a[220:221], v27 offset:24832\n\t"
    "v_mfma_f32_16x16x32_bf16 v[112:115], a[196:199], a[108:111], v[112:115]\n\t"
    "ds_read_b64_tr_b16 a[222:223], v27 offset:25856\n\t"
    "v_mfma_f32_16x16x32_bf16 v[112:115], a[192:195], a[252:255], v[112:115]\n\t"
    "v_mul_f32_e32 v64, s66, v64\n\t"
    "v_mul_f32_e32 v65, s66, v65\n\t"
    "v_mfma_f32_16x16x32_bf16 v[144:147], a[192:195], v[0:3], v[144:147]\n\t"
    "buffer_load_dword v255, v32, s[44:47], s89 offen\n\t"
    "s_add_u32 s89, s89, 0x8000\n\t"
    "v_mfma_f32_16x16x32_bf16 v[144:147], a[196:199], a[112:115], v[144:147]\n\t"
    "v_mul_f32_e32 v66, s66, v66\n\t"
    "v_mul_f32_e32 v67, s66, v67\n\t"
    "v_mfma_f32_16x16x32_bf16 v[176:179], a[196:199], a[116:119], v[176:179]\n\t"
    "v_mul_f32_e32 v68, s66, v68\n\t"
    "v_mul_f32_e32 v69, s66, v69\n\t"
    "v_mfma_f32_16x16x32_bf16 v[176:179], a[192:195], v[4:7], v[176:179]\n\t"
    "v_mul_f32_e32 v70, s66, v70\n\t"
    "v_mul_f32_e32 v71, s66, v71\n\t"
    "v_mfma_f32_16x16x32_bf16 v[208:211], a[192:195], v[8:11], v[208:211]\n\t"
    "s_waitcnt vmcnt(17) lgkmcnt(0)\n\t"
    "s_barrier\n\t"
    "v_mfma_f32_16x16x32_bf16 v[208:211], a[196:199], a[120:123], v[208:211]\n\t"
    "ds_read_b64_tr_b16 a[128:129], v22 offset:0\n\t"
    "v_mul_f32_e32 v72, s66, v72\n\t"
    "v_mfma_f32_16x16x32_bf16 v[240:243], a[196:199], a[124:127], v[240:243]\n\t"
    "ds_read_b64_tr_b16 a[130:131], v22 offset:1024\n\t"
    "v_mul_f32_e32 v73, s66, v73\n\t"
    "v_mfma_f32_16x16x32_bf16 v[240:243], a[192:195], v[12:15], v[240:243]\n\t"
    "ds_read_b128 a[192:195], v30 offset:0\n\t"
    "v_mul_f32_e32 v74, s66, v74\n\t"
    "v_mfma_f32_16x16x32_bf16 v[212:215], a[200:203], v[8:11], v[212:215]\n\t"
    "ds_read_b128 a[196:199], v30 offset:256\n\t"
    "v_mul_f32_e32 v75, s66, v75\n\t"
    "v_mfma_f32_16x16x32_bf16 v[212:215], a[204:207], a[120:123], v[212:215]\n\t"
    "ds_read_b64_tr_b16 a[132:133], v23 offset:0\n\t"
    "v_mul_f32_e32 v76, s66, v76\n\t"
    "v_mfma_f32_16x16x32_bf16 v[180:183], a[204:207], a[116:119], v[180:183]\n\t"
    "ds_read_b64_tr_b16 a[134:135], v23 offset:1024\n\t"
    "v_mul_f32_e32 v77, s66, v77\n\t"
    "v_mfma_f32_16x16x32_bf16 v[180:183], a[200:203], v[4:7], v[180:183]\n\t"
    "ds_read_b64_tr_b16 a[136:137], v22 offset:256\n\t"
    "v_mul_f32_e32 v78, s66, v78\n\t"
    "v_mfma_f32_16x16x32_bf16 v[148:151], a[200:203], v[0:3], v[148:151]\n\t"
    "ds_read_b64_tr_b16 a[138:139], v22 offset:1280\n\t"
    "v_mul_f32_e32 v79, s66, v79\n\t"
    "v_mfma_f32_16x16x32_bf16 v[148:151], a[204:207], a[112:115], v[148:151]\n\t"
    "ds_write_b128 v36, a[96:99] offset:16384\n\t"
    "v_mul_f32_e32 v96, s66, v96\n\t"
    "v_mfma_f32_16x16x32_bf16 v[116:119], a[204:207], a[108:111], v[116:119]\n\t"
    "ds_read_b64_tr_b16 a[140:141], v23 offset:256\n\t"
    "v_mul_f32_e32 v97, s66, v97\n\t"
    "v_mfma_f32_16x16x32_bf16 v[116:119], a[200:203], a[252:255], v[116:119]\n\t"
    "ds_read_b64_tr_b16 a[142:143], v23 offset:1280\n\t"
    "v_mul_f32_e32 v98, s66, v98\n\t"
    "v_mfma_f32_16x16x32_bf16 v[84:87], a[200:203], a[248:251], v[84:87]\n\t"
    "ds_read_b64_tr_b16 a[144:145], v22 offset:512\n\t"
    "v_mul_f32_e32 v99, s66, v99\n\t"
    "v_mfma_f32_16x16x32_bf16 v[84:87], a[204:207], a[104:107], v[84:87]\n\t"
    "ds_read_b128 a[200:203], v30 offset:512\n\t"
    "v_mul_f32_e32 v100, s66, v100\n\t"
    "v_mfma_f32_16x16x32_bf16 v[88:91], a[208:211], a[248:251], v[88:91]\n\t"
    "ds_read_b128 a[204:207], v30 offset:768\n\t"
    "v_mul_f32_e32 v101, s66, v101\n\t"
    "v_mfma_f32_16x16x32_bf16 v[88:91], a[212:215], a[104:107], v[88:91]\n\t"
    "ds_read_b64_tr_b16 a[146:147], v22 offset:1536\n\t"
    "v_mul_f32_e32 v102, s66, v102\n\t"
    "v_mfma_f32_16x16x32_bf16 v[120:123], a[212:215], a[108:111], v[120:123]\n\t"
    "ds_read_b64_tr_b16 a[148:149], v23 offset:512\n\t"
    "v_mul_f32_e32 v103, s66, v103\n\t"
    "v_mfma_f32_16x16x32_bf16 v[120:123], a[208:211], a[252:255], v[120:123]\n\t"
    "ds_read_b64_tr_b16 a[150:151], v23 offset:1536\n\t"
    "v_mul_f32_e32 v104, s66, v104\n\t"
    "v_mfma_f32_16x16x32_bf16 v[152:155], a[208:211], v[0:3], v[152:155]\n\t"
    "ds_read_b64_tr_b16 a[152:153], v22 offset:768\n\t"
    "v_mul_f32_e32 v105, s66, v105\n\t"
    "v_mfma_f32_16x16x32_bf16 v[152:155], a[212:215], a[112:115], v[152:155]\n\t"
    "ds_read_b64_tr_b16 a[154:155], v22 offset:1792\n\t"
    "v_mul_f32_e32 v106, s66, v106\n\t"
    "v_mfma_f32_16x16x32_bf16 v[184:187], a[212:215], a[116:119], v[184:187]\n\t"
    "ds_read_b64_tr_b16 a[156:157], v23 offset:768\n\t"
    "v_mul_f32_e32 v107, s66, v107\n\t"
    "v_mfma_f32_16x16x32_bf16 v[184:187], a[208:211], v[4:7], v[184:187]\n\t"
    "ds_read_b64_tr_b16 a[158:159], v23 offset:1792\n\t"
    "v_mul_f32_e32 v108, s66, v108\n\t"
    "v_mfma_f32_16x16x32_bf16 v[216:219], a[208:211], v[8:11], v[216:219]\n\t"
    "v_mul_f32_e32 v109, s66, v109\n\t"
    "v_mul_f32_e32 v110, s66, v110\n\t"
    "v_mfma_f32_16x16x32_bf16 v[216:219], a[212:215], a[120:123], v[216:219]\n\t"
    "v_mul_f32_e32 v111, s66, v111\n\t"
    "v_mul_f32_e32 v128, s66, v128\n\t"
    "v_mfma_f32_16x16x32_bf16 v[248:251], a[212:215], a[124:127], v[248:251]\n\t"
    "v_mul_f32_e32 v129, s66, v129\n\t"
    "v_mul_f32_e32 v130, s66, v130\n\t"
    "v_mfma_f32_16x16x32_bf16 v[248:251], a[208:211], v[12:15], v[248:251]\n\t"
    "ds_read_b128 a[208:211], v30 offset:1024\n\t"
    "v_mul_f32_e32 v131, s66, v131\n\t"
    "v_mfma_f32_16x16x32_bf16 v[220:223], a[216:219], v[8:11], v[220:223]\n\t"
    "ds_read_b128 a[212:215], v30 offset:1280\n\t"
    "v_mul_f32_e32 v132, s66, v132\n\t"
    "v_mfma_f32_16x16x32_bf16 v[220:223], a[220:223], a[120:123], v[220:223]\n\t"
    "ds_write_b128 v36, a[100:103] offset:17408\n\t"
    "v_mul_f32_e32 v133, s66, v133\n\t"
    "v_mfma_f32_16x16x32_bf16 v[188:191], a[220:223], a[116:119], v[188:191]\n\t"
    "v_mul_f32_e32 v134, s66, v134\n\t"
    "v_mul_f32_e32 v135, s66, v135\n\t"
    "v_mfma_f32_16x16x32_bf16 v[188:191], a[216:219], v[4:7], v[188:191]\n\t"
    "v_mul_f32_e32 v136, s66, v136\n\t"
    "v_mul_f32_e32 v137, s66, v137\n\t"
    "v_mfma_f32_16x16x32_bf16 v[156:159], a[216:219], v[0:3], v[156:159]\n\t"
    "v_mul_f32_e32 v138, s66, v138\n\t"
    "v_mul_f32_e32 v139, s66, v139\n\t"
    "v_mfma_f32_16x16x32_bf16 v[156:159], a[220:223], a[112:115], v[156:159]\n\t"
    "v_mul_f32_e32 v140, s66, v140\n\t"
    "v_mul_f32_e32 v141, s66, v141\n\t"
    "v_mfma_f32_16x16x32_bf16 v[124:127], a[220:223], a[108:111], v[124:127]\n\t"
    "v_mul_f32_e32 v142, s66, v142\n\t"
    "v_mul_f32_e32 v143, s66, v143\n\t"
    "v_mfma_f32_16x16x32_bf16 v[124:127], a[216:219], a[252:255], v[124:127]\n\t"
    "v_mul_f32_e32 v160, s66, v160\n\t"
    "v_mul_f32_e32 v161, s66, v161\n\t"
    "v_mfma_f32_16x16x32_bf16 v[92:95], a[216:219], a[248:251], v[92:95]\n\t"
    "v_mul_f32_e32 v162, s66, v162\n\t"
    "v_mul_f32_e32 v163, s66, v163\n\t"
    "v_mfma_f32_16x16x32_bf16 v[92:95], a[220:223], a[104:107], v[92:95]\n\t"
    "v_mul_f32_e32 v164, s66, v164\n\t"
    "v_mul_f32_e32 v165, s66, v165\n\t"
    "ds_read_b128 a[216:219], v30 offset:1536\n\t"
    "ds_read_b128 a[220:223], v30 offset:1792\n\t"
    "v_pk_mul_f32 v[80:81], v[80:81], s[66:67]\n\t"
    "v_pk_mul_f32 v[82:83], v[82:83], s[66:67]\n\t"
    "v_pk_mul_f32 v[84:85], v[84:85], s[66:67]\n\t"
    "v_pk_mul_f32 v[86:87], v[86:87], s[66:67]\n\t"
    "v_pk_mul_f32 v[112:113], v[112:113], s[66:67]\n\t"
    "v_pk_mul_f32 v[114:115], v[114:115], s[66:67]\n\t"
    "v_pk_mul_f32 v[116:117], v[116:117], s[66:67]\n\t"
    "v_pk_mul_f32 v[118:119], v[118:119], s[66:67]\n\t"
    "v_pk_mul_f32 v[144:145], v[144:145], s[66:67]\n\t"
    "v_pk_mul_f32 v[146:147], v[146:147], s[66:67]\n\t"
    "v_pk_mul_f32 v[148:149], v[148:149], s[66:67]\n\t"
    "v_pk_mul_f32 v[150:151], v[150:151], s[66:67]\n\t"
    "v_pk_mul_f32 v[166:167], v[166:167], s[66:67]\n\t"
    "v_pk_mul_f32 v[168:169], v[168:169], s[66:67]\n\t"
    "v_pk_mul_f32 v[170:171], v[170:171], s[66:67]\n\t"
    "v_pk_mul_f32 v[172:173], v[172:173], s[66:67]\n\t"
    "v_pk_mul_f32 v[174:175], v[174:175], s[66:67]\n\t"
    "v_pk_mul_f32 v[176:177], v[176:177], s[66:67]\n\t"
    "v_pk_mul_f32 v[178:179], v[178:179], s[66:67]\n\t"
    "v_pk_mul_f32 v[180:181], v[180:181], s[66:67]\n\t"
    "v_pk_mul_f32 v[182:183], v[182:183], s[66:67]\n\t"
    "v_pk_mul_f32 v[192:193], v[192:193], s[66:67]\n\t"
    "v_pk_mul_f32 v[194:195], v[194:195], s[66:67]\n\t"
    "v_pk_mul_f32 v[196:197], v[196:197], s[66:67]\n\t"
    "v_pk_mul_f32 v[198:199], v[198:199], s[66:67]\n\t"
    "v_pk_mul_f32 v[200:201], v[200:201], s[66:67]\n\t"
    "v_pk_mul_f32 v[202:203], v[202:203], s[66:67]\n\t"
    "v_pk_mul_f32 v[204:205], v[204:205], s[66:67]\n\t"
    "v_pk_mul_f32 v[206:207], v[206:207], s[66:67]\n\t"
    "v_pk_mul_f32 v[208:209], v[208:209], s[66:67]\n\t"
    "v_pk_mul_f32 v[210:211], v[210:211], s[66:67]\n\t"
    "v_pk_mul_f32 v[212:213], v[212:213], s[66:67]\n\t"
    "v_pk_mul_f32 v[214:215], v[214:215], s[66:67]\n\t"
    "v_pk_mul_f32 v[224:225], v[224:225], s[66:67]\n\t"
    "v_pk_mul_f32 v[226:227], v[226:227], s[66:67]\n\t"
    "v_pk_mul_f32 v[232:233], v[232:233], s[66:67]\n\t"
    "v_pk_mul_f32 v[234:235], v[234:235], s[66:67]\n\t"
    "v_pk_mul_f32 v[240:241], v[240:241], s[66:67]\n\t"
    "v_pk_mul_f32 v[242:243], v[242:243], s[66:67]\n\t"
    "v_pk_mul_f32 v[88:89], v[88:89], s[66:67]\n\t"
    "v_pk_mul_f32 v[90:91], v[90:91], s[66:67]\n\t"
    "v_pk_mul_f32 v[92:93], v[92:93], s[66:67]\n\t"
    "v_pk_mul_f32 v[94:95], v[94:95], s[66:67]\n\t"
    "v_pk_mul_f32 v[120:121], v[120:121], s[66:67]\n\t"
    "v_pk_mul_f32 v[122:123], v[122:123], s[66:67]\n\t"
    "v_pk_mul_f32 v[124:125], v[124:125], s[66:67]\n\t"
    "v_pk_mul_f32 v[126:127], v[126:127], s[66:67]\n\t"
    "v_pk_mul_f32 v[152:153], v[152:153], s[66:67]\n\t"
    "v_pk_mul_f32 v[154:155], v[154:155], s[66:67]\n\t"
    "v_pk_mul_f32 v[156:157], v[156:157], s[66:67]\n\t"
    "v_pk_mul_f32 v[158:159], v[158:159], s[66:67]\n\t"
    "v_pk_mul_f32 v[184:185], v[184:185], s[66:67]\n\t"
    "v_pk_mul_f32 v[186:187], v[186:187], s[66:67]\n\t"
    "v_pk_mul_f32 v[188:189], v[188:189], s[66:67]\n\t"
    "v_pk_mul_f32 v[190:191], v[190:191], s[66:67]\n\t"
    "v_pk_mul_f32 v[216:217], v[216:217], s[66:67]\n\t"
    "v_pk_mul_f32 v[218:219], v[218:219], s[66:67]\n\t"
    "v_pk_mul_f32 v[220:221], v[220:221], s[66:67]\n\t"
    "v_pk_mul_f32 v[222:223], v[222:223], s[66:67]\n\t"
    "v_pk_mul_f32 v[248:249], v[248:249], s[66:67]\n\t"
    "v_pk_mul_f32 v[250:251], v[250:251], s[66:67]\n\t"
    "v_cvt_pk_bf16_f32 v64, v64, v65\n\t"
    "v_cvt_pk_bf16_f32 v65, v66, v67\n\t"
    "v_cvt_pk_bf16_f32 v66, v68, v69\n\t"
    "v_cvt_pk_bf16_f32 v67, v70, v71\n\t"
    "v_cvt_pk_bf16_f32 v68, v72, v73\n\t"
    "v_cvt_pk_bf16_f32 v69, v74, v75\n\t"
    "v_cvt_pk_bf16_f32 v70, v76, v77\n\t"
    "v_cvt_pk_bf16_f32 v71, v78, v79\n\t"
    "v_cvt_pk_bf16_f32 v72, v80, v81\n\t"
    "v_cvt_pk_bf16_f32 v73, v82, v83\n\t"
    "v_cvt_pk_bf16_f32 v74, v84, v85\n\t"
    "v_cvt_pk_bf16_f32 v75, v86, v87\n\t"
    "v_cvt_pk_bf16_f32 v76, v88, v89\n\t"
    "v_cvt_pk_bf16_f32 v77, v90, v91\n\t"
    "v_cvt_pk_bf16_f32 v78, v92, v93\n\t"
    "v_cvt_pk_bf16_f32 v79, v94, v95\n\t"
    "v_permlane32_swap_b32_e32 v64, v66\n\t"
    "v_permlane32_swap_b32_e32 v65, v67\n\t"
    "v_permlane32_swap_b32_e32 v68, v70\n\t"
    "v_permlane32_swap_b32_e32 v69, v71\n\t"
    "v_permlane32_swap_b32_e32 v72, v74\n\t"
    "v_permlane32_swap_b32_e32 v73, v75\n\t"
    "v_permlane32_swap_b32_e32 v76, v78\n\t"
    "v_permlane32_swap_b32_e32 v77, v79\n\t"
    "v_permlane16_swap_b32_e32 v64, v66\n\t"
    "v_permlane16_swap_b32_e32 v65, v67\n\t"
    "v_permlane16_swap_b32_e32 v68, v70\n\t"
    "v_permlane16_swap_b32_e32 v69, v71\n\t"
    "v_permlane16_swap_b32_e32 v72, v74\n\t"
    "v_permlane16_swap_b32_e32 v73, v75\n\t"
    "v_permlane16_swap_b32_e32 v76, v78\n\t"
    "v_permlane16_swap_b32_e32 v77, v79\n\t"
    "s_waitcnt lgkmcnt(0)\n\t"
    "v_cvt_pk_bf16_f32 v80, v96, v97\n\t"
    "v_cvt_pk_bf16_f32 v81, v98, v99\n\t"
    "v_mfma_f32_16x16x32_bf16 v[40:43], a[192:195], v[64:67], 0\n\t"
    "v_cvt_pk_bf16_f32 v82, v100, v101\n\t"
    "v_cvt_pk_bf16_f32 v83, v102, v103\n\t"
    "v_mfma_f32_16x16x32_bf16 v[40:43], a[196:199], v[68:71], v[40:43]\n\t"
    "v_cvt_pk_bf16_f32 v84, v104, v105\n\t"
    "v_cvt_pk_bf16_f32 v85, v106, v107\n\t"
    "v_mfma_f32_16x16x32_bf16 v[40:43], a[200:203], v[72:75], v[40:43]\n\t"
    "v_cvt_pk_bf16_f32 v86, v108, v109\n\t"
    "v_cvt_pk_bf16_f32 v87, v110, v111\n\t"
    "v_mfma_f32_16x16x32_bf16 v[40:43], a[204:207], v[76:79], v[40:43]\n\t"
    "v_cvt_pk_bf16_f32 v88, v112, v113\n\t"
    "v_cvt_pk_bf16_f32 v89, v114, v115\n\t"
    "v_cvt_pk_bf16_f32 v90, v116, v117\n\t"
    "v_cvt_pk_bf16_f32 v91, v118, v119\n\t"
    "v_cvt_pk_bf16_f32 v92, v120, v121\n\t"
    "v_cvt_pk_bf16_f32 v93, v122, v123\n\t"
    "v_cvt_pk_bf16_f32 v94, v124, v125\n\t"
    "v_cvt_pk_bf16_f32 v95, v126, v127\n\t"
    "v_permlane32_swap_b32_e32 v80, v82\n\t"
    "v_permlane32_swap_b32_e32 v81, v83\n\t"
    "v_permlane32_swap_b32_e32 v84, v86\n\t"
    "v_permlane32_swap_b32_e32 v85, v87\n\t"
    "v_permlane32_swap_b32_e32 v88, v90\n\t"
    "v_permlane32_swap_b32_e32 v89, v91\n\t"
    "v_permlane32_swap_b32_e32 v92, v94\n\t"
    "v_permlane32_swap_b32_e32 v93, v95\n\t"
    "v_permlane16_swap_b32_e32 v80, v82\n\t"
    "v_permlane16_swap_b32_e32 v81, v83\n\t"
    "v_permlane16_swap_b32_e32 v84, v86\n\t"
    "v_permlane16_swap_b32_e32 v85, v87\n\t"
    "v_permlane16_swap_b32_e32 v88, v90\n\t"
    "v_permlane16_swap_b32_e32 v89, v91\n\t"
    "v_permlane16_swap_b32_e32 v92, v94\n\t"
    "v_permlane16_swap_b32_e32 v93, v95\n\t"
    "v_cvt_pk_bf16_f32 v96, v128, v129\n\t"
    "v_cvt_pk_bf16_f32 v97, v130, v131\n\t"
    "v_mfma_f32_16x16x32_bf16 v[44:47], a[192:195], v[80:83], 0\n\t"
    "v_cvt_pk_bf16_f32 v98, v132, v133\n\t"
    "v_cvt_pk_bf16_f32 v99, v134, v135\n\t"
    "v_mfma_f32_16x16x32_bf16 v[44:47], a[196:199], v[84:87], v[44:47]\n\t"
    "v_cvt_pk_bf16_f32 v100, v136, v137\n\t"
    "v_cvt_pk_bf16_f32 v101, v138, v139\n\t"
    "v_mfma_f32_16x16x32_bf16 v[44:47], a[200:203], v[88:91], v[44:47]\n\t"
    "v_cvt_pk_bf16_f32 v102, v140, v141\n\t"
    "v_cvt_pk_bf16_f32 v103, v142, v143\n\t"
    "v_mfma_f32_16x16x32_bf16 v[44:47], a[204:207], v[92:95], v[44:47]\n\t"
    "v_cvt_pk_bf16_f32 v104, v144, v145\n\t"
    "v_cvt_pk_bf16_f32 v105, v146, v147\n\t"
    "v_cvt_pk_bf16_f32 v106, v148, v149\n\t"
    "v_cvt_pk_bf16_f32 v107, v150, v151\n\t"
    "v_cvt_pk_bf16_f32 v108, v152, v153\n\t"
    "v_cvt_pk_bf16_f32 v109, v154, v155\n\t"
    "v_cvt_pk_bf16_f32 v110, v156, v157\n\t"
    "v_cvt_pk_bf16_f32 v111, v158, v159\n\t"
    "v_cvt_pk_bf16_f32 v144, v224, v225\n\t"
    "v_cvt_pk_bf16_f32 v145, v226, v227\n\t"
    "v_cvt_pk_bf16_f32 v148, v232, v233\n\t"
    "v_cvt_pk_bf16_f32 v149, v234, v235\n\t"
    "v_cvt_pk_bf16_f32 v152, v240, v241\n\t"
    "v_cvt_pk_bf16_f32 v153, v242, v243\n\t"
    "v_cvt_pk_bf16_f32 v156, v248, v249\n\t"
    "v_cvt_pk_bf16_f32 v157, v250, v251\n\t"
    "ds_write_b64 v35, v[144:145] offset:0\n\t"
    "ds_write_b64 v35, v[148:149] offset:512\n\t"
    "ds_write_b64 v35, v[152:153] offset:1024\n\t"
    "ds_write_b64 v35, v[156:157] offset:1536\n\t"
    "v_permlane32_swap_b32_e32 v96, v98\n\t"
    "v_permlane32_swap_b32_e32 v97, v99\n\t"
    "v_permlane32_swap_b32_e32 v100, v102\n\t"
    "v_permlane32_swap_b32_e32 v101, v103\n\t"
    "v_permlane32_swap_b32_e32 v104, v106\n\t"
    "v_permlane32_swap_b32_e32 v105, v107\n\t"
    "v_permlane32_swap_b32_e32 v108, v110\n\t"
    "v_permlane32_swap_b32_e32 v109, v111\n\t"
    "v_permlane16_swap_b32_e32 v96, v98\n\t"
    "v_permlane16_swap_b32_e32 v97, v99\n\t"
    "v_permlane16_swap_b32_e32 v100, v102\n\t"
    "v_permlane16_swap_b32_e32 v101, v103\n\t"
    "v_permlane16_swap_b32_e32 v104, v106\n\t"
    "v_permlane16_swap_b32_e32 v105, v107\n\t"
    "v_permlane16_swap_b32_e32 v108, v110\n\t"
    "v_permlane16_swap_b32_e32 v109, v111\n\t"
    "v_cvt_pk_bf16_f32 v112, v160, v161\n\t"
    "v_cvt_pk_bf16_f32 v113, v162, v163\n\t"
    "v_mfma_f32_16x16x32_bf16 v[48:51], a[192:195], v[96:99], 0\n\t"
    "v_cvt_pk_bf16_f32 v114, v164, v165\n\t"
    "v_cvt_pk_bf16_f32 v115, v166, v167\n\t"
    "v_mfma_f32_16x16x32_bf16 v[48:51], a[196:199], v[100:103], v[48:51]\n\t"
    "v_cvt_pk_bf16_f32 v116, v168, v169\n\t"
    "v_cvt_pk_bf16_f32 v117, v170, v171\n\t"
    "v_mfma_f32_16x16x32_bf16 v[48:51], a[200:203], v[104:107], v[48:51]\n\t"
    "v_cvt_pk_bf16_f32 v118, v172, v173\n\t"
    "v_cvt_pk_bf16_f32 v119, v174, v175\n\t"
    "v_mfma_f32_16x16x32_bf16 v[48:51], a[204:207], v[108:111], v[48:51]\n\t"
    "v_cvt_pk_bf16_f32 v120, v176, v177\n\t"
    "v_cvt_pk_bf16_f32 v121, v178, v179\n\t"
    "v_cvt_pk_bf16_f32 v122, v180, v181\n\t"
    "v_cvt_pk_bf16_f32 v123, v182, v183\n\t"
    "v_cvt_pk_bf16_f32 v124, v184, v185\n\t"
    "v_cvt_pk_bf16_f32 v125, v186, v187\n\t"
    "v_cvt_pk_bf16_f32 v126, v188, v189\n\t"
    "v_cvt_pk_bf16_f32 v127, v190, v191\n\t"
    "v_permlane32_swap_b32_e32 v112, v114\n\t"
    "v_permlane32_swap_b32_e32 v113, v115\n\t"
    "v_permlane32_swap_b32_e32 v116, v118\n\t"
    "v_permlane32_swap_b32_e32 v117, v119\n\t"
    "v_permlane32_swap_b32_e32 v120, v122\n\t"
    "v_permlane32_swap_b32_e32 v121, v123\n\t"
    "v_permlane32_swap_b32_e32 v124, v126\n\t"
    "v_permlane32_swap_b32_e32 v125, v127\n\t"
    "v_permlane16_swap_b32_e32 v112, v114\n\t"
    "v_permlane16_swap_b32_e32 v113, v115\n\t"
    "v_permlane16_swap_b32_e32 v116, v118\n\t"
    "v_permlane16_swap_b32_e32 v117, v119\n\t"
    "v_permlane16_swap_b32_e32 v120, v122\n\t"
    "v_permlane16_swap_b32_e32 v121, v123\n\t"
    "v_permlane16_swap_b32_e32 v124, v126\n\t"
    "v_permlane16_swap_b32_e32 v125, v127\n\t"
    "buffer_load_dword v255, v32, s[44:47], s89 offen\n\t"
    "s_add_u32 s89, s89, 0x8000\n\t"
    "s_waitcnt lgkmcnt(0)\n\t"
    "s_barrier\n\t"
    "ds_read_b64 v[146:147], v28 offset:0\n\t"
    "ds_read_b64 v[150:151], v28 offset:512\n\t"
    "ds_read_b64 v[154:155], v28 offset:1024\n\t"
    "ds_read_b64 v[158:159], v28 offset:1536\n\t"
    "v_cvt_pk_bf16_f32 v128, v192, v193\n\t"
    "v_cvt_pk_bf16_f32 v129, v194, v195\n\t"
    "v_mfma_f32_16x16x32_bf16 v[52:55], a[192:195], v[112:115], 0\n\t"
    "v_cvt_pk_bf16_f32 v130, v196, v197\n\t"
    "v_cvt_pk_bf16_f32 v131, v198, v199\n\t"
    "v_mfma_f32_16x16x32_bf16 v[52:55], a[196:199], v[116:119], v[52:55]\n\t"
    "v_cvt_pk_bf16_f32 v132, v200, v201\n\t"
    "v_cvt_pk_bf16_f32 v133, v202, v203\n\t"
    "v_mfma_f32_16x16x32_bf16 v[52:55], a[200:203], v[120:123], v[52:55]\n\t"
    "v_cvt_pk_bf16_f32 v134, v204, v205\n\t"
    "v_cvt_pk_bf16_f32 v135, v206, v207\n\t"
    "v_mfma_f32_16x16x32_bf16 v[52:55], a[204:207], v[124:127], v[52:55]\n\t"
    "v_cvt_pk_bf16_f32 v136, v208, v209\n\t"
    "v_cvt_pk_bf16_f32 v137, v210, v211\n\t"
    "v_cvt_pk_bf16_f32 v138, v212, v213\n\t"
    "v_cvt_pk_bf16_f32 v139, v214, v215\n\t"
    "v_cvt_pk_bf16_f32 v140, v216, v217\n\t"
    "v_cvt_pk_bf16_f32 v141, v218, v219\n\t"
    "v_cvt_pk_bf16_f32 v142, v220, v221\n\t"
    "v_cvt_pk_bf16_f32 v143, v222, v223\n\t"
    "v_permlane32_swap_b32_e32 v128, v130\n\t"
    "v_permlane32_swap_b32_e32 v129, v131\n\t"
    "v_permlane32_swap_b32_e32 v132, v134\n\t"
    "v_permlane32_swap_b32_e32 v133, v135\n\t"
    "v_permlane32_swap_b32_e32 v136, v138\n\t"
    "v_permlane32_swap_b32_e32 v137, v139\n\t"
    "v_permlane32_swap_b32_e32 v140, v142\n\t"
    "v_permlane32_swap_b32_e32 v141, v143\n\t"
    "v_permlane16_swap_b32_e32 v128, v130\n\t"
    "v_permlane16_swap_b32_e32 v129, v131\n\t"
    "v_permlane16_swap_b32_e32 v132, v134\n\t"
    "v_permlane16_swap_b32_e32 v133, v135\n\t"
    "v_permlane16_swap_b32_e32 v136, v138\n\t"
    "v_permlane16_swap_b32_e32 v137, v139\n\t"
    "v_permlane16_swap_b32_e32 v140, v142\n\t"
    "v_permlane16_swap_b32_e32 v141, v143\n\t"
    "s_waitcnt lgkmcnt(0)\n\t"
    "v_permlane32_swap_b32_e32 v144, v146\n\t"
    "v_permlane32_swap_b32_e32 v145, v147\n\t"
    "v_mfma_f32_16x16x32_bf16 v[56:59], a[192:195], v[128:131], 0\n\t"
    "v_permlane32_swap_b32_e32 v148, v150\n\t"
    "v_permlane32_swap_b32_e32 v149, v151\n\t"
    "v_mfma_f32_16x16x32_bf16 v[56:59], a[196:199], v[132:135], v[56:59]\n\t"
    "v_permlane32_swap_b32_e32 v152, v154\n\t"
    "v_permlane32_swap_b32_e32 v153, v155\n\t"
    "v_mfma_f32_16x16x32_bf16 v[56:59], a[200:203], v[136:139], v[56:59]\n\t"
    "v_permlane32_swap_b32_e32 v156, v158\n\t"
    "v_permlane32_swap_b32_e32 v157, v159\n\t"
    "v_mfma_f32_16x16x32_bf16 v[56:59], a[204:207], v[140:143], v[56:59]\n\t"
    "v_permlane16_swap_b32_e32 v144, v146\n\t"
    "v_permlane16_swap_b32_e32 v145, v147\n\t"
    "v_permlane16_swap_b32_e32 v148, v150\n\t"
    "v_permlane16_swap_b32_e32 v149, v151\n\t"
    "v_permlane16_swap_b32_e32 v152, v154\n\t"
    "v_permlane16_swap_b32_e32 v153, v155\n\t"
    "v_permlane16_swap_b32_e32 v156, v158\n\t"
    "v_permlane16_swap_b32_e32 v157, v159\n\t"
    "v_max3_f32 v0, v40, v41, v42\n\t"
    "v_max3_f32 v1, v44, v45, v46\n\t"
    "v_mfma_f32_16x16x32_bf16 v[60:63], a[192:195], v[144:147], 0\n\t"
    "v_max3_f32 v2, v48, v49, v50\n\t"
    "v_max3_f32 v3, v52, v53, v54\n\t"
    "v_mfma_f32_16x16x32_bf16 v[60:63], a[196:199], v[148:151], v[60:63]\n\t"
    "v_max3_f32 v4, v56, v57, v58\n\t"
    "v_max_f32_e32 v0, v0, v43\n\t"
    "v_mfma_f32_16x16x32_bf16 v[60:63], a[200:203], v[152:155], v[60:63]\n\t"
    "v_max_f32_e32 v1, v1, v47\n\t"
    "v_max_f32_e32 v2, v2, v51\n\t"
    "v_mfma_f32_16x16x32_bf16 v[60:63], a[204:207], v[156:159], v[60:63]\n\t"
    "v_max_f32_e32 v3, v3, v55\n\t"
    "v_max_f32_e32 v4, v4, v59\n\t"
    "s_cmp_lg_u32 s77, 0\n\t"
    "s_mov_b32 s75, 0x7149f2ca\n\t"
    "s_cselect_b32 s75, s75, 0xf149f2ca\n\t"
    "s_cbranch_scc0 .Lfa_nz5\n\t"
    "v_mov_b32_e32 v144, 0\n\t"
    "v_mov_b32_e32 v145, 0\n\t"
    "v_mov_b32_e32 v146, 0\n\t"
    "v_mov_b32_e32 v147, 0\n\t"
    "v_mov_b32_e32 v148, 0\n\t"
    "v_mov_b32_e32 v149, 0\n\t"
    "v_mov_b32_e32 v150, 0\n\t"
    "v_mov_b32_e32 v151, 0\n\t"
    "v_mov_b32_e32 v152, 0\n\t"
    "v_mov_b32_e32 v153, 0\n\t"
    "v_mov_b32_e32 v154, 0\n\t"
    "v_mov_b32_e32 v155, 0\n\t"
    "v_mov_b32_e32 v156, 0\n\t"
    "v_mov_b32_e32 v157, 0\n\t"
    "v_mov_b32_e32 v158, 0\n\t"
    "v_mov_b32_e32 v159, 0\n\t"
    ".Lfa_nz5:\n\t"
    "s_nop 2\n\t"
    "v_max3_f32 v5, v60, v61, v62\n\t"
    "v_max3_f32 v5, v5, v63, s75\n\t"
    "v_mov_b32_e32 v8, v0\n\t"
    "v_mov_b32_e32 v9, v1\n\t"
    "v_mov_b32_e32 v10, v2\n\t"
    "v_mov_b32_e32 v11, v3\n\t"
    "v_mov_b32_e32 v12, v4\n\t"
    "v_mov_b32_e32 v13, v5\n\t"
    "v_permlane32_swap_b32_e32 v0, v8\n\t"
    "v_permlane32_swap_b32_e32 v1, v9\n\t"
    "v_permlane32_swap_b32_e32 v2, v10\n\t"
    "v_permlane32_swap_b32_e32 v3, v11\n\t"
    "v_permlane32_swap_b32_e32 v4, v12\n\t"
    "v_permlane32_swap_b32_e32 v5, v13\n\t"
    "v_max_f32_e32 v0, v0, v8\n\t"
    "v_max_f32_e32 v1, v1, v9\n\t"
    "v_max_f32_e32 v2, v2, v10\n\t"
    "v_max_f32_e32 v3, v3, v11\n\t"
    "v_max_f32_e32 v4, v4, v12\n\t"
    "v_max_f32_e32 v5, v5, v13\n\t"
    "v_mov_b32_e32 v8, v0\n\t"
    "v_mov_b32_e32 v9, v1\n\t"
    "v_mov_b32_e32 v10, v2\n\t"
    "v_mov_b32_e32 v11, v3\n\t"
    "v_mov_b32_e32 v12, v4\n\t"
    "v_mov_b32_e32 v13, v5\n\t"
    "v_permlane16_swap_b32_e32 v0, v8\n\t"
    "v_permlane16_swap_b32_e32 v1, v9\n\t"
    "v_permlane16_swap_b32_e32 v2, v10\n\t"
    "v_permlane16_swap_b32_e32 v3, v11\n\t"
    "v_permlane16_swap_b32_e32 v4, v12\n\t"
    "v_permlane16_swap_b32_e32 v5, v13\n\t"
    "v_max_f32_e32 v0, v0, v8\n\t"
    "v_max_f32_e32 v1, v1, v9\n\t"
    "v_max_f32_e32 v2, v2, v10\n\t"
    "v_max_f32_e32 v3, v3, v11\n\t"
    "v_max_f32_e32 v4, v4, v12\n\t"
    "v_max_f32_e32 v5, v5, v13\n\t"
    "v_xor_b32_e32 v40, 0x80000000, v0\n\t"
    "v_xor_b32_e32 v41, 0x80000000, v0\n\t"
    "v_xor_b32_e32 v42, 0x80000000, v0\n\t"
    "v_xor_b32_e32 v43, 0x80000000, v0\n\t"
    "v_xor_b32_e32 v44, 0x80000000, v1\n\t"
    "v_xor_b32_e32 v45, 0x80000000, v1\n\t"
    "v_xor_b32_e32 v46, 0x80000000, v1\n\t"
    "v_xor_b32_e32 v47, 0x80000000, v1\n\t"
    "v_xor_b32_e32 v48, 0x80000000, v2\n\t"
    "v_xor_b32_e32 v49, 0x80000000, v2\n\t"
    "v_xor_b32_e32 v50, 0x80000000, v2\n\t"
    "v_xor_b32_e32 v51, 0x80000000, v2\n\t"
    "v_xor_b32_e32 v52, 0x80000000, v3\n\t"
    "v_xor_b32_e32 v53, 0x80000000, v3\n\t"
    "v_xor_b32_e32 v54, 0x80000000, v3\n\t"
    "v_xor_b32_e32 v55, 0x80000000, v3\n\t"
    "v_xor_b32_e32 v56, 0x80000000, v4\n\t"
    "v_xor_b32_e32 v57, 0x80000000, v4\n\t"
    "v_xor_b32_e32 v58, 0x80000000, v4\n\t"
    "v_xor_b32_e32 v59, 0x80000000, v4\n\t"
    "v_xor_b32_e32 v60, 0x80000000, v5\n\t"
    "v_xor_b32_e32 v61, 0x80000000, v5\n\t"
    "v_xor_b32_e32 v62, 0x80000000, v5\n\t"
    "v_xor_b32_e32 v63, 0x80000000, v5\n\t"
    ".Lfa_att:\n\t"
    "s_memrealtime s[30:31]\n\t"
    "s_waitcnt vmcnt(63) lgkmcnt(0)\n\t"
    "s_barrier\n\t"
    "s_add_u32 s29, s30, 0x71\n\t"
    "v_mfma_f32_16x16x32_bf16 v[224:227], a[192:195], v[64:67], v[40:43]\n\t"
    "v_mfma_f32_16x16x32_bf16 v[224:227], a[196:199], v[68:71], v[224:227]\n\t"
    "ds_read_b128 a[224:227], v30 offset:32768\n\t"
    "v_mfma_f32_16x16x32_bf16 v[224:227], a[200:203], v[72:75], v[224:227]\n\t"
    "v_mfma_f32_16x16x32_bf16 v[224:227], a[204:207], v[76:79], v[224:227]\n\t"
    "ds_read_b128 a[228:231], v30 offset:33024\n\t"
    "v_mfma_f32_16x16x32_bf16 v[228:231], a[208:211], v[64:67], v[40:43]\n\t"
    "v_mfma_f32_16x16x32_bf16 v[228:231], a[212:215], v[68:71], v[228:231]\n\t"
    "ds_read_b128 a[232:235], v30 offset:33280\n\t"
    "v_mfma_f32_16x16x32_bf16 v[228:231], a[216:219], v[72:75], v[228:231]\n\t"
    "v_mfma_f32_16x16x32_bf16 v[228:231], a[220:223], v[76:79], v[228:231]\n\t"
    "ds_read_b128 a[236:239], v30 offset:33536\n\t"
    "s_nop 3\n\t"
    "v_mfma_f32_16x16x32_bf16 v[232:235], a[192:195], v[80:83], v[44:47]\n\t"
    "v_exp_f32_e32 v224, v224\n\t"
    "v_mfma_f32_16x16x32_bf16 v[232:235], a[196:199], v[84:87], v[232:235]\n\t"
    "ds_read_b128 a[240:243], v30 offset:33792\n\t"
    "v_mov_b32_e32 v32, v224\n\t"
    "v_mfma_f32_16x16x32_bf16 v[232:235], a[200:203], v[88:91], v[232:235]\n\t"
    "v_exp_f32_e32 v225, v225\n\t"
    "v_mfma_f32_16x16x32_bf16 v[232:235], a[204:207], v[92:95], v[232:235]\n\t"
    "ds_read_b128 a[244:247], v30 offset:34048\n\t"
    "v_add_f32_e32 v32, v225, v32\n\t"
    "v_mfma_f32_16x16x32_bf16 v[236:239], a[208:211], v[80:83], v[44:47]\n\t"
    "v_exp_f32_e32 v226, v226\n\t"
    "v_mfma_f32_16x16x32_bf16 v[236:239], a[212:215], v[84:87], v[236:239]\n\t"
    "ds_read_b128 a[248:251], v30 offset:34304\n\t"
    "v_add_f32_e32 v32, v226, v32\n\t"
    "v_mfma_f32_16x16x32_bf16 v[236:239], a[216:219], v[88:91], v[236:239]\n\t"
    "v_exp_f32_e32 v227, v227\n\t"
    "v_mfma_f32_16x16x32_bf16 v[236:239], a[220:223], v[92:95], v[236:239]\n\t"
    "ds_read_b128 a[252:255], v30 offset:34560\n\t"
    "v_add_f32_e32 v32, v227, v32\n\t"
    "v_exp_f32_e32 v228, v228\n\t"
    "v_exp_f32_e32 v229, v229\n\t"
    "v_exp_f32_e32 v230, v230\n\t"
    "v_exp_f32_e32 v231, v231\n\t"
    "v_add_f32_e32 v32, v228, v32\n\t"
    "v_cvt_pk_bf16_f32 v224, v224, v225\n\t"
    "v_add_f32_e32 v32, v229, v32\n\t"
    "v_cvt_pk_bf16_f32 v225, v226, v227\n\t"
    "v_add_f32_e32 v32, v230, v32\n\t"
    "v_cvt_pk_bf16_f32 v226, v228, v229\n\t"
    "v_add_f32_e32 v32, v231, v32\n\t"
    "v_cvt_pk_bf16_f32 v227, v230, v231\n\t"
    "v_mfma_f32_16x16x32_bf16 v[240:243], a[192:195], v[96:99], v[48:51]\n\t"
    "s_mov_b32 s85, 0x28000\n\t"
    "v_exp_f32_e32 v232, v232\n\t"
    "v_mfma_f32_16x16x32_bf16 v[240:243], a[196:199], v[100:103], v[240:243]\n\t"
    "ds_read_b64_tr_b16 a[160:161], v22 offset:32768\n\t"
    "v_mov_b32_e32 v33, v232\n\t"
    "v_mfma_f32_16x16x32_bf16 v[240:243], a[200:203], v[104:107], v[240:243]\n\t"
    "v_exp_f32_e32 v233, v233\n\t"
    "v_mfma_f32_16x16x32_bf16 v[240:243], a[204:207], v[108:111], v[240:243]\n\t"
    "ds_read_b64_tr_b16 a[162:163], v22 offset:33792\n\t"
    "v_add_f32_e32 v33, v233, v33\n\t"
    "v_mfma_f32_16x16x32_bf16 v[244:247], a[208:211], v[96:99], v[48:51]\n\t"
    "v_exp_f32_e32 v234, v234\n\t"
    "v_mfma_f32_16x16x32_bf16 v[244:247], a[212:215], v[100:103], v[244:247]\n\t"
    "ds_read_b64_tr_b16 a[164:165], v23 offset:32768\n\t"
    "v_add_f32_e32 v33, v234, v33\n\t"
    "v_mfma_f32_16x16x32_bf16 v[244:247], a[216:219], v[104:107], v[244:247]\n\t"
    "v_exp_f32_e32 v235, v235\n\t"
    "v_mfma_f32_16x16x32_bf16 v[244:247], a[220:223], v[108:111], v[244:247]\n\t"
    "ds_read_b64_tr_b16 a[166:167], v23 offset:33792\n\t"
    "v_add_f32_e32 v33, v235, v33\n\t"
    "v_mfma_f32_16x16x32_bf16 a[0:3], a[128:131], v[224:227], 0\n\t"
    "v_exp_f32_e32 v236, v236\n\t"
    "v_mfma_f32_16x16x32_bf16 a[4:7], a[132:135], v[224:227], 0\n\t"
    "s_add_u32 m0, s65, 0x26000\n\t"
    "v_add_f32_e32 v33, v236, v33\n\t"
    "v_cvt_pk_bf16_f32 v232, v232, v233\n\t"
    "v_mfma_f32_16x16x32_bf16 a[8:11], a[136:139], v[224:227], 0\n\t"
    "buffer_load_dwordx4 v16, s[48:51], s85 offen nt lds\n\t"
    "v_exp_f32_e32 v237, v237\n\t"
    "v_mfma_f32_16x16x32_bf16 a[12:15], a[140:143], v[224:227], 0\n\t"
    "v_add_f32_e32 v33, v237, v33\n\t"
    "v_cvt_pk_bf16_f32 v233, v234, v235\n\t"
    "v_mfma_f32_16x16x32_bf16 a[16:19], a[144:147], v[224:227], 0\n\t"
    "v_exp_f32_e32 v238, v238\n\t"
    "v_mfma_f32_16x16x32_bf16 a[20:23], a[148:151], v[224:227], 0\n\t"
    "v_add_f32_e32 v33, v238, v33\n\t"
    "v_cvt_pk_bf16_f32 v234, v236, v237\n\t"
    "v_mfma_f32_16x16x32_bf16 a[24:27], a[152:155], v[224:227], 0\n\t"
    "buffer_load_dwordx4 v16, s[48:51], s85 offen offset:1024 nt lds\n\t"
    "v_exp_f32_e32 v239, v239\n\t"
    "v_mfma_f32_16x16x32_bf16 a[28:31], a[156:159], v[224:227], 0\n\t"
    "v_add_f32_e32 v33, v239, v33\n\t"
    "v_cvt_pk_bf16_f32 v235, v238, v239\n\t"
    "v_and_b32_e32 v0, 0x70, v31\n\t"
    "v_and_b32_e32 v25, 0x380, v31\n\t"
    "v_lshlrev_b32_e32 v0, 3, v0\n\t"
    "v_lshl_or_b32 v25, v25, 4, v0\n\t"
    "v_cmp_le_u32_e32 vcc, 256, v31\n\t"
    "v_mov_b32_e32 v0, 0x40000000\n\t"
    "v_xor_b32_e32 v26, 48, v29\n\t"
    "v_cndmask_b32_e32 v25, v25, v0, vcc\n\t"
    "v_mfma_f32_16x16x32_bf16 v[224:227], a[192:195], v[112:115], v[52:55]\n\t"
    "s_mov_b32 s84, 0xa000\n\t"
    "v_exp_f32_e32 v240, v240\n\t"
    "v_mfma_f32_16x16x32_bf16 v[224:227], a[196:199], v[116:119], v[224:227]\n\t"
    "ds_read_b64_tr_b16 a[168:169], v22 offset:33024\n\t"
    "v_mov_b32_e32 v34, v240\n\t"
    "v_mfma_f32_16x16x32_bf16 v[224:227], a[200:203], v[120:123], v[224:227]\n\t"
    "v_exp_f32_e32 v241, v241\n\t"
    "v_mfma_f32_16x16x32_bf16 v[224:227], a[204:207], v[124:127], v[224:227]\n\t"
    "ds_read_b64_tr_b16 a[170:171], v22 offset:34048\n\t"
    "v_add_f32_e32 v34, v241, v34\n\t"
    "v_mfma_f32_16x16x32_bf16 v[228:231], a[208:211], v[112:115], v[52:55]\n\t"
    "v_exp_f32_e32 v242, v242\n\t"
    "v_mfma_f32_16x16x32_bf16 v[228:231], a[212:215], v[116:119], v[228:231]\n\t"
    "ds_read_b64_tr_b16 a[172:173], v23 offset:33024\n\t"
    "v_add_f32_e32 v34, v242, v34\n\t"
    "v_mfma_f32_16x16x32_bf16 v[228:231], a[216:219], v[120:123], v[228:231]\n\t"
    "v_exp_f32_e32 v243, v243\n\t"
    "v_mfma_f32_16x16x32_bf16 v[228:231], a[220:223], v[124:127], v[228:231]\n\t"
    "ds_read_b64_tr_b16 a[174:175], v23 offset:34048\n\t"
    "v_add_f32_e32 v34, v243, v34\n\t"
    "v_mfma_f32_16x16x32_bf16 a[32:35], a[128:131], v[232:235], 0\n\t"
    "v_exp_f32_e32 v244, v244\n\t"
    "v_mfma_f32_16x16x32_bf16 a[36:39], a[132:135], v[232:235], 0\n\t"
    "v_add_f32_e32 v34, v244, v34\n\t"
    "v_cvt_pk_bf16_f32 v240, v240, v241\n\t"
    "v_mfma_f32_16x16x32_bf16 a[40:43], a[136:139], v[232:235], 0\n\t"
    "v_exp_f32_e32 v245, v245\n\t"
    "v_mfma_f32_16x16x32_bf16 a[44:47], a[140:143], v[232:235], 0\n\t"
    "s_add_u32 m0, s65, 0\n\t"
    "v_add_f32_e32 v34, v245, v34\n\t"
    "v_cvt_pk_bf16_f32 v241, v242, v243\n\t"
    "v_mfma_f32_16x16x32_bf16 a[48:51], a[144:147], v[232:235], 0\n\t"
    "buffer_load_dwordx4 v16, s[44:47], s84 offen nt lds\n\t"
    "v_exp_f32_e32 v246, v246\n\t"
    "v_mfma_f32_16x16x32_bf16 a[52:55], a[148:151], v[232:235], 0\n\t"
    "v_add_f32_e32 v34, v246, v34\n\t"
    "v_cvt_pk_bf16_f32 v242, v244, v245\n\t"
    "v_mfma_f32_16x16x32_bf16 a[56:59], a[152:155], v[232:235], 0\n\t"
    "v_exp_f32_e32 v247, v247\n\t"
    "v_mfma_f32_16x16x32_bf16 a[60:63], a[156:159], v[232:235], 0\n\t"
    "v_add_f32_e32 v34, v247, v34\n\t"
    "v_cvt_pk_bf16_f32 v243, v246, v247\n\t"
    "v_add_u32_e32 v26, 0x2000, v26\n\t"
    "v_xor_b32_e32 v27, 32, v29\n\t"
    "v_add_u32_e32 v27, 0x4000, v27\n\t"
    "v_xor_b32_e32 v28, 16, v29\n\t"
    "v_add_u32_e32 v28, 0x6000, v28\n\t"
    "v_mfma_f32_16x16x32_bf16 v[232:235], a[192:195], v[128:131], v[56:59]\n\t"
    "s_mov_b32 s85, 0x2a000\n\t"
    "v_exp_f32_e32 v224, v224\n\t"
    "v_mfma_f32_16x16x32_bf16 v[232:235], a[196:199], v[132:135], v[232:235]\n\t"
    "ds_read_b64_tr_b16 a[176:177], v22 offset:33280\n\t"
    "v_mov_b32_e32 v35, v224\n\t"
    "v_mfma_f32_16x16x32_bf16 v[232:235], a[200:203], v[136:139], v[232:235]\n\t"
    "v_exp_f32_e32 v225, v225\n\t"
    "v_mfma_f32_16x16x32_bf16 v[232:235], a[204:207], v[140:143], v[232:235]\n\t"
    "ds_read_b64_tr_b16 a[178:179], v22 offset:34304\n\t"
    "v_add_f32_e32 v35, v225, v35\n\t"
    "v_mfma_f32_16x16x32_bf16 v[236:239], a[208:211], v[128:131], v[56:59]\n\t"
    "v_exp_f32_e32 v226, v226\n\t"
    "v_mfma_f32_16x16x32_bf16 v[236:239], a[212:215], v[132:135], v[236:239]\n\t"
    "ds_read_b64_tr_b16 a[180:181], v23 offset:33280\n\t"
    "v_add_f32_e32 v35, v226, v35\n\t"
    "v_mfma_f32_16x16x32_bf16 v[236:239], a[216:219], v[136:139], v[236:239]\n\t"
    "v_exp_f32_e32 v227, v227\n\t"
    "v_mfma_f32_16x16x32_bf16 v[236:239], a[220:223], v[140:143], v[236:239]\n\t"
    "ds_read_b64_tr_b16 a[182:183], v23 offset:34304\n\t"
    "v_add_f32_e32 v35, v227, v35\n\t"
    "v_mfma_f32_16x16x32_bf16 a[64:67], a[128:131], v[240:243], 0\n\t"
    "buffer_load_dwordx4 v16, s[44:47], s84 offen offset:1024 nt lds\n\t"
    "v_exp_f32_e32 v228, v228\n\t"
    "v_mfma_f32_16x16x32_bf16 a[68:71], a[132:135], v[240:243], 0\n\t"
    "v_add_f32_e32 v35, v228, v35\n\t"
    "v_cvt_pk_bf16_f32 v224, v224, v225\n\t"
    "v_mfma_f32_16x16x32_bf16 a[72:75], a[136:139], v[240:243], 0\n\t"
    "v_exp_f32_e32 v229, v229\n\t"
    "v_mfma_f32_16x16x32_bf16 a[76:79], a[140:143], v[240:243], 0\n\t"
    "v_add_f32_e32 v35, v229, v35\n\t"
    "v_cvt_pk_bf16_f32 v225, v226, v227\n\t"
    "v_mfma_f32_16x16x32_bf16 a[80:83], a[144:147], v[240:243], 0\n\t"
    "v_exp_f32_e32 v230, v230\n\t"
    "v_mfma_f32_16x16x32_bf16 a[84:87], a[148:151], v[240:243], 0\n\t"
    "s_add_u32 m0, s65, 0x2000\n\t"
    "v_add_f32_e32 v35, v230, v35\n\t"
    "v_cvt_pk_bf16_f32 v226, v228, v229\n\t"
    "v_mfma_f32_16x16x32_bf16 a[88:91], a[152:155], v[240:243], 0\n\t"
    "buffer_load_dwordx4 v16, s[44:47], s85 offen nt lds\n\t"
    "v_exp_f32_e32 v231, v231\n\t"
    "v_mfma_f32_16x16x32_bf16 a[92:95], a[156:159], v[240:243], 0\n\t"
    "v_add_f32_e32 v35, v231, v35\n\t"
    "v_cvt_pk_bf16_f32 v227, v230, v231\n\t"
    "v_mfma_f32_16x16x32_bf16 v[240:243], a[192:195], v[144:147], v[60:63]\n\t"
    "v_exp_f32_e32 v232, v232\n\t"
    "v_mfma_f32_16x16x32_bf16 v[240:243], a[196:199], v[148:151], v[240:243]\n\t"
    "ds_read_b64_tr_b16 a[184:185], v22 offset:33536\n\t"
    "v_mov_b32_e32 v36, v232\n\t"
    "v_mfma_f32_16x16x32_bf16 v[240:243], a[200:203], v[152:155], v[240:243]\n\t"
    "v_exp_f32_e32 v233, v233\n\t"
    "v_mfma_f32_16x16x32_bf16 v[240:243], a[204:207], v[156:159], v[240:243]\n\t"
    "ds_read_b64_tr_b16 a[186:187], v22 offset:34560\n\t"
    "v_add_f32_e32 v36, v233, v36\n\t"
    "v_mfma_f32_16x16x32_bf16 v[244:247], a[208:211], v[144:147], v[60:63]\n\t"
    "v_exp_f32_e32 v234, v234\n\t"
    "v_mfma_f32_16x16x32_bf16 v[244:247], a[212:215], v[148:151], v[244:247]\n\t"
    "ds_read_b64_tr_b16 a[188:189], v23 offset:33536\n\t"
    "v_add_f32_e32 v36, v234, v36\n\t"
    "v_mfma_f32_16x16x32_bf16 v[244:247], a[216:219], v[152:155], v[244:247]\n\t"
    "v_exp_f32_e32 v235, v235\n\t"
    "v_mfma_f32_16x16x32_bf16 v[244:247], a[220:223], v[156:159], v[244:247]\n\t"
    "ds_read_b64_tr_b16 a[190:191], v23 offset:34560\n\t"
    "v_add_f32_e32 v36, v235, v36\n\t"
    "v_mfma_f32_16x16x32_bf16 a[96:99], a[128:131], v[224:227], 0\n\t"
    "v_exp_f32_e32 v236, v236\n\t"
    "v_mfma_f32_16x16x32_bf16 a[100:103], a[132:135], v[224:227], 0\n\t"
    "s_memrealtime s[30:31]\n\t"
    "v_add_f32_e32 v36, v236, v36\n\t"
    "v_cvt_pk_bf16_f32 v232, v232, v233\n\t"
    "v_mfma_f32_16x16x32_bf16 a[104:107], a[136:139], v[224:227], 0\n\t"
    "buffer_load_dwordx4 v16, s[44:47], s85 offen offset:1024 nt lds\n\t"
    "v_exp_f32_e32 v237, v237\n\t"
    "v_mfma_f32_16x16x32_bf16 a[108:111], a[140:143], v[224:227], 0\n\t"
    "v_add_f32_e32 v36, v237, v36\n\t"
    "v_cvt_pk_bf16_f32 v233, v234, v235\n\t"
    "v_mfma_f32_16x16x32_bf16 a[112:115], a[144:147], v[224:227], 0\n\t"
    "v_exp_f32_e32 v238, v238\n\t"
    "v_mfma_f32_16x16x32_bf16 a[116:119], a[148:151], v[224:227], 0\n\t"
    "v_add_f32_e32 v36, v238, v36\n\t"
    "v_cvt_pk_bf16_f32 v234, v236, v237\n\t"
    "v_mfma_f32_16x16x32_bf16 a[120:123], a[152:155], v[224:227], 0\n\t"
    "s_waitcnt vmcnt(63) lgkmcnt(0)\n\t"
    "v_exp_f32_e32 v239, v239\n\t"
    "v_mfma_f32_16x16x32_bf16 a[124:127], a[156:159], v[224:227], 0\n\t"
    "s_barrier\n\t"
    "v_add_f32_e32 v36, v239, v36\n\t"
    "v_cvt_pk_bf16_f32 v235, v238, v239\n\t"
    "s_sub_i32 s31, s29, s30\n\t"
    "s_cmp_gt_i32 s31, 0\n\t"
    "s_cselect_b32 s29, s29, s30\n\t"
    "s_cbranch_scc0 .Lfa_pa5e\n\t"
    "s_min_i32 s31, s31, 0xc8\n\t"
    ".Lfa_pa5:\n\t"
    "s_sub_u32 s31, s31, 2\n\t"
    "s_cmp_gt_i32 s31, 0\n\t"
    "s_cbranch_scc1 .Lfa_pa5\n\t"
    ".Lfa_pa5e:\n\t"
    "s_add_u32 s29, s29, 0x71\n\t"
    "v_mfma_f32_16x16x32_bf16 v[224:227], a[224:227], v[64:67], v[40:43]\n\t"
    "s_mov_b32 s84, 0xa000\n\t"
    "v_exp_f32_e32 v240, v240\n\t"
    "v_mfma_f32_16x16x32_bf16 v[224:227], a[228:231], v[68:71], v[224:227]\n\t"
    "ds_read_b128 a[192:195], v17 offset:0\n\t"
    "v_mov_b32_e32 v37, v240\n\t"
    "v_mfma_f32_16x16x32_bf16 v[224:227], a[232:235], v[72:75], v[224:227]\n\t"
    "v_exp_f32_e32 v241, v241\n\t"
    "v_mfma_f32_16x16x32_bf16 v[224:227], a[236:239], v[76:79], v[224:227]\n\t"
    "ds_read_b128 a[196:199], v17 offset:256\n\t"
    "v_add_f32_e32 v37, v241, v37\n\t"
    "v_mfma_f32_16x16x32_bf16 v[228:231], a[240:243], v[64:67], v[40:43]\n\t"
    "v_exp_f32_e32 v242, v242\n\t"
    "v_mfma_f32_16x16x32_bf16 v[228:231], a[244:247], v[68:71], v[228:231]\n\t"
    "ds_read_b128 a[200:203], v17 offset:512\n\t"
    "v_add_f32_e32 v37, v242, v37\n\t"
    "v_mfma_f32_16x16x32_bf16 v[228:231], a[248:251], v[72:75], v[228:231]\n\t"
    "v_exp_f32_e32 v243, v243\n\t"
    "v_mfma_f32_16x16x32_bf16 v[228:231], a[252:255], v[76:79], v[228:231]\n\t"
    "ds_read_b128 a[204:207], v17 offset:768\n\t"
    "v_add_f32_e32 v37, v243, v37\n\t"
    "v_mfma_f32_16x16x32_bf16 v[160:163], a[128:131], v[232:235], 0\n\t"
    "v_exp_f32_e32 v244, v244\n\t"
    "v_mfma_f32_16x16x32_bf16 v[164:167], a[132:135], v[232:235], 0\n\t"
    "s_add_u32 m0, s65, 0x4000\n\t"
    "v_add_f32_e32 v37, v244, v37\n\t"
    "v_cvt_pk_bf16_f32 v240, v240, v241\n\t"
    "v_mfma_f32_16x16x32_bf16 v[168:171], a[136:139], v[232:235], 0\n\t"
    "buffer_load_dwordx4 v16, s[48:51], s84 offen nt lds\n\t"
    "v_exp_f32_e32 v245, v245\n\t"
    "v_mfma_f32_16x16x32_bf16 v[172:175], a[140:143], v[232:235], 0\n\t"
    "v_add_f32_e32 v37, v245, v37\n\t"
    "v_cvt_pk_bf16_f32 v241, v242, v243\n\t"
    "v_mfma_f32_16x16x32_bf16 v[176:179], a[144:147], v[232:235], 0\n\t"
    "v_exp_f32_e32 v246, v246\n\t"
    "v_mfma_f32_16x16x32_bf16 v[180:183], a[148:151], v[232:235], 0\n\t"
    "v_add_f32_e32 v37, v246, v37\n\t"
    "v_cvt_pk_bf16_f32 v242, v244, v245\n\t"
    "v_mfma_f32_16x16x32_bf16 v[184:187], a[152:155], v[232:235], 0\n\t"
    "buffer_load_dwordx4 v16, s[48:51], s84 offen offset:1024 nt lds\n\t"
    "v_exp_f32_e32 v247, v247\n\t"
    "v_mfma_f32_16x16x32_bf16 v[188:191], a[156:159], v[232:235], 0\n\t"
    "v_add_f32_e32 v37, v247, v37\n\t"
    "v_cvt_pk_bf16_f32 v243, v246, v247\n\t"
    "v_mfma_f32_16x16x32_bf16 v[232:235], a[224:227], v[80:83], v[44:47]\n\t"
    "s_mov_b32 s85, 0x2a000\n\t"
    "v_exp_f32_e32 v224, v224\n\t"
    "v_mfma_f32_16x16x32_bf16 v[232:235], a[228:231], v[84:87], v[232:235]\n\t"
    "ds_read_b128 a[208:211], v17 offset:1024\n\t"
    "v_add_f32_e32 v32, v224, v32\n\t"
    "v_mfma_f32_16x16x32_bf16 v[232:235], a[232:235], v[88:91], v[232:235]\n\t"
    "v_exp_f32_e32 v225, v225\n\t"
    "v_mfma_f32_16x16x32_bf16 v[232:235], a[236:239], v[92:95], v[232:235]\n\t"
    "ds_read_b128 a[212:215], v17 offset:1280\n\t"
    "v_add_f32_e32 v32, v225, v32\n\t"
    "v_mfma_f32_16x16x32_bf16 v[236:239], a[240:243], v[80:83], v[44:47]\n\t"
    "v_exp_f32_e32 v226, v226\n\t"
    "v_mfma_f32_16x16x32_bf16 v[236:239], a[244:247], v[84:87], v[236:239]\n\t"
    "ds_read_b128 a[216:219], v17 offset:1536\n\t"
    "v_add_f32_e32 v32, v226, v32\n\t"
    "v_mfma_f32_16x16x32_bf16 v[236:239], a[248:251], v[88:91], v[236:239]\n\t"
    "v_exp_f32_e32 v227, v227\n\t"
    "v_mfma_f32_16x16x32_bf16 v[236:239], a[252:255], v[92:95], v[236:239]\n\t"
    "ds_read_b128 a[220:223], v17 offset:1792\n\t"
    "v_add_f32_e32 v32, v227, v32\n\t"
    "v_mfma_f32_16x16x32_bf16 v[192:195], a[128:131], v[240:243], 0\n\t"
    "v_exp_f32_e32 v228, v228\n\t"
    "v_mfma_f32_16x16x32_bf16 v[196:199], a[132:135], v[240:243], 0\n\t"
    "v_add_f32_e32 v32, v228, v32\n\t"
    "v_cvt_pk_bf16_f32 v224, v224, v225\n\t"
    "v_mfma_f32_16x16x32_bf16 v[200:203], a[136:139], v[240:243], 0\n\t"
    "v_exp_f32_e32 v229, v229\n\t"
    "v_mfma_f32_16x16x32_bf16 v[204:207], a[140:143], v[240:243], 0\n\t"
    "s_add_u32 m0, s65, 0x6000\n\t"
    "v_add_f32_e32 v32, v229, v32\n\t"
    "v_cvt_pk_bf16_f32 v225, v226, v227\n\t"
    "v_mfma_f32_16x16x32_bf16 v[208:211], a[144:147], v[240:243], 0\n\t"
    "buffer_load_dwordx4 v16, s[48:51], s85 offen nt lds\n\t"
    "v_exp_f32_e32 v230, v230\n\t"
    "v_mfma_f32_16x16x32_bf16 v[212:215], a[148:151], v[240:243], 0\n\t"
    "v_add_f32_e32 v32, v230, v32\n\t"
    "v_cvt_pk_bf16_f32 v226, v228, v229\n\t"
    "v_mfma_f32_16x16x32_bf16 v[216:219], a[152:155], v[240:243], 0\n\t"
    "v_exp_f32_e32 v231, v231\n\t"
    "v_mfma_f32_16x16x32_bf16 v[220:223], a[156:159], v[240:243], 0\n\t"
    "v_add_f32_e32 v32, v231, v32\n\t"
    "v_cvt_pk_bf16_f32 v227, v230, v231\n\t"
    "v_mfma_f32_16x16x32_bf16 v[240:243], a[224:227], v[96:99], v[48:51]\n\t"
    "s_add_u32 s88, s88, 0x80000\n\t"
    "v_exp_f32_e32 v232, v232\n\t"
    "v_mfma_f32_16x16x32_bf16 v[240:243], a[228:231], v[100:103], v[240:243]\n\t"
    "ds_read_b64_tr_b16 a[128:129], v19 offset:0\n\t"
    "v_add_f32_e32 v33, v232, v33\n\t"
    "v_mfma_f32_16x16x32_bf16 v[240:243], a[232:235], v[104:107], v[240:243]\n\t"
    "s_and_b32 s88, s88, 0x3fffff\n\t"
    "v_exp_f32_e32 v233, v233\n\t"
    "v_mfma_f32_16x16x32_bf16 v[240:243], a[236:239], v[108:111], v[240:243]\n\t"
    "ds_read_b64_tr_b16 a[130:131], v19 offset:1024\n\t"
    "v_add_f32_e32 v33, v233, v33\n\t"
    "v_mfma_f32_16x16x32_bf16 v[244:247], a[240:243], v[96:99], v[48:51]\n\t"
    "v_exp_f32_e32 v234, v234\n\t"
    "v_mfma_f32_16x16x32_bf16 v[244:247], a[244:247], v[100:103], v[244:247]\n\t"
    "ds_read_b64_tr_b16 a[132:133], v21 offset:0\n\t"
    "v_add_f32_e32 v33, v234, v33\n\t"
    "v_mfma_f32_16x16x32_bf16 v[244:247], a[248:251], v[104:107], v[244:247]\n\t"
    "v_exp_f32_e32 v235, v235\n\t"
    "v_mfma_f32_16x16x32_bf16 v[244:247], a[252:255], v[108:111], v[244:247]\n\t"
    "ds_read_b64_tr_b16 a[134:135], v21 offset:1024\n\t"
    "v_add_f32_e32 v33, v235, v33\n\t"
    "v_mfma_f32_16x16x32_bf16 a[0:3], a[160:163], v[224:227], a[0:3]\n\t"
    "buffer_load_dwordx4 v16, s[48:51], s85 offen offset:1024 nt lds\n\t"
    "v_exp_f32_e32 v236, v236\n\t"
    "v_mfma_f32_16x16x32_bf16 a[4:7], a[164:167], v[224:227], a[4:7]\n\t"
    "v_add_f32_e32 v33, v236, v33\n\t"
    "v_cvt_pk_bf16_f32 v232, v232, v233\n\t"
    "v_mfma_f32_16x16x32_bf16 a[8:11], a[168:171], v[224:227], a[8:11]\n\t"
    "v_exp_f32_e32 v237, v237\n\t"
    "v_mfma_f32_16x16x32_bf16 a[12:15], a[172:175], v[224:227], a[12:15]\n\t"
    "v_add_f32_e32 v33, v237, v33\n\t"
    "v_cvt_pk_bf16_f32 v233, v234, v235\n\t"
    "v_mfma_f32_16x16x32_bf16 a[16:19], a[176:179], v[224:227], a[16:19]\n\t"
    "v_exp_f32_e32 v238, v238\n\t"
    "v_mfma_f32_16x16x32_bf16 a[20:23], a[180:183], v[224:227], a[20:23]\n\t"
    "v_add_f32_e32 v33, v238, v33\n\t"
    "v_cvt_pk_bf16_f32 v234, v236, v237\n\t"
    "v_mfma_f32_16x16x32_bf16 a[24:27], a[184:187], v[224:227], a[24:27]\n\t"
    "buffer_load_dword v15, v25, s[56:59], s88 offen\n\t"
    "v_exp_f32_e32 v239, v239\n\t"
    "v_mfma_f32_16x16x32_bf16 a[28:31], a[188:191], v[224:227], a[28:31]\n\t"
    "v_add_f32_e32 v33, v239, v33\n\t"
    "v_cvt_pk_bf16_f32 v235, v238, v239\n\t"
    "v_mfma_f32_16x16x32_bf16 v[224:227], a[224:227], v[112:115], v[52:55]\n\t"
    "s_mov_b32 s84, 0xc000\n\t"
    "v_exp_f32_e32 v240, v240\n\t"
    "v_mfma_f32_16x16x32_bf16 v[224:227], a[228:231], v[116:119], v[224:227]\n\t"
    "ds_read_b64_tr_b16 a[136:137], v19 offset:256\n\t"
    "v_add_f32_e32 v34, v240, v34\n\t"
    "v_mfma_f32_16x16x32_bf16 v[224:227], a[232:235], v[120:123], v[224:227]\n\t"
    "v_exp_f32_e32 v241, v241\n\t"
    "v_mfma_f32_16x16x32_bf16 v[224:227], a[236:239], v[124:127], v[224:227]\n\t"
    "ds_read_b64_tr_b16 a[138:139], v19 offset:1280\n\t"
    "v_add_f32_e32 v34, v241, v34\n\t"
    "v_mfma_f32_16x16x32_bf16 v[228:231], a[240:243], v[112:115], v[52:55]\n\t"
    "v_exp_f32_e32 v242, v242\n\t"
    "v_mfma_f32_16x16x32_bf16 v[228:231], a[244:247], v[116:119], v[228:231]\n\t"
    "ds_read_b64_tr_b16 a[140:141], v21 offset:256\n\t"
    "v_add_f32_e32 v34, v242, v34\n\t"
    "v_mfma_f32_16x16x32_bf16 v[228:231], a[248:251], v[120:123], v[228:231]\n\t"
    "v_exp_f32_e32 v243, v243\n\t"
    "v_mfma_f32_16x16x32_bf16 v[228:231], a[252:255], v[124:127], v[228:231]\n\t"
    "ds_read_b64_tr_b16 a[142:143], v21 offset:1280\n\t"
    "v_add_f32_e32 v34, v243, v34\n\t"
    "v_mfma_f32_16x16x32_bf16 a[32:35], a[160:163], v[232:235], a[32:35]\n\t"
    "v_exp_f32_e32 v244, v244\n\t"
    "v_mfma_f32_16x16x32_bf16 a[36:39], a[164:167], v[232:235], a[36:39]\n\t"
    "v_add_f32_e32 v34, v244, v34\n\t"
    "v_cvt_pk_bf16_f32 v240, v240, v241\n\t"
    "v_mfma_f32_16x16x32_bf16 a[40:43], a[168:171], v[232:235], a[40:43]\n\t"
    "v_exp_f32_e32 v245, v245\n\t"
    "v_mfma_f32_16x16x32_bf16 a[44:47], a[172:175], v[232:235], a[44:47]\n\t"
    "s_add_u32 m0, s65, 0x8000\n\t"
    "v_add_f32_e32 v34, v245, v34\n\t"
    "v_cvt_pk_bf16_f32 v241, v242, v243\n\t"
    "v_mfma_f32_16x16x32_bf16 a[48:51], a[176:179], v[232:235], a[48:51]\n\t"
    "buffer_load_dwordx4 v16, s[44:47], s84 offen nt lds\n\t"
    "v_exp_f32_e32 v246, v246\n\t"
    "v_mfma_f32_16x16x32_bf16 a[52:55], a[180:183], v[232:235], a[52:55]\n\t"
    "v_add_f32_e32 v34, v246, v34\n\t"
    "v_cvt_pk_bf16_f32 v242, v244, v245\n\t"
    "v_mfma_f32_16x16x32_bf16 a[56:59], a[184:187], v[232:235], a[56:59]\n\t"
    "v_exp_f32_e32 v247, v247\n\t"
    "v_mfma_f32_16x16x32_bf16 a[60:63], a[188:191], v[232:235], a[60:63]\n\t"
    "v_add_f32_e32 v34, v247, v34\n\t"
    "v_cvt_pk_bf16_f32 v243, v246, v247\n\t"
    "v_mfma_f32_16x16x32_bf16 v[232:235], a[224:227], v[128:131], v[56:59]\n\t"
    "s_mov_b32 s85, 0x2c000\n\t"
    "v_exp_f32_e32 v224, v224\n\t"
    "v_mfma_f32_16x16x32_bf16 v[232:235], a[228:231], v[132:135], v[232:235]\n\t"
    "ds_read_b64_tr_b16 a[144:145], v19 offset:512\n\t"
    "v_add_f32_e32 v35, v224, v35\n\t"
    "v_mfma_f32_16x16x32_bf16 v[232:235], a[232:235], v[136:139], v[232:235]\n\t"
    "v_exp_f32_e32 v225, v225\n\t"
    "v_mfma_f32_16x16x32_bf16 v[232:235], a[236:239], v[140:143], v[232:235]\n\t"
    "ds_read_b64_tr_b16 a[146:147], v19 offset:1536\n\t"
    "v_add_f32_e32 v35, v225, v35\n\t"
    "v_mfma_f32_16x16x32_bf16 v[236:239], a[240:243], v[128:131], v[56:59]\n\t"
    "v_exp_f32_e32 v226, v226\n\t"
    "v_mfma_f32_16x16x32_bf16 v[236:239], a[244:247], v[132:135], v[236:239]\n\t"
    "ds_read_b64_tr_b16 a[148:149], v21 offset:512\n\t"
    "v_add_f32_e32 v35, v226, v35\n\t"
    "v_mfma_f32_16x16x32_bf16 v[236:239], a[248:251], v[136:139], v[236:239]\n\t"
    "v_exp_f32_e32 v227, v227\n\t"
    "v_mfma_f32_16x16x32_bf16 v[236:239], a[252:255], v[140:143], v[236:239]\n\t"
    "ds_read_b64_tr_b16 a[150:151], v21 offset:1536\n\t"
    "v_add_f32_e32 v35, v227, v35\n\t"
    "v_mfma_f32_16x16x32_bf16 a[64:67], a[160:163], v[240:243], a[64:67]\n\t"
    "buffer_load_dwordx4 v16, s[44:47], s84 offen offset:1024 nt lds\n\t"
    "v_exp_f32_e32 v228, v228\n\t"
    "v_mfma_f32_16x16x32_bf16 a[68:71], a[164:167], v[240:243], a[68:71]\n\t"
    "v_add_f32_e32 v35, v228, v35\n\t"
    "v_cvt_pk_bf16_f32 v224, v224, v225\n\t"
    "v_mfma_f32_16x16x32_bf16 a[72:75], a[168:171], v[240:243], a[72:75]\n\t"
    "v_exp_f32_e32 v229, v229\n\t"
    "v_mfma_f32_16x16x32_bf16 a[76:79], a[172:175], v[240:243], a[76:79]\n\t"
    "v_add_f32_e32 v35, v229, v35\n\t"
    "v_cvt_pk_bf16_f32 v225, v226, v227\n\t"
    "v_mfma_f32_16x16x32_bf16 a[80:83], a[176:179], v[240:243], a[80:83]\n\t"
    "v_exp_f32_e32 v230, v230\n\t"
    "v_mfma_f32_16x16x32_bf16 a[84:87], a[180:183], v[240:243], a[84:87]\n\t"
    "s_add_u32 m0, s65, 0xa000\n\t"
    "v_add_f32_e32 v35, v230, v35\n\t"
    "v_cvt_pk_bf16_f32 v226, v228, v229\n\t"
    "v_mfma_f32_16x16x32_bf16 a[88:91], a[184:187], v[240:243], a[88:91]\n\t"
    "buffer_load_dwordx4 v16, s[44:47], s85 offen nt lds\n\t"
    "v_exp_f32_e32 v231, v231\n\t"
    "v_mfma_f32_16x16x32_bf16 a[92:95], a[188:191], v[240:243], a[92:95]\n\t"
    "v_add_f32_e32 v35, v231, v35\n\t"
    "v_cvt_pk_bf16_f32 v227, v230, v231\n\t"
    "v_mfma_f32_16x16x32_bf16 v[240:243], a[224:227], v[144:147], v[60:63]\n\t"
    "v_exp_f32_e32 v232, v232\n\t"
    "v_mfma_f32_16x16x32_bf16 v[240:243], a[228:231], v[148:151], v[240:243]\n\t"
    "ds_read_b64_tr_b16 a[152:153], v19 offset:768\n\t"
    "v_add_f32_e32 v36, v232, v36\n\t"
    "v_mfma_f32_16x16x32_bf16 v[240:243], a[232:235], v[152:155], v[240:243]\n\t"
    "v_exp_f32_e32 v233, v233\n\t"
    "v_mfma_f32_16x16x32_bf16 v[240:243], a[236:239], v[156:159], v[240:243]\n\t"
    "ds_read_b64_tr_b16 a[154:155], v19 offset:1792\n\t"
    "v_add_f32_e32 v36, v233, v36\n\t"
    "v_mfma_f32_16x16x32_bf16 v[244:247], a[240:243], v[144:147], v[60:63]\n\t"
    "v_exp_f32_e32 v234, v234\n\t"
    "v_mfma_f32_16x16x32_bf16 v[244:247], a[244:247], v[148:151], v[244:247]\n\t"
    "ds_read_b64_tr_b16 a[156:157], v21 offset:768\n\t"
    "v_add_f32_e32 v36, v234, v36\n\t"
    "v_mfma_f32_16x16x32_bf16 v[244:247], a[248:251], v[152:155], v[244:247]\n\t"
    "v_exp_f32_e32 v235, v235\n\t"
    "v_mfma_f32_16x16x32_bf16 v[244:247], a[252:255], v[156:159], v[244:247]\n\t"
    "ds_read_b64_tr_b16 a[158:159], v21 offset:1792\n\t"
    "v_add_f32_e32 v36, v235, v36\n\t"
    "v_mfma_f32_16x16x32_bf16 a[96:99], a[160:163], v[224:227], a[96:99]\n\t"
    "v_exp_f32_e32 v236, v236\n\t"
    "v_mfma_f32_16x16x32_bf16 a[100:103], a[164:167], v[224:227], a[100:103]\n\t"
    "s_memrealtime s[30:31]\n\t"
    "v_add_f32_e32 v36, v236, v36\n\t"
    "v_cvt_pk_bf16_f32 v232, v232, v233\n\t"
    "v_mfma_f32_16x16x32_bf16 a[104:107], a[168:171], v[224:227], a[104:107]\n\t"
    "buffer_load_dwordx4 v16, s[44:47], s85 offen offset:1024 nt lds\n\t"
    "v_exp_f32_e32 v237, v237\n\t"
    "v_mfma_f32_16x16x32_bf16 a[108:111], a[172:175], v[224:227], a[108:111]\n\t"
    "v_add_f32_e32 v36, v237, v36\n\t"
    "v_cvt_pk_bf16_f32 v233, v234, v235\n\t"
    "v_mfma_f32_16x16x32_bf16 a[112:115], a[176:179], v[224:227], a[112:115]\n\t"
    "v_exp_f32_e32 v238, v238\n\t"
    "v_mfma_f32_16x16x32_bf16 a[116:119], a[180:183], v[224:227], a[116:119]\n\t"
    "v_add_f32_e32 v36, v238, v36\n\t"
    "v_cvt_pk_bf16_f32 v234, v236, v237\n\t"
    "v_mfma_f32_16x16x32_bf16 a[120:123], a[184:187], v[224:227], a[120:123]\n\t"
    "s_waitcnt vmcnt(63) lgkmcnt(0)\n\t"
    "v_exp_f32_e32 v239, v239\n\t"
    "v_mfma_f32_16x16x32_bf16 a[124:127], a[188:191], v[224:227], a[124:127]\n\t"
    "s_barrier\n\t"
    "v_add_f32_e32 v36, v239, v36\n\t"
    "v_cvt_pk_bf16_f32 v235, v238, v239\n\t"
    "s_sub_i32 s31, s29, s30\n\t"
    "s_cmp_gt_i32 s31, 0\n\t"
    "s_cselect_b32 s29, s29, s30\n\t"
    "s_cbranch_scc0 .Lfa_pa11e\n\t"
    "s_min_i32 s31, s31, 0xc8\n\t"
    ".Lfa_pa11:\n\t"
    "s_sub_u32 s31, s31, 2\n\t"
    "s_cmp_gt_i32 s31, 0\n\t"
    "s_cbranch_scc1 .Lfa_pa11\n\t"
    ".Lfa_pa11e:\n\t"
    "s_add_u32 s29, s29, 0x71\n\t"
    "v_mfma_f32_16x16x32_bf16 v[224:227], a[192:195], v[64:67], v[40:43]\n\t"
    "s_mov_b32 s84, 0xc000\n\t"
    "v_exp_f32_e32 v240, v240\n\t"
    "v_mfma_f32_16x16x32_bf16 v[224:227], a[196:199], v[68:71], v[224:227]\n\t"
    "ds_read_b128 a[224:227], v17 offset:32768\n\t"
    "v_add_f32_e32 v37, v240, v37\n\t"
    "v_mfma_f32_16x16x32_bf16 v[224:227], a[200:203], v[72:75], v[224:227]\n\t"
    "v_exp_f32_e32 v241, v241\n\t"
    "v_mfma_f32_16x16x32_bf16 v[224:227], a[204:207], v[76:79], v[224:227]\n\t"
    "ds_read_b128 a[228:231], v17 offset:33024\n\t"
    "v_add_f32_e32 v37, v241, v37\n\t"
    "v_mfma_f32_16x16x32_bf16 v[228:231], a[208:211], v[64:67], v[40:43]\n\t"
    "v_exp_f32_e32 v242, v242\n\t"
    "v_mfma_f32_16x16x32_bf16 v[228:231], a[212:215], v[68:71], v[228:231]\n\t"
    "ds_read_b128 a[232:235], v17 offset:33280\n\t"
    "v_add_f32_e32 v37, v242, v37\n\t"
    "v_mfma_f32_16x16x32_bf16 v[228:231], a[216:219], v[72:75], v[228:231]\n\t"
    "v_exp_f32_e32 v243, v243\n\t"
    "v_mfma_f32_16x16x32_bf16 v[228:231], a[220:223], v[76:79], v[228:231]\n\t"
    "ds_read_b128 a[236:239], v17 offset:33536\n\t"
    "v_add_f32_e32 v37, v243, v37\n\t"
    "v_mfma_f32_16x16x32_bf16 v[160:163], a[160:163], v[232:235], v[160:163]\n\t"
    "v_exp_f32_e32 v244, v244\n\t"
    "v_mfma_f32_16x16x32_bf16 v[164:167], a[164:167], v[232:235], v[164:167]\n\t"
    "s_add_u32 m0, s65, 0xc000\n\t"
    "v_add_f32_e32 v37, v244, v37\n\t"
    "v_cvt_pk_bf16_f32 v240, v240, v241\n\t"
    "v_mfma_f32_16x16x32_bf16 v[168:171], a[168:171], v[232:235], v[168:171]\n\t"
    "buffer_load_dwordx4 v16, s[48:51], s84 offen nt lds\n\t"
    "v_exp_f32_e32 v245, v245\n\t"
    "v_mfma_f32_16x16x32_bf16 v[172:175], a[172:175], v[232:235], v[172:175]\n\t"
    "v_add_f32_e32 v37, v245, v37\n\t"
    "v_cvt_pk_bf16_f32 v241, v242, v243\n\t"
    "v_mfma_f32_16x16x32_bf16 v[176:179], a[176:179], v[232:235], v[176:179]\n\t"
    "v_exp_f32_e32 v246, v246\n\t"
    "v_mfma_f32_16x16x32_bf16 v[180:183], a[180:183], v[232:235], v[180:183]\n\t"
    "v_add_f32_e32 v37, v246, v37\n\t"
    "v_cvt_pk_bf16_f32 v242, v244, v245\n\t"
    "v_mfma_f32_16x16x32_bf16 v[184:187], a[184:187], v[232:235], v[184:187]\n\t"
    "v_exp_f32_e32 v247, v247\n\t"
    "v_mfma_f32_16x16x32_bf16 v[188:191], a[188:191], v[232:235], v[188:191]\n\t"
    "v_add_f32_e32 v37, v247, v37\n\t"
    "v_cvt_pk_bf16_f32 v243, v246, v247\n\t"
    "v_mfma_f32_16x16x32_bf16 v[232:235], a[192:195], v[80:83], v[44:47]\n\t"
    "v_exp_f32_e32 v224, v224\n\t"
    "v_mfma_f32_16x16x32_bf16 v[232:235], a[196:199], v[84:87], v[232:235]\n\t"
    "ds_read_b128 a[240:243], v17 offset:33792\n\t"
    "v_add_f32_e32 v32, v224, v32\n\t"
    "v_mfma_f32_16x16x32_bf16 v[232:235], a[200:203], v[88:91], v[232:235]\n\t"
    "v_exp_f32_e32 v225, v225\n\t"
    "v_mfma_f32_16x16x32_bf16 v[232:235], a[204:207], v[92:95], v[232:235]\n\t"
    "ds_read_b128 a[244:247], v17 offset:34048\n\t"
    "v_add_f32_e32 v32, v225, v32\n\t"
    "v_mfma_f32_16x16x32_bf16 v[236:239], a[208:211], v[80:83], v[44:47]\n\t"
    "v_exp_f32_e32 v226, v226\n\t"
    "v_mfma_f32_16x16x32_bf16 v[236:239], a[212:215], v[84:87], v[236:239]\n\t"
    "ds_read_b128 a[248:251], v17 offset:34304\n\t"
    "v_add_f32_e32 v32, v226, v32\n\t"
    "v_mfma_f32_16x16x32_bf16 v[236:239], a[216:219], v[88:91], v[236:239]\n\t"
    "v_exp_f32_e32 v227, v227\n\t"
    "v_mfma_f32_16x16x32_bf16 v[236:239], a[220:223], v[92:95], v[236:239]\n\t"
    "ds_read_b128 a[252:255], v17 offset:34560\n\t"
    "v_add_f32_e32 v32, v227, v32\n\t"
    "v_mfma_f32_16x16x32_bf16 v[192:195], a[160:163], v[240:243], v[192:195]\n\t"
    "v_exp_f32_e32 v228, v228\n\t"
    "v_mfma_f32_16x16x32_bf16 v[196:199], a[164:167], v[240:243], v[196:199]\n\t"
    "v_add_f32_e32 v32, v228, v32\n\t"
    "v_cvt_pk_bf16_f32 v224, v224, v225\n\t"
    "v_mfma_f32_16x16x32_bf16 v[200:203], a[168:171], v[240:243], v[200:203]\n\t"
    "buffer_load_dwordx4 v16, s[48:51], s84 offen offset:1024 nt lds\n\t"
    "v_exp_f32_e32 v229, v229\n\t"
    "v_mfma_f32_16x16x32_bf16 v[204:207], a[172:175], v[240:243], v[204:207]\n\t"
    "v_add_f32_e32 v32, v229, v32\n\t"
    "v_cvt_pk_bf16_f32 v225, v226, v227\n\t"
    "v_mfma_f32_16x16x32_bf16 v[208:211], a[176:179], v[240:243], v[208:211]\n\t"
    "v_exp_f32_e32 v230, v230\n\t"
    "v_mfma_f32_16x16x32_bf16 v[212:215], a[180:183], v[240:243], v[212:215]\n\t"
    "v_add_f32_e32 v32, v230, v32\n\t"
    "v_cvt_pk_bf16_f32 v226, v228, v229\n\t"
    "v_mfma_f32_16x16x32_bf16 v[216:219], a[184:187], v[240:243], v[216:219]\n\t"
    "v_exp_f32_e32 v231, v231\n\t"
    "v_mfma_f32_16x16x32_bf16 v[220:223], a[188:191], v[240:243], v[220:223]\n\t"
    "v_add_f32_e32 v32, v231, v32\n\t"
    "v_cvt_pk_bf16_f32 v227, v230, v231\n\t"
    "v_mfma_f32_16x16x32_bf16 v[240:243], a[192:195], v[96:99], v[48:51]\n\t"
    "s_mov_b32 s85, 0x2c000\n\t"
    "v_exp_f32_e32 v232, v232\n\t"
    "v_mfma_f32_16x16x32_bf16 v[240:243], a[196:199], v[100:103], v[240:243]\n\t"
    "ds_read_b64_tr_b16 a[160:161], v19 offset:32768\n\t"
    "v_add_f32_e32 v33, v232, v33\n\t"
    "v_mfma_f32_16x16x32_bf16 v[240:243], a[200:203], v[104:107], v[240:243]\n\t"
    "v_exp_f32_e32 v233, v233\n\t"
    "v_mfma_f32_16x16x32_bf16 v[240:243], a[204:207], v[108:111], v[240:243]\n\t"
    "ds_read_b64_tr_b16 a[162:163], v19 offset:33792\n\t"
    "v_add_f32_e32 v33, v233, v33\n\t"
    "v_mfma_f32_16x16x32_bf16 v[244:247], a[208:211], v[96:99], v[48:51]\n\t"
    "v_exp_f32_e32 v234, v234\n\t"
    "v_mfma_f32_16x16x32_bf16 v[244:247], a[212:215], v[100:103], v[244:247]\n\t"
    "ds_read_b64_tr_b16 a[164:165], v21 offset:32768\n\t"
    "v_add_f32_e32 v33, v234, v33\n\t"
    "v_mfma_f32_16x16x32_bf16 v[244:247], a[216:219], v[104:107], v[244:247]\n\t"
    "v_exp_f32_e32 v235, v235\n\t"
    "v_mfma_f32_16x16x32_bf16 v[244:247], a[220:223], v[108:111], v[244:247]\n\t"
    "ds_read_b64_tr_b16 a[166:167], v21 offset:33792\n\t"
    "v_add_f32_e32 v33, v235, v33\n\t"
    "v_mfma_f32_16x16x32_bf16 a[0:3], a[128:131], v[224:227], a[0:3]\n\t"
    "v_exp_f32_e32 v236, v236\n\t"
    "v_mfma_f32_16x16x32_bf16 a[4:7], a[132:135], v[224:227], a[4:7]\n\t"
    "s_add_u32 m0, s65, 0xe000\n\t"
    "v_add_f32_e32 v33, v236, v33\n\t"
    "v_cvt_pk_bf16_f32 v232, v232, v233\n\t"
    "v_mfma_f32_16x16x32_bf16 a[8:11], a[136:139], v[224:227], a[8:11]\n\t"
    "buffer_load_dwordx4 v16, s[48:51], s85 offen nt lds\n\t"
    "v_exp_f32_e32 v237, v237\n\t"
    "v_mfma_f32_16x16x32_bf16 a[12:15], a[140:143], v[224:227], a[12:15]\n\t"
    "v_add_f32_e32 v33, v237, v33\n\t"
    "v_cvt_pk_bf16_f32 v233, v234, v235\n\t"
    "v_mfma_f32_16x16x32_bf16 a[16:19], a[144:147], v[224:227], a[16:19]\n\t"
    "v_exp_f32_e32 v238, v238\n\t"
    "v_mfma_f32_16x16x32_bf16 a[20:23], a[148:151], v[224:227], a[20:23]\n\t"
    "v_add_f32_e32 v33, v238, v33\n\t"
    "v_cvt_pk_bf16_f32 v234, v236, v237\n\t"
    "v_mfma_f32_16x16x32_bf16 a[24:27], a[152:155], v[224:227], a[24:27]\n\t"
    "v_exp_f32_e32 v239, v239\n\t"
    "v_mfma_f32_16x16x32_bf16 a[28:31], a[156:159], v[224:227], a[28:31]\n\t"
    "v_add_f32_e32 v33, v239, v33\n\t"
    "v_cvt_pk_bf16_f32 v235, v238, v239\n\t"
    "v_mfma_f32_16x16x32_bf16 v[224:227], a[192:195], v[112:115], v[52:55]\n\t"
    "v_exp_f32_e32 v240, v240\n\t"
    "v_mfma_f32_16x16x32_bf16 v[224:227], a[196:199], v[116:119], v[224:227]\n\t"
    "ds_read_b64_tr_b16 a[168:169], v19 offset:33024\n\t"
    "v_add_f32_e32 v34, v240, v34\n\t"
    "v_mfma_f32_16x16x32_bf16 v[224:227], a[200:203], v[120:123], v[224:227]\n\t"
    "v_exp_f32_e32 v241, v241\n\t"
    "v_mfma_f32_16x16x32_bf16 v[224:227], a[204:207], v[124:127], v[224:227]\n\t"
    "ds_read_b64_tr_b16 a[170:171], v19 offset:34048\n\t"
    "v_add_f32_e32 v34, v241, v34\n\t"
    "v_mfma_f32_16x16x32_bf16 v[228:231], a[208:211], v[112:115], v[52:55]\n\t"
    "v_exp_f32_e32 v242, v242\n\t"
    "v_mfma_f32_16x16x32_bf16 v[228:231], a[212:215], v[116:119], v[228:231]\n\t"
    "ds_read_b64_tr_b16 a[172:173], v21 offset:33024\n\t"
    "v_add_f32_e32 v34, v242, v34\n\t"
    "v_mfma_f32_16x16x32_bf16 v[228:231], a[216:219], v[120:123], v[228:231]\n\t"
    "v_exp_f32_e32 v243, v243\n\t"
    "v_mfma_f32_16x16x32_bf16 v[228:231], a[220:223], v[124:127], v[228:231]\n\t"
    "ds_read_b64_tr_b16 a[174:175], v21 offset:34048\n\t"
    "v_add_f32_e32 v34, v243, v34\n\t"
    "v_mfma_f32_16x16x32_bf16 a[32:35], a[128:131], v[232:235], a[32:35]\n\t"
    "v_exp_f32_e32 v244, v244\n\t"
    "v_mfma_f32_16x16x32_bf16 a[36:39], a[132:135], v[232:235], a[36:39]\n\t"
    "v_add_f32_e32 v34, v244, v34\n\t"
    "v_cvt_pk_bf16_f32 v240, v240, v241\n\t"
    "v_mfma_f32_16x16x32_bf16 a[40:43], a[136:139], v[232:235], a[40:43]\n\t"
    "buffer_load_dwordx4 v16, s[48:51], s85 offen offset:1024 nt lds\n\t"
    "v_exp_f32_e32 v245, v245\n\t"
    "v_mfma_f32_16x16x32_bf16 a[44:47], a[140:143], v[232:235], a[44:47]\n\t"
    "v_add_f32_e32 v34, v245, v34\n\t"
    "v_cvt_pk_bf16_f32 v241, v242, v243\n\t"
    "v_mfma_f32_16x16x32_bf16 a[48:51], a[144:147], v[232:235], a[48:51]\n\t"
    "v_exp_f32_e32 v246, v246\n\t"
    "v_mfma_f32_16x16x32_bf16 a[52:55], a[148:151], v[232:235], a[52:55]\n\t"
    "v_add_f32_e32 v34, v246, v34\n\t"
    "v_cvt_pk_bf16_f32 v242, v244, v245\n\t"
    "v_mfma_f32_16x16x32_bf16 a[56:59], a[152:155], v[232:235], a[56:59]\n\t"
    "v_exp_f32_e32 v247, v247\n\t"
    "v_mfma_f32_16x16x32_bf16 a[60:63], a[156:159], v[232:235], a[60:63]\n\t"
    "v_add_f32_e32 v34, v247, v34\n\t"
    "v_cvt_pk_bf16_f32 v243, v246, v247\n\t"
    "v_mfma_f32_16x16x32_bf16 v[232:235], a[192:195], v[128:131], v[56:59]\n\t"
    "s_mov_b32 s84, 0xe000\n\t"
    "v_exp_f32_e32 v224, v224\n\t"
    "v_mfma_f32_16x16x32_bf16 v[232:235], a[196:199], v[132:135], v[232:235]\n\t"
    "ds_read_b64_tr_b16 a[176:177], v19 offset:33280\n\t"
    "v_add_f32_e32 v35, v224, v35\n\t"
    "v_mfma_f32_16x16x32_bf16 v[232:235], a[200:203], v[136:139], v[232:235]\n\t"
    "v_exp_f32_e32 v225, v225\n\t"
    "v_mfma_f32_16x16x32_bf16 v[232:235], a[204:207], v[140:143], v[232:235]\n\t"
    "ds_read_b64_tr_b16 a[178:179], v19 offset:34304\n\t"
    "v_add_f32_e32 v35, v225, v35\n\t"
    "v_mfma_f32_16x16x32_bf16 v[236:239], a[208:211], v[128:131], v[56:59]\n\t"
    "v_exp_f32_e32 v226, v226\n\t"
    "v_mfma_f32_16x16x32_bf16 v[236:239], a[212:215], v[132:135], v[236:239]\n\t"
    "ds_read_b64_tr_b16 a[180:181], v21 offset:33280\n\t"
    "v_add_f32_e32 v35, v226, v35\n\t"
    "v_mfma_f32_16x16x32_bf16 v[236:239], a[216:219], v[136:139], v[236:239]\n\t"
    "v_exp_f32_e32 v227, v227\n\t"
    "v_mfma_f32_16x16x32_bf16 v[236:239], a[220:223], v[140:143], v[236:239]\n\t"
    "ds_read_b64_tr_b16 a[182:183], v21 offset:34304\n\t"
    "v_add_f32_e32 v35, v227, v35\n\t"
    "v_mfma_f32_16x16x32_bf16 a[64:67], a[128:131], v[240:243], a[64:67]\n\t"
    "v_exp_f32_e32 v228, v228\n\t"
    "v_mfma_f32_16x16x32_bf16 a[68:71], a[132:135], v[240:243], a[68:71]\n\t"
    "s_add_u32 m0, s65, 0x10000\n\t"
    "v_add_f32_e32 v35, v228, v35\n\t"
    "v_cvt_pk_bf16_f32 v224, v224, v225\n\t"
    "v_mfma_f32_16x16x32_bf16 a[72:75], a[136:139], v[240:243], a[72:75]\n\t"
    "buffer_load_dwordx4 v16, s[44:47], s84 offen nt lds\n\t"
    "v_exp_f32_e32 v229, v229\n\t"
    "v_mfma_f32_16x16x32_bf16 a[76:79], a[140:143], v[240:243], a[76:79]\n\t"
    "v_add_f32_e32 v35, v229, v35\n\t"
    "v_cvt_pk_bf16_f32 v225, v226, v227\n\t"
    "v_mfma_f32_16x16x32_bf16 a[80:83], a[144:147], v[240:243], a[80:83]\n\t"
    "v_exp_f32_e32 v230, v230\n\t"
    "v_mfma_f32_16x16x32_bf16 a[84:87], a[148:151], v[240:243], a[84:87]\n\t"
    "v_add_f32_e32 v35, v230, v35\n\t"
    "v_cvt_pk_bf16_f32 v226, v228, v229\n\t"
    "v_mfma_f32_16x16x32_bf16 a[88:91], a[152:155], v[240:243], a[88:91]\n\t"
    "v_exp_f32_e32 v231, v231\n\t"
    "v_mfma_f32_16x16x32_bf16 a[92:95], a[156:159], v[240:243], a[92:95]\n\t"
    "v_add_f32_e32 v35, v231, v35\n\t"
    "v_cvt_pk_bf16_f32 v227, v230, v231\n\t"
    "v_mfma_f32_16x16x32_bf16 v[240:243], a[192:195], v[144:147], v[60:63]\n\t"
    "v_exp_f32_e32 v232, v232\n\t"
    "v_mfma_f32_16x16x32_bf16 v[240:243], a[196:199], v[148:151], v[240:243]\n\t"
    "ds_read_b64_tr_b16 a[184:185], v19 offset:33536\n\t"
    "v_add_f32_e32 v36, v232, v36\n\t"
    "v_mfma_f32_16x16x32_bf16 v[240:243], a[200:203], v[152:155], v[240:243]\n\t"
    "v_exp_f32_e32 v233, v233\n\t"
    "v_mfma_f32_16x16x32_bf16 v[240:243], a[204:207], v[156:159], v[240:243]\n\t"
    "ds_read_b64_tr_b16 a[186:187], v19 offset:34560\n\t"
    "v_add_f32_e32 v36, v233, v36\n\t"
    "v_mfma_f32_16x16x32_bf16 v[244:247], a[208:211], v[144:147], v[60:63]\n\t"
    "v_exp_f32_e32 v234, v234\n\t"
    "v_mfma_f32_16x16x32_bf16 v[244:247], a[212:215], v[148:151], v[244:247]\n\t"
    "ds_read_b64_tr_b16 a[188:189], v21 offset:33536\n\t"
    "v_add_f32_e32 v36, v234, v36\n\t"
    "v_mfma_f32_16x16x32_bf16 v[244:247], a[216:219], v[152:155], v[244:247]\n\t"
    "v_exp_f32_e32 v235, v235\n\t"
    "v_mfma_f32_16x16x32_bf16 v[244:247], a[220:223], v[156:159], v[244:247]\n\t"
    "ds_read_b64_tr_b16 a[190:191], v21 offset:34560\n\t"
    "v_add_f32_e32 v36, v235, v36\n\t"
    "v_mfma_f32_16x16x32_bf16 a[96:99], a[128:131], v[224:227], a[96:99]\n\t"
    "v_exp_f32_e32 v236, v236\n\t"
    "v_mfma_f32_16x16x32_bf16 a[100:103], a[132:135], v[224:227], a[100:103]\n\t"
    "s_memrealtime s[30:31]\n\t"
    "v_add_f32_e32 v36, v236, v36\n\t"
    "v_cvt_pk_bf16_f32 v232, v232, v233\n\t"
    "v_mfma_f32_16x16x32_bf16 a[104:107], a[136:139], v[224:227], a[104:107]\n\t"
    "buffer_load_dwordx4 v16, s[44:47], s84 offen offset:1024 nt lds\n\t"
    "v_exp_f32_e32 v237, v237\n\t"
    "v_mfma_f32_16x16x32_bf16 a[108:111], a[140:143], v[224:227], a[108:111]\n\t"
    "v_add_f32_e32 v36, v237, v36\n\t"
    "v_cvt_pk_bf16_f32 v233, v234, v235\n\t"
    "v_mfma_f32_16x16x32_bf16 a[112:115], a[144:147], v[224:227], a[112:115]\n\t"
    "v_exp_f32_e32 v238, v238\n\t"
    "v_mfma_f32_16x16x32_bf16 a[116:119], a[148:151], v[224:227], a[116:119]\n\t"
    "v_add_f32_e32 v36, v238, v36\n\t"
    "v_cvt_pk_bf16_f32 v234, v236, v237\n\t"
    "v_mfma_f32_16x16x32_bf16 a[120:123], a[152:155], v[224:227], a[120:123]\n\t"
    "s_waitcnt vmcnt(19) lgkmcnt(0)\n\t"
    "v_exp_f32_e32 v239, v239\n\t"
    "v_mfma_f32_16x16x32_bf16 a[124:127], a[156:159], v[224:227], a[124:127]\n\t"
    "s_barrier\n\t"
    "v_add_f32_e32 v36, v239, v36\n\t"
    "v_cvt_pk_bf16_f32 v235, v238, v239\n\t"
    "s_sub_i32 s31, s29, s30\n\t"
    "s_cmp_gt_i32 s31, 0\n\t"
    "s_cselect_b32 s29, s29, s30\n\t"
    "s_cbranch_scc0 .Lfa_pa17e\n\t"
    "s_min_i32 s31, s31, 0xc8\n\t"
    ".Lfa_pa17:\n\t"
    "s_sub_u32 s31, s31, 2\n\t"
    "s_cmp_gt_i32 s31, 0\n\t"
    "s_cbranch_scc1 .Lfa_pa17\n\t"
    ".Lfa_pa17e:\n\t"
    "s_add_u32 s29, s29, 0x71\n\t"
    "v_mfma_f32_16x16x32_bf16 v[224:227], a[224:227], v[64:67], v[40:43]\n\t"
    "s_mov_b32 s85, 0x2e000\n\t"
    "v_exp_f32_e32 v240, v240\n\t"
    "v_mfma_f32_16x16x32_bf16 v[224:227], a[228:231], v[68:71], v[224:227]\n\t"
    "ds_read_b128 a[192:195], v18 offset:0\n\t"
    "v_add_f32_e32 v37, v240, v37\n\t"
    "v_mfma_f32_16x16x32_bf16 v[224:227], a[232:235], v[72:75], v[224:227]\n\t"
    "v_exp_f32_e32 v241, v241\n\t"
    "v_mfma_f32_16x16x32_bf16 v[224:227], a[236:239], v[76:79], v[224:227]\n\t"
    "ds_read_b128 a[196:199], v18 offset:256\n\t"
    "v_add_f32_e32 v37, v241, v37\n\t"
    "v_mfma_f32_16x16x32_bf16 v[228:231], a[240:243], v[64:67], v[40:43]\n\t"
    "v_exp_f32_e32 v242, v242\n\t"
    "v_mfma_f32_16x16x32_bf16 v[228:231], a[244:247], v[68:71], v[228:231]\n\t"
    "ds_read_b128 a[200:203], v18 offset:512\n\t"
    "v_add_f32_e32 v37, v242, v37\n\t"
    "v_mfma_f32_16x16x32_bf16 v[228:231], a[248:251], v[72:75], v[228:231]\n\t"
    "v_exp_f32_e32 v243, v243\n\t"
    "v_mfma_f32_16x16x32_bf16 v[228:231], a[252:255], v[76:79], v[228:231]\n\t"
    "ds_read_b128 a[204:207], v18 offset:768\n\t"
    "v_add_f32_e32 v37, v243, v37\n\t"
    "v_mfma_f32_16x16x32_bf16 v[160:163], a[128:131], v[232:235], v[160:163]\n\t"
    "v_exp_f32_e32 v244, v244\n\t"
    "v_mfma_f32_16x16x32_bf16 v[164:167], a[132:135], v[232:235], v[164:167]\n\t"
    "s_add_u32 m0, s65, 0x12000\n\t"
    "v_add_f32_e32 v37, v244, v37\n\t"
    "v_cvt_pk_bf16_f32 v240, v240, v241\n\t"
    "v_mfma_f32_16x16x32_bf16 v[168:171], a[136:139], v[232:235], v[168:171]\n\t"
    "buffer_load_dwordx4 v16, s[44:47], s85 offen nt lds\n\t"
    "v_exp_f32_e32 v245, v245\n\t"
    "v_mfma_f32_16x16x32_bf16 v[172:175], a[140:143], v[232:235], v[172:175]\n\t"
    "v_add_f32_e32 v37, v245, v37\n\t"
    "v_cvt_pk_bf16_f32 v241, v242, v243\n\t"
    "v_mfma_f32_16x16x32_bf16 v[176:179], a[144:147], v[232:235], v[176:179]\n\t"
    "v_exp_f32_e32 v246, v246\n\t"
    "v_mfma_f32_16x16x32_bf16 v[180:183], a[148:151], v[232:235], v[180:183]\n\t"
    "v_add_f32_e32 v37, v246, v37\n\t"
    "v_cvt_pk_bf16_f32 v242, v244, v245\n\t"
    "v_mfma_f32_16x16x32_bf16 v[184:187], a[152:155], v[232:235], v[184:187]\n\t"
    "buffer_load_dwordx4 v16, s[44:47], s85 offen offset:1024 nt lds\n\t"
    "v_exp_f32_e32 v247, v247\n\t"
    "v_mfma_f32_16x16x32_bf16 v[188:191], a[156:159], v[232:235], v[188:191]\n\t"
    "v_add_f32_e32 v37, v247, v37\n\t"
    "v_cvt_pk_bf16_f32 v243, v246, v247\n\t"
    "v_mfma_f32_16x16x32_bf16 v[232:235], a[224:227], v[80:83], v[44:47]\n\t"
    "s_mov_b32 s84, 0xe000\n\t"
    "v_exp_f32_e32 v224, v224\n\t"
    "v_mfma_f32_16x16x32_bf16 v[232:235], a[228:231], v[84:87], v[232:235]\n\t"
    "ds_read_b128 a[208:211], v18 offset:1024\n\t"
    "v_add_f32_e32 v32, v224, v32\n\t"
    "v_mfma_f32_16x16x32_bf16 v[232:235], a[232:235], v[88:91], v[232:235]\n\t"
    "v_exp_f32_e32 v225, v225\n\t"
    "v_mfma_f32_16x16x32_bf16 v[232:235], a[236:239], v[92:95], v[232:235]\n\t"
    "ds_read_b128 a[212:215], v18 offset:1280\n\t"
    "v_add_f32_e32 v32, v225, v32\n\t"
    "v_mfma_f32_16x16x32_bf16 v[236:239], a[240:243], v[80:83], v[44:47]\n\t"
    "v_exp_f32_e32 v226, v226\n\t"
    "v_mfma_f32_16x16x32_bf16 v[236:239], a[244:247], v[84:87], v[236:239]\n\t"
    "ds_read_b128 a[216:219], v18 offset:1536\n\t"
    "v_add_f32_e32 v32, v226, v32\n\t"
    "v_mfma_f32_16x16x32_bf16 v[236:239], a[248:251], v[88:91], v[236:239]\n\t"
    "v_exp_f32_e32 v227, v227\n\t"
    "v_mfma_f32_16x16x32_bf16 v[236:239], a[252:255], v[92:95], v[236:239]\n\t"
    "ds_read_b128 a[220:223], v18 offset:1792\n\t"
    "v_add_f32_e32 v32, v227, v32\n\t"
    "v_mfma_f32_16x16x32_bf16 v[192:195], a[128:131], v[240:243], v[192:195]\n\t"
    "v_exp_f32_e32 v228, v228\n\t"
    "v_mfma_f32_16x16x32_bf16 v[196:199], a[132:135], v[240:243], v[196:199]\n\t"
    "v_add_f32_e32 v32, v228, v32\n\t"
    "v_cvt_pk_bf16_f32 v224, v224, v225\n\t"
    "v_mfma_f32_16x16x32_bf16 v[200:203], a[136:139], v[240:243], v[200:203]\n\t"
    "v_exp_f32_e32 v229, v229\n\t"
    "v_mfma_f32_16x16x32_bf16 v[204:207], a[140:143], v[240:243], v[204:207]\n\t"
    "s_add_u32 m0, s65, 0x14000\n\t"
    "v_add_f32_e32 v32, v229, v32\n\t"
    "v_cvt_pk_bf16_f32 v225, v226, v227\n\t"
    "v_mfma_f32_16x16x32_bf16 v[208:211], a[144:147], v[240:243], v[208:211]\n\t"
    "buffer_load_dwordx4 v16, s[48:51], s84 offen nt lds\n\t"
    "v_exp_f32_e32 v230, v230\n\t"
    "v_mfma_f32_16x16x32_bf16 v[212:215], a[148:151], v[240:243], v[212:215]\n\t"
    "v_add_f32_e32 v32, v230, v32\n\t"
    "v_cvt_pk_bf16_f32 v226, v228, v229\n\t"
    "v_mfma_f32_16x16x32_bf16 v[216:219], a[152:155], v[240:243], v[216:219]\n\t"
    "v_exp_f32_e32 v231, v231\n\t"
    "v_mfma_f32_16x16x32_bf16 v[220:223], a[156:159], v[240:243], v[220:223]\n\t"
    "v_add_f32_e32 v32, v231, v32\n\t"
    "v_cvt_pk_bf16_f32 v227, v230, v231\n\t"
    "v_mfma_f32_16x16x32_bf16 v[240:243], a[224:227], v[96:99], v[48:51]\n\t"
    "s_add_u32 s88, s88, 0x80000\n\t"
    "v_exp_f32_e32 v232, v232\n\t"
    "v_mfma_f32_16x16x32_bf16 v[240:243], a[228:231], v[100:103], v[240:243]\n\t"
    "ds_read_b64_tr_b16 a[128:129], v20 offset:0\n\t"
    "v_add_f32_e32 v33, v232, v33\n\t"
    "v_mfma_f32_16x16x32_bf16 v[240:243], a[232:235], v[104:107], v[240:243]\n\t"
    "s_and_b32 s88, s88, 0x3fffff\n\t"
    "v_exp_f32_e32 v233, v233\n\t"
    "v_mfma_f32_16x16x32_bf16 v[240:243], a[236:239], v[108:111], v[240:243]\n\t"
    "ds_read_b64_tr_b16 a[130:131], v20 offset:1024\n\t"
    "v_add_f32_e32 v33, v233, v33\n\t"
    "v_mfma_f32_16x16x32_bf16 v[244:247], a[240:243], v[96:99], v[48:51]\n\t"
    "v_exp_f32_e32 v234, v234\n\t"
    "v_mfma_f32_16x16x32_bf16 v[244:247], a[244:247], v[100:103], v[244:247]\n\t"
    "ds_read_b64_tr_b16 a[132:133], v24 offset:0\n\t"
    "v_add_f32_e32 v33, v234, v33\n\t"
    "v_mfma_f32_16x16x32_bf16 v[244:247], a[248:251], v[104:107], v[244:247]\n\t"
    "v_exp_f32_e32 v235, v235\n\t"
    "v_mfma_f32_16x16x32_bf16 v[244:247], a[252:255], v[108:111], v[244:247]\n\t"
    "ds_read_b64_tr_b16 a[134:135], v24 offset:1024\n\t"
    "v_add_f32_e32 v33, v235, v33\n\t"
    "v_mfma_f32_16x16x32_bf16 a[0:3], a[160:163], v[224:227], a[0:3]\n\t"
    "buffer_load_dwordx4 v16, s[48:51], s84 offen offset:1024 nt lds\n\t"
    "v_exp_f32_e32 v236, v236\n\t"
    "v_mfma_f32_16x16x32_bf16 a[4:7], a[164:167], v[224:227], a[4:7]\n\t"
    "v_add_f32_e32 v33, v236, v33\n\t"
    "v_cvt_pk_bf16_f32 v232, v232, v233\n\t"
    "v_mfma_f32_16x16x32_bf16 a[8:11], a[168:171], v[224:227], a[8:11]\n\t"
    "v_exp_f32_e32 v237, v237\n\t"
    "v_mfma_f32_16x16x32_bf16 a[12:15], a[172:175], v[224:227], a[12:15]\n\t"
    "v_add_f32_e32 v33, v237, v33\n\t"
    "v_cvt_pk_bf16_f32 v233, v234, v235\n\t"
    "v_mfma_f32_16x16x32_bf16 a[16:19], a[176:179], v[224:227], a[16:19]\n\t"
    "v_exp_f32_e32 v238, v238\n\t"
    "v_mfma_f32_16x16x32_bf16 a[20:23], a[180:183], v[224:227], a[20:23]\n\t"
    "v_add_f32_e32 v33, v238, v33\n\t"
    "v_cvt_pk_bf16_f32 v234, v236, v237\n\t"
    "v_mfma_f32_16x16x32_bf16 a[24:27], a[184:187], v[224:227], a[24:27]\n\t"
    "buffer_load_dword v15, v25, s[56:59], s88 offen\n\t"
    "v_exp_f32_e32 v239, v239\n\t"
    "v_mfma_f32_16x16x32_bf16 a[28:31], a[188:191], v[224:227], a[28:31]\n\t"
    "v_add_f32_e32 v33, v239, v33\n\t"
    "v_cvt_pk_bf16_f32 v235, v238, v239\n\t"
    "v_mfma_f32_16x16x32_bf16 v[224:227], a[224:227], v[112:115], v[52:55]\n\t"
    "s_mov_b32 s85, 0x2e000\n\t"
    "v_exp_f32_e32 v240, v240\n\t"
    "v_mfma_f32_16x16x32_bf16 v[224:227], a[228:231], v[116:119], v[224:227]\n\t"
    "ds_read_b64_tr_b16 a[136:137], v20 offset:256\n\t"
    "v_add_f32_e32 v34, v240, v34\n\t"
    "v_mfma_f32_16x16x32_bf16 v[224:227], a[232:235], v[120:123], v[224:227]\n\t"
    "v_exp_f32_e32 v241, v241\n\t"
    "v_mfma_f32_16x16x32_bf16 v[224:227], a[236:239], v[124:127], v[224:227]\n\t"
    "ds_read_b64_tr_b16 a[138:139], v20 offset:1280\n\t"
    "v_add_f32_e32 v34, v241, v34\n\t"
    "v_mfma_f32_16x16x32_bf16 v[228:231], a[240:243], v[112:115], v[52:55]\n\t"
    "v_exp_f32_e32 v242, v242\n\t"
    "v_mfma_f32_16x16x32_bf16 v[228:231], a[244:247], v[116:119], v[228:231]\n\t"
    "ds_read_b64_tr_b16 a[140:141], v24 offset:256\n\t"
    "v_add_f32_e32 v34, v242, v34\n\t"
    "v_mfma_f32_16x16x32_bf16 v[228:231], a[248:251], v[120:123], v[228:231]\n\t"
    "v_exp_f32_e32 v243, v243\n\t"
    "v_mfma_f32_16x16x32_bf16 v[228:231], a[252:255], v[124:127], v[228:231]\n\t"
    "ds_read_b64_tr_b16 a[142:143], v24 offset:1280\n\t"
    "v_add_f32_e32 v34, v243, v34\n\t"
    "v_mfma_f32_16x16x32_bf16 a[32:35], a[160:163], v[232:235], a[32:35]\n\t"
    "v_exp_f32_e32 v244, v244\n\t"
    "v_mfma_f32_16x16x32_bf16 a[36:39], a[164:167], v[232:235], a[36:39]\n\t"
    "v_add_f32_e32 v34, v244, v34\n\t"
    "v_cvt_pk_bf16_f32 v240, v240, v241\n\t"
    "v_mfma_f32_16x16x32_bf16 a[40:43], a[168:171], v[232:235], a[40:43]\n\t"
    "v_exp_f32_e32 v245, v245\n\t"
    "v_mfma_f32_16x16x32_bf16 a[44:47], a[172:175], v[232:235], a[44:47]\n\t"
    "s_add_u32 m0, s65, 0x16000\n\t"
    "v_add_f32_e32 v34, v245, v34\n\t"
    "v_cvt_pk_bf16_f32 v241, v242, v243\n\t"
    "v_mfma_f32_16x16x32_bf16 a[48:51], a[176:179], v[232:235], a[48:51]\n\t"
    "buffer_load_dwordx4 v16, s[48:51], s85 offen nt lds\n\t"
    "v_exp_f32_e32 v246, v246\n\t"
    "v_mfma_f32_16x16x32_bf16 a[52:55], a[180:183], v[232:235], a[52:55]\n\t"
    "v_add_f32_e32 v34, v246, v34\n\t"
    "v_cvt_pk_bf16_f32 v242, v244, v245\n\t"
    "v_mfma_f32_16x16x32_bf16 a[56:59], a[184:187], v[232:235], a[56:59]\n\t"
    "v_exp_f32_e32 v247, v247\n\t"
    "v_mfma_f32_16x16x32_bf16 a[60:63], a[188:191], v[232:235], a[60:63]\n\t"
    "v_add_f32_e32 v34, v247, v34\n\t"
    "v_cvt_pk_bf16_f32 v243, v246, v247\n\t"
    "v_mfma_f32_16x16x32_bf16 v[232:235], a[224:227], v[128:131], v[56:59]\n\t"
    "s_mov_b32 s84, 0x10000\n\t"
    "v_exp_f32_e32 v224, v224\n\t"
    "v_mfma_f32_16x16x32_bf16 v[232:235], a[228:231], v[132:135], v[232:235]\n\t"
    "ds_read_b64_tr_b16 a[144:145], v20 offset:512\n\t"
    "v_add_f32_e32 v35, v224, v35\n\t"
    "v_mfma_f32_16x16x32_bf16 v[232:235], a[232:235], v[136:139], v[232:235]\n\t"
    "v_exp_f32_e32 v225, v225\n\t"
    "v_mfma_f32_16x16x32_bf16 v[232:235], a[236:239], v[140:143], v[232:235]\n\t"
    "ds_read_b64_tr_b16 a[146:147], v20 offset:1536\n\t"
    "v_add_f32_e32 v35, v225, v35\n\t"
    "v_mfma_f32_16x16x32_bf16 v[236:239], a[240:243], v[128:131], v[56:59]\n\t"
    "v_exp_f32_e32 v226, v226\n\t"
    "v_mfma_f32_16x16x32_bf16 v[236:239], a[244:247], v[132:135], v[236:239]\n\t"
    "ds_read_b64_tr_b16 a[148:149], v24 offset:512\n\t"
    "v_add_f32_e32 v35, v226, v35\n\t"
    "v_mfma_f32_16x16x32_bf16 v[236:239], a[248:251], v[136:139], v[236:239]\n\t"
    "v_exp_f32_e32 v227, v227\n\t"
    "v_mfma_f32_16x16x32_bf16 v[236:239], a[252:255], v[140:143], v[236:239]\n\t"
    "ds_read_b64_tr_b16 a[150:151], v24 offset:1536\n\t"
    "v_add_f32_e32 v35, v227, v35\n\t"
    "v_mfma_f32_16x16x32_bf16 a[64:67], a[160:163], v[240:243], a[64:67]\n\t"
    "buffer_load_dwordx4 v16, s[48:51], s85 offen offset:1024 nt lds\n\t"
    "v_exp_f32_e32 v228, v228\n\t"
    "v_mfma_f32_16x16x32_bf16 a[68:71], a[164:167], v[240:243], a[68:71]\n\t"
    "v_add_f32_e32 v35, v228, v35\n\t"
    "v_cvt_pk_bf16_f32 v224, v224, v225\n\t"
    "v_mfma_f32_16x16x32_bf16 a[72:75], a[168:171], v[240:243], a[72:75]\n\t"
    "v_exp_f32_e32 v229, v229\n\t"
    "v_mfma_f32_16x16x32_bf16 a[76:79], a[172:175], v[240:243], a[76:79]\n\t"
    "v_add_f32_e32 v35, v229, v35\n\t"
    "v_cvt_pk_bf16_f32 v225, v226, v227\n\t"
    "v_mfma_f32_16x16x32_bf16 a[80:83], a[176:179], v[240:243], a[80:83]\n\t"
    "v_exp_f32_e32 v230, v230\n\t"
    "v_mfma_f32_16x16x32_bf16 a[84:87], a[180:183], v[240:243], a[84:87]\n\t"
    "s_add_u32 m0, s65, 0x18000\n\t"
    "v_add_f32_e32 v35, v230, v35\n\t"
    "v_cvt_pk_bf16_f32 v226, v228, v229\n\t"
    "v_mfma_f32_16x16x32_bf16 a[88:91], a[184:187], v[240:243], a[88:91]\n\t"
    "buffer_load_dwordx4 v16, s[44:47], s84 offen nt lds\n\t"
    "v_exp_f32_e32 v231, v231\n\t"
    "v_mfma_f32_16x16x32_bf16 a[92:95], a[188:191], v[240:243], a[92:95]\n\t"
    "v_add_f32_e32 v35, v231, v35\n\t"
    "v_cvt_pk_bf16_f32 v227, v230, v231\n\t"
    "v_mfma_f32_16x16x32_bf16 v[240:243], a[224:227], v[144:147], v[60:63]\n\t"
    "v_exp_f32_e32 v232, v232\n\t"
    "v_mfma_f32_16x16x32_bf16 v[240:243], a[228:231], v[148:151], v[240:243]\n\t"
    "ds_read_b64_tr_b16 a[152:153], v20 offset:768\n\t"
    "v_add_f32_e32 v36, v232, v36\n\t"
    "v_mfma_f32_16x16x32_bf16 v[240:243], a[232:235], v[152:155], v[240:243]\n\t"
    "v_exp_f32_e32 v233, v233\n\t"
    "v_mfma_f32_16x16x32_bf16 v[240:243], a[236:239], v[156:159], v[240:243]\n\t"
    "ds_read_b64_tr_b16 a[154:155], v20 offset:1792\n\t"
    "v_add_f32_e32 v36, v233, v36\n\t"
    "v_mfma_f32_16x16x32_bf16 v[244:247], a[240:243], v[144:147], v[60:63]\n\t"
    "v_exp_f32_e32 v234, v234\n\t"
    "v_mfma_f32_16x16x32_bf16 v[244:247], a[244:247], v[148:151], v[244:247]\n\t"
    "ds_read_b64_tr_b16 a[156:157], v24 offset:768\n\t"
    "v_add_f32_e32 v36, v234, v36\n\t"
    "v_mfma_f32_16x16x32_bf16 v[244:247], a[248:251], v[152:155], v[244:247]\n\t"
    "v_exp_f32_e32 v235, v235\n\t"
    "v_mfma_f32_16x16x32_bf16 v[244:247], a[252:255], v[156:159], v[244:247]\n\t"
    "ds_read_b64_tr_b16 a[158:159], v24 offset:1792\n\t"
    "v_add_f32_e32 v36, v235, v36\n\t"
    "v_mfma_f32_16x16x32_bf16 a[96:99], a[160:163], v[224:227], a[96:99]\n\t"
    "v_exp_f32_e32 v236, v236\n\t"
    "v_mfma_f32_16x16x32_bf16 a[100:103], a[164:167], v[224:227], a[100:103]\n\t"
    "s_memrealtime s[30:31]\n\t"
    "v_add_f32_e32 v36, v236, v36\n\t"
    "v_cvt_pk_bf16_f32 v232, v232, v233\n\t"
    "v_mfma_f32_16x16x32_bf16 a[104:107], a[168:171], v[224:227], a[104:107]\n\t"
    "buffer_load_dwordx4 v16, s[44:47], s84 offen offset:1024 nt lds\n\t"
    "v_exp_f32_e32 v237, v237\n\t"
    "v_mfma_f32_16x16x32_bf16 a[108:111], a[172:175], v[224:227], a[108:111]\n\t"
    "v_add_f32_e32 v36, v237, v36\n\t"
    "v_cvt_pk_bf16_f32 v233, v234, v235\n\t"
    "v_mfma_f32_16x16x32_bf16 a[112:115], a[176:179], v[224:227], a[112:115]\n\t"
    "v_exp_f32_e32 v238, v238\n\t"
    "v_mfma_f32_16x16x32_bf16 a[116:119], a[180:183], v[224:227], a[116:119]\n\t"
    "v_add_f32_e32 v36, v238, v36\n\t"
    "v_cvt_pk_bf16_f32 v234, v236, v237\n\t"
    "v_mfma_f32_16x16x32_bf16 a[120:123], a[184:187], v[224:227], a[120:123]\n\t"
    "s_waitcnt vmcnt(20) lgkmcnt(0)\n\t"
    "v_exp_f32_e32 v239, v239\n\t"
    "v_mfma_f32_16x16x32_bf16 a[124:127], a[188:191], v[224:227], a[124:127]\n\t"
    "s_barrier\n\t"
    "v_add_f32_e32 v36, v239, v36\n\t"
    "v_cvt_pk_bf16_f32 v235, v238, v239\n\t"
    "s_sub_i32 s31, s29, s30\n\t"
    "s_cmp_gt_i32 s31, 0\n\t"
    "s_cselect_b32 s29, s29, s30\n\t"
    "s_cbranch_scc0 .Lfa_pa23e\n\t"
    "s_min_i32 s31, s31, 0xc8\n\t"
    ".Lfa_pa23:\n\t"
    "s_sub_u32 s31, s31, 2\n\t"
    "s_cmp_gt_i32 s31, 0\n\t"
    "s_cbranch_scc1 .Lfa_pa23\n\t"
    ".Lfa_pa23e:\n\t"
    "s_add_u32 s29, s29, 0x71\n\t"
    "v_mfma_f32_16x16x32_bf16 v[224:227], a[192:195], v[64:67], v[40:43]\n\t"
    "s_mov_b32 s85, 0x30000\n\t"
    "v_exp_f32_e32 v240, v240\n\t"
    "v_mfma_f32_16x16x32_bf16 v[224:227], a[196:199], v[68:71], v[224:227]\n\t"
    "ds_read_b128 a[224:227], v30 offset:0\n\t"
    "v_add_f32_e32 v37, v240, v37\n\t"
    "v_mfma_f32_16x16x32_bf16 v[224:227], a[200:203], v[72:75], v[224:227]\n\t"
    "v_exp_f32_e32 v241, v241\n\t"
    "v_mfma_f32_16x16x32_bf16 v[224:227], a[204:207], v[76:79], v[224:227]\n\t"
    "ds_read_b128 a[228:231], v30 offset:256\n\t"
    "v_add_f32_e32 v37, v241, v37\n\t"
    "v_mfma_f32_16x16x32_bf16 v[228:231], a[208:211], v[64:67], v[40:43]\n\t"
    "v_exp_f32_e32 v242, v242\n\t"
    "v_mfma_f32_16x16x32_bf16 v[228:231], a[212:215], v[68:71], v[228:231]\n\t"
    "ds_read_b128 a[232:235], v30 offset:512\n\t"
    "v_add_f32_e32 v37, v242, v37\n\t"
    "v_mfma_f32_16x16x32_bf16 v[228:231], a[216:219], v[72:75], v[228:231]\n\t"
    "v_exp_f32_e32 v243, v243\n\t"
    "v_mfma_f32_16x16x32_bf16 v[228:231], a[220:223], v[76:79], v[228:231]\n\t"
    "ds_read_b128 a[236:239], v30 offset:768\n\t"
    "v_add_f32_e32 v37, v243, v37\n\t"
    "v_mfma_f32_16x16x32_bf16 v[160:163], a[160:163], v[232:235], v[160:163]\n\t"
    "v_exp_f32_e32 v244, v244\n\t"
    "v_mfma_f32_16x16x32_bf16 v[164:167], a[164:167], v[232:235], v[164:167]\n\t"
    "s_add_u32 m0, s65, 0x1a000\n\t"
    "v_add_f32_e32 v37, v244, v37\n\t"
    "v_cvt_pk_bf16_f32 v240, v240, v241\n\t"
    "v_mfma_f32_16x16x32_bf16 v[168:171], a[168:171], v[232:235], v[168:171]\n\t"
    "buffer_load_dwordx4 v16, s[44:47], s85 offen nt lds\n\t"
    "v_exp_f32_e32 v245, v245\n\t"
    "v_mfma_f32_16x16x32_bf16 v[172:175], a[172:175], v[232:235], v[172:175]\n\t"
    "v_add_f32_e32 v37, v245, v37\n\t"
    "v_cvt_pk_bf16_f32 v241, v242, v243\n\t"
    "v_mfma_f32_16x16x32_bf16 v[176:179], a[176:179], v[232:235], v[176:179]\n\t"
    "v_exp_f32_e32 v246, v246\n\t"
    "v_mfma_f32_16x16x32_bf16 v[180:183], a[180:183], v[232:235], v[180:183]\n\t"
    "v_add_f32_e32 v37, v246, v37\n\t"
    "v_cvt_pk_bf16_f32 v242, v244, v245\n\t"
    "v_mfma_f32_16x16x32_bf16 v[184:187], a[184:187], v[232:235], v[184:187]\n\t"
    "v_exp_f32_e32 v247, v247\n\t"
    "v_mfma_f32_16x16x32_bf16 v[188:191], a[188:191], v[232:235], v[188:191]\n\t"
    "v_add_f32_e32 v37, v247, v37\n\t"
    "v_cvt_pk_bf16_f32 v243, v246, v247\n\t"
    "v_mfma_f32_16x16x32_bf16 v[232:235], a[192:195], v[80:83], v[44:47]\n\t"
    "v_exp_f32_e32 v224, v224\n\t"
    "v_mfma_f32_16x16x32_bf16 v[232:235], a[196:199], v[84:87], v[232:235]\n\t"
    "ds_read_b128 a[240:243], v30 offset:1024\n\t"
    "v_add_f32_e32 v32, v224, v32\n\t"
    "v_mfma_f32_16x16x32_bf16 v[232:235], a[200:203], v[88:91], v[232:235]\n\t"
    "v_exp_f32_e32 v225, v225\n\t"
    "v_mfma_f32_16x16x32_bf16 v[232:235], a[204:207], v[92:95], v[232:235]\n\t"
    "ds_read_b128 a[244:247], v30 offset:1280\n\t"
    "v_add_f32_e32 v32, v225, v32\n\t"
    "v_mfma_f32_16x16x32_bf16 v[236:239], a[208:211], v[80:83], v[44:47]\n\t"
    "v_exp_f32_e32 v226, v226\n\t"
    "v_mfma_f32_16x16x32_bf16 v[236:239], a[212:215], v[84:87], v[236:239]\n\t"
    "ds_read_b128 a[248:251], v30 offset:1536\n\t"
    "v_add_f32_e32 v32, v226, v32\n\t"
    "v_mfma_f32_16x16x32_bf16 v[236:239], a[216:219], v[88:91], v[236:239]\n\t"
    "v_exp_f32_e32 v227, v227\n\t"
    "v_mfma_f32_16x16x32_bf16 v[236:239], a[220:223], v[92:95], v[236:239]\n\t"
    "ds_read_b128 a[252:255], v30 offset:1792\n\t"
    "v_add_f32_e32 v32, v227, v32\n\t"
    "v_mfma_f32_16x16x32_bf16 v[192:195], a[160:163], v[240:243], v[192:195]\n\t"
    "buffer_load_dwordx4 v16, s[44:47], s85 offen offset:1024 nt lds\n\t"
    "v_exp_f32_e32 v228, v228\n\t"
    "v_mfma_f32_16x16x32_bf16 v[196:199], a[164:167], v[240:243], v[196:199]\n\t"
    "v_add_f32_e32 v32, v228, v32\n\t"
    "v_cvt_pk_bf16_f32 v224, v224, v225\n\t"
    "v_mfma_f32_16x16x32_bf16 v[200:203], a[168:171], v[240:243], v[200:203]\n\t"
    "v_exp_f32_e32 v229, v229\n\t"
    "v_mfma_f32_16x16x32_bf16 v[204:207], a[172:175], v[240:243], v[204:207]\n\t"
    "v_add_f32_e32 v32, v229, v32\n\t"
    "v_cvt_pk_bf16_f32 v225, v226, v227\n\t"
    "v_mfma_f32_16x16x32_bf16 v[208:211], a[176:179], v[240:243], v[208:211]\n\t"
    "v_exp_f32_e32 v230, v230\n\t"
    "v_mfma_f32_16x16x32_bf16 v[212:215], a[180:183], v[240:243], v[212:215]\n\t"
    "v_add_f32_e32 v32, v230, v32\n\t"
    "v_cvt_pk_bf16_f32 v226, v228, v229\n\t"
    "v_mfma_f32_16x16x32_bf16 v[216:219], a[184:187], v[240:243], v[216:219]\n\t"
    "v_exp_f32_e32 v231, v231\n\t"
    "v_mfma_f32_16x16x32_bf16 v[220:223], a[188:191], v[240:243], v[220:223]\n\t"
    "v_add_f32_e32 v32, v231, v32\n\t"
    "v_cvt_pk_bf16_f32 v227, v230, v231\n\t"
    "v_mfma_f32_16x16x32_bf16 v[240:243], a[192:195], v[96:99], v[48:51]\n\t"
    "s_mov_b32 s84, 0x10000\n\t"
    "v_exp_f32_e32 v232, v232\n\t"
    "v_mfma_f32_16x16x32_bf16 v[240:243], a[196:199], v[100:103], v[240:243]\n\t"
    "ds_read_b64_tr_b16 a[160:161], v22 offset:0\n\t"
    "v_add_f32_e32 v33, v232, v33\n\t"
    "v_mfma_f32_16x16x32_bf16 v[240:243], a[200:203], v[104:107], v[240:243]\n\t"
    "s_add_u32 s88, s88, 0x80000\n\t"
    "v_exp_f32_e32 v233, v233\n\t"
    "v_mfma_f32_16x16x32_bf16 v[240:243], a[204:207], v[108:111], v[240:243]\n\t"
    "ds_read_b64_tr_b16 a[162:163], v22 offset:1024\n\t"
    "v_add_f32_e32 v33, v233, v33\n\t"
    "v_mfma_f32_16x16x32_bf16 v[244:247], a[208:211], v[96:99], v[48:51]\n\t"
    "s_and_b32 s88, s88, 0x3fffff\n\t"
    "v_exp_f32_e32 v234, v234\n\t"
    "v_mfma_f32_16x16x32_bf16 v[244:247], a[212:215], v[100:103], v[244:247]\n\t"
    "ds_read_b64_tr_b16 a[164:165], v23 offset:0\n\t"
    "v_add_f32_e32 v33, v234, v33\n\t"
    "v_mfma_f32_16x16x32_bf16 v[244:247], a[216:219], v[104:107], v[244:247]\n\t"
    "s_add_u32 m0, s65, 0x1c000\n\t"
    "v_exp_f32_e32 v235, v235\n\t"
    "v_mfma_f32_16x16x32_bf16 v[244:247], a[220:223], v[108:111], v[244:247]\n\t"
    "ds_read_b64_tr_b16 a[166:167], v23 offset:1024\n\t"
    "v_add_f32_e32 v33, v235, v33\n\t"
    "v_mfma_f32_16x16x32_bf16 a[0:3], a[128:131], v[224:227], a[0:3]\n\t"
    "buffer_load_dwordx4 v16, s[48:51], s84 offen nt lds\n\t"
    "v_exp_f32_e32 v236, v236\n\t"
    "v_mfma_f32_16x16x32_bf16 a[4:7], a[132:135], v[224:227], a[4:7]\n\t"
    "v_add_f32_e32 v33, v236, v33\n\t"
    "v_cvt_pk_bf16_f32 v232, v232, v233\n\t"
    "v_mfma_f32_16x16x32_bf16 a[8:11], a[136:139], v[224:227], a[8:11]\n\t"
    "v_exp_f32_e32 v237, v237\n\t"
    "v_mfma_f32_16x16x32_bf16 a[12:15], a[140:143], v[224:227], a[12:15]\n\t"
    "v_add_f32_e32 v33, v237, v33\n\t"
    "v_cvt_pk_bf16_f32 v233, v234, v235\n\t"
    "v_mfma_f32_16x16x32_bf16 a[16:19], a[144:147], v[224:227], a[16:19]\n\t"
    "v_exp_f32_e32 v238, v238\n\t"
    "v_mfma_f32_16x16x32_bf16 a[20:23], a[148:151], v[224:227], a[20:23]\n\t"
    "v_add_f32_e32 v33, v238, v33\n\t"
    "v_cvt_pk_bf16_f32 v234, v236, v237\n\t"
    "v_mfma_f32_16x16x32_bf16 a[24:27], a[152:155], v[224:227], a[24:27]\n\t"
    "buffer_load_dword v15, v25, s[56:59], s88 offen\n\t"
    "v_exp_f32_e32 v239, v239\n\t"
    "v_mfma_f32_16x16x32_bf16 a[28:31], a[156:159], v[224:227], a[28:31]\n\t"
    "v_add_f32_e32 v33, v239, v33\n\t"
    "v_cvt_pk_bf16_f32 v235, v238, v239\n\t"
    "v_mfma_f32_16x16x32_bf16 v[224:227], a[192:195], v[112:115], v[52:55]\n\t"
    "v_exp_f32_e32 v240, v240\n\t"
    "v_mfma_f32_16x16x32_bf16 v[224:227], a[196:199], v[116:119], v[224:227]\n\t"
    "ds_read_b64_tr_b16 a[168:169], v22 offset:256\n\t"
    "v_add_f32_e32 v34, v240, v34\n\t"
    "v_mfma_f32_16x16x32_bf16 v[224:227], a[200:203], v[120:123], v[224:227]\n\t"
    "v_exp_f32_e32 v241, v241\n\t"
    "v_mfma_f32_16x16x32_bf16 v[224:227], a[204:207], v[124:127], v[224:227]\n\t"
    "ds_read_b64_tr_b16 a[170:171], v22 offset:1280\n\t"
    "v_add_f32_e32 v34, v241, v34\n\t"
    "v_mfma_f32_16x16x32_bf16 v[228:231], a[208:211], v[112:115], v[52:55]\n\t"
    "v_exp_f32_e32 v242, v242\n\t"
    "v_mfma_f32_16x16x32_bf16 v[228:231], a[212:215], v[116:119], v[228:231]\n\t"
    "ds_read_b64_tr_b16 a[172:173], v23 offset:256\n\t"
    "v_add_f32_e32 v34, v242, v34\n\t"
    "v_mfma_f32_16x16x32_bf16 v[228:231], a[216:219], v[120:123], v[228:231]\n\t"
    "v_exp_f32_e32 v243, v243\n\t"
    "v_mfma_f32_16x16x32_bf16 v[228:231], a[220:223], v[124:127], v[228:231]\n\t"
    "ds_read_b64_tr_b16 a[174:175], v23 offset:1280\n\t"
    "v_add_f32_e32 v34, v243, v34\n\t"
    "v_mfma_f32_16x16x32_bf16 a[32:35], a[128:131], v[232:235], a[32:35]\n\t"
    "v_exp_f32_e32 v244, v244\n\t"
    "v_mfma_f32_16x16x32_bf16 a[36:39], a[132:135], v[232:235], a[36:39]\n\t"
    "v_add_f32_e32 v34, v244, v34\n\t"
    "v_cvt_pk_bf16_f32 v240, v240, v241\n\t"
    "v_mfma_f32_16x16x32_bf16 a[40:43], a[136:139], v[232:235], a[40:43]\n\t"
    "v_exp_f32_e32 v245, v245\n\t"
    "v_mfma_f32_16x16x32_bf16 a[44:47], a[140:143], v[232:235], a[44:47]\n\t"
    "v_add_f32_e32 v34, v245, v34\n\t"
    "v_cvt_pk_bf16_f32 v241, v242, v243\n\t"
    "v_mfma_f32_16x16x32_bf16 a[48:51], a[144:147], v[232:235], a[48:51]\n\t"
    "buffer_load_dwordx4 v16, s[48:51], s84 offen offset:1024 nt lds\n\t"
    "v_exp_f32_e32 v246, v246\n\t"
    "v_mfma_f32_16x16x32_bf16 a[52:55], a[148:151], v[232:235], a[52:55]\n\t"
    "v_add_f32_e32 v34, v246, v34\n\t"
    "v_cvt_pk_bf16_f32 v242, v244, v245\n\t"
    "v_mfma_f32_16x16x32_bf16 a[56:59], a[152:155], v[232:235], a[56:59]\n\t"
    "v_exp_f32_e32 v247, v247\n\t"
    "v_mfma_f32_16x16x32_bf16 a[60:63], a[156:159], v[232:235], a[60:63]\n\t"
    "v_add_f32_e32 v34, v247, v34\n\t"
    "v_cvt_pk_bf16_f32 v243, v246, v247\n\t"
    "v_mfma_f32_16x16x32_bf16 v[232:235], a[192:195], v[128:131], v[56:59]\n\t"
    "s_mov_b32 s85, 0x30000\n\t"
    "v_exp_f32_e32 v224, v224\n\t"
    "v_mfma_f32_16x16x32_bf16 v[232:235], a[196:199], v[132:135], v[232:235]\n\t"
    "ds_read_b64_tr_b16 a[176:177], v22 offset:512\n\t"
    "v_add_f32_e32 v35, v224, v35\n\t"
    "v_mfma_f32_16x16x32_bf16 v[232:235], a[200:203], v[136:139], v[232:235]\n\t"
    "v_exp_f32_e32 v225, v225\n\t"
    "v_mfma_f32_16x16x32_bf16 v[232:235], a[204:207], v[140:143], v[232:235]\n\t"
    "ds_read_b64_tr_b16 a[178:179], v22 offset:1536\n\t"
    "v_add_f32_e32 v35, v225, v35\n\t"
    "v_mfma_f32_16x16x32_bf16 v[236:239], a[208:211], v[128:131], v[56:59]\n\t"
    "v_exp_f32_e32 v226, v226\n\t"
    "v_mfma_f32_16x16x32_bf16 v[236:239], a[212:215], v[132:135], v[236:239]\n\t"
    "ds_read_b64_tr_b16 a[180:181], v23 offset:512\n\t"
    "v_add_f32_e32 v35, v226, v35\n\t"
    "v_mfma_f32_16x16x32_bf16 v[236:239], a[216:219], v[136:139], v[236:239]\n\t"
    "v_exp_f32_e32 v227, v227\n\t"
    "v_mfma_f32_16x16x32_bf16 v[236:239], a[220:223], v[140:143], v[236:239]\n\t"
    "ds_read_b64_tr_b16 a[182:183], v23 offset:1536\n\t"
    "v_add_f32_e32 v35, v227, v35\n\t"
    "v_mfma_f32_16x16x32_bf16 a[64:67], a[128:131], v[240:243], a[64:67]\n\t"
    "v_exp_f32_e32 v228, v228\n\t"
    "v_mfma_f32_16x16x32_bf16 a[68:71], a[132:135], v[240:243], a[68:71]\n\t"
    "v_add_f32_e32 v35, v228, v35\n\t"
    "v_cvt_pk_bf16_f32 v224, v224, v225\n\t"
    "v_mfma_f32_16x16x32_bf16 a[72:75], a[136:139], v[240:243], a[72:75]\n\t"
    "v_exp_f32_e32 v229, v229\n\t"
    "v_mfma_f32_16x16x32_bf16 a[76:79], a[140:143], v[240:243], a[76:79]\n\t"
    "s_add_u32 m0, s65, 0x1e000\n\t"
    "v_add_f32_e32 v35, v229, v35\n\t"
    "v_cvt_pk_bf16_f32 v225, v226, v227\n\t"
    "v_mfma_f32_16x16x32_bf16 a[80:83], a[144:147], v[240:243], a[80:83]\n\t"
    "buffer_load_dwordx4 v16, s[48:51], s85 offen nt lds\n\t"
    "v_exp_f32_e32 v230, v230\n\t"
    "v_mfma_f32_16x16x32_bf16 a[84:87], a[148:151], v[240:243], a[84:87]\n\t"
    "v_add_f32_e32 v35, v230, v35\n\t"
    "v_cvt_pk_bf16_f32 v226, v228, v229\n\t"
    "v_mfma_f32_16x16x32_bf16 a[88:91], a[152:155], v[240:243], a[88:91]\n\t"
    "v_exp_f32_e32 v231, v231\n\t"
    "v_mfma_f32_16x16x32_bf16 a[92:95], a[156:159], v[240:243], a[92:95]\n\t"
    "v_add_f32_e32 v35, v231, v35\n\t"
    "v_cvt_pk_bf16_f32 v227, v230, v231\n\t"
    "v_mfma_f32_16x16x32_bf16 v[240:243], a[192:195], v[144:147], v[60:63]\n\t"
    "v_exp_f32_e32 v232, v232\n\t"
    "v_mfma_f32_16x16x32_bf16 v[240:243], a[196:199], v[148:151], v[240:243]\n\t"
    "ds_read_b64_tr_b16 a[184:185], v22 offset:768\n\t"
    "v_add_f32_e32 v36, v232, v36\n\t"
    "v_mfma_f32_16x16x32_bf16 v[240:243], a[200:203], v[152:155], v[240:243]\n\t"
    "v_exp_f32_e32 v233, v233\n\t"
    "v_mfma_f32_16x16x32_bf16 v[240:243], a[204:207], v[156:159], v[240:243]\n\t"
    "ds_read_b64_tr_b16 a[186:187], v22 offset:1792\n\t"
    "v_add_f32_e32 v36, v233, v36\n\t"
    "v_mfma_f32_16x16x32_bf16 v[244:247], a[208:211], v[144:147], v[60:63]\n\t"
    "v_exp_f32_e32 v234, v234\n\t"
    "v_mfma_f32_16x16x32_bf16 v[244:247], a[212:215], v[148:151], v[244:247]\n\t"
    "ds_read_b64_tr_b16 a[188:189], v23 offset:768\n\t"
    "v_add_f32_e32 v36, v234, v36\n\t"
    "v_mfma_f32_16x16x32_bf16 v[244:247], a[216:219], v[152:155], v[244:247]\n\t"
    "v_exp_f32_e32 v235, v235\n\t"
    "v_mfma_f32_16x16x32_bf16 v[244:247], a[220:223], v[156:159], v[244:247]\n\t"
    "ds_read_b64_tr_b16 a[190:191], v23 offset:1792\n\t"
    "v_add_f32_e32 v36, v235, v36\n\t"
    "v_mfma_f32_16x16x32_bf16 a[96:99], a[128:131], v[224:227], a[96:99]\n\t"
    "v_exp_f32_e32 v236, v236\n\t"
    "v_mfma_f32_16x16x32_bf16 a[100:103], a[132:135], v[224:227], a[100:103]\n\t"
    "s_memrealtime s[30:31]\n\t"
    "v_add_f32_e32 v36, v236, v36\n\t"
    "v_cvt_pk_bf16_f32 v232, v232, v233\n\t"
    "v_mfma_f32_16x16x32_bf16 a[104:107], a[136:139], v[224:227], a[104:107]\n\t"
    "buffer_load_dwordx4 v16, s[48:51], s85 offen offset:1024 nt lds\n\t"
    "v_exp_f32_e32 v237, v237\n\t"
    "v_mfma_f32_16x16x32_bf16 a[108:111], a[140:143], v[224:227], a[108:111]\n\t"
    "v_add_f32_e32 v36, v237, v36\n\t"
    "v_cvt_pk_bf16_f32 v233, v234, v235\n\t"
    "v_mfma_f32_16x16x32_bf16 a[112:115], a[144:147], v[224:227], a[112:115]\n\t"
    "v_exp_f32_e32 v238, v238\n\t"
    "v_mfma_f32_16x16x32_bf16 a[116:119], a[148:151], v[224:227], a[116:119]\n\t"
    "v_add_f32_e32 v36, v238, v36\n\t"
    "v_cvt_pk_bf16_f32 v234, v236, v237\n\t"
    "v_mfma_f32_16x16x32_bf16 a[120:123], a[152:155], v[224:227], a[120:123]\n\t"
    "s_waitcnt vmcnt(18) lgkmcnt(0)\n\t"
    "v_exp_f32_e32 v239, v239\n\t"
    "v_mfma_f32_16x16x32_bf16 a[124:127], a[156:159], v[224:227], a[124:127]\n\t"
    "s_barrier\n\t"
    "v_add_f32_e32 v36, v239, v36\n\t"
    "v_cvt_pk_bf16_f32 v235, v238, v239\n\t"
    "s_sub_i32 s31, s29, s30\n\t"
    "s_cmp_gt_i32 s31, 0\n\t"
    "s_cselect_b32 s29, s29, s30\n\t"
    "s_cbranch_scc0 .Lfa_pa29e\n\t"
    "s_min_i32 s31, s31, 0xc8\n\t"
    ".Lfa_pa29:\n\t"
    "s_sub_u32 s31, s31, 2\n\t"
    "s_cmp_gt_i32 s31, 0\n\t"
    "s_cbranch_scc1 .Lfa_pa29\n\t"
    ".Lfa_pa29e:\n\t"
    "s_add_u32 s29, s29, 0x71\n\t"
    "v_mfma_f32_16x16x32_bf16 v[224:227], a[224:227], v[64:67], v[40:43]\n\t"
    "s_mov_b32 s84, 0x12000\n\t"
    "v_exp_f32_e32 v240, v240\n\t"
    "v_mfma_f32_16x16x32_bf16 v[224:227], a[228:231], v[68:71], v[224:227]\n\t"
    "ds_read_b128 a[192:195], v30 offset:32768\n\t"
    "v_add_f32_e32 v37, v240, v37\n\t"
    "v_mfma_f32_16x16x32_bf16 v[224:227], a[232:235], v[72:75], v[224:227]\n\t"
    "v_exp_f32_e32 v241, v241\n\t"
    "v_mfma_f32_16x16x32_bf16 v[224:227], a[236:239], v[76:79], v[224:227]\n\t"
    "ds_read_b128 a[196:199], v30 offset:33024\n\t"
    "v_add_f32_e32 v37, v241, v37\n\t"
    "v_mfma_f32_16x16x32_bf16 v[228:231], a[240:243], v[64:67], v[40:43]\n\t"
    "v_exp_f32_e32 v242, v242\n\t"
    "v_mfma_f32_16x16x32_bf16 v[228:231], a[244:247], v[68:71], v[228:231]\n\t"
    "ds_read_b128 a[200:203], v30 offset:33280\n\t"
    "v_add_f32_e32 v37, v242, v37\n\t"
    "v_mfma_f32_16x16x32_bf16 v[228:231], a[248:251], v[72:75], v[228:231]\n\t"
    "v_exp_f32_e32 v243, v243\n\t"
    "v_mfma_f32_16x16x32_bf16 v[228:231], a[252:255], v[76:79], v[228:231]\n\t"
    "ds_read_b128 a[204:207], v30 offset:33536\n\t"
    "v_add_f32_e32 v37, v243, v37\n\t"
    "v_mfma_f32_16x16x32_bf16 v[160:163], a[128:131], v[232:235], v[160:163]\n\t"
    "v_exp_f32_e32 v244, v244\n\t"
    "v_mfma_f32_16x16x32_bf16 v[164:167], a[132:135], v[232:235], v[164:167]\n\t"
    "s_add_u32 m0, s65, 0x20000\n\t"
    "v_add_f32_e32 v37, v244, v37\n\t"
    "v_cvt_pk_bf16_f32 v240, v240, v241\n\t"
    "v_mfma_f32_16x16x32_bf16 v[168:171], a[136:139], v[232:235], v[168:171]\n\t"
    "buffer_load_dwordx4 v16, s[44:47], s84 offen nt lds\n\t"
    "v_exp_f32_e32 v245, v245\n\t"
    "v_mfma_f32_16x16x32_bf16 v[172:175], a[140:143], v[232:235], v[172:175]\n\t"
    "v_add_f32_e32 v37, v245, v37\n\t"
    "v_cvt_pk_bf16_f32 v241, v242, v243\n\t"
    "v_mfma_f32_16x16x32_bf16 v[176:179], a[144:147], v[232:235], v[176:179]\n\t"
    "v_exp_f32_e32 v246, v246\n\t"
    "v_mfma_f32_16x16x32_bf16 v[180:183], a[148:151], v[232:235], v[180:183]\n\t"
    "v_add_f32_e32 v37, v246, v37\n\t"
    "v_cvt_pk_bf16_f32 v242, v244, v245\n\t"
    "v_mfma_f32_16x16x32_bf16 v[184:187], a[152:155], v[232:235], v[184:187]\n\t"
    "v_exp_f32_e32 v247, v247\n\t"
    "v_mfma_f32_16x16x32_bf16 v[188:191], a[156:159], v[232:235], v[188:191]\n\t"
    "v_add_f32_e32 v37, v247, v37\n\t"
    "v_cvt_pk_bf16_f32 v243, v246, v247\n\t"
    "v_mfma_f32_16x16x32_bf16 v[232:235], a[224:227], v[80:83], v[44:47]\n\t"
    "s_mov_b32 s85, 0x32000\n\t"
    "v_exp_f32_e32 v224, v224\n\t"
    "v_mfma_f32_16x16x32_bf16 v[232:235], a[228:231], v[84:87], v[232:235]\n\t"
    "ds_read_b128 a[208:211], v30 offset:33792\n\t"
    "v_add_f32_e32 v32, v224, v32\n\t"
    "v_mfma_f32_16x16x32_bf16 v[232:235], a[232:235], v[88:91], v[232:235]\n\t"
    "v_exp_f32_e32 v225, v225\n\t"
    "v_mfma_f32_16x16x32_bf16 v[232:235], a[236:239], v[92:95], v[232:235]\n\t"
    "ds_read_b128 a[212:215], v30 offset:34048\n\t"
    "v_add_f32_e32 v32, v225, v32\n\t"
    "v_mfma_f32_16x16x32_bf16 v[236:239], a[240:243], v[80:83], v[44:47]\n\t"
    "v_exp_f32_e32 v226, v226\n\t"
    "v_mfma_f32_16x16x32_bf16 v[236:239], a[244:247], v[84:87], v[236:239]\n\t"
    "ds_read_b128 a[216:219], v30 offset:34304\n\t"
    "v_add_f32_e32 v32, v226, v32\n\t"
    "v_mfma_f32_16x16x32_bf16 v[236:239], a[248:251], v[88:91], v[236:239]\n\t"
    "v_exp_f32_e32 v227, v227\n\t"
    "v_mfma_f32_16x16x32_bf16 v[236:239], a[252:255], v[92:95], v[236:239]\n\t"
    "ds_read_b128 a[220:223], v30 offset:34560\n\t"
    "v_add_f32_e32 v32, v227, v32\n\t"
    "v_mfma_f32_16x16x32_bf16 v[192:195], a[128:131], v[240:243], v[192:195]\n\t"
    "buffer_load_dwordx4 v16, s[44:47], s84 offen offset:1024 nt lds\n\t"
    "v_exp_f32_e32 v228, v228\n\t"
    "v_mfma_f32_16x16x32_bf16 v[196:199], a[132:135], v[240:243], v[196:199]\n\t"
    "v_add_f32_e32 v32, v228, v32\n\t"
    "v_cvt_pk_bf16_f32 v224, v224, v225\n\t"
    "v_mfma_f32_16x16x32_bf16 v[200:203], a[136:139], v[240:243], v[200:203]\n\t"
    "v_exp_f32_e32 v229, v229\n\t"
    "v_mfma_f32_16x16x32_bf16 v[204:207], a[140:143], v[240:243], v[204:207]\n\t"
    "v_add_f32_e32 v32, v229, v32\n\t"
    "v_cvt_pk_bf16_f32 v225, v226, v227\n\t"
    "v_mfma_f32_16x16x32_bf16 v[208:211], a[144:147], v[240:243], v[208:211]\n\t"
    "v_exp_f32_e32 v230, v230\n\t"
    "v_mfma_f32_16x16x32_bf16 v[212:215], a[148:151], v[240:243], v[212:215]\n\t"
    "s_add_u32 m0, s65, 0x22000\n\t"
    "v_add_f32_e32 v32, v230, v32\n\t"
    "v_cvt_pk_bf16_f32 v226, v228, v229\n\t"
    "v_mfma_f32_16x16x32_bf16 v[216:219], a[152:155], v[240:243], v[216:219]\n\t"
    "buffer_load_dwordx4 v16, s[44:47], s85 offen nt lds\n\t"
    "v_exp_f32_e32 v231, v231\n\t"
    "v_mfma_f32_16x16x32_bf16 v[220:223], a[156:159], v[240:243], v[220:223]\n\t"
    "v_add_f32_e32 v32, v231, v32\n\t"
    "v_cvt_pk_bf16_f32 v227, v230, v231\n\t"
    "v_mfma_f32_16x16x32_bf16 v[240:243], a[224:227], v[96:99], v[48:51]\n\t"
    "v_exp_f32_e32 v232, v232\n\t"
    "v_mfma_f32_16x16x32_bf16 v[240:243], a[228:231], v[100:103], v[240:243]\n\t"
    "ds_read_b64_tr_b16 a[128:129], v22 offset:32768\n\t"
    "v_add_f32_e32 v33, v232, v33\n\t"
    "v_mfma_f32_16x16x32_bf16 v[240:243], a[232:235], v[104:107], v[240:243]\n\t"
    "v_exp_f32_e32 v233, v233\n\t"
    "v_mfma_f32_16x16x32_bf16 v[240:243], a[236:239], v[108:111], v[240:243]\n\t"
    "ds_read_b64_tr_b16 a[130:131], v22 offset:33792\n\t"
    "v_add_f32_e32 v33, v233, v33\n\t"
    "v_mfma_f32_16x16x32_bf16 v[244:247], a[240:243], v[96:99], v[48:51]\n\t"
    "v_exp_f32_e32 v234, v234\n\t"
    "v_mfma_f32_16x16x32_bf16 v[244:247], a[244:247], v[100:103], v[244:247]\n\t"
    "ds_read_b64_tr_b16 a[132:133], v23 offset:32768\n\t"
    "v_add_f32_e32 v33, v234, v33\n\t"
    "v_mfma_f32_16x16x32_bf16 v[244:247], a[248:251], v[104:107], v[244:247]\n\t"
    "v_exp_f32_e32 v235, v235\n\t"
    "v_mfma_f32_16x16x32_bf16 v[244:247], a[252:255], v[108:111], v[244:247]\n\t"
    "ds_read_b64_tr_b16 a[134:135], v23 offset:33792\n\t"
    "v_add_f32_e32 v33, v235, v33\n\t"
    "v_mfma_f32_16x16x32_bf16 a[0:3], a[160:163], v[224:227], a[0:3]\n\t"
    "v_exp_f32_e32 v236, v236\n\t"
    "v_mfma_f32_16x16x32_bf16 a[4:7], a[164:167], v[224:227], a[4:7]\n\t"
    "v_add_f32_e32 v33, v236, v33\n\t"
    "v_cvt_pk_bf16_f32 v232, v232, v233\n\t"
    "v_mfma_f32_16x16x32_bf16 a[8:11], a[168:171], v[224:227], a[8:11]\n\t"
    "v_exp_f32_e32 v237, v237\n\t"
    "v_mfma_f32_16x16x32_bf16 a[12:15], a[172:175], v[224:227], a[12:15]\n\t"
    "v_add_f32_e32 v33, v237, v33\n\t"
    "v_cvt_pk_bf16_f32 v233, v234, v235\n\t"
    "v_mfma_f32_16x16x32_bf16 a[16:19], a[176:179], v[224:227], a[16:19]\n\t"
    "buffer_load_dwordx4 v16, s[44:47], s85 offen offset:1024 nt lds\n\t"
    "v_exp_f32_e32 v238, v238\n\t"
    "v_mfma_f32_16x16x32_bf16 a[20:23], a[180:183], v[224:227], a[20:23]\n\t"
    "v_add_f32_e32 v33, v238, v33\n\t"
    "v_cvt_pk_bf16_f32 v234, v236, v237\n\t"
    "v_mfma_f32_16x16x32_bf16 a[24:27], a[184:187], v[224:227], a[24:27]\n\t"
    "v_exp_f32_e32 v239, v239\n\t"
    "v_mfma_f32_16x16x32_bf16 a[28:31], a[188:191], v[224:227], a[28:31]\n\t"
    "v_add_f32_e32 v33, v239, v33\n\t"
    "v_cvt_pk_bf16_f32 v235, v238, v239\n\t"
    "v_mfma_f32_16x16x32_bf16 v[224:227], a[224:227], v[112:115], v[52:55]\n\t"
    "s_mov_b32 s84, 0x12000\n\t"
    "v_exp_f32_e32 v240, v240\n\t"
    "v_mfma_f32_16x16x32_bf16 v[224:227], a[228:231], v[116:119], v[224:227]\n\t"
    "ds_read_b64_tr_b16 a[136:137], v22 offset:33024\n\t"
    "v_add_f32_e32 v34, v240, v34\n\t"
    "v_mfma_f32_16x16x32_bf16 v[224:227], a[232:235], v[120:123], v[224:227]\n\t"
    "v_exp_f32_e32 v241, v241\n\t"
    "v_mfma_f32_16x16x32_bf16 v[224:227], a[236:239], v[124:127], v[224:227]\n\t"
    "ds_read_b64_tr_b16 a[138:139], v22 offset:34048\n\t"
    "v_add_f32_e32 v34, v241, v34\n\t"
    "v_mfma_f32_16x16x32_bf16 v[228:231], a[240:243], v[112:115], v[52:55]\n\t"
    "v_exp_f32_e32 v242, v242\n\t"
    "v_mfma_f32_16x16x32_bf16 v[228:231], a[244:247], v[116:119], v[228:231]\n\t"
    "ds_read_b64_tr_b16 a[140:141], v23 offset:33024\n\t"
    "v_add_f32_e32 v34, v242, v34\n\t"
    "v_mfma_f32_16x16x32_bf16 v[228:231], a[248:251], v[120:123], v[228:231]\n\t"
    "s_add_u32 m0, s65, 0x24000\n\t"
    "v_exp_f32_e32 v243, v243\n\t"
    "v_mfma_f32_16x16x32_bf16 v[228:231], a[252:255], v[124:127], v[228:231]\n\t"
    "ds_read_b64_tr_b16 a[142:143], v23 offset:34048\n\t"
    "v_add_f32_e32 v34, v243, v34\n\t"
    "v_mfma_f32_16x16x32_bf16 a[32:35], a[160:163], v[232:235], a[32:35]\n\t"
    "buffer_load_dwordx4 v16, s[48:51], s84 offen nt lds\n\t"
    "v_exp_f32_e32 v244, v244\n\t"
    "v_mfma_f32_16x16x32_bf16 a[36:39], a[164:167], v[232:235], a[36:39]\n\t"
    "v_add_f32_e32 v34, v244, v34\n\t"
    "v_cvt_pk_bf16_f32 v240, v240, v241\n\t"
    "v_mfma_f32_16x16x32_bf16 a[40:43], a[168:171], v[232:235], a[40:43]\n\t"
    "v_exp_f32_e32 v245, v245\n\t"
    "v_mfma_f32_16x16x32_bf16 a[44:47], a[172:175], v[232:235], a[44:47]\n\t"
    "v_add_f32_e32 v34, v245, v34\n\t"
    "v_cvt_pk_bf16_f32 v241, v242, v243\n\t"
    "v_mfma_f32_16x16x32_bf16 a[48:51], a[176:179], v[232:235], a[48:51]\n\t"
    "v_exp_f32_e32 v246, v246\n\t"
    "v_mfma_f32_16x16x32_bf16 a[52:55], a[180:183], v[232:235], a[52:55]\n\t"
    "v_add_f32_e32 v34, v246, v34\n\t"
    "v_cvt_pk_bf16_f32 v242, v244, v245\n\t"
    "v_mfma_f32_16x16x32_bf16 a[56:59], a[184:187], v[232:235], a[56:59]\n\t"
    "buffer_load_dwordx4 v16, s[48:51], s84 offen offset:1024 nt lds\n\t"
    "v_exp_f32_e32 v247, v247\n\t"
    "v_mfma_f32_16x16x32_bf16 a[60:63], a[188:191], v[232:235], a[60:63]\n\t"
    "v_add_f32_e32 v34, v247, v34\n\t"
    "v_cvt_pk_bf16_f32 v243, v246, v247\n\t"
    "v_mfma_f32_16x16x32_bf16 v[232:235], a[224:227], v[128:131], v[56:59]\n\t"
    "s_mov_b32 s85, 0x32000\n\t"
    "v_exp_f32_e32 v224, v224\n\t"
    "v_mfma_f32_16x16x32_bf16 v[232:235], a[228:231], v[132:135], v[232:235]\n\t"
    "ds_read_b64_tr_b16 a[144:145], v22 offset:33280\n\t"
    "v_add_f32_e32 v35, v224, v35\n\t"
    "v_mfma_f32_16x16x32_bf16 v[232:235], a[232:235], v[136:139], v[232:235]\n\t"
    "v_exp_f32_e32 v225, v225\n\t"
    "v_mfma_f32_16x16x32_bf16 v[232:235], a[236:239], v[140:143], v[232:235]\n\t"
    "ds_read_b64_tr_b16 a[146:147], v22 offset:34304\n\t"
    "v_add_f32_e32 v35, v225, v35\n\t"
    "v_mfma_f32_16x16x32_bf16 v[236:239], a[240:243], v[128:131], v[56:59]\n\t"
    "v_exp_f32_e32 v226, v226\n\t"
    "v_mfma_f32_16x16x32_bf16 v[236:239], a[244:247], v[132:135], v[236:239]\n\t"
    "ds_read_b64_tr_b16 a[148:149], v23 offset:33280\n\t"
    "v_add_f32_e32 v35, v226, v35\n\t"
    "v_mfma_f32_16x16x32_bf16 v[236:239], a[248:251], v[136:139], v[236:239]\n\t"
    "v_exp_f32_e32 v227, v227\n\t"
    "v_mfma_f32_16x16x32_bf16 v[236:239], a[252:255], v[140:143], v[236:239]\n\t"
    "ds_read_b64_tr_b16 a[150:151], v23 offset:34304\n\t"
    "v_add_f32_e32 v35, v227, v35\n\t"
    "v_mfma_f32_16x16x32_bf16 a[64:67], a[160:163], v[240:243], a[64:67]\n\t"
    "v_exp_f32_e32 v228, v228\n\t"
    "v_mfma_f32_16x16x32_bf16 a[68:71], a[164:167], v[240:243], a[68:71]\n\t"
    "v_add_f32_e32 v35, v228, v35\n\t"
    "v_cvt_pk_bf16_f32 v224, v224, v225\n\t"
    "v_mfma_f32_16x16x32_bf16 a[72:75], a[168:171], v[240:243], a[72:75]\n\t"
    "v_exp_f32_e32 v229, v229\n\t"
    "v_mfma_f32_16x16x32_bf16 a[76:79], a[172:175], v[240:243], a[76:79]\n\t"
    "s_add_u32 m0, s65, 0x26000\n\t"
    "v_add_f32_e32 v35, v229, v35\n\t"
    "v_cvt_pk_bf16_f32 v225, v226, v227\n\t"
    "v_mfma_f32_16x16x32_bf16 a[80:83], a[176:179], v[240:243], a[80:83]\n\t"
    "buffer_load_dwordx4 v16, s[48:51], s85 offen nt lds\n\t"
    "v_exp_f32_e32 v230, v230\n\t"
    "v_mfma_f32_16x16x32_bf16 a[84:87], a[180:183], v[240:243], a[84:87]\n\t"
    "v_add_f32_e32 v35, v230, v35\n\t"
    "v_cvt_pk_bf16_f32 v226, v228, v229\n\t"
    "v_mfma_f32_16x16x32_bf16 a[88:91], a[184:187], v[240:243], a[88:91]\n\t"
    "v_exp_f32_e32 v231, v231\n\t"
    "v_mfma_f32_16x16x32_bf16 a[92:95], a[188:191], v[240:243], a[92:95]\n\t"
    "v_add_f32_e32 v35, v231, v35\n\t"
    "v_cvt_pk_bf16_f32 v227, v230, v231\n\t"
    "v_mfma_f32_16x16x32_bf16 v[240:243], a[224:227], v[144:147], v[60:63]\n\t"
    "v_exp_f32_e32 v232, v232\n\t"
    "v_mfma_f32_16x16x32_bf16 v[240:243], a[228:231], v[148:151], v[240:243]\n\t"
    "ds_read_b64_tr_b16 a[152:153], v22 offset:33536\n\t"
    "v_add_f32_e32 v36, v232, v36\n\t"
    "v_mfma_f32_16x16x32_bf16 v[240:243], a[232:235], v[152:155], v[240:243]\n\t"
    "v_exp_f32_e32 v233, v233\n\t"
    "v_mfma_f32_16x16x32_bf16 v[240:243], a[236:239], v[156:159], v[240:243]\n\t"
    "ds_read_b64_tr_b16 a[154:155], v22 offset:34560\n\t"
    "v_add_f32_e32 v36, v233, v36\n\t"
    "v_mfma_f32_16x16x32_bf16 v[244:247], a[240:243], v[144:147], v[60:63]\n\t"
    "v_exp_f32_e32 v234, v234\n\t"
    "v_mfma_f32_16x16x32_bf16 v[244:247], a[244:247], v[148:151], v[244:247]\n\t"
    "ds_read_b64_tr_b16 a[156:157], v23 offset:33536\n\t"
    "v_add_f32_e32 v36, v234, v36\n\t"
    "v_mfma_f32_16x16x32_bf16 v[244:247], a[248:251], v[152:155], v[244:247]\n\t"
    "v_exp_f32_e32 v235, v235\n\t"
    "v_mfma_f32_16x16x32_bf16 v[244:247], a[252:255], v[156:159], v[244:247]\n\t"
    "ds_read_b64_tr_b16 a[158:159], v23 offset:34560\n\t"
    "v_add_f32_e32 v36, v235, v36\n\t"
    "v_mfma_f32_16x16x32_bf16 a[96:99], a[160:163], v[224:227], a[96:99]\n\t"
    "v_exp_f32_e32 v236, v236\n\t"
    "v_mfma_f32_16x16x32_bf16 a[100:103], a[164:167], v[224:227], a[100:103]\n\t"
    "s_memrealtime s[30:31]\n\t"
    "v_add_f32_e32 v36, v236, v36\n\t"
    "v_cvt_pk_bf16_f32 v232, v232, v233\n\t"
    "v_mfma_f32_16x16x32_bf16 a[104:107], a[168:171], v[224:227], a[104:107]\n\t"
    "buffer_load_dwordx4 v16, s[48:51], s85 offen offset:1024 nt lds\n\t"
    "v_exp_f32_e32 v237, v237\n\t"
    "v_mfma_f32_16x16x32_bf16 a[108:111], a[172:175], v[224:227], a[108:111]\n\t"
    "v_add_f32_e32 v36, v237, v36\n\t"
    "v_cvt_pk_bf16_f32 v233, v234, v235\n\t"
    "v_mfma_f32_16x16x32_bf16 a[112:115], a[176:179], v[224:227], a[112:115]\n\t"
    "v_exp_f32_e32 v238, v238\n\t"
    "v_mfma_f32_16x16x32_bf16 a[116:119], a[180:183], v[224:227], a[116:119]\n\t"
    "v_add_f32_e32 v36, v238, v36\n\t"
    "v_cvt_pk_bf16_f32 v234, v236, v237\n\t"
    "v_mfma_f32_16x16x32_bf16 a[120:123], a[184:187], v[224:227], a[120:123]\n\t"
    "s_waitcnt vmcnt(17) lgkmcnt(0)\n\t"
    "v_exp_f32_e32 v239, v239\n\t"
    "v_mfma_f32_16x16x32_bf16 a[124:127], a[188:191], v[224:227], a[124:127]\n\t"
    "s_barrier\n\t"
    "v_add_f32_e32 v36, v239, v36\n\t"
    "v_cvt_pk_bf16_f32 v235, v238, v239\n\t"
    "s_sub_i32 s31, s29, s30\n\t"
    "s_cmp_gt_i32 s31, 0\n\t"
    "s_cselect_b32 s29, s29, s30\n\t"
    "s_cbranch_scc0 .Lfa_pa35e\n\t"
    "s_min_i32 s31, s31, 0xc8\n\t"
    ".Lfa_pa35:\n\t"
    "s_sub_u32 s31, s31, 2\n\t"
    "s_cmp_gt_i32 s31, 0\n\t"
    "s_cbranch_scc1 .Lfa_pa35\n\t"
    ".Lfa_pa35e:\n\t"
    "s_add_u32 s29, s29, 0x71\n\t"
    "v_mfma_f32_16x16x32_bf16 v[224:227], a[192:195], v[64:67], v[40:43]\n\t"
    "s_mov_b32 s84, 0x14000\n\t"
    "v_exp_f32_e32 v240, v240\n\t"
    "v_mfma_f32_16x16x32_bf16 v[224:227], a[196:199], v[68:71], v[224:227]\n\t"
    "ds_read_b128 a[224:227], v17 offset:0\n\t"
    "v_add_f32_e32 v37, v240, v37\n\t"
    "v_mfma_f32_16x16x32_bf16 v[224:227], a[200:203], v[72:75], v[224:227]\n\t"
    "v_exp_f32_e32 v241, v241\n\t"
    "v_mfma_f32_16x16x32_bf16 v[224:227], a[204:207], v[76:79], v[224:227]\n\t"
    "ds_read_b128 a[228:231], v17 offset:256\n\t"
    "v_add_f32_e32 v37, v241, v37\n\t"
    "v_mfma_f32_16x16x32_bf16 v[228:231], a[208:211], v[64:67], v[40:43]\n\t"
    "v_exp_f32_e32 v242, v242\n\t"
    "v_mfma_f32_16x16x32_bf16 v[228:231], a[212:215], v[68:71], v[228:231]\n\t"
    "ds_read_b128 a[232:235], v17 offset:512\n\t"
    "v_add_f32_e32 v37, v242, v37\n\t"
    "v_mfma_f32_16x16x32_bf16 v[228:231], a[216:219], v[72:75], v[228:231]\n\t"
    "v_exp_f32_e32 v243, v243\n\t"
    "v_mfma_f32_16x16x32_bf16 v[228:231], a[220:223], v[76:79], v[228:231]\n\t"
    "ds_read_b128 a[236:239], v17 offset:768\n\t"
    "v_add_f32_e32 v37, v243, v37\n\t"
    "v_mfma_f32_16x16x32_bf16 v[160:163], a[160:163], v[232:235], v[160:163]\n\t"
    "v_exp_f32_e32 v244, v244\n\t"
    "v_mfma_f32_16x16x32_bf16 v[164:167], a[164:167], v[232:235], v[164:167]\n\t"
    "s_add_u32 m0, s65, 0\n\t"
    "v_add_f32_e32 v37, v244, v37\n\t"
    "v_cvt_pk_bf16_f32 v240, v240, v241\n\t"
    "v_mfma_f32_16x16x32_bf16 v[168:171], a[168:171], v[232:235], v[168:171]\n\t"
    "buffer_load_dwordx4 v16, s[44:47], s84 offen nt lds\n\t"
    "v_exp_f32_e32 v245, v245\n\t"
    "v_mfma_f32_16x16x32_bf16 v[172:175], a[172:175], v[232:235], v[172:175]\n\t"
    "v_add_f32_e32 v37, v245, v37\n\t"
    "v_cvt_pk_bf16_f32 v241, v242, v243\n\t"
    "v_mfma_f32_16x16x32_bf16 v[176:179], a[176:179], v[232:235], v[176:179]\n\t"
    "v_exp_f32_e32 v246, v246\n\t"
    "v_mfma_f32_16x16x32_bf16 v[180:183], a[180:183], v[232:235], v[180:183]\n\t"
    "v_add_f32_e32 v37, v246, v37\n\t"
    "v_cvt_pk_bf16_f32 v242, v244, v245\n\t"
    "v_mfma_f32_16x16x32_bf16 v[184:187], a[184:187], v[232:235], v[184:187]\n\t"
    "v_exp_f32_e32 v247, v247\n\t"
    "v_mfma_f32_16x16x32_bf16 v[188:191], a[188:191], v[232:235], v[188:191]\n\t"
    "v_add_f32_e32 v37, v247, v37\n\t"
    "v_cvt_pk_bf16_f32 v243, v246, v247\n\t"
    "v_mfma_f32_16x16x32_bf16 v[232:235], a[192:195], v[80:83], v[44:47]\n\t"
    "v_exp_f32_e32 v224, v224\n\t"
    "v_mfma_f32_16x16x32_bf16 v[232:235], a[196:199], v[84:87], v[232:235]\n\t"
    "ds_read_b128 a[240:243], v17 offset:1024\n\t"
    "v_add_f32_e32 v32, v224, v32\n\t"
    "v_mfma_f32_16x16x32_bf16 v[232:235], a[200:203], v[88:91], v[232:235]\n\t"
    "v_exp_f32_e32 v225, v225\n\t"
    "v_mfma_f32_16x16x32_bf16 v[232:235], a[204:207], v[92:95], v[232:235]\n\t"
    "ds_read_b128 a[244:247], v17 offset:1280\n\t"
    "v_add_f32_e32 v32, v225, v32\n\t"
    "v_mfma_f32_16x16x32_bf16 v[236:239], a[208:211], v[80:83], v[44:47]\n\t"
    "v_exp_f32_e32 v226, v226\n\t"
    "v_mfma_f32_16x16x32_bf16 v[236:239], a[212:215], v[84:87], v[236:239]\n\t"
    "ds_read_b128 a[248:251], v17 offset:1536\n\t"
    "v_add_f32_e32 v32, v226, v32\n\t"
    "v_mfma_f32_16x16x32_bf16 v[236:239], a[216:219], v[88:91], v[236:239]\n\t"
    "v_exp_f32_e32 v227, v227\n\t"
    "v_mfma_f32_16x16x32_bf16 v[236:239], a[220:223], v[92:95], v[236:239]\n\t"
    "ds_read_b128 a[252:255], v17 offset:1792\n\t"
    "v_add_f32_e32 v32, v227, v32\n\t"
    "v_mfma_f32_16x16x32_bf16 v[192:195], a[160:163], v[240:243], v[192:195]\n\t"
    "buffer_load_dwordx4 v16, s[44:47], s84 offen offset:1024 nt lds\n\t"
    "v_exp_f32_e32 v228, v228\n\t"
    "v_mfma_f32_16x16x32_bf16 v[196:199], a[164:167], v[240:243], v[196:199]\n\t"
    "v_add_f32_e32 v32, v228, v32\n\t"
    "v_cvt_pk_bf16_f32 v224, v224, v225\n\t"
    "v_mfma_f32_16x16x32_bf16 v[200:203], a[168:171], v[240:243], v[200:203]\n\t"
    "v_exp_f32_e32 v229, v229\n\t"
    "v_mfma_f32_16x16x32_bf16 v[204:207], a[172:175], v[240:243], v[204:207]\n\t"
    "v_add_f32_e32 v32, v229, v32\n\t"
    "v_cvt_pk_bf16_f32 v225, v226, v227\n\t"
    "v_mfma_f32_16x16x32_bf16 v[208:211], a[176:179], v[240:243], v[208:211]\n\t"
    "v_exp_f32_e32 v230, v230\n\t"
    "v_mfma_f32_16x16x32_bf16 v[212:215], a[180:183], v[240:243], v[212:215]\n\t"
    "v_add_f32_e32 v32, v230, v32\n\t"
    "v_cvt_pk_bf16_f32 v226, v228, v229\n\t"
    "v_mfma_f32_16x16x32_bf16 v[216:219], a[184:187], v[240:243], v[216:219]\n\t"
    "v_exp_f32_e32 v231, v231\n\t"
    "v_mfma_f32_16x16x32_bf16 v[220:223], a[188:191], v[240:243], v[220:223]\n\t"
    "v_add_f32_e32 v32, v231, v32\n\t"
    "v_cvt_pk_bf16_f32 v227, v230, v231\n\t"
    "v_mfma_f32_16x16x32_bf16 v[240:243], a[192:195], v[96:99], v[48:51]\n\t"
    "s_mov_b32 s85, 0x34000\n\t"
    "v_exp_f32_e32 v232, v232\n\t"
    "v_mfma_f32_16x16x32_bf16 v[240:243], a[196:199], v[100:103], v[240:243]\n\t"
    "ds_read_b64_tr_b16 a[160:161], v19 offset:0\n\t"
    "v_add_f32_e32 v33, v232, v33\n\t"
    "v_mfma_f32_16x16x32_bf16 v[240:243], a[200:203], v[104:107], v[240:243]\n\t"
    "s_add_u32 s88, s88, 0x80000\n\t"
    "v_exp_f32_e32 v233, v233\n\t"
    "v_mfma_f32_16x16x32_bf16 v[240:243], a[204:207], v[108:111], v[240:243]\n\t"
    "ds_read_b64_tr_b16 a[162:163], v19 offset:1024\n\t"
    "v_add_f32_e32 v33, v233, v33\n\t"
    "v_mfma_f32_16x16x32_bf16 v[244:247], a[208:211], v[96:99], v[48:51]\n\t"
    "s_and_b32 s88, s88, 0x3fffff\n\t"
    "v_exp_f32_e32 v234, v234\n\t"
    "v_mfma_f32_16x16x32_bf16 v[244:247], a[212:215], v[100:103], v[244:247]\n\t"
    "ds_read_b64_tr_b16 a[164:165], v21 offset:0\n\t"
    "v_add_f32_e32 v33, v234, v33\n\t"
    "v_mfma_f32_16x16x32_bf16 v[244:247], a[216:219], v[104:107], v[244:247]\n\t"
    "s_add_u32 m0, s65, 0x2000\n\t"
    "v_exp_f32_e32 v235, v235\n\t"
    "v_mfma_f32_16x16x32_bf16 v[244:247], a[220:223], v[108:111], v[244:247]\n\t"
    "ds_read_b64_tr_b16 a[166:167], v21 offset:1024\n\t"
    "v_add_f32_e32 v33, v235, v33\n\t"
    "v_mfma_f32_16x16x32_bf16 a[0:3], a[128:131], v[224:227], a[0:3]\n\t"
    "buffer_load_dwordx4 v16, s[44:47], s85 offen nt lds\n\t"
    "v_exp_f32_e32 v236, v236\n\t"
    "v_mfma_f32_16x16x32_bf16 a[4:7], a[132:135], v[224:227], a[4:7]\n\t"
    "v_add_f32_e32 v33, v236, v33\n\t"
    "v_cvt_pk_bf16_f32 v232, v232, v233\n\t"
    "v_mfma_f32_16x16x32_bf16 a[8:11], a[136:139], v[224:227], a[8:11]\n\t"
    "v_exp_f32_e32 v237, v237\n\t"
    "v_mfma_f32_16x16x32_bf16 a[12:15], a[140:143], v[224:227], a[12:15]\n\t"
    "v_add_f32_e32 v33, v237, v33\n\t"
    "v_cvt_pk_bf16_f32 v233, v234, v235\n\t"
    "v_mfma_f32_16x16x32_bf16 a[16:19], a[144:147], v[224:227], a[16:19]\n\t"
    "v_exp_f32_e32 v238, v238\n\t"
    "v_mfma_f32_16x16x32_bf16 a[20:23], a[148:151], v[224:227], a[20:23]\n\t"
    "v_add_f32_e32 v33, v238, v33\n\t"
    "v_cvt_pk_bf16_f32 v234, v236, v237\n\t"
    "v_mfma_f32_16x16x32_bf16 a[24:27], a[152:155], v[224:227], a[24:27]\n\t"
    "buffer_load_dword v15, v25, s[56:59], s88 offen\n\t"
    "v_exp_f32_e32 v239, v239\n\t"
    "v_mfma_f32_16x16x32_bf16 a[28:31], a[156:159], v[224:227], a[28:31]\n\t"
    "v_add_f32_e32 v33, v239, v33\n\t"
    "v_cvt_pk_bf16_f32 v235, v238, v239\n\t"
    "v_mfma_f32_16x16x32_bf16 v[224:227], a[192:195], v[112:115], v[52:55]\n\t"
    "v_exp_f32_e32 v240, v240\n\t"
    "v_mfma_f32_16x16x32_bf16 v[224:227], a[196:199], v[116:119], v[224:227]\n\t"
    "ds_read_b64_tr_b16 a[168:169], v19 offset:256\n\t"
    "v_add_f32_e32 v34, v240, v34\n\t"
    "v_mfma_f32_16x16x32_bf16 v[224:227], a[200:203], v[120:123], v[224:227]\n\t"
    "v_exp_f32_e32 v241, v241\n\t"
    "v_mfma_f32_16x16x32_bf16 v[224:227], a[204:207], v[124:127], v[224:227]\n\t"
    "ds_read_b64_tr_b16 a[170:171], v19 offset:1280\n\t"
    "v_add_f32_e32 v34, v241, v34\n\t"
    "v_mfma_f32_16x16x32_bf16 v[228:231], a[208:211], v[112:115], v[52:55]\n\t"
    "v_exp_f32_e32 v242, v242\n\t"
    "v_mfma_f32_16x16x32_bf16 v[228:231], a[212:215], v[116:119], v[228:231]\n\t"
    "ds_read_b64_tr_b16 a[172:173], v21 offset:256\n\t"
    "v_add_f32_e32 v34, v242, v34\n\t"
    "v_mfma_f32_16x16x32_bf16 v[228:231], a[216:219], v[120:123], v[228:231]\n\t"
    "v_exp_f32_e32 v243, v243\n\t"
    "v_mfma_f32_16x16x32_bf16 v[228:231], a[220:223], v[124:127], v[228:231]\n\t"
    "ds_read_b64_tr_b16 a[174:175], v21 offset:1280\n\t"
    "v_add_f32_e32 v34, v243, v34\n\t"
    "v_mfma_f32_16x16x32_bf16 a[32:35], a[128:131], v[232:235], a[32:35]\n\t"
    "v_exp_f32_e32 v244, v244\n\t"
    "v_mfma_f32_16x16x32_bf16 a[36:39], a[132:135], v[232:235], a[36:39]\n\t"
    "v_add_f32_e32 v34, v244, v34\n\t"
    "v_cvt_pk_bf16_f32 v240, v240, v241\n\t"
    "v_mfma_f32_16x16x32_bf16 a[40:43], a[136:139], v[232:235], a[40:43]\n\t"
    "v_exp_f32_e32 v245, v245\n\t"
    "v_mfma_f32_16x16x32_bf16 a[44:47], a[140:143], v[232:235], a[44:47]\n\t"
    "v_add_f32_e32 v34, v245, v34\n\t"
    "v_cvt_pk_bf16_f32 v241, v242, v243\n\t"
    "v_mfma_f32_16x16x32_bf16 a[48:51], a[144:147], v[232:235], a[48:51]\n\t"
    "buffer_load_dwordx4 v16, s[44:47], s85 offen offset:1024 nt lds\n\t"
    "v_exp_f32_e32 v246, v246\n\t"
    "v_mfma_f32_16x16x32_bf16 a[52:55], a[148:151], v[232:235], a[52:55]\n\t"
    "v_add_f32_e32 v34, v246, v34\n\t"
    "v_cvt_pk_bf16_f32 v242, v244, v245\n\t"
    "v_mfma_f32_16x16x32_bf16 a[56:59], a[152:155], v[232:235], a[56:59]\n\t"
    "v_exp_f32_e32 v247, v247\n\t"
    "v_mfma_f32_16x16x32_bf16 a[60:63], a[156:159], v[232:235], a[60:63]\n\t"
    "v_add_f32_e32 v34, v247, v34\n\t"
    "v_cvt_pk_bf16_f32 v243, v246, v247\n\t"
    "v_mfma_f32_16x16x32_bf16 v[232:235], a[192:195], v[128:131], v[56:59]\n\t"
    "s_mov_b32 s84, 0x14000\n\t"
    "v_exp_f32_e32 v224, v224\n\t"
    "v_mfma_f32_16x16x32_bf16 v[232:235], a[196:199], v[132:135], v[232:235]\n\t"
    "ds_read_b64_tr_b16 a[176:177], v19 offset:512\n\t"
    "v_add_f32_e32 v35, v224, v35\n\t"
    "v_mfma_f32_16x16x32_bf16 v[232:235], a[200:203], v[136:139], v[232:235]\n\t"
    "v_exp_f32_e32 v225, v225\n\t"
    "v_mfma_f32_16x16x32_bf16 v[232:235], a[204:207], v[140:143], v[232:235]\n\t"
    "ds_read_b64_tr_b16 a[178:179], v19 offset:1536\n\t"
    "v_add_f32_e32 v35, v225, v35\n\t"
    "v_mfma_f32_16x16x32_bf16 v[236:239], a[208:211], v[128:131], v[56:59]\n\t"
    "v_exp_f32_e32 v226, v226\n\t"
    "v_mfma_f32_16x16x32_bf16 v[236:239], a[212:215], v[132:135], v[236:239]\n\t"
    "ds_read_b64_tr_b16 a[180:181], v21 offset:512\n\t"
    "v_add_f32_e32 v35, v226, v35\n\t"
    "v_mfma_f32_16x16x32_bf16 v[236:239], a[216:219], v[136:139], v[236:239]\n\t"
    "v_exp_f32_e32 v227, v227\n\t"
    "v_mfma_f32_16x16x32_bf16 v[236:239], a[220:223], v[140:143], v[236:239]\n\t"
    "ds_read_b64_tr_b16 a[182:183], v21 offset:1536\n\t"
    "v_add_f32_e32 v35, v227, v35\n\t"
    "v_mfma_f32_16x16x32_bf16 a[64:67], a[128:131], v[240:243], a[64:67]\n\t"
    "v_exp_f32_e32 v228, v228\n\t"
    "v_mfma_f32_16x16x32_bf16 a[68:71], a[132:135], v[240:243], a[68:71]\n\t"
    "v_add_f32_e32 v35, v228, v35\n\t"
    "v_cvt_pk_bf16_f32 v224, v224, v225\n\t"
    "v_mfma_f32_16x16x32_bf16 a[72:75], a[136:139], v[240:243], a[72:75]\n\t"
    "v_exp_f32_e32 v229, v229\n\t"
    "v_mfma_f32_16x16x32_bf16 a[76:79], a[140:143], v[240:243], a[76:79]\n\t"
    "s_add_u32 m0, s65, 0x4000\n\t"
    "v_add_f32_e32 v35, v229, v35\n\t"
    "v_cvt_pk_bf16_f32 v225, v226, v227\n\t"
    "v_mfma_f32_16x16x32_bf16 a[80:83], a[144:147], v[240:243], a[80:83]\n\t"
    "buffer_load_dwordx4 v16, s[48:51], s84 offen nt lds\n\t"
    "v_exp_f32_e32 v230, v230\n\t"
    "v_mfma_f32_16x16x32_bf16 a[84:87], a[148:151], v[240:243], a[84:87]\n\t"
    "v_add_f32_e32 v35, v230, v35\n\t"
    "v_cvt_pk_bf16_f32 v226, v228, v229\n\t"
    "v_mfma_f32_16x16x32_bf16 a[88:91], a[152:155], v[240:243], a[88:91]\n\t"
    "v_exp_f32_e32 v231, v231\n\t"
    "v_mfma_f32_16x16x32_bf16 a[92:95], a[156:159], v[240:243], a[92:95]\n\t"
    "v_add_f32_e32 v35, v231, v35\n\t"
    "v_cvt_pk_bf16_f32 v227, v230, v231\n\t"
    "v_mfma_f32_16x16x32_bf16 v[240:243], a[192:195], v[144:147], v[60:63]\n\t"
    "v_exp_f32_e32 v232, v232\n\t"
    "v_mfma_f32_16x16x32_bf16 v[240:243], a[196:199], v[148:151], v[240:243]\n\t"
    "ds_read_b64_tr_b16 a[184:185], v19 offset:768\n\t"
    "v_add_f32_e32 v36, v232, v36\n\t"
    "v_mfma_f32_16x16x32_bf16 v[240:243], a[200:203], v[152:155], v[240:243]\n\t"
    "v_exp_f32_e32 v233, v233\n\t"
    "v_mfma_f32_16x16x32_bf16 v[240:243], a[204:207], v[156:159], v[240:243]\n\t"
    "ds_read_b64_tr_b16 a[186:187], v19 offset:1792\n\t"
    "v_add_f32_e32 v36, v233, v36\n\t"
    "v_mfma_f32_16x16x32_bf16 v[244:247], a[208:211], v[144:147], v[60:63]\n\t"
    "v_exp_f32_e32 v234, v234\n\t"
    "v_mfma_f32_16x16x32_bf16 v[244:247], a[212:215], v[148:151], v[244:247]\n\t"
    "ds_read_b64_tr_b16 a[188:189], v21 offset:768\n\t"
    "v_add_f32_e32 v36, v234, v36\n\t"
    "v_mfma_f32_16x16x32_bf16 v[244:247], a[216:219], v[152:155], v[244:247]\n\t"
    "v_exp_f32_e32 v235, v235\n\t"
    "v_mfma_f32_16x16x32_bf16 v[244:247], a[220:223], v[156:159], v[244:247]\n\t"
    "ds_read_b64_tr_b16 a[190:191], v21 offset:1792\n\t"
    "v_add_f32_e32 v36, v235, v36\n\t"
    "v_mfma_f32_16x16x32_bf16 a[96:99], a[128:131], v[224:227], a[96:99]\n\t"
    "v_exp_f32_e32 v236, v236\n\t"
    "v_mfma_f32_16x16x32_bf16 a[100:103], a[132:135], v[224:227], a[100:103]\n\t"
    "s_memrealtime s[30:31]\n\t"
    "v_add_f32_e32 v36, v236, v36\n\t"
    "v_cvt_pk_bf16_f32 v232, v232, v233\n\t"
    "v_mfma_f32_16x16x32_bf16 a[104:107], a[136:139], v[224:227], a[104:107]\n\t"
    "buffer_load_dwordx4 v16, s[48:51], s84 offen offset:1024 nt lds\n\t"
    "v_exp_f32_e32 v237, v237\n\t"
    "v_mfma_f32_16x16x32_bf16 a[108:111], a[140:143], v[224:227], a[108:111]\n\t"
    "v_add_f32_e32 v36, v237, v36\n\t"
    "v_cvt_pk_bf16_f32 v233, v234, v235\n\t"
    "v_mfma_f32_16x16x32_bf16 a[112:115], a[144:147], v[224:227], a[112:115]\n\t"
    "v_exp_f32_e32 v238, v238\n\t"
    "v_mfma_f32_16x16x32_bf16 a[116:119], a[148:151], v[224:227], a[116:119]\n\t"
    "v_add_f32_e32 v36, v238, v36\n\t"
    "v_cvt_pk_bf16_f32 v234, v236, v237\n\t"
    "v_mfma_f32_16x16x32_bf16 a[120:123], a[152:155], v[224:227], a[120:123]\n\t"
    "s_waitcnt vmcnt(15) lgkmcnt(0)\n\t"
    "v_exp_f32_e32 v239, v239\n\t"
    "v_mfma_f32_16x16x32_bf16 a[124:127], a[156:159], v[224:227], a[124:127]\n\t"
    "s_barrier\n\t"
    "v_add_f32_e32 v36, v239, v36\n\t"
    "v_cvt_pk_bf16_f32 v235, v238, v239\n\t"
    "s_sub_i32 s31, s29, s30\n\t"
    "s_cmp_gt_i32 s31, 0\n\t"
    "s_cselect_b32 s29, s29, s30\n\t"
    "s_cbranch_scc0 .Lfa_pa41e\n\t"
    "s_min_i32 s31, s31, 0xc8\n\t"
    ".Lfa_pa41:\n\t"
    "s_sub_u32 s31, s31, 2\n\t"
    "s_cmp_gt_i32 s31, 0\n\t"
    "s_cbranch_scc1 .Lfa_pa41\n\t"
    ".Lfa_pa41e:\n\t"
    "s_add_u32 s29, s29, 0x71\n\t"
    "v_mfma_f32_16x16x32_bf16 v[224:227], a[224:227], v[64:67], v[40:43]\n\t"
    "s_mov_b32 s85, 0x34000\n\t"
    "v_exp_f32_e32 v240, v240\n\t"
    "v_mfma_f32_16x16x32_bf16 v[224:227], a[228:231], v[68:71], v[224:227]\n\t"
    "ds_read_b128 a[192:195], v17 offset:32768\n\t"
    "v_add_f32_e32 v37, v240, v37\n\t"
    "v_mfma_f32_16x16x32_bf16 v[224:227], a[232:235], v[72:75], v[224:227]\n\t"
    "v_exp_f32_e32 v241, v241\n\t"
    "v_mfma_f32_16x16x32_bf16 v[224:227], a[236:239], v[76:79], v[224:227]\n\t"
    "ds_read_b128 a[196:199], v17 offset:33024\n\t"
    "v_add_f32_e32 v37, v241, v37\n\t"
    "v_mfma_f32_16x16x32_bf16 v[228:231], a[240:243], v[64:67], v[40:43]\n\t"
    "v_exp_f32_e32 v242, v242\n\t"
    "v_mfma_f32_16x16x32_bf16 v[228:231], a[244:247], v[68:71], v[228:231]\n\t"
    "ds_read_b128 a[200:203], v17 offset:33280\n\t"
    "v_add_f32_e32 v37, v242, v37\n\t"
    "v_mfma_f32_16x16x32_bf16 v[228:231], a[248:251], v[72:75], v[228:231]\n\t"
    "v_exp_f32_e32 v243, v243\n\t"
    "v_mfma_f32_16x16x32_bf16 v[228:231], a[252:255], v[76:79], v[228:231]\n\t"
    "ds_read_b128 a[204:207], v17 offset:33536\n\t"
    "v_add_f32_e32 v37, v243, v37\n\t"
    "v_mfma_f32_16x16x32_bf16 v[160:163], a[128:131], v[232:235], v[160:163]\n\t"
    "v_exp_f32_e32 v244, v244\n\t"
    "v_mfma_f32_16x16x32_bf16 v[164:167], a[132:135], v[232:235], v[164:167]\n\t"
    "s_add_u32 m0, s65, 0x6000\n\t"
    "v_add_f32_e32 v37, v244, v37\n\t"
    "v_cvt_pk_bf16_f32 v240, v240, v241\n\t"
    "v_mfma_f32_16x16x32_bf16 v[168:171], a[136:139], v[232:235], v[168:171]\n\t"
    "buffer_load_dwordx4 v16, s[48:51], s85 offen nt lds\n\t"
    "v_exp_f32_e32 v245, v245\n\t"
    "v_mfma_f32_16x16x32_bf16 v[172:175], a[140:143], v[232:235], v[172:175]\n\t"
    "v_add_f32_e32 v37, v245, v37\n\t"
    "v_cvt_pk_bf16_f32 v241, v242, v243\n\t"
    "v_mfma_f32_16x16x32_bf16 v[176:179], a[144:147], v[232:235], v[176:179]\n\t"
    "v_exp_f32_e32 v246, v246\n\t"
    "v_mfma_f32_16x16x32_bf16 v[180:183], a[148:151], v[232:235], v[180:183]\n\t"
    "v_add_f32_e32 v37, v246, v37\n\t"
    "v_cvt_pk_bf16_f32 v242, v244, v245\n\t"
    "v_mfma_f32_16x16x32_bf16 v[184:187], a[152:155], v[232:235], v[184:187]\n\t"
    "buffer_load_dwordx4 v16, s[48:51], s85 offen offset:1024 nt lds\n\t"
    "v_exp_f32_e32 v247, v247\n\t"
    "v_mfma_f32_16x16x32_bf16 v[188:191], a[156:159], v[232:235], v[188:191]\n\t"
    "v_add_f32_e32 v37, v247, v37\n\t"
    "v_cvt_pk_bf16_f32 v243, v246, v247\n\t"
    "v_mfma_f32_16x16x32_bf16 v[232:235], a[224:227], v[80:83], v[44:47]\n\t"
    "s_mov_b32 s84, 0x16000\n\t"
    "v_exp_f32_e32 v224, v224\n\t"
    "v_mfma_f32_16x16x32_bf16 v[232:235], a[228:231], v[84:87], v[232:235]\n\t"
    "ds_read_b128 a[208:211], v17 offset:33792\n\t"
    "v_add_f32_e32 v32, v224, v32\n\t"
    "v_mfma_f32_16x16x32_bf16 v[232:235], a[232:235], v[88:91], v[232:235]\n\t"
    "v_exp_f32_e32 v225, v225\n\t"
    "v_mfma_f32_16x16x32_bf16 v[232:235], a[236:239], v[92:95], v[232:235]\n\t"
    "ds_read_b128 a[212:215], v17 offset:34048\n\t"
    "v_add_f32_e32 v32, v225, v32\n\t"
    "v_mfma_f32_16x16x32_bf16 v[236:239], a[240:243], v[80:83], v[44:47]\n\t"
    "v_exp_f32_e32 v226, v226\n\t"
    "v_mfma_f32_16x16x32_bf16 v[236:239], a[244:247], v[84:87], v[236:239]\n\t"
    "ds_read_b128 a[216:219], v17 offset:34304\n\t"
    "v_add_f32_e32 v32, v226, v32\n\t"
    "v_mfma_f32_16x16x32_bf16 v[236:239], a[248:251], v[88:91], v[236:239]\n\t"
    "v_exp_f32_e32 v227, v227\n\t"
    "v_mfma_f32_16x16x32_bf16 v[236:239], a[252:255], v[92:95], v[236:239]\n\t"
    "ds_read_b128 a[220:223], v17 offset:34560\n\t"
    "v_add_f32_e32 v32, v227, v32\n\t"
    "v_mfma_f32_16x16x32_bf16 v[192:195], a[128:131], v[240:243], v[192:195]\n\t"
    "v_exp_f32_e32 v228, v228\n\t"
    "v_mfma_f32_16x16x32_bf16 v[196:199], a[132:135], v[240:243], v[196:199]\n\t"
    "v_add_f32_e32 v32, v228, v32\n\t"
    "v_cvt_pk_bf16_f32 v224, v224, v225\n\t"
    "v_mfma_f32_16x16x32_bf16 v[200:203], a[136:139], v[240:243], v[200:203]\n\t"
    "v_exp_f32_e32 v229, v229\n\t"
    "v_mfma_f32_16x16x32_bf16 v[204:207], a[140:143], v[240:243], v[204:207]\n\t"
    "s_add_u32 m0, s65, 0x8000\n\t"
    "v_add_f32_e32 v32, v229, v32\n\t"
    "v_cvt_pk_bf16_f32 v225, v226, v227\n\t"
    "v_mfma_f32_16x16x32_bf16 v[208:211], a[144:147], v[240:243], v[208:211]\n\t"
    "buffer_load_dwordx4 v16, s[44:47], s84 offen nt lds\n\t"
    "v_exp_f32_e32 v230, v230\n\t"
    "v_mfma_f32_16x16x32_bf16 v[212:215], a[148:151], v[240:243], v[212:215]\n\t"
    "v_add_f32_e32 v32, v230, v32\n\t"
    "v_cvt_pk_bf16_f32 v226, v228, v229\n\t"
    "v_mfma_f32_16x16x32_bf16 v[216:219], a[152:155], v[240:243], v[216:219]\n\t"
    "v_exp_f32_e32 v231, v231\n\t"
    "v_mfma_f32_16x16x32_bf16 v[220:223], a[156:159], v[240:243], v[220:223]\n\t"
    "v_add_f32_e32 v32, v231, v32\n\t"
    "v_cvt_pk_bf16_f32 v227, v230, v231\n\t"
    "v_mfma_f32_16x16x32_bf16 v[240:243], a[224:227], v[96:99], v[48:51]\n\t"
    "s_add_u32 s88, s88, 0x80000\n\t"
    "v_exp_f32_e32 v232, v232\n\t"
    "v_mfma_f32_16x16x32_bf16 v[240:243], a[228:231], v[100:103], v[240:243]\n\t"
    "ds_read_b64_tr_b16 a[128:129], v19 offset:32768\n\t"
    "v_add_f32_e32 v33, v232, v33\n\t"
    "v_mfma_f32_16x16x32_bf16 v[240:243], a[232:235], v[104:107], v[240:243]\n\t"
    "s_and_b32 s88, s88, 0x3fffff\n\t"
    "v_exp_f32_e32 v233, v233\n\t"
    "v_mfma_f32_16x16x32_bf16 v[240:243], a[236:239], v[108:111], v[240:243]\n\t"
    "ds_read_b64_tr_b16 a[130:131], v19 offset:33792\n\t"
    "v_add_f32_e32 v33, v233, v33\n\t"
    "v_mfma_f32_16x16x32_bf16 v[244:247], a[240:243], v[96:99], v[48:51]\n\t"
    "v_exp_f32_e32 v234, v234\n\t"
    "v_mfma_f32_16x16x32_bf16 v[244:247], a[244:247], v[100:103], v[244:247]\n\t"
    "ds_read_b64_tr_b16 a[132:133], v21 offset:32768\n\t"
    "v_add_f32_e32 v33, v234, v33\n\t"
    "v_mfma_f32_16x16x32_bf16 v[244:247], a[248:251], v[104:107], v[244:247]\n\t"
    "v_exp_f32_e32 v235, v235\n\t"
    "v_mfma_f32_16x16x32_bf16 v[244:247], a[252:255], v[108:111], v[244:247]\n\t"
    "ds_read_b64_tr_b16 a[134:135], v21 offset:33792\n\t"
    "v_add_f32_e32 v33, v235, v33\n\t"
    "v_mfma_f32_16x16x32_bf16 a[0:3], a[160:163], v[224:227], a[0:3]\n\t"
    "buffer_load_dwordx4 v16, s[44:47], s84 offen offset:1024 nt lds\n\t"
    "v_exp_f32_e32 v236, v236\n\t"
    "v_mfma_f32_16x16x32_bf16 a[4:7], a[164:167], v[224:227], a[4:7]\n\t"
    "v_add_f32_e32 v33, v236, v33\n\t"
    "v_cvt_pk_bf16_f32 v232, v232, v233\n\t"
    "v_mfma_f32_16x16x32_bf16 a[8:11], a[168:171], v[224:227], a[8:11]\n\t"
    "v_exp_f32_e32 v237, v237\n\t"
    "v_mfma_f32_16x16x32_bf16 a[12:15], a[172:175], v[224:227], a[12:15]\n\t"
    "v_add_f32_e32 v33, v237, v33\n\t"
    "v_cvt_pk_bf16_f32 v233, v234, v235\n\t"
    "v_mfma_f32_16x16x32_bf16 a[16:19], a[176:179], v[224:227], a[16:19]\n\t"
    "v_exp_f32_e32 v238, v238\n\t"
    "v_mfma_f32_16x16x32_bf16 a[20:23], a[180:183], v[224:227], a[20:23]\n\t"
    "v_add_f32_e32 v33, v238, v33\n\t"
    "v_cvt_pk_bf16_f32 v234, v236, v237\n\t"
    "v_mfma_f32_16x16x32_bf16 a[24:27], a[184:187], v[224:227], a[24:27]\n\t"
    "buffer_load_dword v15, v25, s[56:59], s88 offen\n\t"
    "v_exp_f32_e32 v239, v239\n\t"
    "v_mfma_f32_16x16x32_bf16 a[28:31], a[188:191], v[224:227], a[28:31]\n\t"
    "v_add_f32_e32 v33, v239, v33\n\t"
    "v_cvt_pk_bf16_f32 v235, v238, v239\n\t"
    "v_mfma_f32_16x16x32_bf16 v[224:227], a[224:227], v[112:115], v[52:55]\n\t"
    "s_mov_b32 s85, 0x36000\n\t"
    "v_exp_f32_e32 v240, v240\n\t"
    "v_mfma_f32_16x16x32_bf16 v[224:227], a[228:231], v[116:119], v[224:227]\n\t"
    "ds_read_b64_tr_b16 a[136:137], v19 offset:33024\n\t"
    "v_add_f32_e32 v34, v240, v34\n\t"
    "v_mfma_f32_16x16x32_bf16 v[224:227], a[232:235], v[120:123], v[224:227]\n\t"
    "v_exp_f32_e32 v241, v241\n\t"
    "v_mfma_f32_16x16x32_bf16 v[224:227], a[236:239], v[124:127], v[224:227]\n\t"
    "ds_read_b64_tr_b16 a[138:139], v19 offset:34048\n\t"
    "v_add_f32_e32 v34, v241, v34\n\t"
    "v_mfma_f32_16x16x32_bf16 v[228:231], a[240:243], v[112:115], v[52:55]\n\t"
    "v_exp_f32_e32 v242, v242\n\t"
    "v_mfma_f32_16x16x32_bf16 v[228:231], a[244:247], v[116:119], v[228:231]\n\t"
    "ds_read_b64_tr_b16 a[140:141], v21 offset:33024\n\t"
    "v_add_f32_e32 v34, v242, v34\n\t"
    "v_mfma_f32_16x16x32_bf16 v[228:231], a[248:251], v[120:123], v[228:231]\n\t"
    "v_exp_f32_e32 v243, v243\n\t"
    "v_mfma_f32_16x16x32_bf16 v[228:231], a[252:255], v[124:127], v[228:231]\n\t"
    "ds_read_b64_tr_b16 a[142:143], v21 offset:34048\n\t"
    "v_add_f32_e32 v34, v243, v34\n\t"
    "v_mfma_f32_16x16x32_bf16 a[32:35], a[160:163], v[232:235], a[32:35]\n\t"
    "v_exp_f32_e32 v244, v244\n\t"
    "v_mfma_f32_16x16x32_bf16 a[36:39], a[164:167], v[232:235], a[36:39]\n\t"
    "v_add_f32_e32 v34, v244, v34\n\t"
    "v_cvt_pk_bf16_f32 v240, v240, v241\n\t"
    "v_mfma_f32_16x16x32_bf16 a[40:43], a[168:171], v[232:235], a[40:43]\n\t"
    "v_exp_f32_e32 v245, v245\n\t"
    "v_mfma_f32_16x16x32_bf16 a[44:47], a[172:175], v[232:235], a[44:47]\n\t"
    "s_add_u32 m0, s65, 0xa000\n\t"
    "v_add_f32_e32 v34, v245, v34\n\t"
    "v_cvt_pk_bf16_f32 v241, v242, v243\n\t"
    "v_mfma_f32_16x16x32_bf16 a[48:51], a[176:179], v[232:235], a[48:51]\n\t"
    "buffer_load_dwordx4 v16, s[44:47], s85 offen nt lds\n\t"
    "v_exp_f32_e32 v246, v246\n\t"
    "v_mfma_f32_16x16x32_bf16 a[52:55], a[180:183], v[232:235], a[52:55]\n\t"
    "v_add_f32_e32 v34, v246, v34\n\t"
    "v_cvt_pk_bf16_f32 v242, v244, v245\n\t"
    "v_mfma_f32_16x16x32_bf16 a[56:59], a[184:187], v[232:235], a[56:59]\n\t"
    "v_exp_f32_e32 v247, v247\n\t"
    "v_mfma_f32_16x16x32_bf16 a[60:63], a[188:191], v[232:235], a[60:63]\n\t"
    "v_add_f32_e32 v34, v247, v34\n\t"
    "v_cvt_pk_bf16_f32 v243, v246, v247\n\t"
    "v_mfma_f32_16x16x32_bf16 v[232:235], a[224:227], v[128:131], v[56:59]\n\t"
    "s_mov_b32 s84, 0x16000\n\t"
    "v_exp_f32_e32 v224, v224\n\t"
    "v_mfma_f32_16x16x32_bf16 v[232:235], a[228:231], v[132:135], v[232:235]\n\t"
    "ds_read_b64_tr_b16 a[144:145], v19 offset:33280\n\t"
    "v_add_f32_e32 v35, v224, v35\n\t"
    "v_mfma_f32_16x16x32_bf16 v[232:235], a[232:235], v[136:139], v[232:235]\n\t"
    "v_exp_f32_e32 v225, v225\n\t"
    "v_mfma_f32_16x16x32_bf16 v[232:235], a[236:239], v[140:143], v[232:235]\n\t"
    "ds_read_b64_tr_b16 a[146:147], v19 offset:34304\n\t"
    "v_add_f32_e32 v35, v225, v35\n\t"
    "v_mfma_f32_16x16x32_bf16 v[236:239], a[240:243], v[128:131], v[56:59]\n\t"
    "v_exp_f32_e32 v226, v226\n\t"
    "v_mfma_f32_16x16x32_bf16 v[236:239], a[244:247], v[132:135], v[236:239]\n\t"
    "ds_read_b64_tr_b16 a[148:149], v21 offset:33280\n\t"
    "v_add_f32_e32 v35, v226, v35\n\t"
    "v_mfma_f32_16x16x32_bf16 v[236:239], a[248:251], v[136:139], v[236:239]\n\t"
    "v_exp_f32_e32 v227, v227\n\t"
    "v_mfma_f32_16x16x32_bf16 v[236:239], a[252:255], v[140:143], v[236:239]\n\t"
    "ds_read_b64_tr_b16 a[150:151], v21 offset:34304\n\t"
    "v_add_f32_e32 v35, v227, v35\n\t"
    "v_mfma_f32_16x16x32_bf16 a[64:67], a[160:163], v[240:243], a[64:67]\n\t"
    "buffer_load_dwordx4 v16, s[44:47], s85 offen offset:1024 nt lds\n\t"
    "v_exp_f32_e32 v228, v228\n\t"
    "v_mfma_f32_16x16x32_bf16 a[68:71], a[164:167], v[240:243], a[68:71]\n\t"
    "v_add_f32_e32 v35, v228, v35\n\t"
    "v_cvt_pk_bf16_f32 v224, v224, v225\n\t"
    "v_mfma_f32_16x16x32_bf16 a[72:75], a[168:171], v[240:243], a[72:75]\n\t"
    "v_exp_f32_e32 v229, v229\n\t"
    "v_mfma_f32_16x16x32_bf16 a[76:79], a[172:175], v[240:243], a[76:79]\n\t"
    "v_add_f32_e32 v35, v229, v35\n\t"
    "v_cvt_pk_bf16_f32 v225, v226, v227\n\t"
    "v_mfma_f32_16x16x32_bf16 a[80:83], a[176:179], v[240:243], a[80:83]\n\t"
    "v_exp_f32_e32 v230, v230\n\t"
    "v_mfma_f32_16x16x32_bf16 a[84:87], a[180:183], v[240:243], a[84:87]\n\t"
    "s_add_u32 m0, s65, 0xc000\n\t"
    "v_add_f32_e32 v35, v230, v35\n\t"
    "v_cvt_pk_bf16_f32 v226, v228, v229\n\t"
    "v_mfma_f32_16x16x32_bf16 a[88:91], a[184:187], v[240:243], a[88:91]\n\t"
    "buffer_load_dwordx4 v16, s[48:51], s84 offen nt lds\n\t"
    "v_exp_f32_e32 v231, v231\n\t"
    "v_mfma_f32_16x16x32_bf16 a[92:95], a[188:191], v[240:243], a[92:95]\n\t"
    "v_add_f32_e32 v35, v231, v35\n\t"
    "v_cvt_pk_bf16_f32 v227, v230, v231\n\t"
    "v_mfma_f32_16x16x32_bf16 v[240:243], a[224:227], v[144:147], v[60:63]\n\t"
    "v_exp_f32_e32 v232, v232\n\t"
    "v_mfma_f32_16x16x32_bf16 v[240:243], a[228:231], v[148:151], v[240:243]\n\t"
    "ds_read_b64_tr_b16 a[152:153], v19 offset:33536\n\t"
    "v_add_f32_e32 v36, v232, v36\n\t"
    "v_mfma_f32_16x16x32_bf16 v[240:243], a[232:235], v[152:155], v[240:243]\n\t"
    "v_exp_f32_e32 v233, v233\n\t"
    "v_mfma_f32_16x16x32_bf16 v[240:243], a[236:239], v[156:159], v[240:243]\n\t"
    "ds_read_b64_tr_b16 a[154:155], v19 offset:34560\n\t"
    "v_add_f32_e32 v36, v233, v36\n\t"
    "v_mfma_f32_16x16x32_bf16 v[244:247], a[240:243], v[144:147], v[60:63]\n\t"
    "v_exp_f32_e32 v234, v234\n\t"
    "v_mfma_f32_16x16x32_bf16 v[244:247], a[244:247], v[148:151], v[244:247]\n\t"
    "ds_read_b64_tr_b16 a[156:157], v21 offset:33536\n\t"
    "v_add_f32_e32 v36, v234, v36\n\t"
    "v_mfma_f32_16x16x32_bf16 v[244:247], a[248:251], v[152:155], v[244:247]\n\t"
    "v_exp_f32_e32 v235, v235\n\t"
    "v_mfma_f32_16x16x32_bf16 v[244:247], a[252:255], v[156:159], v[244:247]\n\t"
    "ds_read_b64_tr_b16 a[158:159], v21 offset:34560\n\t"
    "v_add_f32_e32 v36, v235, v36\n\t"
    "v_mfma_f32_16x16x32_bf16 a[96:99], a[160:163], v[224:227], a[96:99]\n\t"
    "v_exp_f32_e32 v236, v236\n\t"
    "v_mfma_f32_16x16x32_bf16 a[100:103], a[164:167], v[224:227], a[100:103]\n\t"
    "s_memrealtime s[30:31]\n\t"
    "v_add_f32_e32 v36, v236, v36\n\t"
    "v_cvt_pk_bf16_f32 v232, v232, v233\n\t"
    "v_mfma_f32_16x16x32_bf16 a[104:107], a[168:171], v[224:227], a[104:107]\n\t"
    "buffer_load_dwordx4 v16, s[48:51], s84 offen offset:1024 nt lds\n\t"
    "v_exp_f32_e32 v237, v237\n\t"
    "v_mfma_f32_16x16x32_bf16 a[108:111], a[172:175], v[224:227], a[108:111]\n\t"
    "v_add_f32_e32 v36, v237, v36\n\t"
    "v_cvt_pk_bf16_f32 v233, v234, v235\n\t"
    "v_mfma_f32_16x16x32_bf16 a[112:115], a[176:179], v[224:227], a[112:115]\n\t"
    "v_exp_f32_e32 v238, v238\n\t"
    "v_mfma_f32_16x16x32_bf16 a[116:119], a[180:183], v[224:227], a[116:119]\n\t"
    "v_add_f32_e32 v36, v238, v36\n\t"
    "v_cvt_pk_bf16_f32 v234, v236, v237\n\t"
    "v_mfma_f32_16x16x32_bf16 a[120:123], a[184:187], v[224:227], a[120:123]\n\t"
    "s_waitcnt vmcnt(16) lgkmcnt(0)\n\t"
    "v_exp_f32_e32 v239, v239\n\t"
    "v_mfma_f32_16x16x32_bf16 a[124:127], a[188:191], v[224:227], a[124:127]\n\t"
    "s_barrier\n\t"
    "v_add_f32_e32 v36, v239, v36\n\t"
    "v_cvt_pk_bf16_f32 v235, v238, v239\n\t"
    "s_sub_i32 s31, s29, s30\n\t"
    "s_cmp_gt_i32 s31, 0\n\t"
    "s_cselect_b32 s29, s29, s30\n\t"
    "s_cbranch_scc0 .Lfa_pa47e\n\t"
    "s_min_i32 s31, s31, 0xc8\n\t"
    ".Lfa_pa47:\n\t"
    "s_sub_u32 s31, s31, 2\n\t"
    "s_cmp_gt_i32 s31, 0\n\t"
    "s_cbranch_scc1 .Lfa_pa47\n\t"
    ".Lfa_pa47e:\n\t"
    "s_add_u32 s29, s29, 0x71\n\t"
    "v_mfma_f32_16x16x32_bf16 v[224:227], a[192:195], v[64:67], v[40:43]\n\t"
    "s_mov_b32 s85, 0x36000\n\t"
    "v_exp_f32_e32 v240, v240\n\t"
    "v_mfma_f32_16x16x32_bf16 v[224:227], a[196:199], v[68:71], v[224:227]\n\t"
    "ds_read_b128 a[224:227], v18 offset:0\n\t"
    "v_add_f32_e32 v37, v240, v37\n\t"
    "v_mfma_f32_16x16x32_bf16 v[224:227], a[200:203], v[72:75], v[224:227]\n\t"
    "v_exp_f32_e32 v241, v241\n\t"
    "v_mfma_f32_16x16x32_bf16 v[224:227], a[204:207], v[76:79], v[224:227]\n\t"
    "ds_read_b128 a[228:231], v18 offset:256\n\t"
    "v_add_f32_e32 v37, v241, v37\n\t"
    "v_mfma_f32_16x16x32_bf16 v[228:231], a[208:211], v[64:67], v[40:43]\n\t"
    "v_exp_f32_e32 v242, v242\n\t"
    "v_mfma_f32_16x16x32_bf16 v[228:231], a[212:215], v[68:71], v[228:231]\n\t"
    "ds_read_b128 a[232:235], v18 offset:512\n\t"
    "v_add_f32_e32 v37, v242, v37\n\t"
    "v_mfma_f32_16x16x32_bf16 v[228:231], a[216:219], v[72:75], v[228:231]\n\t"
    "v_exp_f32_e32 v243, v243\n\t"
    "v_mfma_f32_16x16x32_bf16 v[228:231], a[220:223], v[76:79], v[228:231]\n\t"
    "ds_read_b128 a[236:239], v18 offset:768\n\t"
    "v_add_f32_e32 v37, v243, v37\n\t"
    "v_mfma_f32_16x16x32_bf16 v[160:163], a[160:163], v[232:235], v[160:163]\n\t"
    "v_exp_f32_e32 v244, v244\n\t"
    "v_mfma_f32_16x16x32_bf16 v[164:167], a[164:167], v[232:235], v[164:167]\n\t"
    "s_add_u32 m0, s65, 0xe000\n\t"
    "v_add_f32_e32 v37, v244, v37\n\t"
    "v_cvt_pk_bf16_f32 v240, v240, v241\n\t"
    "v_mfma_f32_16x16x32_bf16 v[168:171], a[168:171], v[232:235], v[168:171]\n\t"
    "buffer_load_dwordx4 v16, s[48:51], s85 offen nt lds\n\t"
    "v_exp_f32_e32 v245, v245\n\t"
    "v_mfma_f32_16x16x32_bf16 v[172:175], a[172:175], v[232:235], v[172:175]\n\t"
    "v_add_f32_e32 v37, v245, v37\n\t"
    "v_cvt_pk_bf16_f32 v241, v242, v243\n\t"
    "v_mfma_f32_16x16x32_bf16 v[176:179], a[176:179], v[232:235], v[176:179]\n\t"
    "v_exp_f32_e32 v246, v246\n\t"
    "v_mfma_f32_16x16x32_bf16 v[180:183], a[180:183], v[232:235], v[180:183]\n\t"
    "v_add_f32_e32 v37, v246, v37\n\t"
    "v_cvt_pk_bf16_f32 v242, v244, v245\n\t"
    "v_mfma_f32_16x16x32_bf16 v[184:187], a[184:187], v[232:235], v[184:187]\n\t"
    "v_exp_f32_e32 v247, v247\n\t"
    "v_mfma_f32_16x16x32_bf16 v[188:191], a[188:191], v[232:235], v[188:191]\n\t"
    "v_add_f32_e32 v37, v247, v37\n\t"
    "v_cvt_pk_bf16_f32 v243, v246, v247\n\t"
    "v_mfma_f32_16x16x32_bf16 v[232:235], a[192:195], v[80:83], v[44:47]\n\t"
    "v_exp_f32_e32 v224, v224\n\t"
    "v_mfma_f32_16x16x32_bf16 v[232:235], a[196:199], v[84:87], v[232:235]\n\t"
    "ds_read_b128 a[240:243], v18 offset:1024\n\t"
    "v_add_f32_e32 v32, v224, v32\n\t"
    "v_mfma_f32_16x16x32_bf16 v[232:235], a[200:203], v[88:91], v[232:235]\n\t"
    "v_exp_f32_e32 v225, v225\n\t"
    "v_mfma_f32_16x16x32_bf16 v[232:235], a[204:207], v[92:95], v[232:235]\n\t"
    "ds_read_b128 a[244:247], v18 offset:1280\n\t"
    "v_add_f32_e32 v32, v225, v32\n\t"
    "v_mfma_f32_16x16x32_bf16 v[236:239], a[208:211], v[80:83], v[44:47]\n\t"
    "v_exp_f32_e32 v226, v226\n\t"
    "v_mfma_f32_16x16x32_bf16 v[236:239], a[212:215], v[84:87], v[236:239]\n\t"
    "ds_read_b128 a[248:251], v18 offset:1536\n\t"
    "v_add_f32_e32 v32, v226, v32\n\t"
    "v_mfma_f32_16x16x32_bf16 v[236:239], a[216:219], v[88:91], v[236:239]\n\t"
    "v_exp_f32_e32 v227, v227\n\t"
    "v_mfma_f32_16x16x32_bf16 v[236:239], a[220:223], v[92:95], v[236:239]\n\t"
    "ds_read_b128 a[252:255], v18 offset:1792\n\t"
    "v_add_f32_e32 v32, v227, v32\n\t"
    "v_mfma_f32_16x16x32_bf16 v[192:195], a[160:163], v[240:243], v[192:195]\n\t"
    "v_exp_f32_e32 v228, v228\n\t"
    "v_mfma_f32_16x16x32_bf16 v[196:199], a[164:167], v[240:243], v[196:199]\n\t"
    "v_add_f32_e32 v32, v228, v32\n\t"
    "v_cvt_pk_bf16_f32 v224, v224, v225\n\t"
    "v_mfma_f32_16x16x32_bf16 v[200:203], a[168:171], v[240:243], v[200:203]\n\t"
    "buffer_load_dwordx4 v16, s[48:51], s85 offen offset:1024 nt lds\n\t"
    "v_exp_f32_e32 v229, v229\n\t"
    "v_mfma_f32_16x16x32_bf16 v[204:207], a[172:175], v[240:243], v[204:207]\n\t"
    "v_add_f32_e32 v32, v229, v32\n\t"
    "v_cvt_pk_bf16_f32 v225, v226, v227\n\t"
    "v_mfma_f32_16x16x32_bf16 v[208:211], a[176:179], v[240:243], v[208:211]\n\t"
    "v_exp_f32_e32 v230, v230\n\t"
    "v_mfma_f32_16x16x32_bf16 v[212:215], a[180:183], v[240:243], v[212:215]\n\t"
    "v_add_f32_e32 v32, v230, v32\n\t"
    "v_cvt_pk_bf16_f32 v226, v228, v229\n\t"
    "v_mfma_f32_16x16x32_bf16 v[216:219], a[184:187], v[240:243], v[216:219]\n\t"
    "v_exp_f32_e32 v231, v231\n\t"
    "v_mfma_f32_16x16x32_bf16 v[220:223], a[188:191], v[240:243], v[220:223]\n\t"
    "v_add_f32_e32 v32, v231, v32\n\t"
    "v_cvt_pk_bf16_f32 v227, v230, v231\n\t"
    "v_mfma_f32_16x16x32_bf16 v[240:243], a[192:195], v[96:99], v[48:51]\n\t"
    "s_mov_b32 s84, 0x18000\n\t"
    "v_exp_f32_e32 v232, v232\n\t"
    "v_mfma_f32_16x16x32_bf16 v[240:243], a[196:199], v[100:103], v[240:243]\n\t"
    "ds_read_b64_tr_b16 a[160:161], v20 offset:0\n\t"
    "v_add_f32_e32 v33, v232, v33\n\t"
    "v_mfma_f32_16x16x32_bf16 v[240:243], a[200:203], v[104:107], v[240:243]\n\t"
    "v_exp_f32_e32 v233, v233\n\t"
    "v_mfma_f32_16x16x32_bf16 v[240:243], a[204:207], v[108:111], v[240:243]\n\t"
    "ds_read_b64_tr_b16 a[162:163], v20 offset:1024\n\t"
    "v_add_f32_e32 v33, v233, v33\n\t"
    "v_mfma_f32_16x16x32_bf16 v[244:247], a[208:211], v[96:99], v[48:51]\n\t"
    "v_exp_f32_e32 v234, v234\n\t"
    "v_mfma_f32_16x16x32_bf16 v[244:247], a[212:215], v[100:103], v[244:247]\n\t"
    "ds_read_b64_tr_b16 a[164:165], v24 offset:0\n\t"
    "v_add_f32_e32 v33, v234, v33\n\t"
    "v_mfma_f32_16x16x32_bf16 v[244:247], a[216:219], v[104:107], v[244:247]\n\t"
    "v_exp_f32_e32 v235, v235\n\t"
    "v_mfma_f32_16x16x32_bf16 v[244:247], a[220:223], v[108:111], v[244:247]\n\t"
    "ds_read_b64_tr_b16 a[166:167], v24 offset:1024\n\t"
    "v_add_f32_e32 v33, v235, v33\n\t"
    "v_mfma_f32_16x16x32_bf16 a[0:3], a[128:131], v[224:227], a[0:3]\n\t"
    "v_exp_f32_e32 v236, v236\n\t"
    "v_mfma_f32_16x16x32_bf16 a[4:7], a[132:135], v[224:227], a[4:7]\n\t"
    "s_add_u32 m0, s65, 0x10000\n\t"
    "v_add_f32_e32 v33, v236, v33\n\t"
    "v_cvt_pk_bf16_f32 v232, v232, v233\n\t"
    "v_mfma_f32_16x16x32_bf16 a[8:11], a[136:139], v[224:227], a[8:11]\n\t"
    "buffer_load_dwordx4 v16, s[44:47], s84 offen nt lds\n\t"
    "v_exp_f32_e32 v237, v237\n\t"
    "v_mfma_f32_16x16x32_bf16 a[12:15], a[140:143], v[224:227], a[12:15]\n\t"
    "v_add_f32_e32 v33, v237, v33\n\t"
    "v_cvt_pk_bf16_f32 v233, v234, v235\n\t"
    "v_mfma_f32_16x16x32_bf16 a[16:19], a[144:147], v[224:227], a[16:19]\n\t"
    "v_exp_f32_e32 v238, v238\n\t"
    "v_mfma_f32_16x16x32_bf16 a[20:23], a[148:151], v[224:227], a[20:23]\n\t"
    "v_add_f32_e32 v33, v238, v33\n\t"
    "v_cvt_pk_bf16_f32 v234, v236, v237\n\t"
    "v_mfma_f32_16x16x32_bf16 a[24:27], a[152:155], v[224:227], a[24:27]\n\t"
    "v_exp_f32_e32 v239, v239\n\t"
    "v_mfma_f32_16x16x32_bf16 a[28:31], a[156:159], v[224:227], a[28:31]\n\t"
    "v_add_f32_e32 v33, v239, v33\n\t"
    "v_cvt_pk_bf16_f32 v235, v238, v239\n\t"
    "v_mfma_f32_16x16x32_bf16 v[224:227], a[192:195], v[112:115], v[52:55]\n\t"
    "v_exp_f32_e32 v240, v240\n\t"
    "v_mfma_f32_16x16x32_bf16 v[224:227], a[196:199], v[116:119], v[224:227]\n\t"
    "ds_read_b64_tr_b16 a[168:169], v20 offset:256\n\t"
    "v_add_f32_e32 v34, v240, v34\n\t"
    "v_mfma_f32_16x16x32_bf16 v[224:227], a[200:203], v[120:123], v[224:227]\n\t"
    "v_exp_f32_e32 v241, v241\n\t"
    "v_mfma_f32_16x16x32_bf16 v[224:227], a[204:207], v[124:127], v[224:227]\n\t"
    "ds_read_b64_tr_b16 a[170:171], v20 offset:1280\n\t"
    "v_add_f32_e32 v34, v241, v34\n\t"
    "v_mfma_f32_16x16x32_bf16 v[228:231], a[208:211], v[112:115], v[52:55]\n\t"
    "v_exp_f32_e32 v242, v242\n\t"
    "v_mfma_f32_16x16x32_bf16 v[228:231], a[212:215], v[116:119], v[228:231]\n\t"
    "ds_read_b64_tr_b16 a[172:173], v24 offset:256\n\t"
    "v_add_f32_e32 v34, v242, v34\n\t"
    "v_mfma_f32_16x16x32_bf16 v[228:231], a[216:219], v[120:123], v[228:231]\n\t"
    "v_exp_f32_e32 v243, v243\n\t"
    "v_mfma_f32_16x16x32_bf16 v[228:231], a[220:223], v[124:127], v[228:231]\n\t"
    "ds_read_b64_tr_b16 a[174:175], v24 offset:1280\n\t"
    "v_add_f32_e32 v34, v243, v34\n\t"
    "v_mfma_f32_16x16x32_bf16 a[32:35], a[128:131], v[232:235], a[32:35]\n\t"
    "v_exp_f32_e32 v244, v244\n\t"
    "v_mfma_f32_16x16x32_bf16 a[36:39], a[132:135], v[232:235], a[36:39]\n\t"
    "v_add_f32_e32 v34, v244, v34\n\t"
    "v_cvt_pk_bf16_f32 v240, v240, v241\n\t"
    "v_mfma_f32_16x16x32_bf16 a[40:43], a[136:139], v[232:235], a[40:43]\n\t"
    "buffer_load_dwordx4 v16, s[44:47], s84 offen offset:1024 nt lds\n\t"
    "v_exp_f32_e32 v245, v245\n\t"
    "v_mfma_f32_16x16x32_bf16 a[44:47], a[140:143], v[232:235], a[44:47]\n\t"
    "v_add_f32_e32 v34, v245, v34\n\t"
    "v_cvt_pk_bf16_f32 v241, v242, v243\n\t"
    "v_mfma_f32_16x16x32_bf16 a[48:51], a[144:147], v[232:235], a[48:51]\n\t"
    "v_exp_f32_e32 v246, v246\n\t"
    "v_mfma_f32_16x16x32_bf16 a[52:55], a[148:151], v[232:235], a[52:55]\n\t"
    "v_add_f32_e32 v34, v246, v34\n\t"
    "v_cvt_pk_bf16_f32 v242, v244, v245\n\t"
    "v_mfma_f32_16x16x32_bf16 a[56:59], a[152:155], v[232:235], a[56:59]\n\t"
    "v_exp_f32_e32 v247, v247\n\t"
    "v_mfma_f32_16x16x32_bf16 a[60:63], a[156:159], v[232:235], a[60:63]\n\t"
    "v_add_f32_e32 v34, v247, v34\n\t"
    "v_cvt_pk_bf16_f32 v243, v246, v247\n\t"
    "v_mfma_f32_16x16x32_bf16 v[232:235], a[192:195], v[128:131], v[56:59]\n\t"
    "s_mov_b32 s85, 0x38000\n\t"
    "v_exp_f32_e32 v224, v224\n\t"
    "v_mfma_f32_16x16x32_bf16 v[232:235], a[196:199], v[132:135], v[232:235]\n\t"
    "ds_read_b64_tr_b16 a[176:177], v20 offset:512\n\t"
    "v_add_f32_e32 v35, v224, v35\n\t"
    "v_mfma_f32_16x16x32_bf16 v[232:235], a[200:203], v[136:139], v[232:235]\n\t"
    "v_exp_f32_e32 v225, v225\n\t"
    "v_mfma_f32_16x16x32_bf16 v[232:235], a[204:207], v[140:143], v[232:235]\n\t"
    "ds_read_b64_tr_b16 a[178:179], v20 offset:1536\n\t"
    "v_add_f32_e32 v35, v225, v35\n\t"
    "v_mfma_f32_16x16x32_bf16 v[236:239], a[208:211], v[128:131], v[56:59]\n\t"
    "v_exp_f32_e32 v226, v226\n\t"
    "v_mfma_f32_16x16x32_bf16 v[236:239], a[212:215], v[132:135], v[236:239]\n\t"
    "ds_read_b64_tr_b16 a[180:181], v24 offset:512\n\t"
    "v_add_f32_e32 v35, v226, v35\n\t"
    "v_mfma_f32_16x16x32_bf16 v[236:239], a[216:219], v[136:139], v[236:239]\n\t"
    "v_exp_f32_e32 v227, v227\n\t"
    "v_mfma_f32_16x16x32_bf16 v[236:239], a[220:223], v[140:143], v[236:239]\n\t"
    "ds_read_b64_tr_b16 a[182:183], v24 offset:1536\n\t"
    "v_add_f32_e32 v35, v227, v35\n\t"
    "v_mfma_f32_16x16x32_bf16 a[64:67], a[128:131], v[240:243], a[64:67]\n\t"
    "v_exp_f32_e32 v228, v228\n\t"
    "v_mfma_f32_16x16x32_bf16 a[68:71], a[132:135], v[240:243], a[68:71]\n\t"
    "s_add_u32 m0, s65, 0x12000\n\t"
    "v_add_f32_e32 v35, v228, v35\n\t"
    "v_cvt_pk_bf16_f32 v224, v224, v225\n\t"
    "v_mfma_f32_16x16x32_bf16 a[72:75], a[136:139], v[240:243], a[72:75]\n\t"
    "buffer_load_dwordx4 v16, s[44:47], s85 offen nt lds\n\t"
    "v_exp_f32_e32 v229, v229\n\t"
    "v_mfma_f32_16x16x32_bf16 a[76:79], a[140:143], v[240:243], a[76:79]\n\t"
    "v_add_f32_e32 v35, v229, v35\n\t"
    "v_cvt_pk_bf16_f32 v225, v226, v227\n\t"
    "v_mfma_f32_16x16x32_bf16 a[80:83], a[144:147], v[240:243], a[80:83]\n\t"
    "v_exp_f32_e32 v230, v230\n\t"
    "v_mfma_f32_16x16x32_bf16 a[84:87], a[148:151], v[240:243], a[84:87]\n\t"
    "v_add_f32_e32 v35, v230, v35\n\t"
    "v_cvt_pk_bf16_f32 v226, v228, v229\n\t"
    "v_mfma_f32_16x16x32_bf16 a[88:91], a[152:155], v[240:243], a[88:91]\n\t"
    "v_exp_f32_e32 v231, v231\n\t"
    "v_mfma_f32_16x16x32_bf16 a[92:95], a[156:159], v[240:243], a[92:95]\n\t"
    "v_add_f32_e32 v35, v231, v35\n\t"
    "v_cvt_pk_bf16_f32 v227, v230, v231\n\t"
    "v_mfma_f32_16x16x32_bf16 v[240:243], a[192:195], v[144:147], v[60:63]\n\t"
    "v_exp_f32_e32 v232, v232\n\t"
    "v_mfma_f32_16x16x32_bf16 v[240:243], a[196:199], v[148:151], v[240:243]\n\t"
    "ds_read_b64_tr_b16 a[184:185], v20 offset:768\n\t"
    "v_add_f32_e32 v36, v232, v36\n\t"
    "v_mfma_f32_16x16x32_bf16 v[240:243], a[200:203], v[152:155], v[240:243]\n\t"
    "v_exp_f32_e32 v233, v233\n\t"
    "v_mfma_f32_16x16x32_bf16 v[240:243], a[204:207], v[156:159], v[240:243]\n\t"
    "ds_read_b64_tr_b16 a[186:187], v20 offset:1792\n\t"
    "v_add_f32_e32 v36, v233, v36\n\t"
    "v_mfma_f32_16x16x32_bf16 v[244:247], a[208:211], v[144:147], v[60:63]\n\t"
    "v_exp_f32_e32 v234, v234\n\t"
    "v_mfma_f32_16x16x32_bf16 v[244:247], a[212:215], v[148:151], v[244:247]\n\t"
    "ds_read_b64_tr_b16 a[188:189], v24 offset:768\n\t"
    "v_add_f32_e32 v36, v234, v36\n\t"
    "v_mfma_f32_16x16x32_bf16 v[244:247], a[216:219], v[152:155], v[244:247]\n\t"
    "v_exp_f32_e32 v235, v235\n\t"
    "v_mfma_f32_16x16x32_bf16 v[244:247], a[220:223], v[156:159], v[244:247]\n\t"
    "ds_read_b64_tr_b16 a[190:191], v24 offset:1792\n\t"
    "v_add_f32_e32 v36, v235, v36\n\t"
    "v_mfma_f32_16x16x32_bf16 a[96:99], a[128:131], v[224:227], a[96:99]\n\t"
    "v_exp_f32_e32 v236, v236\n\t"
    "v_mfma_f32_16x16x32_bf16 a[100:103], a[132:135], v[224:227], a[100:103]\n\t"
    "s_memrealtime s[30:31]\n\t"
    "v_add_f32_e32 v36, v236, v36\n\t"
    "v_cvt_pk_bf16_f32 v232, v232, v233\n\t"
    "v_mfma_f32_16x16x32_bf16 a[104:107], a[136:139], v[224:227], a[104:107]\n\t"
    "buffer_load_dwordx4 v16, s[44:47], s85 offen offset:1024 nt lds\n\t"
    "v_exp_f32_e32 v237, v237\n\t"
    "v_mfma_f32_16x16x32_bf16 a[108:111], a[140:143], v[224:227], a[108:111]\n\t"
    "v_add_f32_e32 v36, v237, v36\n\t"
    "v_cvt_pk_bf16_f32 v233, v234, v235\n\t"
    "v_mfma_f32_16x16x32_bf16 a[112:115], a[144:147], v[224:227], a[112:115]\n\t"
    "v_exp_f32_e32 v238, v238\n\t"
    "v_mfma_f32_16x16x32_bf16 a[116:119], a[148:151], v[224:227], a[116:119]\n\t"
    "v_add_f32_e32 v36, v238, v36\n\t"
    "v_cvt_pk_bf16_f32 v234, v236, v237\n\t"
    "v_mfma_f32_16x16x32_bf16 a[120:123], a[152:155], v[224:227], a[120:123]\n\t"
    "s_waitcnt vmcnt(13) lgkmcnt(0)\n\t"
    "v_exp_f32_e32 v239, v239\n\t"
    "v_mfma_f32_16x16x32_bf16 a[124:127], a[156:159], v[224:227], a[124:127]\n\t"
    "s_barrier\n\t"
    "v_add_f32_e32 v36, v239, v36\n\t"
    "v_cvt_pk_bf16_f32 v235, v238, v239\n\t"
    "s_sub_i32 s31, s29, s30\n\t"
    "s_cmp_gt_i32 s31, 0\n\t"
    "s_cselect_b32 s29, s29, s30\n\t"
    "s_cbranch_scc0 .Lfa_pa53e\n\t"
    "s_min_i32 s31, s31, 0xc8\n\t"
    ".Lfa_pa53:\n\t"
    "s_sub_u32 s31, s31, 2\n\t"
    "s_cmp_gt_i32 s31, 0\n\t"
    "s_cbranch_scc1 .Lfa_pa53\n\t"
    ".Lfa_pa53e:\n\t"
    "s_add_u32 s29, s29, 0x71\n\t"
    "v_mfma_f32_16x16x32_bf16 v[224:227], a[224:227], v[64:67], v[40:43]\n\t"
    "s_mov_b32 s84, 0x18000\n\t"
    "v_exp_f32_e32 v240, v240\n\t"
    "v_mfma_f32_16x16x32_bf16 v[224:227], a[228:231], v[68:71], v[224:227]\n\t"
    "ds_read_b128 a[192:195], v30 offset:0\n\t"
    "v_add_f32_e32 v37, v240, v37\n\t"
    "v_mfma_f32_16x16x32_bf16 v[224:227], a[232:235], v[72:75], v[224:227]\n\t"
    "v_exp_f32_e32 v241, v241\n\t"
    "v_mfma_f32_16x16x32_bf16 v[224:227], a[236:239], v[76:79], v[224:227]\n\t"
    "ds_read_b128 a[196:199], v30 offset:256\n\t"
    "v_add_f32_e32 v37, v241, v37\n\t"
    "v_mfma_f32_16x16x32_bf16 v[228:231], a[240:243], v[64:67], v[40:43]\n\t"
    "v_exp_f32_e32 v242, v242\n\t"
    "v_mfma_f32_16x16x32_bf16 v[228:231], a[244:247], v[68:71], v[228:231]\n\t"
    "ds_read_b128 a[200:203], v30 offset:512\n\t"
    "v_add_f32_e32 v37, v242, v37\n\t"
    "v_mfma_f32_16x16x32_bf16 v[228:231], a[248:251], v[72:75], v[228:231]\n\t"
    "v_exp_f32_e32 v243, v243\n\t"
    "v_mfma_f32_16x16x32_bf16 v[228:231], a[252:255], v[76:79], v[228:231]\n\t"
    "ds_read_b128 a[204:207], v30 offset:768\n\t"
    "v_add_f32_e32 v37, v243, v37\n\t"
    "v_mfma_f32_16x16x32_bf16 v[160:163], a[128:131], v[232:235], v[160:163]\n\t"
    "v_exp_f32_e32 v244, v244\n\t"
    "v_mfma_f32_16x16x32_bf16 v[164:167], a[132:135], v[232:235], v[164:167]\n\t"
    "s_add_u32 m0, s65, 0x14000\n\t"
    "v_add_f32_e32 v37, v244, v37\n\t"
    "v_cvt_pk_bf16_f32 v240, v240, v241\n\t"
    "v_mfma_f32_16x16x32_bf16 v[168:171], a[136:139], v[232:235], v[168:171]\n\t"
    "buffer_load_dwordx4 v16, s[48:51], s84 offen nt lds\n\t"
    "v_exp_f32_e32 v245, v245\n\t"
    "v_mfma_f32_16x16x32_bf16 v[172:175], a[140:143], v[232:235], v[172:175]\n\t"
    "v_add_f32_e32 v37, v245, v37\n\t"
    "v_cvt_pk_bf16_f32 v241, v242, v243\n\t"
    "v_mfma_f32_16x16x32_bf16 v[176:179], a[144:147], v[232:235], v[176:179]\n\t"
    "v_exp_f32_e32 v246, v246\n\t"
    "v_mfma_f32_16x16x32_bf16 v[180:183], a[148:151], v[232:235], v[180:183]\n\t"
    "v_add_f32_e32 v37, v246, v37\n\t"
    "v_cvt_pk_bf16_f32 v242, v244, v245\n\t"
    "v_mfma_f32_16x16x32_bf16 v[184:187], a[152:155], v[232:235], v[184:187]\n\t"
    "buffer_load_dwordx4 v16, s[48:51], s84 offen offset:1024 nt lds\n\t"
    "v_exp_f32_e32 v247, v247\n\t"
    "v_mfma_f32_16x16x32_bf16 v[188:191], a[156:159], v[232:235], v[188:191]\n\t"
    "v_add_f32_e32 v37, v247, v37\n\t"
    "v_cvt_pk_bf16_f32 v243, v246, v247\n\t"
    "v_mfma_f32_16x16x32_bf16 v[232:235], a[224:227], v[80:83], v[44:47]\n\t"
    "s_mov_b32 s85, 0x38000\n\t"
    "v_exp_f32_e32 v224, v224\n\t"
    "v_mfma_f32_16x16x32_bf16 v[232:235], a[228:231], v[84:87], v[232:235]\n\t"
    "ds_read_b128 a[208:211], v30 offset:1024\n\t"
    "v_add_f32_e32 v32, v224, v32\n\t"
    "v_mfma_f32_16x16x32_bf16 v[232:235], a[232:235], v[88:91], v[232:235]\n\t"
    "v_exp_f32_e32 v225, v225\n\t"
    "v_mfma_f32_16x16x32_bf16 v[232:235], a[236:239], v[92:95], v[232:235]\n\t"
    "ds_read_b128 a[212:215], v30 offset:1280\n\t"
    "v_add_f32_e32 v32, v225, v32\n\t"
    "v_mfma_f32_16x16x32_bf16 v[236:239], a[240:243], v[80:83], v[44:47]\n\t"
    "v_exp_f32_e32 v226, v226\n\t"
    "v_mfma_f32_16x16x32_bf16 v[236:239], a[244:247], v[84:87], v[236:239]\n\t"
    "ds_read_b128 a[216:219], v30 offset:1536\n\t"
    "v_add_f32_e32 v32, v226, v32\n\t"
    "v_mfma_f32_16x16x32_bf16 v[236:239], a[248:251], v[88:91], v[236:239]\n\t"
    "v_exp_f32_e32 v227, v227\n\t"
    "v_mfma_f32_16x16x32_bf16 v[236:239], a[252:255], v[92:95], v[236:239]\n\t"
    "ds_read_b128 a[220:223], v30 offset:1792\n\t"
    "v_add_f32_e32 v32, v227, v32\n\t"
    "v_mfma_f32_16x16x32_bf16 v[192:195], a[128:131], v[240:243], v[192:195]\n\t"
    "v_exp_f32_e32 v228, v228\n\t"
    "v_mfma_f32_16x16x32_bf16 v[196:199], a[132:135], v[240:243], v[196:199]\n\t"
    "v_add_f32_e32 v32, v228, v32\n\t"
    "v_cvt_pk_bf16_f32 v224, v224, v225\n\t"
    "v_mfma_f32_16x16x32_bf16 v[200:203], a[136:139], v[240:243], v[200:203]\n\t"
    "v_exp_f32_e32 v229, v229\n\t"
    "v_mfma_f32_16x16x32_bf16 v[204:207], a[140:143], v[240:243], v[204:207]\n\t"
    "s_add_u32 m0, s65, 0x16000\n\t"
    "v_add_f32_e32 v32, v229, v32\n\t"
    "v_cvt_pk_bf16_f32 v225, v226, v227\n\t"
    "v_mfma_f32_16x16x32_bf16 v[208:211], a[144:147], v[240:243], v[208:211]\n\t"
    "buffer_load_dwordx4 v16, s[48:51], s85 offen nt lds\n\t"
    "v_exp_f32_e32 v230, v230\n\t"
    "v_mfma_f32_16x16x32_bf16 v[212:215], a[148:151], v[240:243], v[212:215]\n\t"
    "v_add_f32_e32 v32, v230, v32\n\t"
    "v_cvt_pk_bf16_f32 v226, v228, v229\n\t"
    "v_mfma_f32_16x16x32_bf16 v[216:219], a[152:155], v[240:243], v[216:219]\n\t"
    "v_exp_f32_e32 v231, v231\n\t"
    "v_mfma_f32_16x16x32_bf16 v[220:223], a[156:159], v[240:243], v[220:223]\n\t"
    "v_add_f32_e32 v32, v231, v32\n\t"
    "v_cvt_pk_bf16_f32 v227, v230, v231\n\t"
    "v_mfma_f32_16x16x32_bf16 v[240:243], a[224:227], v[96:99], v[48:51]\n\t"
    "s_add_u32 s88, s88, 0x80000\n\t"
    "v_exp_f32_e32 v232, v232\n\t"
    "v_mfma_f32_16x16x32_bf16 v[240:243], a[228:231], v[100:103], v[240:243]\n\t"
    "ds_read_b64_tr_b16 a[128:129], v22 offset:0\n\t"
    "v_add_f32_e32 v33, v232, v33\n\t"
    "v_mfma_f32_16x16x32_bf16 v[240:243], a[232:235], v[104:107], v[240:243]\n\t"
    "s_and_b32 s88, s88, 0x3fffff\n\t"
    "v_exp_f32_e32 v233, v233\n\t"
    "v_mfma_f32_16x16x32_bf16 v[240:243], a[236:239], v[108:111], v[240:243]\n\t"
    "ds_read_b64_tr_b16 a[130:131], v22 offset:1024\n\t"
    "v_add_f32_e32 v33, v233, v33\n\t"
    "v_mfma_f32_16x16x32_bf16 v[244:247], a[240:243], v[96:99], v[48:51]\n\t"
    "v_exp_f32_e32 v234, v234\n\t"
    "v_mfma_f32_16x16x32_bf16 v[244:247], a[244:247], v[100:103], v[244:247]\n\t"
    "ds_read_b64_tr_b16 a[132:133], v23 offset:0\n\t"
    "v_add_f32_e32 v33, v234, v33\n\t"
    "v_mfma_f32_16x16x32_bf16 v[244:247], a[248:251], v[104:107], v[244:247]\n\t"
    "v_exp_f32_e32 v235, v235\n\t"
    "v_mfma_f32_16x16x32_bf16 v[244:247], a[252:255], v[108:111], v[244:247]\n\t"
    "ds_read_b64_tr_b16 a[134:135], v23 offset:1024\n\t"
    "v_add_f32_e32 v33, v235, v33\n\t"
    "v_mfma_f32_16x16x32_bf16 a[0:3], a[160:163], v[224:227], a[0:3]\n\t"
    "buffer_load_dwordx4 v16, s[48:51], s85 offen offset:1024 nt lds\n\t"
    "v_exp_f32_e32 v236, v236\n\t"
    "v_mfma_f32_16x16x32_bf16 a[4:7], a[164:167], v[224:227], a[4:7]\n\t"
    "v_add_f32_e32 v33, v236, v33\n\t"
    "v_cvt_pk_bf16_f32 v232, v232, v233\n\t"
    "v_mfma_f32_16x16x32_bf16 a[8:11], a[168:171], v[224:227], a[8:11]\n\t"
    "v_exp_f32_e32 v237, v237\n\t"
    "v_mfma_f32_16x16x32_bf16 a[12:15], a[172:175], v[224:227], a[12:15]\n\t"
    "v_add_f32_e32 v33, v237, v33\n\t"
    "v_cvt_pk_bf16_f32 v233, v234, v235\n\t"
    "v_mfma_f32_16x16x32_bf16 a[16:19], a[176:179], v[224:227], a[16:19]\n\t"
    "v_exp_f32_e32 v238, v238\n\t"
    "v_mfma_f32_16x16x32_bf16 a[20:23], a[180:183], v[224:227], a[20:23]\n\t"
    "v_add_f32_e32 v33, v238, v33\n\t"
    "v_cvt_pk_bf16_f32 v234, v236, v237\n\t"
    "v_mfma_f32_16x16x32_bf16 a[24:27], a[184:187], v[224:227], a[24:27]\n\t"
    "buffer_load_dword v15, v25, s[56:59], s88 offen\n\t"
    "v_exp_f32_e32 v239, v239\n\t"
    "v_mfma_f32_16x16x32_bf16 a[28:31], a[188:191], v[224:227], a[28:31]\n\t"
    "v_add_f32_e32 v33, v239, v33\n\t"
    "v_cvt_pk_bf16_f32 v235, v238, v239\n\t"
    "v_mfma_f32_16x16x32_bf16 v[224:227], a[224:227], v[112:115], v[52:55]\n\t"
    "s_mov_b32 s84, 0x1a000\n\t"
    "v_exp_f32_e32 v240, v240\n\t"
    "v_mfma_f32_16x16x32_bf16 v[224:227], a[228:231], v[116:119], v[224:227]\n\t"
    "ds_read_b64_tr_b16 a[136:137], v22 offset:256\n\t"
    "v_add_f32_e32 v34, v240, v34\n\t"
    "v_mfma_f32_16x16x32_bf16 v[224:227], a[232:235], v[120:123], v[224:227]\n\t"
    "v_exp_f32_e32 v241, v241\n\t"
    "v_mfma_f32_16x16x32_bf16 v[224:227], a[236:239], v[124:127], v[224:227]\n\t"
    "ds_read_b64_tr_b16 a[138:139], v22 offset:1280\n\t"
    "v_add_f32_e32 v34, v241, v34\n\t"
    "v_mfma_f32_16x16x32_bf16 v[228:231], a[240:243], v[112:115], v[52:55]\n\t"
    "v_exp_f32_e32 v242, v242\n\t"
    "v_mfma_f32_16x16x32_bf16 v[228:231], a[244:247], v[116:119], v[228:231]\n\t"
    "ds_read_b64_tr_b16 a[140:141], v23 offset:256\n\t"
    "v_add_f32_e32 v34, v242, v34\n\t"
    "v_mfma_f32_16x16x32_bf16 v[228:231], a[248:251], v[120:123], v[228:231]\n\t"
    "v_exp_f32_e32 v243, v243\n\t"
    "v_mfma_f32_16x16x32_bf16 v[228:231], a[252:255], v[124:127], v[228:231]\n\t"
    "ds_read_b64_tr_b16 a[142:143], v23 offset:1280\n\t"
    "v_add_f32_e32 v34, v243, v34\n\t"
    "v_mfma_f32_16x16x32_bf16 a[32:35], a[160:163], v[232:235], a[32:35]\n\t"
    "v_exp_f32_e32 v244, v244\n\t"
    "v_mfma_f32_16x16x32_bf16 a[36:39], a[164:167], v[232:235], a[36:39]\n\t"
    "v_add_f32_e32 v34, v244, v34\n\t"
    "v_cvt_pk_bf16_f32 v240, v240, v241\n\t"
    "v_mfma_f32_16x16x32_bf16 a[40:43], a[168:171], v[232:235], a[40:43]\n\t"
    "v_exp_f32_e32 v245, v245\n\t"
    "v_mfma_f32_16x16x32_bf16 a[44:47], a[172:175], v[232:235], a[44:47]\n\t"
    "s_add_u32 m0, s65, 0x18000\n\t"
    "v_add_f32_e32 v34, v245, v34\n\t"
    "v_cvt_pk_bf16_f32 v241, v242, v243\n\t"
    "v_mfma_f32_16x16x32_bf16 a[48:51], a[176:179], v[232:235], a[48:51]\n\t"
    "buffer_load_dwordx4 v16, s[44:47], s84 offen nt lds\n\t"
    "v_exp_f32_e32 v246, v246\n\t"
    "v_mfma_f32_16x16x32_bf16 a[52:55], a[180:183], v[232:235], a[52:55]\n\t"
    "v_add_f32_e32 v34, v246, v34\n\t"
    "v_cvt_pk_bf16_f32 v242, v244, v245\n\t"
    "v_mfma_f32_16x16x32_bf16 a[56:59], a[184:187], v[232:235], a[56:59]\n\t"
    "v_exp_f32_e32 v247, v247\n\t"
    "v_mfma_f32_16x16x32_bf16 a[60:63], a[188:191], v[232:235], a[60:63]\n\t"
    "v_add_f32_e32 v34, v247, v34\n\t"
    "v_cvt_pk_bf16_f32 v243, v246, v247\n\t"
    "v_mfma_f32_16x16x32_bf16 v[232:235], a[224:227], v[128:131], v[56:59]\n\t"
    "s_mov_b32 s85, 0x3a000\n\t"
    "v_exp_f32_e32 v224, v224\n\t"
    "v_mfma_f32_16x16x32_bf16 v[232:235], a[228:231], v[132:135], v[232:235]\n\t"
    "ds_read_b64_tr_b16 a[144:145], v22 offset:512\n\t"
    "v_add_f32_e32 v35, v224, v35\n\t"
    "v_mfma_f32_16x16x32_bf16 v[232:235], a[232:235], v[136:139], v[232:235]\n\t"
    "v_exp_f32_e32 v225, v225\n\t"
    "v_mfma_f32_16x16x32_bf16 v[232:235], a[236:239], v[140:143], v[232:235]\n\t"
    "ds_read_b64_tr_b16 a[146:147], v22 offset:1536\n\t"
    "v_add_f32_e32 v35, v225, v35\n\t"
    "v_mfma_f32_16x16x32_bf16 v[236:239], a[240:243], v[128:131], v[56:59]\n\t"
    "v_exp_f32_e32 v226, v226\n\t"
    "v_mfma_f32_16x16x32_bf16 v[236:239], a[244:247], v[132:135], v[236:239]\n\t"
    "ds_read_b64_tr_b16 a[148:149], v23 offset:512\n\t"
    "v_add_f32_e32 v35, v226, v35\n\t"
    "v_mfma_f32_16x16x32_bf16 v[236:239], a[248:251], v[136:139], v[236:239]\n\t"
    "v_exp_f32_e32 v227, v227\n\t"
    "v_mfma_f32_16x16x32_bf16 v[236:239], a[252:255], v[140:143], v[236:239]\n\t"
    "ds_read_b64_tr_b16 a[150:151], v23 offset:1536\n\t"
    "v_add_f32_e32 v35, v227, v35\n\t"
    "v_mfma_f32_16x16x32_bf16 a[64:67], a[160:163], v[240:243], a[64:67]\n\t"
    "buffer_load_dwordx4 v16, s[44:47], s84 offen offset:1024 nt lds\n\t"
    "v_exp_f32_e32 v228, v228\n\t"
    "v_mfma_f32_16x16x32_bf16 a[68:71], a[164:167], v[240:243], a[68:71]\n\t"
    "v_add_f32_e32 v35, v228, v35\n\t"
    "v_cvt_pk_bf16_f32 v224, v224, v225\n\t"
    "v_mfma_f32_16x16x32_bf16 a[72:75], a[168:171], v[240:243], a[72:75]\n\t"
    "v_exp_f32_e32 v229, v229\n\t"
    "v_mfma_f32_16x16x32_bf16 a[76:79], a[172:175], v[240:243], a[76:79]\n\t"
    "v_add_f32_e32 v35, v229, v35\n\t"
    "v_cvt_pk_bf16_f32 v225, v226, v227\n\t"
    "v_mfma_f32_16x16x32_bf16 a[80:83], a[176:179], v[240:243], a[80:83]\n\t"
    "v_exp_f32_e32 v230, v230\n\t"
    "v_mfma_f32_16x16x32_bf16 a[84:87], a[180:183], v[240:243], a[84:87]\n\t"
    "s_add_u32 m0, s65, 0x1a000\n\t"
    "v_add_f32_e32 v35, v230, v35\n\t"
    "v_cvt_pk_bf16_f32 v226, v228, v229\n\t"
    "v_mfma_f32_16x16x32_bf16 a[88:91], a[184:187], v[240:243], a[88:91]\n\t"
    "buffer_load_dwordx4 v16, s[44:47], s85 offen nt lds\n\t"
    "v_exp_f32_e32 v231, v231\n\t"
    "v_mfma_f32_16x16x32_bf16 a[92:95], a[188:191], v[240:243], a[92:95]\n\t"
    "v_add_f32_e32 v35, v231, v35\n\t"
    "v_cvt_pk_bf16_f32 v227, v230, v231\n\t"
    "v_mfma_f32_16x16x32_bf16 v[240:243], a[224:227], v[144:147], v[60:63]\n\t"
    "v_exp_f32_e32 v232, v232\n\t"
    "v_mfma_f32_16x16x32_bf16 v[240:243], a[228:231], v[148:151], v[240:243]\n\t"
    "ds_read_b64_tr_b16 a[152:153], v22 offset:768\n\t"
    "v_add_f32_e32 v36, v232, v36\n\t"
    "v_mfma_f32_16x16x32_bf16 v[240:243], a[232:235], v[152:155], v[240:243]\n\t"
    "v_exp_f32_e32 v233, v233\n\t"
    "v_mfma_f32_16x16x32_bf16 v[240:243], a[236:239], v[156:159], v[240:243]\n\t"
    "ds_read_b64_tr_b16 a[154:155], v22 offset:1792\n\t"
    "v_add_f32_e32 v36, v233, v36\n\t"
    "v_mfma_f32_16x16x32_bf16 v[244:247], a[240:243], v[144:147], v[60:63]\n\t"
    "v_exp_f32_e32 v234, v234\n\t"
    "v_mfma_f32_16x16x32_bf16 v[244:247], a[244:247], v[148:151], v[244:247]\n\t"
    "ds_read_b64_tr_b16 a[156:157], v23 offset:768\n\t"
    "v_add_f32_e32 v36, v234, v36\n\t"
    "v_mfma_f32_16x16x32_bf16 v[244:247], a[248:251], v[152:155], v[244:247]\n\t"
    "v_exp_f32_e32 v235, v235\n\t"
    "v_mfma_f32_16x16x32_bf16 v[244:247], a[252:255], v[156:159], v[244:247]\n\t"
    "ds_read_b64_tr_b16 a[158:159], v23 offset:1792\n\t"
    "v_add_f32_e32 v36, v235, v36\n\t"
    "v_mfma_f32_16x16x32_bf16 a[96:99], a[160:163], v[224:227], a[96:99]\n\t"
    "v_exp_f32_e32 v236, v236\n\t"
    "v_mfma_f32_16x16x32_bf16 a[100:103], a[164:167], v[224:227], a[100:103]\n\t"
    "s_memrealtime s[30:31]\n\t"
    "v_add_f32_e32 v36, v236, v36\n\t"
    "v_cvt_pk_bf16_f32 v232, v232, v233\n\t"
    "v_mfma_f32_16x16x32_bf16 a[104:107], a[168:171], v[224:227], a[104:107]\n\t"
    "buffer_load_dwordx4 v16, s[44:47], s85 offen offset:1024 nt lds\n\t"
    "v_exp_f32_e32 v237, v237\n\t"
    "v_mfma_f32_16x16x32_bf16 a[108:111], a[172:175], v[224:227], a[108:111]\n\t"
    "v_add_f32_e32 v36, v237, v36\n\t"
    "v_cvt_pk_bf16_f32 v233, v234, v235\n\t"
    "v_mfma_f32_16x16x32_bf16 a[112:115], a[176:179], v[224:227], a[112:115]\n\t"
    "v_exp_f32_e32 v238, v238\n\t"
    "v_mfma_f32_16x16x32_bf16 a[116:119], a[180:183], v[224:227], a[116:119]\n\t"
    "v_add_f32_e32 v36, v238, v36\n\t"
    "v_cvt_pk_bf16_f32 v234, v236, v237\n\t"
    "v_mfma_f32_16x16x32_bf16 a[120:123], a[184:187], v[224:227], a[120:123]\n\t"
    "s_waitcnt vmcnt(13) lgkmcnt(0)\n\t"
    "v_exp_f32_e32 v239, v239\n\t"
    "v_mfma_f32_16x16x32_bf16 a[124:127], a[188:191], v[224:227], a[124:127]\n\t"
    "s_barrier\n\t"
    "v_add_f32_e32 v36, v239, v36\n\t"
    "v_cvt_pk_bf16_f32 v235, v238, v239\n\t"
    "s_sub_i32 s31, s29, s30\n\t"
    "s_cmp_gt_i32 s31, 0\n\t"
    "s_cselect_b32 s29, s29, s30\n\t"
    "s_cbranch_scc0 .Lfa_pa59e\n\t"
    "s_min_i32 s31, s31, 0xc8\n\t"
    ".Lfa_pa59:\n\t"
    "s_sub_u32 s31, s31, 2\n\t"
    "s_cmp_gt_i32 s31, 0\n\t"
    "s_cbranch_scc1 .Lfa_pa59\n\t"
    ".Lfa_pa59e:\n\t"
    "s_add_u32 s29, s29, 0x71\n\t"
    "v_mfma_f32_16x16x32_bf16 v[224:227], a[192:195], v[64:67], v[40:43]\n\t"
    "s_mov_b32 s84, 0x1a000\n\t"
    "v_exp_f32_e32 v240, v240\n\t"
    "v_mfma_f32_16x16x32_bf16 v[224:227], a[196:199], v[68:71], v[224:227]\n\t"
    "ds_read_b128 a[224:227], v30 offset:32768\n\t"
    "v_add_f32_e32 v37, v240, v37\n\t"
    "v_mfma_f32_16x16x32_bf16 v[224:227], a[200:203], v[72:75], v[224:227]\n\t"
    "v_exp_f32_e32 v241, v241\n\t"
    "v_mfma_f32_16x16x32_bf16 v[224:227], a[204:207], v[76:79], v[224:227]\n\t"
    "ds_read_b128 a[228:231], v30 offset:33024\n\t"
    "v_add_f32_e32 v37, v241, v37\n\t"
    "v_mfma_f32_16x16x32_bf16 v[228:231], a[208:211], v[64:67], v[40:43]\n\t"
    "v_exp_f32_e32 v242, v242\n\t"
    "v_mfma_f32_16x16x32_bf16 v[228:231], a[212:215], v[68:71], v[228:231]\n\t"
    "ds_read_b128 a[232:235], v30 offset:33280\n\t"
    "v_add_f32_e32 v37, v242, v37\n\t"
    "v_mfma_f32_16x16x32_bf16 v[228:231], a[216:219], v[72:75], v[228:231]\n\t"
    "v_exp_f32_e32 v243, v243\n\t"
    "v_mfma_f32_16x16x32_bf16 v[228:231], a[220:223], v[76:79], v[228:231]\n\t"
    "ds_read_b128 a[236:239], v30 offset:33536\n\t"
    "v_add_f32_e32 v37, v243, v37\n\t"
    "v_mfma_f32_16x16x32_bf16 v[160:163], a[160:163], v[232:235], v[160:163]\n\t"
    "v_exp_f32_e32 v244, v244\n\t"
    "v_mfma_f32_16x16x32_bf16 v[164:167], a[164:167], v[232:235], v[164:167]\n\t"
    "s_add_u32 m0, s65, 0x1c000\n\t"
    "v_add_f32_e32 v37, v244, v37\n\t"
    "v_cvt_pk_bf16_f32 v240, v240, v241\n\t"
    "v_mfma_f32_16x16x32_bf16 v[168:171], a[168:171], v[232:235], v[168:171]\n\t"
    "buffer_load_dwordx4 v16, s[48:51], s84 offen nt lds\n\t"
    "v_exp_f32_e32 v245, v245\n\t"
    "v_mfma_f32_16x16x32_bf16 v[172:175], a[172:175], v[232:235], v[172:175]\n\t"
    "v_add_f32_e32 v37, v245, v37\n\t"
    "v_cvt_pk_bf16_f32 v241, v242, v243\n\t"
    "v_mfma_f32_16x16x32_bf16 v[176:179], a[176:179], v[232:235], v[176:179]\n\t"
    "v_exp_f32_e32 v246, v246\n\t"
    "v_mfma_f32_16x16x32_bf16 v[180:183], a[180:183], v[232:235], v[180:183]\n\t"
    "v_add_f32_e32 v37, v246, v37\n\t"
    "v_cvt_pk_bf16_f32 v242, v244, v245\n\t"
    "v_mfma_f32_16x16x32_bf16 v[184:187], a[184:187], v[232:235], v[184:187]\n\t"
    "v_exp_f32_e32 v247, v247\n\t"
    "v_mfma_f32_16x16x32_bf16 v[188:191], a[188:191], v[232:235], v[188:191]\n\t"
    "v_add_f32_e32 v37, v247, v37\n\t"
    "v_cvt_pk_bf16_f32 v243, v246, v247\n\t"
    "v_mfma_f32_16x16x32_bf16 v[232:235], a[192:195], v[80:83], v[44:47]\n\t"
    "v_exp_f32_e32 v224, v224\n\t"
    "v_mfma_f32_16x16x32_bf16 v[232:235], a[196:199], v[84:87], v[232:235]\n\t"
    "ds_read_b128 a[240:243], v30 offset:33792\n\t"
    "v_add_f32_e32 v32, v224, v32\n\t"
    "v_mfma_f32_16x16x32_bf16 v[232:235], a[200:203], v[88:91], v[232:235]\n\t"
    "v_exp_f32_e32 v225, v225\n\t"
    "v_mfma_f32_16x16x32_bf16 v[232:235], a[204:207], v[92:95], v[232:235]\n\t"
    "ds_read_b128 a[244:247], v30 offset:34048\n\t"
    "v_add_f32_e32 v32, v225, v32\n\t"
    "v_mfma_f32_16x16x32_bf16 v[236:239], a[208:211], v[80:83], v[44:47]\n\t"
    "v_exp_f32_e32 v226, v226\n\t"
    "v_mfma_f32_16x16x32_bf16 v[236:239], a[212:215], v[84:87], v[236:239]\n\t"
    "ds_read_b128 a[248:251], v30 offset:34304\n\t"
    "v_add_f32_e32 v32, v226, v32\n\t"
    "v_mfma_f32_16x16x32_bf16 v[236:239], a[216:219], v[88:91], v[236:239]\n\t"
    "v_exp_f32_e32 v227, v227\n\t"
    "v_mfma_f32_16x16x32_bf16 v[236:239], a[220:223], v[92:95], v[236:239]\n\t"
    "ds_read_b128 a[252:255], v30 offset:34560\n\t"
    "v_add_f32_e32 v32, v227, v32\n\t"
    "v_mfma_f32_16x16x32_bf16 v[192:195], a[160:163], v[240:243], v[192:195]\n\t"
    "buffer_load_dwordx4 v16, s[48:51], s84 offen offset:1024 nt lds\n\t"
    "v_exp_f32_e32 v228, v228\n\t"
    "v_mfma_f32_16x16x32_bf16 v[196:199], a[164:167], v[240:243], v[196:199]\n\t"
    "v_add_f32_e32 v32, v228, v32\n\t"
    "v_cvt_pk_bf16_f32 v224, v224, v225\n\t"
    "v_mfma_f32_16x16x32_bf16 v[200:203], a[168:171], v[240:243], v[200:203]\n\t"
    "v_exp_f32_e32 v229, v229\n\t"
    "v_mfma_f32_16x16x32_bf16 v[204:207], a[172:175], v[240:243], v[204:207]\n\t"
    "v_add_f32_e32 v32, v229, v32\n\t"
    "v_cvt_pk_bf16_f32 v225, v226, v227\n\t"
    "v_mfma_f32_16x16x32_bf16 v[208:211], a[176:179], v[240:243], v[208:211]\n\t"
    "v_exp_f32_e32 v230, v230\n\t"
    "v_mfma_f32_16x16x32_bf16 v[212:215], a[180:183], v[240:243], v[212:215]\n\t"
    "v_add_f32_e32 v32, v230, v32\n\t"
    "v_cvt_pk_bf16_f32 v226, v228, v229\n\t"
    "v_mfma_f32_16x16x32_bf16 v[216:219], a[184:187], v[240:243], v[216:219]\n\t"
    "v_exp_f32_e32 v231, v231\n\t"
    "v_mfma_f32_16x16x32_bf16 v[220:223], a[188:191], v[240:243], v[220:223]\n\t"
    "v_add_f32_e32 v32, v231, v32\n\t"
    "v_cvt_pk_bf16_f32 v227, v230, v231\n\t"
    "v_mfma_f32_16x16x32_bf16 v[240:243], a[192:195], v[96:99], v[48:51]\n\t"
    "s_mov_b32 s85, 0x3a000\n\t"
    "v_exp_f32_e32 v232, v232\n\t"
    "v_mfma_f32_16x16x32_bf16 v[240:243], a[196:199], v[100:103], v[240:243]\n\t"
    "ds_read_b64_tr_b16 a[160:161], v22 offset:32768\n\t"
    "v_add_f32_e32 v33, v232, v33\n\t"
    "v_mfma_f32_16x16x32_bf16 v[240:243], a[200:203], v[104:107], v[240:243]\n\t"
    "s_add_u32 s88, s88, 0x80000\n\t"
    "v_exp_f32_e32 v233, v233\n\t"
    "v_mfma_f32_16x16x32_bf16 v[240:243], a[204:207], v[108:111], v[240:243]\n\t"
    "ds_read_b64_tr_b16 a[162:163], v22 offset:33792\n\t"
    "v_add_f32_e32 v33, v233, v33\n\t"
    "v_mfma_f32_16x16x32_bf16 v[244:247], a[208:211], v[96:99], v[48:51]\n\t"
    "s_and_b32 s88, s88, 0x3fffff\n\t"
    "v_exp_f32_e32 v234, v234\n\t"
    "v_mfma_f32_16x16x32_bf16 v[244:247], a[212:215], v[100:103], v[244:247]\n\t"
    "ds_read_b64_tr_b16 a[164:165], v23 offset:32768\n\t"
    "v_add_f32_e32 v33, v234, v33\n\t"
    "v_mfma_f32_16x16x32_bf16 v[244:247], a[216:219], v[104:107], v[244:247]\n\t"
    "s_add_u32 m0, s65, 0x1e000\n\t"
    "v_exp_f32_e32 v235, v235\n\t"
    "v_mfma_f32_16x16x32_bf16 v[244:247], a[220:223], v[108:111], v[244:247]\n\t"
    "ds_read_b64_tr_b16 a[166:167], v23 offset:33792\n\t"
    "v_add_f32_e32 v33, v235, v33\n\t"
    "v_mfma_f32_16x16x32_bf16 a[0:3], a[128:131], v[224:227], a[0:3]\n\t"
    "buffer_load_dwordx4 v16, s[48:51], s85 offen nt lds\n\t"
    "v_exp_f32_e32 v236, v236\n\t"
    "v_mfma_f32_16x16x32_bf16 a[4:7], a[132:135], v[224:227], a[4:7]\n\t"
    "v_add_f32_e32 v33, v236, v33\n\t"
    "v_cvt_pk_bf16_f32 v232, v232, v233\n\t"
    "v_mfma_f32_16x16x32_bf16 a[8:11], a[136:139], v[224:227], a[8:11]\n\t"
    "v_exp_f32_e32 v237, v237\n\t"
    "v_mfma_f32_16x16x32_bf16 a[12:15], a[140:143], v[224:227], a[12:15]\n\t"
    "v_add_f32_e32 v33, v237, v33\n\t"
    "v_cvt_pk_bf16_f32 v233, v234, v235\n\t"
    "v_mfma_f32_16x16x32_bf16 a[16:19], a[144:147], v[224:227], a[16:19]\n\t"
    "v_exp_f32_e32 v238, v238\n\t"
    "v_mfma_f32_16x16x32_bf16 a[20:23], a[148:151], v[224:227], a[20:23]\n\t"
    "v_add_f32_e32 v33, v238, v33\n\t"
    "v_cvt_pk_bf16_f32 v234, v236, v237\n\t"
    "v_mfma_f32_16x16x32_bf16 a[24:27], a[152:155], v[224:227], a[24:27]\n\t"
    "buffer_load_dword v15, v25, s[56:59], s88 offen\n\t"
    "v_exp_f32_e32 v239, v239\n\t"
    "v_mfma_f32_16x16x32_bf16 a[28:31], a[156:159], v[224:227], a[28:31]\n\t"
    "v_add_f32_e32 v33, v239, v33\n\t"
    "v_cvt_pk_bf16_f32 v235, v238, v239\n\t"
    "v_mfma_f32_16x16x32_bf16 v[224:227], a[192:195], v[112:115], v[52:55]\n\t"
    "v_exp_f32_e32 v240, v240\n\t"
    "v_mfma_f32_16x16x32_bf16 v[224:227], a[196:199], v[116:119], v[224:227]\n\t"
    "ds_read_b64_tr_b16 a[168:169], v22 offset:33024\n\t"
    "v_add_f32_e32 v34, v240, v34\n\t"
    "v_mfma_f32_16x16x32_bf16 v[224:227], a[200:203], v[120:123], v[224:227]\n\t"
    "v_exp_f32_e32 v241, v241\n\t"
    "v_mfma_f32_16x16x32_bf16 v[224:227], a[204:207], v[124:127], v[224:227]\n\t"
    "ds_read_b64_tr_b16 a[170:171], v22 offset:34048\n\t"
    "v_add_f32_e32 v34, v241, v34\n\t"
    "v_mfma_f32_16x16x32_bf16 v[228:231], a[208:211], v[112:115], v[52:55]\n\t"
    "v_exp_f32_e32 v242, v242\n\t"
    "v_mfma_f32_16x16x32_bf16 v[228:231], a[212:215], v[116:119], v[228:231]\n\t"
    "ds_read_b64_tr_b16 a[172:173], v23 offset:33024\n\t"
    "v_add_f32_e32 v34, v242, v34\n\t"
    "v_mfma_f32_16x16x32_bf16 v[228:231], a[216:219], v[120:123], v[228:231]\n\t"
    "v_exp_f32_e32 v243, v243\n\t"
    "v_mfma_f32_16x16x32_bf16 v[228:231], a[220:223], v[124:127], v[228:231]\n\t"
    "ds_read_b64_tr_b16 a[174:175], v23 offset:34048\n\t"
    "v_add_f32_e32 v34, v243, v34\n\t"
    "v_mfma_f32_16x16x32_bf16 a[32:35], a[128:131], v[232:235], a[32:35]\n\t"
    "v_exp_f32_e32 v244, v244\n\t"
    "v_mfma_f32_16x16x32_bf16 a[36:39], a[132:135], v[232:235], a[36:39]\n\t"
    "v_add_f32_e32 v34, v244, v34\n\t"
    "v_cvt_pk_bf16_f32 v240, v240, v241\n\t"
    "v_mfma_f32_16x16x32_bf16 a[40:43], a[136:139], v[232:235], a[40:43]\n\t"
    "v_exp_f32_e32 v245, v245\n\t"
    "v_mfma_f32_16x16x32_bf16 a[44:47], a[140:143], v[232:235], a[44:47]\n\t"
    "v_add_f32_e32 v34, v245, v34\n\t"
    "v_cvt_pk_bf16_f32 v241, v242, v243\n\t"
    "v_mfma_f32_16x16x32_bf16 a[48:51], a[144:147], v[232:235], a[48:51]\n\t"
    "buffer_load_dwordx4 v16, s[48:51], s85 offen offset:1024 nt lds\n\t"
    "v_exp_f32_e32 v246, v246\n\t"
    "v_mfma_f32_16x16x32_bf16 a[52:55], a[148:151], v[232:235], a[52:55]\n\t"
    "v_add_f32_e32 v34, v246, v34\n\t"
    "v_cvt_pk_bf16_f32 v242, v244, v245\n\t"
    "v_mfma_f32_16x16x32_bf16 a[56:59], a[152:155], v[232:235], a[56:59]\n\t"
    "v_exp_f32_e32 v247, v247\n\t"
    "v_mfma_f32_16x16x32_bf16 a[60:63], a[156:159], v[232:235], a[60:63]\n\t"
    "v_add_f32_e32 v34, v247, v34\n\t"
    "v_cvt_pk_bf16_f32 v243, v246, v247\n\t"
    "v_mfma_f32_16x16x32_bf16 v[232:235], a[192:195], v[128:131], v[56:59]\n\t"
    "s_mov_b32 s84, 0x1c000\n\t"
    "v_exp_f32_e32 v224, v224\n\t"
    "v_mfma_f32_16x16x32_bf16 v[232:235], a[196:199], v[132:135], v[232:235]\n\t"
    "ds_read_b64_tr_b16 a[176:177], v22 offset:33280\n\t"
    "v_add_f32_e32 v35, v224, v35\n\t"
    "v_mfma_f32_16x16x32_bf16 v[232:235], a[200:203], v[136:139], v[232:235]\n\t"
    "v_exp_f32_e32 v225, v225\n\t"
    "v_mfma_f32_16x16x32_bf16 v[232:235], a[204:207], v[140:143], v[232:235]\n\t"
    "ds_read_b64_tr_b16 a[178:179], v22 offset:34304\n\t"
    "v_add_f32_e32 v35, v225, v35\n\t"
    "v_mfma_f32_16x16x32_bf16 v[236:239], a[208:211], v[128:131], v[56:59]\n\t"
    "v_exp_f32_e32 v226, v226\n\t"
    "v_mfma_f32_16x16x32_bf16 v[236:239], a[212:215], v[132:135], v[236:239]\n\t"
    "ds_read_b64_tr_b16 a[180:181], v23 offset:33280\n\t"
    "v_add_f32_e32 v35, v226, v35\n\t"
    "v_mfma_f32_16x16x32_bf16 v[236:239], a[216:219], v[136:139], v[236:239]\n\t"
    "v_exp_f32_e32 v227, v227\n\t"
    "v_mfma_f32_16x16x32_bf16 v[236:239], a[220:223], v[140:143], v[236:239]\n\t"
    "ds_read_b64_tr_b16 a[182:183], v23 offset:34304\n\t"
    "v_add_f32_e32 v35, v227, v35\n\t"
    "v_mfma_f32_16x16x32_bf16 a[64:67], a[128:131], v[240:243], a[64:67]\n\t"
    "v_exp_f32_e32 v228, v228\n\t"
    "v_mfma_f32_16x16x32_bf16 a[68:71], a[132:135], v[240:243], a[68:71]\n\t"
    "v_add_f32_e32 v35, v228, v35\n\t"
    "v_cvt_pk_bf16_f32 v224, v224, v225\n\t"
    "v_mfma_f32_16x16x32_bf16 a[72:75], a[136:139], v[240:243], a[72:75]\n\t"
    "v_exp_f32_e32 v229, v229\n\t"
    "v_mfma_f32_16x16x32_bf16 a[76:79], a[140:143], v[240:243], a[76:79]\n\t"
    "s_add_u32 m0, s65, 0x20000\n\t"
    "v_add_f32_e32 v35, v229, v35\n\t"
    "v_cvt_pk_bf16_f32 v225, v226, v227\n\t"
    "v_mfma_f32_16x16x32_bf16 a[80:83], a[144:147], v[240:243], a[80:83]\n\t"
    "buffer_load_dwordx4 v16, s[44:47], s84 offen nt lds\n\t"
    "v_exp_f32_e32 v230, v230\n\t"
    "v_mfma_f32_16x16x32_bf16 a[84:87], a[148:151], v[240:243], a[84:87]\n\t"
    "v_add_f32_e32 v35, v230, v35\n\t"
    "v_cvt_pk_bf16_f32 v226, v228, v229\n\t"
    "v_mfma_f32_16x16x32_bf16 a[88:91], a[152:155], v[240:243], a[88:91]\n\t"
    "v_exp_f32_e32 v231, v231\n\t"
    "v_mfma_f32_16x16x32_bf16 a[92:95], a[156:159], v[240:243], a[92:95]\n\t"
    "v_add_f32_e32 v35, v231, v35\n\t"
    "v_cvt_pk_bf16_f32 v227, v230, v231\n\t"
    "v_mfma_f32_16x16x32_bf16 v[240:243], a[192:195], v[144:147], v[60:63]\n\t"
    "v_exp_f32_e32 v232, v232\n\t"
    "v_mfma_f32_16x16x32_bf16 v[240:243], a[196:199], v[148:151], v[240:243]\n\t"
    "ds_read_b64_tr_b16 a[184:185], v22 offset:33536\n\t"
    "v_add_f32_e32 v36, v232, v36\n\t"
    "v_mfma_f32_16x16x32_bf16 v[240:243], a[200:203], v[152:155], v[240:243]\n\t"
    "v_exp_f32_e32 v233, v233\n\t"
    "v_mfma_f32_16x16x32_bf16 v[240:243], a[204:207], v[156:159], v[240:243]\n\t"
    "ds_read_b64_tr_b16 a[186:187], v22 offset:34560\n\t"
    "v_add_f32_e32 v36, v233, v36\n\t"
    "v_mfma_f32_16x16x32_bf16 v[244:247], a[208:211], v[144:147], v[60:63]\n\t"
    "v_exp_f32_e32 v234, v234\n\t"
    "v_mfma_f32_16x16x32_bf16 v[244:247], a[212:215], v[148:151], v[244:247]\n\t"
    "ds_read_b64_tr_b16 a[188:189], v23 offset:33536\n\t"
    "v_add_f32_e32 v36, v234, v36\n\t"
    "v_mfma_f32_16x16x32_bf16 v[244:247], a[216:219], v[152:155], v[244:247]\n\t"
    "v_exp_f32_e32 v235, v235\n\t"
    "v_mfma_f32_16x16x32_bf16 v[244:247], a[220:223], v[156:159], v[244:247]\n\t"
    "ds_read_b64_tr_b16 a[190:191], v23 offset:34560\n\t"
    "v_add_f32_e32 v36, v235, v36\n\t"
    "v_mfma_f32_16x16x32_bf16 a[96:99], a[128:131], v[224:227], a[96:99]\n\t"
    "v_exp_f32_e32 v236, v236\n\t"
    "v_mfma_f32_16x16x32_bf16 a[100:103], a[132:135], v[224:227], a[100:103]\n\t"
    "s_memrealtime s[30:31]\n\t"
    "v_add_f32_e32 v36, v236, v36\n\t"
    "v_cvt_pk_bf16_f32 v232, v232, v233\n\t"
    "v_mfma_f32_16x16x32_bf16 a[104:107], a[136:139], v[224:227], a[104:107]\n\t"
    "buffer_load_dwordx4 v16, s[44:47], s84 offen offset:1024 nt lds\n\t"
    "v_exp_f32_e32 v237, v237\n\t"
    "v_mfma_f32_16x16x32_bf16 a[108:111], a[140:143], v[224:227], a[108:111]\n\t"
    "v_add_f32_e32 v36, v237, v36\n\t"
    "v_cvt_pk_bf16_f32 v233, v234, v235\n\t"
    "v_mfma_f32_16x16x32_bf16 a[112:115], a[144:147], v[224:227], a[112:115]\n\t"
    "v_exp_f32_e32 v238, v238\n\t"
    "v_mfma_f32_16x16x32_bf16 a[116:119], a[148:151], v[224:227], a[116:119]\n\t"
    "v_add_f32_e32 v36, v238, v36\n\t"
    "v_cvt_pk_bf16_f32 v234, v236, v237\n\t"
    "v_mfma_f32_16x16x32_bf16 a[120:123], a[152:155], v[224:227], a[120:123]\n\t"
    "s_waitcnt vmcnt(12) lgkmcnt(0)\n\t"
    "v_exp_f32_e32 v239, v239\n\t"
    "v_mfma_f32_16x16x32_bf16 a[124:127], a[156:159], v[224:227], a[124:127]\n\t"
    "s_barrier\n\t"
    "v_add_f32_e32 v36, v239, v36\n\t"
    "v_cvt_pk_bf16_f32 v235, v238, v239\n\t"
    "s_sub_i32 s31, s29, s30\n\t"
    "s_cmp_gt_i32 s31, 0\n\t"
    "s_cselect_b32 s29, s29, s30\n\t"
    "s_cbranch_scc0 .Lfa_pa65e\n\t"
    "s_min_i32 s31, s31, 0xc8\n\t"
    ".Lfa_pa65:\n\t"
    "s_sub_u32 s31, s31, 2\n\t"
    "s_cmp_gt_i32 s31, 0\n\t"
    "s_cbranch_scc1 .Lfa_pa65\n\t"
    ".Lfa_pa65e:\n\t"
    "s_add_u32 s29, s29, 0x71\n\t"
    "v_mfma_f32_16x16x32_bf16 v[224:227], a[224:227], v[64:67], v[40:43]\n\t"
    "s_mov_b32 s85, 0x3c000\n\t"
    "v_exp_f32_e32 v240, v240\n\t"
    "v_mfma_f32_16x16x32_bf16 v[224:227], a[228:231], v[68:71], v[224:227]\n\t"
    "ds_read_b128 a[192:195], v17 offset:0\n\t"
    "v_add_f32_e32 v37, v240, v37\n\t"
    "v_mfma_f32_16x16x32_bf16 v[224:227], a[232:235], v[72:75], v[224:227]\n\t"
    "s_bfe_u32 s60, s71, 0x30001\n\t"
    "v_exp_f32_e32 v241, v241\n\t"
    "v_mfma_f32_16x16x32_bf16 v[224:227], a[236:239], v[76:79], v[224:227]\n\t"
    "ds_read_b128 a[196:199], v17 offset:256\n\t"
    "v_add_f32_e32 v37, v241, v37\n\t"
    "v_mfma_f32_16x16x32_bf16 v[228:231], a[240:243], v[64:67], v[40:43]\n\t"
    "s_bfe_u32 s75, s71, 0x10000\n\t"
    "s_lshl2_add_u32 s60, s60, s75\n\t"
    "s_lshl_b32 s60, s60, 11\n\t"
    "s_add_u32 s60, s60, s25\n\t"
    "s_lshl2_add_u32 s60, s60, s79\n\t"
    "s_lshl_b32 s60, s60, 4\n\t"
    "s_add_u32 s61, s60, 0x8000\n\t"
    "v_exp_f32_e32 v242, v242\n\t"
    "v_mfma_f32_16x16x32_bf16 v[228:231], a[244:247], v[68:71], v[228:231]\n\t"
    "ds_read_b128 a[200:203], v17 offset:512\n\t"
    "v_add_f32_e32 v37, v242, v37\n\t"
    "v_mfma_f32_16x16x32_bf16 v[228:231], a[248:251], v[72:75], v[228:231]\n\t"
    "s_add_u32 m0, s65, 0x22000\n\t"
    "v_exp_f32_e32 v243, v243\n\t"
    "v_mfma_f32_16x16x32_bf16 v[228:231], a[252:255], v[76:79], v[228:231]\n\t"
    "ds_read_b128 a[204:207], v17 offset:768\n\t"
    "v_add_f32_e32 v37, v243, v37\n\t"
    "v_mfma_f32_16x16x32_bf16 v[160:163], a[128:131], v[232:235], v[160:163]\n\t"
    "buffer_load_dwordx4 v16, s[44:47], s85 offen nt lds\n\t"
    "v_exp_f32_e32 v244, v244\n\t"
    "v_mfma_f32_16x16x32_bf16 v[164:167], a[132:135], v[232:235], v[164:167]\n\t"
    "v_add_f32_e32 v37, v244, v37\n\t"
    "v_cvt_pk_bf16_f32 v240, v240, v241\n\t"
    "v_mfma_f32_16x16x32_bf16 v[168:171], a[136:139], v[232:235], v[168:171]\n\t"
    "v_exp_f32_e32 v245, v245\n\t"
    "v_mfma_f32_16x16x32_bf16 v[172:175], a[140:143], v[232:235], v[172:175]\n\t"
    "s_add_u32 m0, s14, 0x8000\n\t"
    "v_add_f32_e32 v37, v245, v37\n\t"
    "v_cvt_pk_bf16_f32 v241, v242, v243\n\t"
    "v_mfma_f32_16x16x32_bf16 v[176:179], a[144:147], v[232:235], v[176:179]\n\t"
    "buffer_load_dwordx4 v29, s[16:19], s60 offen lds\n\t"
    "v_exp_f32_e32 v246, v246\n\t"
    "v_mfma_f32_16x16x32_bf16 v[180:183], a[148:151], v[232:235], v[180:183]\n\t"
    "s_add_u32 m0, s65, 0x22000\n\t"
    "v_add_f32_e32 v37, v246, v37\n\t"
    "v_cvt_pk_bf16_f32 v242, v244, v245\n\t"
    "v_mfma_f32_16x16x32_bf16 v[184:187], a[152:155], v[232:235], v[184:187]\n\t"
    "buffer_load_dwordx4 v16, s[44:47], s85 offen offset:1024 nt lds\n\t"
    "v_exp_f32_e32 v247, v247\n\t"
    "v_mfma_f32_16x16x32_bf16 v[188:191], a[156:159], v[232:235], v[188:191]\n\t"
    "v_add_f32_e32 v37, v247, v37\n\t"
    "v_cvt_pk_bf16_f32 v243, v246, v247\n\t"
    "v_mfma_f32_16x16x32_bf16 v[232:235], a[224:227], v[80:83], v[44:47]\n\t"
    "s_mov_b32 s84, 0x1c000\n\t"
    "v_exp_f32_e32 v224, v224\n\t"
    "v_mfma_f32_16x16x32_bf16 v[232:235], a[228:231], v[84:87], v[232:235]\n\t"
    "ds_read_b128 a[208:211], v17 offset:1024\n\t"
    "v_add_f32_e32 v32, v224, v32\n\t"
    "v_mfma_f32_16x16x32_bf16 v[232:235], a[232:235], v[88:91], v[232:235]\n\t"
    "v_exp_f32_e32 v225, v225\n\t"
    "v_mfma_f32_16x16x32_bf16 v[232:235], a[236:239], v[92:95], v[232:235]\n\t"
    "ds_read_b128 a[212:215], v17 offset:1280\n\t"
    "v_add_f32_e32 v32, v225, v32\n\t"
    "v_mfma_f32_16x16x32_bf16 v[236:239], a[240:243], v[80:83], v[44:47]\n\t"
    "v_exp_f32_e32 v226, v226\n\t"
    "v_mfma_f32_16x16x32_bf16 v[236:239], a[244:247], v[84:87], v[236:239]\n\t"
    "ds_read_b128 a[216:219], v17 offset:1536\n\t"
    "v_add_f32_e32 v32, v226, v32\n\t"
    "v_mfma_f32_16x16x32_bf16 v[236:239], a[248:251], v[88:91], v[236:239]\n\t"
    "v_exp_f32_e32 v227, v227\n\t"
    "v_mfma_f32_16x16x32_bf16 v[236:239], a[252:255], v[92:95], v[236:239]\n\t"
    "ds_read_b128 a[220:223], v17 offset:1792\n\t"
    "v_add_f32_e32 v32, v227, v32\n\t"
    "v_mfma_f32_16x16x32_bf16 v[192:195], a[128:131], v[240:243], v[192:195]\n\t"
    "v_exp_f32_e32 v228, v228\n\t"
    "v_mfma_f32_16x16x32_bf16 v[196:199], a[132:135], v[240:243], v[196:199]\n\t"
    "s_add_u32 m0, s14, 0x8400\n\t"
    "v_add_f32_e32 v32, v228, v32\n\t"
    "v_cvt_pk_bf16_f32 v224, v224, v225\n\t"
    "v_mfma_f32_16x16x32_bf16 v[200:203], a[136:139], v[240:243], v[200:203]\n\t"
    "buffer_load_dwordx4 v29, s[16:19], s61 offen lds\n\t"
    "v_exp_f32_e32 v229, v229\n\t"
    "v_mfma_f32_16x16x32_bf16 v[204:207], a[140:143], v[240:243], v[204:207]\n\t"
    "s_add_u32 m0, s65, 0x24000\n\t"
    "v_add_f32_e32 v32, v229, v32\n\t"
    "v_cvt_pk_bf16_f32 v225, v226, v227\n\t"
    "v_mfma_f32_16x16x32_bf16 v[208:211], a[144:147], v[240:243], v[208:211]\n\t"
    "buffer_load_dwordx4 v16, s[48:51], s84 offen nt lds\n\t"
    "v_exp_f32_e32 v230, v230\n\t"
    "v_mfma_f32_16x16x32_bf16 v[212:215], a[148:151], v[240:243], v[212:215]\n\t"
    "s_add_u32 m0, s14, 0x8800\n\t"
    "v_add_f32_e32 v32, v230, v32\n\t"
    "v_cvt_pk_bf16_f32 v226, v228, v229\n\t"
    "v_mfma_f32_16x16x32_bf16 v[216:219], a[152:155], v[240:243], v[216:219]\n\t"
    "buffer_load_dwordx4 v26, s[16:19], s60 offen lds\n\t"
    "v_exp_f32_e32 v231, v231\n\t"
    "v_mfma_f32_16x16x32_bf16 v[220:223], a[156:159], v[240:243], v[220:223]\n\t"
    "v_add_f32_e32 v32, v231, v32\n\t"
    "v_cvt_pk_bf16_f32 v227, v230, v231\n\t"
    "v_mfma_f32_16x16x32_bf16 v[240:243], a[224:227], v[96:99], v[48:51]\n\t"
    "v_exp_f32_e32 v232, v232\n\t"
    "v_mfma_f32_16x16x32_bf16 v[240:243], a[228:231], v[100:103], v[240:243]\n\t"
    "ds_read_b64_tr_b16 a[128:129], v19 offset:0\n\t"
    "v_add_f32_e32 v33, v232, v33\n\t"
    "v_mfma_f32_16x16x32_bf16 v[240:243], a[232:235], v[104:107], v[240:243]\n\t"
    "v_exp_f32_e32 v233, v233\n\t"
    "v_mfma_f32_16x16x32_bf16 v[240:243], a[236:239], v[108:111], v[240:243]\n\t"
    "ds_read_b64_tr_b16 a[130:131], v19 offset:1024\n\t"
    "v_add_f32_e32 v33, v233, v33\n\t"
    "v_mfma_f32_16x16x32_bf16 v[244:247], a[240:243], v[96:99], v[48:51]\n\t"
    "v_exp_f32_e32 v234, v234\n\t"
    "v_mfma_f32_16x16x32_bf16 v[244:247], a[244:247], v[100:103], v[244:247]\n\t"
    "ds_read_b64_tr_b16 a[132:133], v21 offset:0\n\t"
    "v_add_f32_e32 v33, v234, v33\n\t"
    "v_mfma_f32_16x16x32_bf16 v[244:247], a[248:251], v[104:107], v[244:247]\n\t"
    "v_exp_f32_e32 v235, v235\n\t"
    "v_mfma_f32_16x16x32_bf16 v[244:247], a[252:255], v[108:111], v[244:247]\n\t"
    "ds_read_b64_tr_b16 a[134:135], v21 offset:1024\n\t"
    "v_add_f32_e32 v33, v235, v33\n\t"
    "v_mfma_f32_16x16x32_bf16 a[0:3], a[160:163], v[224:227], a[0:3]\n\t"
    "v_exp_f32_e32 v236, v236\n\t"
    "v_mfma_f32_16x16x32_bf16 a[4:7], a[164:167], v[224:227], a[4:7]\n\t"
    "s_add_u32 m0, s65, 0x24000\n\t"
    "v_add_f32_e32 v33, v236, v33\n\t"
    "v_cvt_pk_bf16_f32 v232, v232, v233\n\t"
    "v_mfma_f32_16x16x32_bf16 a[8:11], a[168:171], v[224:227], a[8:11]\n\t"
    "buffer_load_dwordx4 v16, s[48:51], s84 offen offset:1024 nt lds\n\t"
    "v_exp_f32_e32 v237, v237\n\t"
    "v_mfma_f32_16x16x32_bf16 a[12:15], a[172:175], v[224:227], a[12:15]\n\t"
    "s_add_u32 m0, s14, 0x8c00\n\t"
    "v_add_f32_e32 v33, v237, v33\n\t"
    "v_cvt_pk_bf16_f32 v233, v234, v235\n\t"
    "v_mfma_f32_16x16x32_bf16 a[16:19], a[176:179], v[224:227], a[16:19]\n\t"
    "buffer_load_dwordx4 v26, s[16:19], s61 offen lds\n\t"
    "v_exp_f32_e32 v238, v238\n\t"
    "v_mfma_f32_16x16x32_bf16 a[20:23], a[180:183], v[224:227], a[20:23]\n\t"
    "v_add_f32_e32 v33, v238, v33\n\t"
    "v_cvt_pk_bf16_f32 v234, v236, v237\n\t"
    "v_mfma_f32_16x16x32_bf16 a[24:27], a[184:187], v[224:227], a[24:27]\n\t"
    "v_exp_f32_e32 v239, v239\n\t"
    "v_mfma_f32_16x16x32_bf16 a[28:31], a[188:191], v[224:227], a[28:31]\n\t"
    "v_add_f32_e32 v33, v239, v33\n\t"
    "v_cvt_pk_bf16_f32 v235, v238, v239\n\t"
    "v_mfma_f32_16x16x32_bf16 v[224:227], a[224:227], v[112:115], v[52:55]\n\t"
    "s_mov_b32 s85, 0x3c000\n\t"
    "v_exp_f32_e32 v240, v240\n\t"
    "v_mfma_f32_16x16x32_bf16 v[224:227], a[228:231], v[116:119], v[224:227]\n\t"
    "ds_read_b64_tr_b16 a[136:137], v19 offset:256\n\t"
    "v_add_f32_e32 v34, v240, v34\n\t"
    "v_mfma_f32_16x16x32_bf16 v[224:227], a[232:235], v[120:123], v[224:227]\n\t"
    "v_exp_f32_e32 v241, v241\n\t"
    "v_mfma_f32_16x16x32_bf16 v[224:227], a[236:239], v[124:127], v[224:227]\n\t"
    "ds_read_b64_tr_b16 a[138:139], v19 offset:1280\n\t"
    "v_add_f32_e32 v34, v241, v34\n\t"
    "v_mfma_f32_16x16x32_bf16 v[228:231], a[240:243], v[112:115], v[52:55]\n\t"
    "v_exp_f32_e32 v242, v242\n\t"
    "v_mfma_f32_16x16x32_bf16 v[228:231], a[244:247], v[116:119], v[228:231]\n\t"
    "ds_read_b64_tr_b16 a[140:141], v21 offset:256\n\t"
    "v_add_f32_e32 v34, v242, v34\n\t"
    "v_mfma_f32_16x16x32_bf16 v[228:231], a[248:251], v[120:123], v[228:231]\n\t"
    "s_add_u32 m0, s65, 0x26000\n\t"
    "v_exp_f32_e32 v243, v243\n\t"
    "v_mfma_f32_16x16x32_bf16 v[228:231], a[252:255], v[124:127], v[228:231]\n\t"
    "ds_read_b64_tr_b16 a[142:143], v21 offset:1280\n\t"
    "v_add_f32_e32 v34, v243, v34\n\t"
    "v_mfma_f32_16x16x32_bf16 a[32:35], a[160:163], v[232:235], a[32:35]\n\t"
    "buffer_load_dwordx4 v16, s[48:51], s85 offen nt lds\n\t"
    "v_exp_f32_e32 v244, v244\n\t"
    "v_mfma_f32_16x16x32_bf16 a[36:39], a[164:167], v[232:235], a[36:39]\n\t"
    "s_add_u32 m0, s14, 0x9000\n\t"
    "v_add_f32_e32 v34, v244, v34\n\t"
    "v_cvt_pk_bf16_f32 v240, v240, v241\n\t"
    "v_mfma_f32_16x16x32_bf16 a[40:43], a[168:171], v[232:235], a[40:43]\n\t"
    "buffer_load_dwordx4 v27, s[16:19], s60 offen lds\n\t"
    "v_exp_f32_e32 v245, v245\n\t"
    "v_mfma_f32_16x16x32_bf16 a[44:47], a[172:175], v[232:235], a[44:47]\n\t"
    "v_add_f32_e32 v34, v245, v34\n\t"
    "v_cvt_pk_bf16_f32 v241, v242, v243\n\t"
    "v_mfma_f32_16x16x32_bf16 a[48:51], a[176:179], v[232:235], a[48:51]\n\t"
    "v_exp_f32_e32 v246, v246\n\t"
    "v_mfma_f32_16x16x32_bf16 a[52:55], a[180:183], v[232:235], a[52:55]\n\t"
    "s_add_u32 m0, s65, 0x26000\n\t"
    "v_add_f32_e32 v34, v246, v34\n\t"
    "v_cvt_pk_bf16_f32 v242, v244, v245\n\t"
    "v_mfma_f32_16x16x32_bf16 a[56:59], a[184:187], v[232:235], a[56:59]\n\t"
    "buffer_load_dwordx4 v16, s[48:51], s85 offen offset:1024 nt lds\n\t"
    "v_exp_f32_e32 v247, v247\n\t"
    "v_mfma_f32_16x16x32_bf16 a[60:63], a[188:191], v[232:235], a[60:63]\n\t"
    "v_add_f32_e32 v34, v247, v34\n\t"
    "v_cvt_pk_bf16_f32 v243, v246, v247\n\t"
    "v_mfma_f32_16x16x32_bf16 v[232:235], a[224:227], v[128:131], v[56:59]\n\t"
    "s_mov_b32 s84, 0x1e000\n\t"
    "v_exp_f32_e32 v224, v224\n\t"
    "v_mfma_f32_16x16x32_bf16 v[232:235], a[228:231], v[132:135], v[232:235]\n\t"
    "ds_read_b64_tr_b16 a[144:145], v19 offset:512\n\t"
    "v_add_f32_e32 v35, v224, v35\n\t"
    "v_mfma_f32_16x16x32_bf16 v[232:235], a[232:235], v[136:139], v[232:235]\n\t"
    "v_exp_f32_e32 v225, v225\n\t"
    "v_mfma_f32_16x16x32_bf16 v[232:235], a[236:239], v[140:143], v[232:235]\n\t"
    "ds_read_b64_tr_b16 a[146:147], v19 offset:1536\n\t"
    "v_add_f32_e32 v35, v225, v35\n\t"
    "v_mfma_f32_16x16x32_bf16 v[236:239], a[240:243], v[128:131], v[56:59]\n\t"
    "v_exp_f32_e32 v226, v226\n\t"
    "v_mfma_f32_16x16x32_bf16 v[236:239], a[244:247], v[132:135], v[236:239]\n\t"
    "ds_read_b64_tr_b16 a[148:149], v21 offset:512\n\t"
    "v_add_f32_e32 v35, v226, v35\n\t"
    "v_mfma_f32_16x16x32_bf16 v[236:239], a[248:251], v[136:139], v[236:239]\n\t"
    "s_add_u32 m0, s14, 0x9400\n\t"
    "v_exp_f32_e32 v227, v227\n\t"
    "v_mfma_f32_16x16x32_bf16 v[236:239], a[252:255], v[140:143], v[236:239]\n\t"
    "ds_read_b64_tr_b16 a[150:151], v21 offset:1536\n\t"
    "v_add_f32_e32 v35, v227, v35\n\t"
    "v_mfma_f32_16x16x32_bf16 a[64:67], a[160:163], v[240:243], a[64:67]\n\t"
    "buffer_load_dwordx4 v27, s[16:19], s61 offen lds\n\t"
    "v_exp_f32_e32 v228, v228\n\t"
    "v_mfma_f32_16x16x32_bf16 a[68:71], a[164:167], v[240:243], a[68:71]\n\t"
    "s_add_u32 m0, s65, 0\n\t"
    "v_add_f32_e32 v35, v228, v35\n\t"
    "v_cvt_pk_bf16_f32 v224, v224, v225\n\t"
    "v_mfma_f32_16x16x32_bf16 a[72:75], a[168:171], v[240:243], a[72:75]\n\t"
    "buffer_load_dwordx4 v16, s[44:47], s84 offen nt lds\n\t"
    "v_exp_f32_e32 v229, v229\n\t"
    "v_mfma_f32_16x16x32_bf16 a[76:79], a[172:175], v[240:243], a[76:79]\n\t"
    "v_add_f32_e32 v35, v229, v35\n\t"
    "v_cvt_pk_bf16_f32 v225, v226, v227\n\t"
    "v_mfma_f32_16x16x32_bf16 a[80:83], a[176:179], v[240:243], a[80:83]\n\t"
    "v_exp_f32_e32 v230, v230\n\t"
    "v_mfma_f32_16x16x32_bf16 a[84:87], a[180:183], v[240:243], a[84:87]\n\t"
    "s_add_u32 m0, s14, 0x9800\n\t"
    "v_add_f32_e32 v35, v230, v35\n\t"
    "v_cvt_pk_bf16_f32 v226, v228, v229\n\t"
    "v_mfma_f32_16x16x32_bf16 a[88:91], a[184:187], v[240:243], a[88:91]\n\t"
    "buffer_load_dwordx4 v28, s[16:19], s60 offen lds\n\t"
    "v_exp_f32_e32 v231, v231\n\t"
    "v_mfma_f32_16x16x32_bf16 a[92:95], a[188:191], v[240:243], a[92:95]\n\t"
    "v_add_f32_e32 v35, v231, v35\n\t"
    "v_cvt_pk_bf16_f32 v227, v230, v231\n\t"
    "v_mfma_f32_16x16x32_bf16 v[240:243], a[224:227], v[144:147], v[60:63]\n\t"
    "v_exp_f32_e32 v232, v232\n\t"
    "v_mfma_f32_16x16x32_bf16 v[240:243], a[228:231], v[148:151], v[240:243]\n\t"
    "ds_read_b64_tr_b16 a[152:153], v19 offset:768\n\t"
    "v_add_f32_e32 v36, v232, v36\n\t"
    "v_mfma_f32_16x16x32_bf16 v[240:243], a[232:235], v[152:155], v[240:243]\n\t"
    "v_exp_f32_e32 v233, v233\n\t"
    "v_mfma_f32_16x16x32_bf16 v[240:243], a[236:239], v[156:159], v[240:243]\n\t"
    "ds_read_b64_tr_b16 a[154:155], v19 offset:1792\n\t"
    "v_add_f32_e32 v36, v233, v36\n\t"
    "v_mfma_f32_16x16x32_bf16 v[244:247], a[240:243], v[144:147], v[60:63]\n\t"
    "v_exp_f32_e32 v234, v234\n\t"
    "v_mfma_f32_16x16x32_bf16 v[244:247], a[244:247], v[148:151], v[244:247]\n\t"
    "ds_read_b64_tr_b16 a[156:157], v21 offset:768\n\t"
    "v_add_f32_e32 v36, v234, v36\n\t"
    "v_mfma_f32_16x16x32_bf16 v[244:247], a[248:251], v[152:155], v[244:247]\n\t"
    "s_add_u32 m0, s65, 0\n\t"
    "v_exp_f32_e32 v235, v235\n\t"
    "v_mfma_f32_16x16x32_bf16 v[244:247], a[252:255], v[156:159], v[244:247]\n\t"
    "ds_read_b64_tr_b16 a[158:159], v21 offset:1792\n\t"
    "v_add_f32_e32 v36, v235, v36\n\t"
    "v_mfma_f32_16x16x32_bf16 a[96:99], a[160:163], v[224:227], a[96:99]\n\t"
    "buffer_load_dwordx4 v16, s[44:47], s84 offen offset:1024 nt lds\n\t"
    "v_exp_f32_e32 v236, v236\n\t"
    "v_mfma_f32_16x16x32_bf16 a[100:103], a[164:167], v[224:227], a[100:103]\n\t"
    "s_memrealtime s[30:31]\n\t"
    "v_add_f32_e32 v36, v236, v36\n\t"
    "v_cvt_pk_bf16_f32 v232, v232, v233\n\t"
    "v_mfma_f32_16x16x32_bf16 a[104:107], a[168:171], v[224:227], a[104:107]\n\t"
    "v_exp_f32_e32 v237, v237\n\t"
    "v_mfma_f32_16x16x32_bf16 a[108:111], a[172:175], v[224:227], a[108:111]\n\t"
    "s_add_u32 m0, s14, 0x9c00\n\t"
    "v_add_f32_e32 v36, v237, v36\n\t"
    "v_cvt_pk_bf16_f32 v233, v234, v235\n\t"
    "v_mfma_f32_16x16x32_bf16 a[112:115], a[176:179], v[224:227], a[112:115]\n\t"
    "buffer_load_dwordx4 v28, s[16:19], s61 offen lds\n\t"
    "v_exp_f32_e32 v238, v238\n\t"
    "v_mfma_f32_16x16x32_bf16 a[116:119], a[180:183], v[224:227], a[116:119]\n\t"
    "v_add_f32_e32 v36, v238, v36\n\t"
    "v_cvt_pk_bf16_f32 v234, v236, v237\n\t"
    "v_mfma_f32_16x16x32_bf16 a[120:123], a[184:187], v[224:227], a[120:123]\n\t"
    "s_waitcnt vmcnt(18) lgkmcnt(0)\n\t"
    "v_exp_f32_e32 v239, v239\n\t"
    "v_mfma_f32_16x16x32_bf16 a[124:127], a[188:191], v[224:227], a[124:127]\n\t"
    "s_barrier\n\t"
    "v_add_f32_e32 v36, v239, v36\n\t"
    "v_cvt_pk_bf16_f32 v235, v238, v239\n\t"
    "s_sub_i32 s31, s29, s30\n\t"
    "s_cmp_gt_i32 s31, 0\n\t"
    "s_cselect_b32 s29, s29, s30\n\t"
    "s_cbranch_scc0 .Lfa_pa71e\n\t"
    "s_min_i32 s31, s31, 0xc8\n\t"
    ".Lfa_pa71:\n\t"
    "s_sub_u32 s31, s31, 2\n\t"
    "s_cmp_gt_i32 s31, 0\n\t"
    "s_cbranch_scc1 .Lfa_pa71\n\t"
    ".Lfa_pa71e:\n\t"
    "s_add_u32 s29, s29, 0x71\n\t"
    "v_mfma_f32_16x16x32_bf16 v[224:227], a[192:195], v[64:67], v[40:43]\n\t"
    "s_mov_b32 s85, 0x3e000\n\t"
    "v_exp_f32_e32 v240, v240\n\t"
    "v_mfma_f32_16x16x32_bf16 v[224:227], a[196:199], v[68:71], v[224:227]\n\t"
    "ds_read_b128 a[224:227], v17 offset:32768\n\t"
    "v_add_f32_e32 v37, v240, v37\n\t"
    "v_mfma_f32_16x16x32_bf16 v[224:227], a[200:203], v[72:75], v[224:227]\n\t"
    "s_bfe_u32 s69, s71, 0x30005\n\t"
    "v_exp_f32_e32 v241, v241\n\t"
    "v_mfma_f32_16x16x32_bf16 v[224:227], a[204:207], v[76:79], v[224:227]\n\t"
    "ds_read_b128 a[228:231], v17 offset:33024\n\t"
    "v_add_f32_e32 v37, v241, v37\n\t"
    "v_mfma_f32_16x16x32_bf16 v[228:231], a[208:211], v[64:67], v[40:43]\n\t"
    "s_bfe_u32 s75, s71, 0x10004\n\t"
    "s_lshl2_add_u32 s69, s69, s75\n\t"
    "s_lshl_b32 s69, s69, 11\n\t"
    "s_add_u32 s69, s69, s25\n\t"
    "s_lshl2_add_u32 s69, s69, s79\n\t"
    "s_lshl_b32 s69, s69, 4\n\t"
    "s_add_u32 s70, s69, 0x8000\n\t"
    "v_exp_f32_e32 v242, v242\n\t"
    "v_mfma_f32_16x16x32_bf16 v[228:231], a[212:215], v[68:71], v[228:231]\n\t"
    "ds_read_b128 a[232:235], v17 offset:33280\n\t"
    "v_add_f32_e32 v37, v242, v37\n\t"
    "v_mfma_f32_16x16x32_bf16 v[228:231], a[216:219], v[72:75], v[228:231]\n\t"
    "s_add_u32 m0, s65, 0x2000\n\t"
    "v_exp_f32_e32 v243, v243\n\t"
    "v_mfma_f32_16x16x32_bf16 v[228:231], a[220:223], v[76:79], v[228:231]\n\t"
    "ds_read_b128 a[236:239], v17 offset:33536\n\t"
    "v_add_f32_e32 v37, v243, v37\n\t"
    "v_mfma_f32_16x16x32_bf16 v[160:163], a[160:163], v[232:235], v[160:163]\n\t"
    "buffer_load_dwordx4 v16, s[44:47], s85 offen nt lds\n\t"
    "v_exp_f32_e32 v244, v244\n\t"
    "v_mfma_f32_16x16x32_bf16 v[164:167], a[164:167], v[232:235], v[164:167]\n\t"
    "v_add_f32_e32 v37, v244, v37\n\t"
    "v_cvt_pk_bf16_f32 v240, v240, v241\n\t"
    "v_mfma_f32_16x16x32_bf16 v[168:171], a[168:171], v[232:235], v[168:171]\n\t"
    "v_exp_f32_e32 v245, v245\n\t"
    "v_mfma_f32_16x16x32_bf16 v[172:175], a[172:175], v[232:235], v[172:175]\n\t"
    "s_add_u32 m0, s14, 0x10000\n\t"
    "v_add_f32_e32 v37, v245, v37\n\t"
    "v_cvt_pk_bf16_f32 v241, v242, v243\n\t"
    "v_mfma_f32_16x16x32_bf16 v[176:179], a[176:179], v[232:235], v[176:179]\n\t"
    "buffer_load_dwordx4 v29, s[16:19], s69 offen lds\n\t"
    "v_exp_f32_e32 v246, v246\n\t"
    "v_mfma_f32_16x16x32_bf16 v[180:183], a[180:183], v[232:235], v[180:183]\n\t"
    "s_add_u32 m0, s14, 0x10400\n\t"
    "v_add_f32_e32 v37, v246, v37\n\t"
    "v_cvt_pk_bf16_f32 v242, v244, v245\n\t"
    "v_mfma_f32_16x16x32_bf16 v[184:187], a[184:187], v[232:235], v[184:187]\n\t"
    "buffer_load_dwordx4 v29, s[16:19], s70 offen lds\n\t"
    "v_exp_f32_e32 v247, v247\n\t"
    "v_mfma_f32_16x16x32_bf16 v[188:191], a[188:191], v[232:235], v[188:191]\n\t"
    "v_add_f32_e32 v37, v247, v37\n\t"
    "v_cvt_pk_bf16_f32 v243, v246, v247\n\t"
    "v_mfma_f32_16x16x32_bf16 v[232:235], a[192:195], v[80:83], v[44:47]\n\t"
    "v_exp_f32_e32 v224, v224\n\t"
    "v_mfma_f32_16x16x32_bf16 v[232:235], a[196:199], v[84:87], v[232:235]\n\t"
    "ds_read_b128 a[240:243], v17 offset:33792\n\t"
    "v_add_f32_e32 v32, v224, v32\n\t"
    "v_mfma_f32_16x16x32_bf16 v[232:235], a[200:203], v[88:91], v[232:235]\n\t"
    "v_exp_f32_e32 v225, v225\n\t"
    "v_mfma_f32_16x16x32_bf16 v[232:235], a[204:207], v[92:95], v[232:235]\n\t"
    "ds_read_b128 a[244:247], v17 offset:34048\n\t"
    "v_add_f32_e32 v32, v225, v32\n\t"
    "v_mfma_f32_16x16x32_bf16 v[236:239], a[208:211], v[80:83], v[44:47]\n\t"
    "v_exp_f32_e32 v226, v226\n\t"
    "v_mfma_f32_16x16x32_bf16 v[236:239], a[212:215], v[84:87], v[236:239]\n\t"
    "ds_read_b128 a[248:251], v17 offset:34304\n\t"
    "v_add_f32_e32 v32, v226, v32\n\t"
    "v_mfma_f32_16x16x32_bf16 v[236:239], a[216:219], v[88:91], v[236:239]\n\t"
    "v_exp_f32_e32 v227, v227\n\t"
    "v_mfma_f32_16x16x32_bf16 v[236:239], a[220:223], v[92:95], v[236:239]\n\t"
    "ds_read_b128 a[252:255], v17 offset:34560\n\t"
    "v_add_f32_e32 v32, v227, v32\n\t"
    "v_mfma_f32_16x16x32_bf16 v[192:195], a[160:163], v[240:243], v[192:195]\n\t"
    "v_exp_f32_e32 v228, v228\n\t"
    "v_mfma_f32_16x16x32_bf16 v[196:199], a[164:167], v[240:243], v[196:199]\n\t"
    "s_add_u32 m0, s65, 0x2000\n\t"
    "v_add_f32_e32 v32, v228, v32\n\t"
    "v_cvt_pk_bf16_f32 v224, v224, v225\n\t"
    "v_mfma_f32_16x16x32_bf16 v[200:203], a[168:171], v[240:243], v[200:203]\n\t"
    "buffer_load_dwordx4 v16, s[44:47], s85 offen offset:1024 nt lds\n\t"
    "v_exp_f32_e32 v229, v229\n\t"
    "v_mfma_f32_16x16x32_bf16 v[204:207], a[172:175], v[240:243], v[204:207]\n\t"
    "s_add_u32 m0, s14, 0x10800\n\t"
    "v_add_f32_e32 v32, v229, v32\n\t"
    "v_cvt_pk_bf16_f32 v225, v226, v227\n\t"
    "v_mfma_f32_16x16x32_bf16 v[208:211], a[176:179], v[240:243], v[208:211]\n\t"
    "buffer_load_dwordx4 v26, s[16:19], s69 offen lds\n\t"
    "v_exp_f32_e32 v230, v230\n\t"
    "v_mfma_f32_16x16x32_bf16 v[212:215], a[180:183], v[240:243], v[212:215]\n\t"
    "v_add_f32_e32 v32, v230, v32\n\t"
    "v_cvt_pk_bf16_f32 v226, v228, v229\n\t"
    "v_mfma_f32_16x16x32_bf16 v[216:219], a[184:187], v[240:243], v[216:219]\n\t"
    "v_exp_f32_e32 v231, v231\n\t"
    "v_mfma_f32_16x16x32_bf16 v[220:223], a[188:191], v[240:243], v[220:223]\n\t"
    "v_add_f32_e32 v32, v231, v32\n\t"
    "v_cvt_pk_bf16_f32 v227, v230, v231\n\t"
    "v_mfma_f32_16x16x32_bf16 v[240:243], a[192:195], v[96:99], v[48:51]\n\t"
    "s_mov_b32 s84, 0x1e000\n\t"
    "v_exp_f32_e32 v232, v232\n\t"
    "v_mfma_f32_16x16x32_bf16 v[240:243], a[196:199], v[100:103], v[240:243]\n\t"
    "ds_read_b64_tr_b16 a[160:161], v19 offset:32768\n\t"
    "v_add_f32_e32 v33, v232, v33\n\t"
    "v_mfma_f32_16x16x32_bf16 v[240:243], a[200:203], v[104:107], v[240:243]\n\t"
    "s_add_u32 s88, s88, 0x80000\n\t"
    "v_exp_f32_e32 v233, v233\n\t"
    "v_mfma_f32_16x16x32_bf16 v[240:243], a[204:207], v[108:111], v[240:243]\n\t"
    "ds_read_b64_tr_b16 a[162:163], v19 offset:33792\n\t"
    "v_add_f32_e32 v33, v233, v33\n\t"
    "v_mfma_f32_16x16x32_bf16 v[244:247], a[208:211], v[96:99], v[48:51]\n\t"
    "s_and_b32 s88, s88, 0x3fffff\n\t"
    "v_exp_f32_e32 v234, v234\n\t"
    "v_mfma_f32_16x16x32_bf16 v[244:247], a[212:215], v[100:103], v[244:247]\n\t"
    "ds_read_b64_tr_b16 a[164:165], v21 offset:32768\n\t"
    "v_add_f32_e32 v33, v234, v33\n\t"
    "v_mfma_f32_16x16x32_bf16 v[244:247], a[216:219], v[104:107], v[244:247]\n\t"
    "s_add_u32 m0, s65, 0x4000\n\t"
    "v_exp_f32_e32 v235, v235\n\t"
    "v_mfma_f32_16x16x32_bf16 v[244:247], a[220:223], v[108:111], v[244:247]\n\t"
    "ds_read_b64_tr_b16 a[166:167], v21 offset:33792\n\t"
    "v_add_f32_e32 v33, v235, v33\n\t"
    "v_mfma_f32_16x16x32_bf16 a[0:3], a[128:131], v[224:227], a[0:3]\n\t"
    "buffer_load_dwordx4 v16, s[48:51], s84 offen nt lds\n\t"
    "v_exp_f32_e32 v236, v236\n\t"
    "v_mfma_f32_16x16x32_bf16 a[4:7], a[132:135], v[224:227], a[4:7]\n\t"
    "s_add_u32 m0, s14, 0x10c00\n\t"
    "v_add_f32_e32 v33, v236, v33\n\t"
    "v_cvt_pk_bf16_f32 v232, v232, v233\n\t"
    "v_mfma_f32_16x16x32_bf16 a[8:11], a[136:139], v[224:227], a[8:11]\n\t"
    "buffer_load_dwordx4 v26, s[16:19], s70 offen lds\n\t"
    "v_exp_f32_e32 v237, v237\n\t"
    "v_mfma_f32_16x16x32_bf16 a[12:15], a[140:143], v[224:227], a[12:15]\n\t"
    "v_add_f32_e32 v33, v237, v33\n\t"
    "v_cvt_pk_bf16_f32 v233, v234, v235\n\t"
    "v_mfma_f32_16x16x32_bf16 a[16:19], a[144:147], v[224:227], a[16:19]\n\t"
    "v_exp_f32_e32 v238, v238\n\t"
    "v_mfma_f32_16x16x32_bf16 a[20:23], a[148:151], v[224:227], a[20:23]\n\t"
    "v_add_f32_e32 v33, v238, v33\n\t"
    "v_cvt_pk_bf16_f32 v234, v236, v237\n\t"
    "v_mfma_f32_16x16x32_bf16 a[24:27], a[152:155], v[224:227], a[24:27]\n\t"
    "buffer_load_dword v15, v25, s[56:59], s88 offen\n\t"
    "v_exp_f32_e32 v239, v239\n\t"
    "v_mfma_f32_16x16x32_bf16 a[28:31], a[156:159], v[224:227], a[28:31]\n\t"
    "v_add_f32_e32 v33, v239, v33\n\t"
    "v_cvt_pk_bf16_f32 v235, v238, v239\n\t"
    "v_mfma_f32_16x16x32_bf16 v[224:227], a[192:195], v[112:115], v[52:55]\n\t"
    "v_exp_f32_e32 v240, v240\n\t"
    "v_mfma_f32_16x16x32_bf16 v[224:227], a[196:199], v[116:119], v[224:227]\n\t"
    "ds_read_b64_tr_b16 a[168:169], v19 offset:33024\n\t"
    "v_add_f32_e32 v34, v240, v34\n\t"
    "v_mfma_f32_16x16x32_bf16 v[224:227], a[200:203], v[120:123], v[224:227]\n\t"
    "v_exp_f32_e32 v241, v241\n\t"
    "v_mfma_f32_16x16x32_bf16 v[224:227], a[204:207], v[124:127], v[224:227]\n\t"
    "ds_read_b64_tr_b16 a[170:171], v19 offset:34048\n\t"
    "v_add_f32_e32 v34, v241, v34\n\t"
    "v_mfma_f32_16x16x32_bf16 v[228:231], a[208:211], v[112:115], v[52:55]\n\t"
    "v_exp_f32_e32 v242, v242\n\t"
    "v_mfma_f32_16x16x32_bf16 v[228:231], a[212:215], v[116:119], v[228:231]\n\t"
    "ds_read_b64_tr_b16 a[172:173], v21 offset:33024\n\t"
    "v_add_f32_e32 v34, v242, v34\n\t"
    "v_mfma_f32_16x16x32_bf16 v[228:231], a[216:219], v[120:123], v[228:231]\n\t"
    "v_exp_f32_e32 v243, v243\n\t"
    "v_mfma_f32_16x16x32_bf16 v[228:231], a[220:223], v[124:127], v[228:231]\n\t"
    "ds_read_b64_tr_b16 a[174:175], v21 offset:34048\n\t"
    "v_add_f32_e32 v34, v243, v34\n\t"
    "v_mfma_f32_16x16x32_bf16 a[32:35], a[128:131], v[232:235], a[32:35]\n\t"
    "v_exp_f32_e32 v244, v244\n\t"
    "v_mfma_f32_16x16x32_bf16 a[36:39], a[132:135], v[232:235], a[36:39]\n\t"
    "s_add_u32 m0, s65, 0x4000\n\t"
    "v_add_f32_e32 v34, v244, v34\n\t"
    "v_cvt_pk_bf16_f32 v240, v240, v241\n\t"
    "v_mfma_f32_16x16x32_bf16 a[40:43], a[136:139], v[232:235], a[40:43]\n\t"
    "buffer_load_dwordx4 v16, s[48:51], s84 offen offset:1024 nt lds\n\t"
    "v_exp_f32_e32 v245, v245\n\t"
    "v_mfma_f32_16x16x32_bf16 a[44:47], a[140:143], v[232:235], a[44:47]\n\t"
    "s_add_u32 m0, s14, 0x11000\n\t"
    "v_add_f32_e32 v34, v245, v34\n\t"
    "v_cvt_pk_bf16_f32 v241, v242, v243\n\t"
    "v_mfma_f32_16x16x32_bf16 a[48:51], a[144:147], v[232:235], a[48:51]\n\t"
    "buffer_load_dwordx4 v27, s[16:19], s69 offen lds\n\t"
    "v_exp_f32_e32 v246, v246\n\t"
    "v_mfma_f32_16x16x32_bf16 a[52:55], a[148:151], v[232:235], a[52:55]\n\t"
    "v_add_f32_e32 v34, v246, v34\n\t"
    "v_cvt_pk_bf16_f32 v242, v244, v245\n\t"
    "v_mfma_f32_16x16x32_bf16 a[56:59], a[152:155], v[232:235], a[56:59]\n\t"
    "v_exp_f32_e32 v247, v247\n\t"
    "v_mfma_f32_16x16x32_bf16 a[60:63], a[156:159], v[232:235], a[60:63]\n\t"
    "v_add_f32_e32 v34, v247, v34\n\t"
    "v_cvt_pk_bf16_f32 v243, v246, v247\n\t"
    "v_mfma_f32_16x16x32_bf16 v[232:235], a[192:195], v[128:131], v[56:59]\n\t"
    "s_mov_b32 s85, 0x3e000\n\t"
    "v_exp_f32_e32 v224, v224\n\t"
    "v_mfma_f32_16x16x32_bf16 v[232:235], a[196:199], v[132:135], v[232:235]\n\t"
    "ds_read_b64_tr_b16 a[176:177], v19 offset:33280\n\t"
    "v_add_f32_e32 v35, v224, v35\n\t"
    "v_mfma_f32_16x16x32_bf16 v[232:235], a[200:203], v[136:139], v[232:235]\n\t"
    "v_exp_f32_e32 v225, v225\n\t"
    "v_mfma_f32_16x16x32_bf16 v[232:235], a[204:207], v[140:143], v[232:235]\n\t"
    "ds_read_b64_tr_b16 a[178:179], v19 offset:34304\n\t"
    "v_add_f32_e32 v35, v225, v35\n\t"
    "v_mfma_f32_16x16x32_bf16 v[236:239], a[208:211], v[128:131], v[56:59]\n\t"
    "v_exp_f32_e32 v226, v226\n\t"
    "v_mfma_f32_16x16x32_bf16 v[236:239], a[212:215], v[132:135], v[236:239]\n\t"
    "ds_read_b64_tr_b16 a[180:181], v21 offset:33280\n\t"
    "v_add_f32_e32 v35, v226, v35\n\t"
    "v_mfma_f32_16x16x32_bf16 v[236:239], a[216:219], v[136:139], v[236:239]\n\t"
    "s_add_u32 m0, s14, 0x11400\n\t"
    "v_exp_f32_e32 v227, v227\n\t"
    "v_mfma_f32_16x16x32_bf16 v[236:239], a[220:223], v[140:143], v[236:239]\n\t"
    "ds_read_b64_tr_b16 a[182:183], v21 offset:34304\n\t"
    "v_add_f32_e32 v35, v227, v35\n\t"
    "v_mfma_f32_16x16x32_bf16 a[64:67], a[128:131], v[240:243], a[64:67]\n\t"
    "buffer_load_dwordx4 v27, s[16:19], s70 offen lds\n\t"
    "v_exp_f32_e32 v228, v228\n\t"
    "v_mfma_f32_16x16x32_bf16 a[68:71], a[132:135], v[240:243], a[68:71]\n\t"
    "s_add_u32 m0, s65, 0x6000\n\t"
    "v_add_f32_e32 v35, v228, v35\n\t"
    "v_cvt_pk_bf16_f32 v224, v224, v225\n\t"
    "v_mfma_f32_16x16x32_bf16 a[72:75], a[136:139], v[240:243], a[72:75]\n\t"
    "buffer_load_dwordx4 v16, s[48:51], s85 offen nt lds\n\t"
    "v_exp_f32_e32 v229, v229\n\t"
    "v_mfma_f32_16x16x32_bf16 a[76:79], a[140:143], v[240:243], a[76:79]\n\t"
    "v_add_f32_e32 v35, v229, v35\n\t"
    "v_cvt_pk_bf16_f32 v225, v226, v227\n\t"
    "v_mfma_f32_16x16x32_bf16 a[80:83], a[144:147], v[240:243], a[80:83]\n\t"
    "v_exp_f32_e32 v230, v230\n\t"
    "v_mfma_f32_16x16x32_bf16 a[84:87], a[148:151], v[240:243], a[84:87]\n\t"
    "s_add_u32 m0, s14, 0x11800\n\t"
    "v_add_f32_e32 v35, v230, v35\n\t"
    "v_cvt_pk_bf16_f32 v226, v228, v229\n\t"
    "v_mfma_f32_16x16x32_bf16 a[88:91], a[152:155], v[240:243], a[88:91]\n\t"
    "buffer_load_dwordx4 v28, s[16:19], s69 offen lds\n\t"
    "v_exp_f32_e32 v231, v231\n\t"
    "v_mfma_f32_16x16x32_bf16 a[92:95], a[156:159], v[240:243], a[92:95]\n\t"
    "v_add_f32_e32 v35, v231, v35\n\t"
    "v_cvt_pk_bf16_f32 v227, v230, v231\n\t"
    "v_mfma_f32_16x16x32_bf16 v[240:243], a[192:195], v[144:147], v[60:63]\n\t"
    "v_exp_f32_e32 v232, v232\n\t"
    "v_mfma_f32_16x16x32_bf16 v[240:243], a[196:199], v[148:151], v[240:243]\n\t"
    "ds_read_b64_tr_b16 a[184:185], v19 offset:33536\n\t"
    "v_add_f32_e32 v36, v232, v36\n\t"
    "v_mfma_f32_16x16x32_bf16 v[240:243], a[200:203], v[152:155], v[240:243]\n\t"
    "v_exp_f32_e32 v233, v233\n\t"
    "v_mfma_f32_16x16x32_bf16 v[240:243], a[204:207], v[156:159], v[240:243]\n\t"
    "ds_read_b64_tr_b16 a[186:187], v19 offset:34560\n\t"
    "v_add_f32_e32 v36, v233, v36\n\t"
    "v_mfma_f32_16x16x32_bf16 v[244:247], a[208:211], v[144:147], v[60:63]\n\t"
    "v_exp_f32_e32 v234, v234\n\t"
    "v_mfma_f32_16x16x32_bf16 v[244:247], a[212:215], v[148:151], v[244:247]\n\t"
    "ds_read_b64_tr_b16 a[188:189], v21 offset:33536\n\t"
    "v_add_f32_e32 v36, v234, v36\n\t"
    "v_mfma_f32_16x16x32_bf16 v[244:247], a[216:219], v[152:155], v[244:247]\n\t"
    "s_add_u32 m0, s65, 0x6000\n\t"
    "v_exp_f32_e32 v235, v235\n\t"
    "v_mfma_f32_16x16x32_bf16 v[244:247], a[220:223], v[156:159], v[244:247]\n\t"
    "ds_read_b64_tr_b16 a[190:191], v21 offset:34560\n\t"
    "v_add_f32_e32 v36, v235, v36\n\t"
    "v_mfma_f32_16x16x32_bf16 a[96:99], a[128:131], v[224:227], a[96:99]\n\t"
    "buffer_load_dwordx4 v16, s[48:51], s85 offen offset:1024 nt lds\n\t"
    "v_exp_f32_e32 v236, v236\n\t"
    "v_mfma_f32_16x16x32_bf16 a[100:103], a[132:135], v[224:227], a[100:103]\n\t"
    "s_memrealtime s[30:31]\n\t"
    "v_add_f32_e32 v36, v236, v36\n\t"
    "v_cvt_pk_bf16_f32 v232, v232, v233\n\t"
    "v_mfma_f32_16x16x32_bf16 a[104:107], a[136:139], v[224:227], a[104:107]\n\t"
    "v_exp_f32_e32 v237, v237\n\t"
    "v_mfma_f32_16x16x32_bf16 a[108:111], a[140:143], v[224:227], a[108:111]\n\t"
    "s_add_u32 m0, s14, 0x11c00\n\t"
    "v_add_f32_e32 v36, v237, v36\n\t"
    "v_cvt_pk_bf16_f32 v233, v234, v235\n\t"
    "v_mfma_f32_16x16x32_bf16 a[112:115], a[144:147], v[224:227], a[112:115]\n\t"
    "buffer_load_dwordx4 v28, s[16:19], s70 offen lds\n\t"
    "v_exp_f32_e32 v238, v238\n\t"
    "v_mfma_f32_16x16x32_bf16 a[116:119], a[148:151], v[224:227], a[116:119]\n\t"
    "v_add_f32_e32 v36, v238, v36\n\t"
    "v_cvt_pk_bf16_f32 v234, v236, v237\n\t"
    "v_mfma_f32_16x16x32_bf16 a[120:123], a[152:155], v[224:227], a[120:123]\n\t"
    "s_waitcnt vmcnt(20) lgkmcnt(0)\n\t"
    "v_exp_f32_e32 v239, v239\n\t"
    "v_mfma_f32_16x16x32_bf16 a[124:127], a[156:159], v[224:227], a[124:127]\n\t"
    "s_barrier\n\t"
    "v_add_f32_e32 v36, v239, v36\n\t"
    "v_cvt_pk_bf16_f32 v235, v238, v239\n\t"
    "s_sub_i32 s31, s29, s30\n\t"
    "s_cmp_gt_i32 s31, 0\n\t"
    "s_cselect_b32 s29, s29, s30\n\t"
    "s_cbranch_scc0 .Lfa_pa77e\n\t"
    "s_min_i32 s31, s31, 0xc8\n\t"
    ".Lfa_pa77:\n\t"
    "s_sub_u32 s31, s31, 2\n\t"
    "s_cmp_gt_i32 s31, 0\n\t"
    "s_cbranch_scc1 .Lfa_pa77\n\t"
    ".Lfa_pa77e:\n\t"
    "s_add_u32 s29, s29, 0x71\n\t"
    "v_mfma_f32_16x16x32_bf16 v[224:227], a[224:227], v[64:67], v[40:43]\n\t"
    "s_bfe_u32 s60, s71, 0x30009\n\t"
    "v_exp_f32_e32 v240, v240\n\t"
    "v_mfma_f32_16x16x32_bf16 v[224:227], a[228:231], v[68:71], v[224:227]\n\t"
    "ds_read_b128 a[192:195], v18 offset:0\n\t"
    "v_add_f32_e32 v37, v240, v37\n\t"
    "v_mfma_f32_16x16x32_bf16 v[224:227], a[232:235], v[72:75], v[224:227]\n\t"
    "s_bfe_u32 s75, s71, 0x10008\n\t"
    "v_exp_f32_e32 v241, v241\n\t"
    "v_mfma_f32_16x16x32_bf16 v[224:227], a[236:239], v[76:79], v[224:227]\n\t"
    "ds_read_b128 a[196:199], v18 offset:256\n\t"
    "v_add_f32_e32 v37, v241, v37\n\t"
    "v_mfma_f32_16x16x32_bf16 v[228:231], a[240:243], v[64:67], v[40:43]\n\t"
    "s_lshl2_add_u32 s60, s60, s75\n\t"
    "v_exp_f32_e32 v242, v242\n\t"
    "v_mfma_f32_16x16x32_bf16 v[228:231], a[244:247], v[68:71], v[228:231]\n\t"
    "ds_read_b128 a[200:203], v18 offset:512\n\t"
    "v_add_f32_e32 v37, v242, v37\n\t"
    "v_mfma_f32_16x16x32_bf16 v[228:231], a[248:251], v[72:75], v[228:231]\n\t"
    "s_lshl_b32 s60, s60, 11\n\t"
    "s_add_u32 s60, s60, s25\n\t"
    "s_lshl2_add_u32 s60, s60, s79\n\t"
    "s_lshl_b32 s60, s60, 4\n\t"
    "s_add_u32 s61, s60, 0x8000\n\t"
    "v_exp_f32_e32 v243, v243\n\t"
    "v_mfma_f32_16x16x32_bf16 v[228:231], a[252:255], v[76:79], v[228:231]\n\t"
    "ds_read_b128 a[204:207], v18 offset:768\n\t"
    "v_add_f32_e32 v37, v243, v37\n\t"
    "v_mfma_f32_16x16x32_bf16 v[160:163], a[128:131], v[232:235], v[160:163]\n\t"
    "v_exp_f32_e32 v244, v244\n\t"
    "v_mfma_f32_16x16x32_bf16 v[164:167], a[132:135], v[232:235], v[164:167]\n\t"
    "s_add_u32 m0, s14, 0x18000\n\t"
    "v_add_f32_e32 v37, v244, v37\n\t"
    "v_cvt_pk_bf16_f32 v240, v240, v241\n\t"
    "v_mfma_f32_16x16x32_bf16 v[168:171], a[136:139], v[232:235], v[168:171]\n\t"
    "buffer_load_dwordx4 v29, s[16:19], s60 offen lds\n\t"
    "v_exp_f32_e32 v245, v245\n\t"
    "v_mfma_f32_16x16x32_bf16 v[172:175], a[140:143], v[232:235], v[172:175]\n\t"
    "v_add_f32_e32 v37, v245, v37\n\t"
    "v_cvt_pk_bf16_f32 v241, v242, v243\n\t"
    "v_mfma_f32_16x16x32_bf16 v[176:179], a[144:147], v[232:235], v[176:179]\n\t"
    "v_exp_f32_e32 v246, v246\n\t"
    "v_mfma_f32_16x16x32_bf16 v[180:183], a[148:151], v[232:235], v[180:183]\n\t"
    "v_add_f32_e32 v37, v246, v37\n\t"
    "v_cvt_pk_bf16_f32 v242, v244, v245\n\t"
    "v_mfma_f32_16x16x32_bf16 v[184:187], a[152:155], v[232:235], v[184:187]\n\t"
    "v_exp_f32_e32 v247, v247\n\t"
    "v_mfma_f32_16x16x32_bf16 v[188:191], a[156:159], v[232:235], v[188:191]\n\t"
    "v_add_f32_e32 v37, v247, v37\n\t"
    "v_cvt_pk_bf16_f32 v243, v246, v247\n\t"
    "v_mfma_f32_16x16x32_bf16 v[232:235], a[224:227], v[80:83], v[44:47]\n\t"
    "v_exp_f32_e32 v224, v224\n\t"
    "v_mfma_f32_16x16x32_bf16 v[232:235], a[228:231], v[84:87], v[232:235]\n\t"
    "ds_read_b128 a[208:211], v18 offset:1024\n\t"
    "v_add_f32_e32 v32, v224, v32\n\t"
    "v_mfma_f32_16x16x32_bf16 v[232:235], a[232:235], v[88:91], v[232:235]\n\t"
    "v_exp_f32_e32 v225, v225\n\t"
    "v_mfma_f32_16x16x32_bf16 v[232:235], a[236:239], v[92:95], v[232:235]\n\t"
    "ds_read_b128 a[212:215], v18 offset:1280\n\t"
    "v_add_f32_e32 v32, v225, v32\n\t"
    "v_mfma_f32_16x16x32_bf16 v[236:239], a[240:243], v[80:83], v[44:47]\n\t"
    "v_exp_f32_e32 v226, v226\n\t"
    "v_mfma_f32_16x16x32_bf16 v[236:239], a[244:247], v[84:87], v[236:239]\n\t"
    "ds_read_b128 a[216:219], v18 offset:1536\n\t"
    "v_add_f32_e32 v32, v226, v32\n\t"
    "v_mfma_f32_16x16x32_bf16 v[236:239], a[248:251], v[88:91], v[236:239]\n\t"
    "s_add_u32 m0, s14, 0x18400\n\t"
    "v_exp_f32_e32 v227, v227\n\t"
    "v_mfma_f32_16x16x32_bf16 v[236:239], a[252:255], v[92:95], v[236:239]\n\t"
    "ds_read_b128 a[220:223], v18 offset:1792\n\t"
    "v_add_f32_e32 v32, v227, v32\n\t"
    "v_mfma_f32_16x16x32_bf16 v[192:195], a[128:131], v[240:243], v[192:195]\n\t"
    "buffer_load_dwordx4 v29, s[16:19], s61 offen lds\n\t"
    "v_exp_f32_e32 v228, v228\n\t"
    "v_mfma_f32_16x16x32_bf16 v[196:199], a[132:135], v[240:243], v[196:199]\n\t"
    "v_add_f32_e32 v32, v228, v32\n\t"
    "v_cvt_pk_bf16_f32 v224, v224, v225\n\t"
    "v_mfma_f32_16x16x32_bf16 v[200:203], a[136:139], v[240:243], v[200:203]\n\t"
    "v_exp_f32_e32 v229, v229\n\t"
    "v_mfma_f32_16x16x32_bf16 v[204:207], a[140:143], v[240:243], v[204:207]\n\t"
    "v_add_f32_e32 v32, v229, v32\n\t"
    "v_cvt_pk_bf16_f32 v225, v226, v227\n\t"
    "v_mfma_f32_16x16x32_bf16 v[208:211], a[144:147], v[240:243], v[208:211]\n\t"
    "v_exp_f32_e32 v230, v230\n\t"
    "v_mfma_f32_16x16x32_bf16 v[212:215], a[148:151], v[240:243], v[212:215]\n\t"
    "s_add_u32 m0, s14, 0x18800\n\t"
    "v_add_f32_e32 v32, v230, v32\n\t"
    "v_cvt_pk_bf16_f32 v226, v228, v229\n\t"
    "v_mfma_f32_16x16x32_bf16 v[216:219], a[152:155], v[240:243], v[216:219]\n\t"
    "buffer_load_dwordx4 v26, s[16:19], s60 offen lds\n\t"
    "v_exp_f32_e32 v231, v231\n\t"
    "v_mfma_f32_16x16x32_bf16 v[220:223], a[156:159], v[240:243], v[220:223]\n\t"
    "v_add_f32_e32 v32, v231, v32\n\t"
    "v_cvt_pk_bf16_f32 v227, v230, v231\n\t"
    "v_mfma_f32_16x16x32_bf16 v[240:243], a[224:227], v[96:99], v[48:51]\n\t"
    "v_exp_f32_e32 v232, v232\n\t"
    "v_mfma_f32_16x16x32_bf16 v[240:243], a[228:231], v[100:103], v[240:243]\n\t"
    "ds_read_b64_tr_b16 a[128:129], v20 offset:0\n\t"
    "v_add_f32_e32 v33, v232, v33\n\t"
    "v_mfma_f32_16x16x32_bf16 v[240:243], a[232:235], v[104:107], v[240:243]\n\t"
    "v_exp_f32_e32 v233, v233\n\t"
    "v_mfma_f32_16x16x32_bf16 v[240:243], a[236:239], v[108:111], v[240:243]\n\t"
    "ds_read_b64_tr_b16 a[130:131], v20 offset:1024\n\t"
    "v_add_f32_e32 v33, v233, v33\n\t"
    "v_mfma_f32_16x16x32_bf16 v[244:247], a[240:243], v[96:99], v[48:51]\n\t"
    "v_exp_f32_e32 v234, v234\n\t"
    "v_mfma_f32_16x16x32_bf16 v[244:247], a[244:247], v[100:103], v[244:247]\n\t"
    "ds_read_b64_tr_b16 a[132:133], v24 offset:0\n\t"
    "v_add_f32_e32 v33, v234, v33\n\t"
    "v_mfma_f32_16x16x32_bf16 v[244:247], a[248:251], v[104:107], v[244:247]\n\t"
    "v_exp_f32_e32 v235, v235\n\t"
    "v_mfma_f32_16x16x32_bf16 v[244:247], a[252:255], v[108:111], v[244:247]\n\t"
    "ds_read_b64_tr_b16 a[134:135], v24 offset:1024\n\t"
    "v_add_f32_e32 v33, v235, v33\n\t"
    "v_mfma_f32_16x16x32_bf16 a[0:3], a[160:163], v[224:227], a[0:3]\n\t"
    "v_exp_f32_e32 v236, v236\n\t"
    "v_mfma_f32_16x16x32_bf16 a[4:7], a[164:167], v[224:227], a[4:7]\n\t"
    "v_add_f32_e32 v33, v236, v33\n\t"
    "v_cvt_pk_bf16_f32 v232, v232, v233\n\t"
    "v_mfma_f32_16x16x32_bf16 a[8:11], a[168:171], v[224:227], a[8:11]\n\t"
    "v_exp_f32_e32 v237, v237\n\t"
    "v_mfma_f32_16x16x32_bf16 a[12:15], a[172:175], v[224:227], a[12:15]\n\t"
    "s_add_u32 m0, s14, 0x18c00\n\t"
    "v_add_f32_e32 v33, v237, v33\n\t"
    "v_cvt_pk_bf16_f32 v233, v234, v235\n\t"
    "v_mfma_f32_16x16x32_bf16 a[16:19], a[176:179], v[224:227], a[16:19]\n\t"
    "buffer_load_dwordx4 v26, s[16:19], s61 offen lds\n\t"
    "v_exp_f32_e32 v238, v238\n\t"
    "v_mfma_f32_16x16x32_bf16 a[20:23], a[180:183], v[224:227], a[20:23]\n\t"
    "v_add_f32_e32 v33, v238, v33\n\t"
    "v_cvt_pk_bf16_f32 v234, v236, v237\n\t"
    "v_mfma_f32_16x16x32_bf16 a[24:27], a[184:187], v[224:227], a[24:27]\n\t"
    "v_exp_f32_e32 v239, v239\n\t"
    "v_mfma_f32_16x16x32_bf16 a[28:31], a[188:191], v[224:227], a[28:31]\n\t"
    "v_add_f32_e32 v33, v239, v33\n\t"
    "v_cvt_pk_bf16_f32 v235, v238, v239\n\t"
    "v_mfma_f32_16x16x32_bf16 v[224:227], a[224:227], v[112:115], v[52:55]\n\t"
    "v_exp_f32_e32 v240, v240\n\t"
    "v_mfma_f32_16x16x32_bf16 v[224:227], a[228:231], v[116:119], v[224:227]\n\t"
    "ds_read_b64_tr_b16 a[136:137], v20 offset:256\n\t"
    "v_add_f32_e32 v34, v240, v34\n\t"
    "v_mfma_f32_16x16x32_bf16 v[224:227], a[232:235], v[120:123], v[224:227]\n\t"
    "v_exp_f32_e32 v241, v241\n\t"
    "v_mfma_f32_16x16x32_bf16 v[224:227], a[236:239], v[124:127], v[224:227]\n\t"
    "ds_read_b64_tr_b16 a[138:139], v20 offset:1280\n\t"
    "v_add_f32_e32 v34, v241, v34\n\t"
    "v_mfma_f32_16x16x32_bf16 v[228:231], a[240:243], v[112:115], v[52:55]\n\t"
    "v_exp_f32_e32 v242, v242\n\t"
    "v_mfma_f32_16x16x32_bf16 v[228:231], a[244:247], v[116:119], v[228:231]\n\t"
    "ds_read_b64_tr_b16 a[140:141], v24 offset:256\n\t"
    "v_add_f32_e32 v34, v242, v34\n\t"
    "v_mfma_f32_16x16x32_bf16 v[228:231], a[248:251], v[120:123], v[228:231]\n\t"
    "s_add_u32 m0, s14, 0x19000\n\t"
    "v_exp_f32_e32 v243, v243\n\t"
    "v_mfma_f32_16x16x32_bf16 v[228:231], a[252:255], v[124:127], v[228:231]\n\t"
    "ds_read_b64_tr_b16 a[142:143], v24 offset:1280\n\t"
    "v_add_f32_e32 v34, v243, v34\n\t"
    "v_mfma_f32_16x16x32_bf16 a[32:35], a[160:163], v[232:235], a[32:35]\n\t"
    "buffer_load_dwordx4 v27, s[16:19], s60 offen lds\n\t"
    "v_exp_f32_e32 v244, v244\n\t"
    "v_mfma_f32_16x16x32_bf16 a[36:39], a[164:167], v[232:235], a[36:39]\n\t"
    "v_add_f32_e32 v34, v244, v34\n\t"
    "v_cvt_pk_bf16_f32 v240, v240, v241\n\t"
    "v_mfma_f32_16x16x32_bf16 a[40:43], a[168:171], v[232:235], a[40:43]\n\t"
    "v_exp_f32_e32 v245, v245\n\t"
    "v_mfma_f32_16x16x32_bf16 a[44:47], a[172:175], v[232:235], a[44:47]\n\t"
    "v_add_f32_e32 v34, v245, v34\n\t"
    "v_cvt_pk_bf16_f32 v241, v242, v243\n\t"
    "v_mfma_f32_16x16x32_bf16 a[48:51], a[176:179], v[232:235], a[48:51]\n\t"
    "v_exp_f32_e32 v246, v246\n\t"
    "v_mfma_f32_16x16x32_bf16 a[52:55], a[180:183], v[232:235], a[52:55]\n\t"
    "s_add_u32 m0, s14, 0x19400\n\t"
    "v_add_f32_e32 v34, v246, v34\n\t"
    "v_cvt_pk_bf16_f32 v242, v244, v245\n\t"
    "v_mfma_f32_16x16x32_bf16 a[56:59], a[184:187], v[232:235], a[56:59]\n\t"
    "buffer_load_dwordx4 v27, s[16:19], s61 offen lds\n\t"
    "v_exp_f32_e32 v247, v247\n\t"
    "v_mfma_f32_16x16x32_bf16 a[60:63], a[188:191], v[232:235], a[60:63]\n\t"
    "v_add_f32_e32 v34, v247, v34\n\t"
    "v_cvt_pk_bf16_f32 v243, v246, v247\n\t"
    "v_mfma_f32_16x16x32_bf16 v[232:235], a[224:227], v[128:131], v[56:59]\n\t"
    "v_exp_f32_e32 v224, v224\n\t"
    "v_mfma_f32_16x16x32_bf16 v[232:235], a[228:231], v[132:135], v[232:235]\n\t"
    "ds_read_b64_tr_b16 a[144:145], v20 offset:512\n\t"
    "v_add_f32_e32 v35, v224, v35\n\t"
    "v_mfma_f32_16x16x32_bf16 v[232:235], a[232:235], v[136:139], v[232:235]\n\t"
    "v_exp_f32_e32 v225, v225\n\t"
    "v_mfma_f32_16x16x32_bf16 v[232:235], a[236:239], v[140:143], v[232:235]\n\t"
    "ds_read_b64_tr_b16 a[146:147], v20 offset:1536\n\t"
    "v_add_f32_e32 v35, v225, v35\n\t"
    "v_mfma_f32_16x16x32_bf16 v[236:239], a[240:243], v[128:131], v[56:59]\n\t"
    "v_exp_f32_e32 v226, v226\n\t"
    "v_mfma_f32_16x16x32_bf16 v[236:239], a[244:247], v[132:135], v[236:239]\n\t"
    "ds_read_b64_tr_b16 a[148:149], v24 offset:512\n\t"
    "v_add_f32_e32 v35, v226, v35\n\t"
    "v_mfma_f32_16x16x32_bf16 v[236:239], a[248:251], v[136:139], v[236:239]\n\t"
    "v_exp_f32_e32 v227, v227\n\t"
    "v_mfma_f32_16x16x32_bf16 v[236:239], a[252:255], v[140:143], v[236:239]\n\t"
    "ds_read_b64_tr_b16 a[150:151], v24 offset:1536\n\t"
    "v_add_f32_e32 v35, v227, v35\n\t"
    "v_mfma_f32_16x16x32_bf16 a[64:67], a[160:163], v[240:243], a[64:67]\n\t"
    "v_exp_f32_e32 v228, v228\n\t"
    "v_mfma_f32_16x16x32_bf16 a[68:71], a[164:167], v[240:243], a[68:71]\n\t"
    "v_add_f32_e32 v35, v228, v35\n\t"
    "v_cvt_pk_bf16_f32 v224, v224, v225\n\t"
    "v_mfma_f32_16x16x32_bf16 a[72:75], a[168:171], v[240:243], a[72:75]\n\t"
    "v_exp_f32_e32 v229, v229\n\t"
    "v_mfma_f32_16x16x32_bf16 a[76:79], a[172:175], v[240:243], a[76:79]\n\t"
    "s_add_u32 m0, s14, 0x19800\n\t"
    "v_add_f32_e32 v35, v229, v35\n\t"
    "v_cvt_pk_bf16_f32 v225, v226, v227\n\t"
    "v_mfma_f32_16x16x32_bf16 a[80:83], a[176:179], v[240:243], a[80:83]\n\t"
    "buffer_load_dwordx4 v28, s[16:19], s60 offen lds\n\t"
    "v_exp_f32_e32 v230, v230\n\t"
    "v_mfma_f32_16x16x32_bf16 a[84:87], a[180:183], v[240:243], a[84:87]\n\t"
    "v_add_f32_e32 v35, v230, v35\n\t"
    "v_cvt_pk_bf16_f32 v226, v228, v229\n\t"
    "v_mfma_f32_16x16x32_bf16 a[88:91], a[184:187], v[240:243], a[88:91]\n\t"
    "v_exp_f32_e32 v231, v231\n\t"
    "v_mfma_f32_16x16x32_bf16 a[92:95], a[188:191], v[240:243], a[92:95]\n\t"
    "v_add_f32_e32 v35, v231, v35\n\t"
    "v_cvt_pk_bf16_f32 v227, v230, v231\n\t"
    "v_mfma_f32_16x16x32_bf16 v[240:243], a[224:227], v[144:147], v[60:63]\n\t"
    "v_exp_f32_e32 v232, v232\n\t"
    "v_mfma_f32_16x16x32_bf16 v[240:243], a[228:231], v[148:151], v[240:243]\n\t"
    "ds_read_b64_tr_b16 a[152:153], v20 offset:768\n\t"
    "v_add_f32_e32 v36, v232, v36\n\t"
    "v_mfma_f32_16x16x32_bf16 v[240:243], a[232:235], v[152:155], v[240:243]\n\t"
    "v_exp_f32_e32 v233, v233\n\t"
    "v_mfma_f32_16x16x32_bf16 v[240:243], a[236:239], v[156:159], v[240:243]\n\t"
    "ds_read_b64_tr_b16 a[154:155], v20 offset:1792\n\t"
    "v_add_f32_e32 v36, v233, v36\n\t"
    "v_mfma_f32_16x16x32_bf16 v[244:247], a[240:243], v[144:147], v[60:63]\n\t"
    "v_exp_f32_e32 v234, v234\n\t"
    "v_mfma_f32_16x16x32_bf16 v[244:247], a[244:247], v[148:151], v[244:247]\n\t"
    "ds_read_b64_tr_b16 a[156:157], v24 offset:768\n\t"
    "v_add_f32_e32 v36, v234, v36\n\t"
    "v_mfma_f32_16x16x32_bf16 v[244:247], a[248:251], v[152:155], v[244:247]\n\t"
    "v_exp_f32_e32 v235, v235\n\t"
    "v_mfma_f32_16x16x32_bf16 v[244:247], a[252:255], v[156:159], v[244:247]\n\t"
    "ds_read_b64_tr_b16 a[158:159], v24 offset:1792\n\t"
    "v_add_f32_e32 v36, v235, v36\n\t"
    "v_mfma_f32_16x16x32_bf16 a[96:99], a[160:163], v[224:227], a[96:99]\n\t"
    "v_exp_f32_e32 v236, v236\n\t"
    "v_mfma_f32_16x16x32_bf16 a[100:103], a[164:167], v[224:227], a[100:103]\n\t"
    "s_memrealtime s[30:31]\n\t"
    "s_add_u32 m0, s14, 0x19c00\n\t"
    "v_add_f32_e32 v36, v236, v36\n\t"
    "v_cvt_pk_bf16_f32 v232, v232, v233\n\t"
    "v_mfma_f32_16x16x32_bf16 a[104:107], a[168:171], v[224:227], a[104:107]\n\t"
    "buffer_load_dwordx4 v28, s[16:19], s61 offen lds\n\t"
    "v_exp_f32_e32 v237, v237\n\t"
    "v_mfma_f32_16x16x32_bf16 a[108:111], a[172:175], v[224:227], a[108:111]\n\t"
    "v_add_f32_e32 v36, v237, v36\n\t"
    "v_cvt_pk_bf16_f32 v233, v234, v235\n\t"
    "v_mfma_f32_16x16x32_bf16 a[112:115], a[176:179], v[224:227], a[112:115]\n\t"
    "v_exp_f32_e32 v238, v238\n\t"
    "v_mfma_f32_16x16x32_bf16 a[116:119], a[180:183], v[224:227], a[116:119]\n\t"
    "v_add_f32_e32 v36, v238, v36\n\t"
    "v_cvt_pk_bf16_f32 v234, v236, v237\n\t"
    "v_mfma_f32_16x16x32_bf16 a[120:123], a[184:187], v[224:227], a[120:123]\n\t"
    "s_waitcnt vmcnt(9) lgkmcnt(0)\n\t"
    "v_exp_f32_e32 v239, v239\n\t"
    "v_mfma_f32_16x16x32_bf16 a[124:127], a[188:191], v[224:227], a[124:127]\n\t"
    "s_barrier\n\t"
    "v_add_f32_e32 v36, v239, v36\n\t"
    "v_cvt_pk_bf16_f32 v235, v238, v239\n\t"
    "s_sub_i32 s31, s29, s30\n\t"
    "s_cmp_gt_i32 s31, 0\n\t"
    "s_cselect_b32 s29, s29, s30\n\t"
    "s_cbranch_scc0 .Lfa_pa83e\n\t"
    "s_min_i32 s31, s31, 0xc8\n\t"
    ".Lfa_pa83:\n\t"
    "s_sub_u32 s31, s31, 2\n\t"
    "s_cmp_gt_i32 s31, 0\n\t"
    "s_cbranch_scc1 .Lfa_pa83\n\t"
    ".Lfa_pa83e:\n\t"
    "s_add_u32 s29, s29, 0x71\n\t"
    "v_mfma_f32_16x16x32_bf16 v[224:227], a[192:195], v[64:67], v[40:43]\n\t"
    "s_bfe_u32 s69, s71, 0x3000d\n\t"
    "v_exp_f32_e32 v240, v240\n\t"
    "v_mfma_f32_16x16x32_bf16 v[224:227], a[196:199], v[68:71], v[224:227]\n\t"
    "ds_read_b128 a[224:227], v30 offset:0\n\t"
    "v_add_f32_e32 v37, v240, v37\n\t"
    "v_mfma_f32_16x16x32_bf16 v[224:227], a[200:203], v[72:75], v[224:227]\n\t"
    "s_bfe_u32 s75, s71, 0x1000c\n\t"
    "v_exp_f32_e32 v241, v241\n\t"
    "v_mfma_f32_16x16x32_bf16 v[224:227], a[204:207], v[76:79], v[224:227]\n\t"
    "ds_read_b128 a[228:231], v30 offset:256\n\t"
    "v_add_f32_e32 v37, v241, v37\n\t"
    "v_mfma_f32_16x16x32_bf16 v[228:231], a[208:211], v[64:67], v[40:43]\n\t"
    "s_lshl2_add_u32 s69, s69, s75\n\t"
    "v_exp_f32_e32 v242, v242\n\t"
    "v_mfma_f32_16x16x32_bf16 v[228:231], a[212:215], v[68:71], v[228:231]\n\t"
    "ds_read_b128 a[232:235], v30 offset:512\n\t"
    "v_add_f32_e32 v37, v242, v37\n\t"
    "v_mfma_f32_16x16x32_bf16 v[228:231], a[216:219], v[72:75], v[228:231]\n\t"
    "s_lshl_b32 s69, s69, 11\n\t"
    "s_add_u32 s69, s69, s25\n\t"
    "s_lshl2_add_u32 s69, s69, s79\n\t"
    "s_lshl_b32 s69, s69, 4\n\t"
    "s_add_u32 s70, s69, 0x8000\n\t"
    "v_exp_f32_e32 v243, v243\n\t"
    "v_mfma_f32_16x16x32_bf16 v[228:231], a[220:223], v[76:79], v[228:231]\n\t"
    "ds_read_b128 a[236:239], v30 offset:768\n\t"
    "v_add_f32_e32 v37, v243, v37\n\t"
    "v_mfma_f32_16x16x32_bf16 v[160:163], a[160:163], v[232:235], v[160:163]\n\t"
    "v_exp_f32_e32 v244, v244\n\t"
    "v_mfma_f32_16x16x32_bf16 v[164:167], a[164:167], v[232:235], v[164:167]\n\t"
    "s_add_u32 m0, s14, 0x20000\n\t"
    "v_add_f32_e32 v37, v244, v37\n\t"
    "v_cvt_pk_bf16_f32 v240, v240, v241\n\t"
    "v_mfma_f32_16x16x32_bf16 v[168:171], a[168:171], v[232:235], v[168:171]\n\t"
    "buffer_load_dwordx4 v29, s[16:19], s69 offen lds\n\t"
    "v_exp_f32_e32 v245, v245\n\t"
    "v_mfma_f32_16x16x32_bf16 v[172:175], a[172:175], v[232:235], v[172:175]\n\t"
    "v_add_f32_e32 v37, v245, v37\n\t"
    "v_cvt_pk_bf16_f32 v241, v242, v243\n\t"
    "v_mfma_f32_16x16x32_bf16 v[176:179], a[176:179], v[232:235], v[176:179]\n\t"
    "v_exp_f32_e32 v246, v246\n\t"
    "v_mfma_f32_16x16x32_bf16 v[180:183], a[180:183], v[232:235], v[180:183]\n\t"
    "s_add_u32 m0, s14, 0x20400\n\t"
    "v_add_f32_e32 v37, v246, v37\n\t"
    "v_cvt_pk_bf16_f32 v242, v244, v245\n\t"
    "v_mfma_f32_16x16x32_bf16 v[184:187], a[184:187], v[232:235], v[184:187]\n\t"
    "buffer_load_dwordx4 v29, s[16:19], s70 offen lds\n\t"
    "v_exp_f32_e32 v247, v247\n\t"
    "v_mfma_f32_16x16x32_bf16 v[188:191], a[188:191], v[232:235], v[188:191]\n\t"
    "v_add_f32_e32 v37, v247, v37\n\t"
    "v_cvt_pk_bf16_f32 v243, v246, v247\n\t"
    "v_mfma_f32_16x16x32_bf16 v[232:235], a[192:195], v[80:83], v[44:47]\n\t"
    "v_exp_f32_e32 v224, v224\n\t"
    "v_mfma_f32_16x16x32_bf16 v[232:235], a[196:199], v[84:87], v[232:235]\n\t"
    "ds_read_b128 a[240:243], v30 offset:1024\n\t"
    "v_add_f32_e32 v32, v224, v32\n\t"
    "v_mfma_f32_16x16x32_bf16 v[232:235], a[200:203], v[88:91], v[232:235]\n\t"
    "v_exp_f32_e32 v225, v225\n\t"
    "v_mfma_f32_16x16x32_bf16 v[232:235], a[204:207], v[92:95], v[232:235]\n\t"
    "ds_read_b128 a[244:247], v30 offset:1280\n\t"
    "v_add_f32_e32 v32, v225, v32\n\t"
    "v_mfma_f32_16x16x32_bf16 v[236:239], a[208:211], v[80:83], v[44:47]\n\t"
    "v_exp_f32_e32 v226, v226\n\t"
    "v_mfma_f32_16x16x32_bf16 v[236:239], a[212:215], v[84:87], v[236:239]\n\t"
    "ds_read_b128 a[248:251], v30 offset:1536\n\t"
    "v_add_f32_e32 v32, v226, v32\n\t"
    "v_mfma_f32_16x16x32_bf16 v[236:239], a[216:219], v[88:91], v[236:239]\n\t"
    "v_exp_f32_e32 v227, v227\n\t"
    "v_mfma_f32_16x16x32_bf16 v[236:239], a[220:223], v[92:95], v[236:239]\n\t"
    "ds_read_b128 a[252:255], v30 offset:1792\n\t"
    "v_add_f32_e32 v32, v227, v32\n\t"
    "v_mfma_f32_16x16x32_bf16 v[192:195], a[160:163], v[240:243], v[192:195]\n\t"
    "v_exp_f32_e32 v228, v228\n\t"
    "v_mfma_f32_16x16x32_bf16 v[196:199], a[164:167], v[240:243], v[196:199]\n\t"
    "v_add_f32_e32 v32, v228, v32\n\t"
    "v_cvt_pk_bf16_f32 v224, v224, v225\n\t"
    "v_mfma_f32_16x16x32_bf16 v[200:203], a[168:171], v[240:243], v[200:203]\n\t"
    "v_exp_f32_e32 v229, v229\n\t"
    "v_mfma_f32_16x16x32_bf16 v[204:207], a[172:175], v[240:243], v[204:207]\n\t"
    "s_add_u32 m0, s14, 0x20800\n\t"
    "v_add_f32_e32 v32, v229, v32\n\t"
    "v_cvt_pk_bf16_f32 v225, v226, v227\n\t"
    "v_mfma_f32_16x16x32_bf16 v[208:211], a[176:179], v[240:243], v[208:211]\n\t"
    "buffer_load_dwordx4 v26, s[16:19], s69 offen lds\n\t"
    "v_exp_f32_e32 v230, v230\n\t"
    "v_mfma_f32_16x16x32_bf16 v[212:215], a[180:183], v[240:243], v[212:215]\n\t"
    "v_add_f32_e32 v32, v230, v32\n\t"
    "v_cvt_pk_bf16_f32 v226, v228, v229\n\t"
    "v_mfma_f32_16x16x32_bf16 v[216:219], a[184:187], v[240:243], v[216:219]\n\t"
    "v_exp_f32_e32 v231, v231\n\t"
    "v_mfma_f32_16x16x32_bf16 v[220:223], a[188:191], v[240:243], v[220:223]\n\t"
    "v_add_f32_e32 v32, v231, v32\n\t"
    "v_cvt_pk_bf16_f32 v227, v230, v231\n\t"
    "v_mfma_f32_16x16x32_bf16 v[240:243], a[192:195], v[96:99], v[48:51]\n\t"
    "v_exp_f32_e32 v232, v232\n\t"
    "v_mfma_f32_16x16x32_bf16 v[240:243], a[196:199], v[100:103], v[240:243]\n\t"
    "ds_read_b64_tr_b16 a[160:161], v22 offset:0\n\t"
    "v_add_f32_e32 v33, v232, v33\n\t"
    "v_mfma_f32_16x16x32_bf16 v[240:243], a[200:203], v[104:107], v[240:243]\n\t"
    "v_exp_f32_e32 v233, v233\n\t"
    "v_mfma_f32_16x16x32_bf16 v[240:243], a[204:207], v[108:111], v[240:243]\n\t"
    "ds_read_b64_tr_b16 a[162:163], v22 offset:1024\n\t"
    "v_add_f32_e32 v33, v233, v33\n\t"
    "v_mfma_f32_16x16x32_bf16 v[244:247], a[208:211], v[96:99], v[48:51]\n\t"
    "v_exp_f32_e32 v234, v234\n\t"
    "v_mfma_f32_16x16x32_bf16 v[244:247], a[212:215], v[100:103], v[244:247]\n\t"
    "ds_read_b64_tr_b16 a[164:165], v23 offset:0\n\t"
    "v_add_f32_e32 v33, v234, v33\n\t"
    "v_mfma_f32_16x16x32_bf16 v[244:247], a[216:219], v[104:107], v[244:247]\n\t"
    "v_exp_f32_e32 v235, v235\n\t"
    "v_mfma_f32_16x16x32_bf16 v[244:247], a[220:223], v[108:111], v[244:247]\n\t"
    "ds_read_b64_tr_b16 a[166:167], v23 offset:1024\n\t"
    "v_add_f32_e32 v33, v235, v33\n\t"
    "v_mfma_f32_16x16x32_bf16 a[0:3], a[128:131], v[224:227], a[0:3]\n\t"
    "v_exp_f32_e32 v236, v236\n\t"
    "v_mfma_f32_16x16x32_bf16 a[4:7], a[132:135], v[224:227], a[4:7]\n\t"
    "s_add_u32 m0, s14, 0x20c00\n\t"
    "v_add_f32_e32 v33, v236, v33\n\t"
    "v_cvt_pk_bf16_f32 v232, v232, v233\n\t"
    "v_mfma_f32_16x16x32_bf16 a[8:11], a[136:139], v[224:227], a[8:11]\n\t"
    "buffer_load_dwordx4 v26, s[16:19], s70 offen lds\n\t"
    "v_exp_f32_e32 v237, v237\n\t"
    "v_mfma_f32_16x16x32_bf16 a[12:15], a[140:143], v[224:227], a[12:15]\n\t"
    "v_add_f32_e32 v33, v237, v33\n\t"
    "v_cvt_pk_bf16_f32 v233, v234, v235\n\t"
    "v_mfma_f32_16x16x32_bf16 a[16:19], a[144:147], v[224:227], a[16:19]\n\t"
    "v_exp_f32_e32 v238, v238\n\t"
    "v_mfma_f32_16x16x32_bf16 a[20:23], a[148:151], v[224:227], a[20:23]\n\t"
    "v_add_f32_e32 v33, v238, v33\n\t"
    "v_cvt_pk_bf16_f32 v234, v236, v237\n\t"
    "v_mfma_f32_16x16x32_bf16 a[24:27], a[152:155], v[224:227], a[24:27]\n\t"
    "s_waitcnt lgkmcnt(4)\n\t"
    "v_exp_f32_e32 v239, v239\n\t"
    "v_mfma_f32_16x16x32_bf16 a[28:31], a[156:159], v[224:227], a[28:31]\n\t"
    "v_add_f32_e32 v33, v239, v33\n\t"
    "v_cvt_pk_bf16_f32 v235, v238, v239\n\t"
    "v_mfma_f32_16x16x32_bf16 v[224:227], a[224:227], v[64:67], v[40:43]\n\t"
    "v_exp_f32_e32 v240, v240\n\t"
    "v_mfma_f32_16x16x32_bf16 v[224:227], a[228:231], v[68:71], v[224:227]\n\t"
    "ds_read_b64_tr_b16 a[168:169], v22 offset:256\n\t"
    "v_add_f32_e32 v34, v240, v34\n\t"
    "v_mfma_f32_16x16x32_bf16 v[224:227], a[232:235], v[72:75], v[224:227]\n\t"
    "v_exp_f32_e32 v241, v241\n\t"
    "v_mfma_f32_16x16x32_bf16 v[224:227], a[236:239], v[76:79], v[224:227]\n\t"
    "ds_read_b64_tr_b16 a[170:171], v22 offset:1280\n\t"
    "v_add_f32_e32 v34, v241, v34\n\t"
    "v_mfma_f32_16x16x32_bf16 v[228:231], a[240:243], v[64:67], v[40:43]\n\t"
    "v_exp_f32_e32 v242, v242\n\t"
    "v_mfma_f32_16x16x32_bf16 v[228:231], a[244:247], v[68:71], v[228:231]\n\t"
    "ds_read_b64_tr_b16 a[172:173], v23 offset:256\n\t"
    "v_add_f32_e32 v34, v242, v34\n\t"
    "v_mfma_f32_16x16x32_bf16 v[228:231], a[248:251], v[72:75], v[228:231]\n\t"
    "s_add_u32 m0, s14, 0x21000\n\t"
    "v_exp_f32_e32 v243, v243\n\t"
    "v_mfma_f32_16x16x32_bf16 v[228:231], a[252:255], v[76:79], v[228:231]\n\t"
    "ds_read_b64_tr_b16 a[174:175], v23 offset:1280\n\t"
    "v_add_f32_e32 v34, v243, v34\n\t"
    "v_mfma_f32_16x16x32_bf16 a[32:35], a[128:131], v[232:235], a[32:35]\n\t"
    "buffer_load_dwordx4 v27, s[16:19], s69 offen lds\n\t"
    "v_exp_f32_e32 v244, v244\n\t"
    "v_mfma_f32_16x16x32_bf16 a[36:39], a[132:135], v[232:235], a[36:39]\n\t"
    "v_add_f32_e32 v34, v244, v34\n\t"
    "v_cvt_pk_bf16_f32 v240, v240, v241\n\t"
    "v_mfma_f32_16x16x32_bf16 a[40:43], a[136:139], v[232:235], a[40:43]\n\t"
    "v_exp_f32_e32 v245, v245\n\t"
    "v_mfma_f32_16x16x32_bf16 a[44:47], a[140:143], v[232:235], a[44:47]\n\t"
    "v_add_f32_e32 v34, v245, v34\n\t"
    "v_cvt_pk_bf16_f32 v241, v242, v243\n\t"
    "v_mfma_f32_16x16x32_bf16 a[48:51], a[144:147], v[232:235], a[48:51]\n\t"
    "v_exp_f32_e32 v246, v246\n\t"
    "v_mfma_f32_16x16x32_bf16 a[52:55], a[148:151], v[232:235], a[52:55]\n\t"
    "s_add_u32 m0, s14, 0x21400\n\t"
    "v_add_f32_e32 v34, v246, v34\n\t"
    "v_cvt_pk_bf16_f32 v242, v244, v245\n\t"
    "v_mfma_f32_16x16x32_bf16 a[56:59], a[152:155], v[232:235], a[56:59]\n\t"
    "buffer_load_dwordx4 v27, s[16:19], s70 offen lds\n\t"
    "v_exp_f32_e32 v247, v247\n\t"
    "v_mfma_f32_16x16x32_bf16 a[60:63], a[156:159], v[232:235], a[60:63]\n\t"
    "v_add_f32_e32 v34, v247, v34\n\t"
    "v_cvt_pk_bf16_f32 v243, v246, v247\n\t"
    "v_mfma_f32_16x16x32_bf16 v[232:235], a[192:195], v[112:115], v[52:55]\n\t"
    "v_exp_f32_e32 v224, v224\n\t"
    "v_mfma_f32_16x16x32_bf16 v[232:235], a[196:199], v[116:119], v[232:235]\n\t"
    "ds_read_b64_tr_b16 a[176:177], v22 offset:512\n\t"
    "v_add_f32_e32 v32, v224, v32\n\t"
    "v_mfma_f32_16x16x32_bf16 v[232:235], a[200:203], v[120:123], v[232:235]\n\t"
    "v_exp_f32_e32 v225, v225\n\t"
    "v_mfma_f32_16x16x32_bf16 v[232:235], a[204:207], v[124:127], v[232:235]\n\t"
    "ds_read_b64_tr_b16 a[178:179], v22 offset:1536\n\t"
    "v_add_f32_e32 v32, v225, v32\n\t"
    "v_mfma_f32_16x16x32_bf16 v[236:239], a[208:211], v[112:115], v[52:55]\n\t"
    "v_exp_f32_e32 v226, v226\n\t"
    "v_mfma_f32_16x16x32_bf16 v[236:239], a[212:215], v[116:119], v[236:239]\n\t"
    "ds_read_b64_tr_b16 a[180:181], v23 offset:512\n\t"
    "v_add_f32_e32 v32, v226, v32\n\t"
    "v_mfma_f32_16x16x32_bf16 v[236:239], a[216:219], v[120:123], v[236:239]\n\t"
    "v_exp_f32_e32 v227, v227\n\t"
    "v_mfma_f32_16x16x32_bf16 v[236:239], a[220:223], v[124:127], v[236:239]\n\t"
    "ds_read_b64_tr_b16 a[182:183], v23 offset:1536\n\t"
    "v_add_f32_e32 v32, v227, v32\n\t"
    "v_mfma_f32_16x16x32_bf16 a[64:67], a[128:131], v[240:243], a[64:67]\n\t"
    "v_exp_f32_e32 v228, v228\n\t"
    "v_mfma_f32_16x16x32_bf16 a[68:71], a[132:135], v[240:243], a[68:71]\n\t"
    "v_add_f32_e32 v32, v228, v32\n\t"
    "v_cvt_pk_bf16_f32 v224, v224, v225\n\t"
    "v_mfma_f32_16x16x32_bf16 a[72:75], a[136:139], v[240:243], a[72:75]\n\t"
    "v_exp_f32_e32 v229, v229\n\t"
    "v_mfma_f32_16x16x32_bf16 a[76:79], a[140:143], v[240:243], a[76:79]\n\t"
    "s_add_u32 m0, s14, 0x21800\n\t"
    "v_add_f32_e32 v32, v229, v32\n\t"
    "v_cvt_pk_bf16_f32 v225, v226, v227\n\t"
    "v_mfma_f32_16x16x32_bf16 a[80:83], a[144:147], v[240:243], a[80:83]\n\t"
    "buffer_load_dwordx4 v28, s[16:19], s69 offen lds\n\t"
    "v_exp_f32_e32 v230, v230\n\t"
    "v_mfma_f32_16x16x32_bf16 a[84:87], a[148:151], v[240:243], a[84:87]\n\t"
    "v_add_f32_e32 v32, v230, v32\n\t"
    "v_cvt_pk_bf16_f32 v226, v228, v229\n\t"
    "v_mfma_f32_16x16x32_bf16 a[88:91], a[152:155], v[240:243], a[88:91]\n\t"
    "v_exp_f32_e32 v231, v231\n\t"
    "v_mfma_f32_16x16x32_bf16 a[92:95], a[156:159], v[240:243], a[92:95]\n\t"
    "v_add_f32_e32 v32, v231, v32\n\t"
    "v_cvt_pk_bf16_f32 v227, v230, v231\n\t"
    "v_mfma_f32_16x16x32_bf16 v[240:243], a[224:227], v[80:83], v[44:47]\n\t"
    "v_exp_f32_e32 v232, v232\n\t"
    "v_mfma_f32_16x16x32_bf16 v[240:243], a[228:231], v[84:87], v[240:243]\n\t"
    "ds_read_b64_tr_b16 a[184:185], v22 offset:768\n\t"
    "v_add_f32_e32 v35, v232, v35\n\t"
    "v_mfma_f32_16x16x32_bf16 v[240:243], a[232:235], v[88:91], v[240:243]\n\t"
    "v_exp_f32_e32 v233, v233\n\t"
    "v_mfma_f32_16x16x32_bf16 v[240:243], a[236:239], v[92:95], v[240:243]\n\t"
    "ds_read_b64_tr_b16 a[186:187], v22 offset:1792\n\t"
    "v_add_f32_e32 v35, v233, v35\n\t"
    "v_mfma_f32_16x16x32_bf16 v[244:247], a[240:243], v[80:83], v[44:47]\n\t"
    "v_exp_f32_e32 v234, v234\n\t"
    "v_mfma_f32_16x16x32_bf16 v[244:247], a[244:247], v[84:87], v[244:247]\n\t"
    "ds_read_b64_tr_b16 a[188:189], v23 offset:768\n\t"
    "v_add_f32_e32 v35, v234, v35\n\t"
    "v_mfma_f32_16x16x32_bf16 v[244:247], a[248:251], v[88:91], v[244:247]\n\t"
    "s_waitcnt lgkmcnt(3)\n\t"
    "s_add_u32 m0, s14, 0x21c00\n\t"
    "v_exp_f32_e32 v235, v235\n\t"
    "v_mfma_f32_16x16x32_bf16 v[244:247], a[252:255], v[92:95], v[244:247]\n\t"
    "ds_read_b64_tr_b16 a[190:191], v23 offset:1792\n\t"
    "v_add_f32_e32 v35, v235, v35\n\t"
    "v_mfma_f32_16x16x32_bf16 a[0:3], a[160:163], v[224:227], a[0:3]\n\t"
    "buffer_load_dwordx4 v28, s[16:19], s70 offen lds\n\t"
    "v_exp_f32_e32 v236, v236\n\t"
    "v_mfma_f32_16x16x32_bf16 a[4:7], a[164:167], v[224:227], a[4:7]\n\t"
    "v_add_f32_e32 v35, v236, v35\n\t"
    "v_cvt_pk_bf16_f32 v232, v232, v233\n\t"
    "v_mfma_f32_16x16x32_bf16 a[8:11], a[168:171], v[224:227], a[8:11]\n\t"
    "v_exp_f32_e32 v237, v237\n\t"
    "v_mfma_f32_16x16x32_bf16 a[12:15], a[172:175], v[224:227], a[12:15]\n\t"
    "v_add_f32_e32 v35, v237, v35\n\t"
    "v_cvt_pk_bf16_f32 v233, v234, v235\n\t"
    "v_mfma_f32_16x16x32_bf16 a[16:19], a[176:179], v[224:227], a[16:19]\n\t"
    "s_waitcnt lgkmcnt(0)\n\t"
    "v_exp_f32_e32 v238, v238\n\t"
    "v_mfma_f32_16x16x32_bf16 a[20:23], a[180:183], v[224:227], a[20:23]\n\t"
    "v_add_f32_e32 v35, v238, v35\n\t"
    "v_cvt_pk_bf16_f32 v234, v236, v237\n\t"
    "v_mfma_f32_16x16x32_bf16 a[24:27], a[184:187], v[224:227], a[24:27]\n\t"
    "s_waitcnt vmcnt(16) lgkmcnt(0)\n\t"
    "v_exp_f32_e32 v239, v239\n\t"
    "v_mfma_f32_16x16x32_bf16 a[28:31], a[188:191], v[224:227], a[28:31]\n\t"
    "s_barrier\n\t"
    "v_add_f32_e32 v35, v239, v35\n\t"
    "v_cvt_pk_bf16_f32 v235, v238, v239\n\t"
    "v_mfma_f32_16x16x32_bf16 v[224:227], a[192:195], v[128:131], v[56:59]\n\t"
    "s_bfe_u32 s60, s71, 0x30011\n\t"
    "v_exp_f32_e32 v240, v240\n\t"
    "v_mfma_f32_16x16x32_bf16 v[224:227], a[196:199], v[132:135], v[224:227]\n\t"
    "v_add_f32_e32 v33, v240, v33\n\t"
    "v_mfma_f32_16x16x32_bf16 v[224:227], a[200:203], v[136:139], v[224:227]\n\t"
    "s_bfe_u32 s75, s71, 0x10010\n\t"
    "v_exp_f32_e32 v241, v241\n\t"
    "v_mfma_f32_16x16x32_bf16 v[224:227], a[204:207], v[140:143], v[224:227]\n\t"
    "v_add_f32_e32 v33, v241, v33\n\t"
    "v_mfma_f32_16x16x32_bf16 v[228:231], a[208:211], v[128:131], v[56:59]\n\t"
    "s_lshl2_add_u32 s60, s60, s75\n\t"
    "v_exp_f32_e32 v242, v242\n\t"
    "v_mfma_f32_16x16x32_bf16 v[228:231], a[212:215], v[132:135], v[228:231]\n\t"
    "v_add_f32_e32 v33, v242, v33\n\t"
    "v_mfma_f32_16x16x32_bf16 v[228:231], a[216:219], v[136:139], v[228:231]\n\t"
    "s_lshl_b32 s60, s60, 11\n\t"
    "s_add_u32 s60, s60, s25\n\t"
    "s_lshl2_add_u32 s60, s60, s79\n\t"
    "s_lshl_b32 s60, s60, 4\n\t"
    "s_add_u32 s61, s60, 0x8000\n\t"
    "v_exp_f32_e32 v243, v243\n\t"
    "v_mfma_f32_16x16x32_bf16 v[228:231], a[220:223], v[140:143], v[228:231]\n\t"
    "v_add_f32_e32 v33, v243, v33\n\t"
    "v_mfma_f32_16x16x32_bf16 a[96:99], a[128:131], v[232:235], a[96:99]\n\t"
    "v_exp_f32_e32 v244, v244\n\t"
    "v_mfma_f32_16x16x32_bf16 a[100:103], a[132:135], v[232:235], a[100:103]\n\t"
    "s_add_u32 m0, s14, 0\n\t"
    "v_add_f32_e32 v33, v244, v33\n\t"
    "v_cvt_pk_bf16_f32 v240, v240, v241\n\t"
    "v_mfma_f32_16x16x32_bf16 a[104:107], a[136:139], v[232:235], a[104:107]\n\t"
    "buffer_load_dwordx4 v29, s[16:19], s60 offen lds\n\t"
    "v_exp_f32_e32 v245, v245\n\t"
    "v_mfma_f32_16x16x32_bf16 a[108:111], a[140:143], v[232:235], a[108:111]\n\t"
    "v_add_f32_e32 v33, v245, v33\n\t"
    "v_cvt_pk_bf16_f32 v241, v242, v243\n\t"
    "v_mfma_f32_16x16x32_bf16 a[112:115], a[144:147], v[232:235], a[112:115]\n\t"
    "v_exp_f32_e32 v246, v246\n\t"
    "v_mfma_f32_16x16x32_bf16 a[116:119], a[148:151], v[232:235], a[116:119]\n\t"
    "v_add_f32_e32 v33, v246, v33\n\t"
    "v_cvt_pk_bf16_f32 v242, v244, v245\n\t"
    "v_mfma_f32_16x16x32_bf16 a[120:123], a[152:155], v[232:235], a[120:123]\n\t"
    "v_exp_f32_e32 v247, v247\n\t"
    "v_mfma_f32_16x16x32_bf16 a[124:127], a[156:159], v[232:235], a[124:127]\n\t"
    "v_add_f32_e32 v33, v247, v33\n\t"
    "v_cvt_pk_bf16_f32 v243, v246, v247\n\t"
    "v_mov_b32_e32 v8, v32\n\t"
    "s_mov_b32 s75, 0x5d800000\n\t"
    "s_add_u32 s68, s76, 0\n\t"
    "v_permlane32_swap_b32_e32 v32, v8\n\t"
    "v_accvgpr_read_b32 v0, a0\n\t"
    "v_add_f32_e32 v32, v32, v8\n\t"
    "v_mov_b32_e32 v8, v32\n\t"
    "v_accvgpr_read_b32 v1, a1\n\t"
    "v_accvgpr_read_b32 v2, a2\n\t"
    "v_permlane16_swap_b32_e32 v32, v8\n\t"
    "v_accvgpr_read_b32 v3, a3\n\t"
    "v_add_f32_e32 v32, v32, v8\n\t"
    "v_rcp_f32_e32 v10, v32\n\t"
    "v_cmp_nge_f32_e32 vcc, s75, v32\n\t"
    "v_accvgpr_read_b32 v4, a4\n\t"
    "v_accvgpr_read_b32 v5, a5\n\t"
    "v_accvgpr_read_b32 v6, a6\n\t"
    "v_accvgpr_read_b32 v7, a7\n\t"
    "v_pk_mul_f32 v[0:1], v[0:1], v[10:11] op_sel_hi:[1,0]\n\t"
    "v_pk_mul_f32 v[2:3], v[2:3], v[10:11] op_sel_hi:[1,0]\n\t"
    "v_pk_mul_f32 v[4:5], v[4:5], v[10:11] op_sel_hi:[1,0]\n\t"
    "v_pk_mul_f32 v[6:7], v[6:7], v[10:11] op_sel_hi:[1,0]\n\t"
    "s_mov_b64 s[94:95], vcc\n\t"
    "v_cvt_pk_bf16_f32 v248, v0, v1\n\t"
    "v_cvt_pk_bf16_f32 v249, v2, v3\n\t"
    "v_cvt_pk_bf16_f32 v250, v4, v5\n\t"
    "v_cvt_pk_bf16_f32 v251, v6, v7\n\t"
    "buffer_store_dwordx4 v[248:251], v31, s[52:55], s68 offen sc1\n\t"
    "v_accvgpr_write_b32 a0, v248\n\t"
    "v_accvgpr_write_b32 a1, v249\n\t"
    "v_accvgpr_write_b32 a2, v250\n\t"
    "v_accvgpr_write_b32 a3, v251\n\t"
    "v_accvgpr_read_b32 v0, a8\n\t"
    "v_accvgpr_read_b32 v1, a9\n\t"
    "v_accvgpr_read_b32 v2, a10\n\t"
    "v_accvgpr_read_b32 v3, a11\n\t"
    "v_accvgpr_read_b32 v4, a12\n\t"
    "v_accvgpr_read_b32 v5, a13\n\t"
    "v_accvgpr_read_b32 v6, a14\n\t"
    "v_accvgpr_read_b32 v7, a15\n\t"
    "v_pk_mul_f32 v[0:1], v[0:1], v[10:11] op_sel_hi:[1,0]\n\t"
    "v_pk_mul_f32 v[2:3], v[2:3], v[10:11] op_sel_hi:[1,0]\n\t"
    "v_pk_mul_f32 v[4:5], v[4:5], v[10:11] op_sel_hi:[1,0]\n\t"
    "v_pk_mul_f32 v[6:7], v[6:7], v[10:11] op_sel_hi:[1,0]\n\t"
    "v_cvt_pk_bf16_f32 v252, v0, v1\n\t"
    "v_cvt_pk_bf16_f32 v253, v2, v3\n\t"
    "v_cvt_pk_bf16_f32 v254, v4, v5\n\t"
    "v_cvt_pk_bf16_f32 v255, v6, v7\n\t"
    "buffer_store_dwordx4 v[252:255], v31, s[52:55], s68 offen offset:1024 sc1\n\t"
    "v_accvgpr_write_b32 a4, v252\n\t"
    "v_accvgpr_write_b32 a5, v253\n\t"
    "v_accvgpr_write_b32 a6, v254\n\t"
    "v_accvgpr_write_b32 a7, v255\n\t"
    "v_accvgpr_read_b32 v0, a16\n\t"
    "v_accvgpr_read_b32 v1, a17\n\t"
    "v_accvgpr_read_b32 v2, a18\n\t"
    "v_accvgpr_read_b32 v3, a19\n\t"
    "v_accvgpr_read_b32 v4, a20\n\t"
    "v_accvgpr_read_b32 v5, a21\n\t"
    "v_accvgpr_read_b32 v6, a22\n\t"
    "v_accvgpr_read_b32 v7, a23\n\t"
    "v_pk_mul_f32 v[0:1], v[0:1], v[10:11] op_sel_hi:[1,0]\n\t"
    "v_pk_mul_f32 v[2:3], v[2:3], v[10:11] op_sel_hi:[1,0]\n\t"
    "v_pk_mul_f32 v[4:5], v[4:5], v[10:11] op_sel_hi:[1,0]\n\t"
    "v_pk_mul_f32 v[6:7], v[6:7], v[10:11] op_sel_hi:[1,0]\n\t"
    "v_cvt_pk_bf16_f32 v248, v0, v1\n\t"
    "v_cvt_pk_bf16_f32 v249, v2, v3\n\t"
    "v_cvt_pk_bf16_f32 v250, v4, v5\n\t"
    "v_cvt_pk_bf16_f32 v251, v6, v7\n\t"
    "buffer_store_dwordx4 v[248:251], v31, s[52:55], s68 offen offset:2048 sc1\n\t"
    "v_accvgpr_write_b32 a8, v248\n\t"
    "v_accvgpr_write_b32 a9, v249\n\t"
    "v_accvgpr_write_b32 a10, v250\n\t"
    "v_accvgpr_write_b32 a11, v251\n\t"
    "v_accvgpr_read_b32 v0, a24\n\t"
    "v_accvgpr_read_b32 v1, a25\n\t"
    "v_accvgpr_read_b32 v2, a26\n\t"
    "v_accvgpr_read_b32 v3, a27\n\t"
    "v_accvgpr_read_b32 v4, a28\n\t"
    "v_accvgpr_read_b32 v5, a29\n\t"
    "v_accvgpr_read_b32 v6, a30\n\t"
    "v_accvgpr_read_b32 v7, a31\n\t"
    "v_pk_mul_f32 v[0:1], v[0:1], v[10:11] op_sel_hi:[1,0]\n\t"
    "v_pk_mul_f32 v[2:3], v[2:3], v[10:11] op_sel_hi:[1,0]\n\t"
    "v_pk_mul_f32 v[4:5], v[4:5], v[10:11] op_sel_hi:[1,0]\n\t"
    "v_pk_mul_f32 v[6:7], v[6:7], v[10:11] op_sel_hi:[1,0]\n\t"
    "v_cvt_pk_bf16_f32 v252, v0, v1\n\t"
    "v_cvt_pk_bf16_f32 v253, v2, v3\n\t"
    "v_cvt_pk_bf16_f32 v254, v4, v5\n\t"
    "v_cvt_pk_bf16_f32 v255, v6, v7\n\t"
    "buffer_store_dwordx4 v[252:255], v31, s[52:55], s68 offen offset:3072 sc1\n\t"
    "v_accvgpr_write_b32 a12, v252\n\t"
    "v_accvgpr_write_b32 a13, v253\n\t"
    "v_accvgpr_write_b32 a14, v254\n\t"
    "v_accvgpr_write_b32 a15, v255\n\t"
    "v_mfma_f32_16x16x32_bf16 v[232:235], a[224:227], v[96:99], v[48:51]\n\t"
    "v_exp_f32_e32 v224, v224\n\t"
    "v_mfma_f32_16x16x32_bf16 v[232:235], a[228:231], v[100:103], v[232:235]\n\t"
    "v_add_f32_e32 v36, v224, v36\n\t"
    "v_mfma_f32_16x16x32_bf16 v[232:235], a[232:235], v[104:107], v[232:235]\n\t"
    "v_exp_f32_e32 v225, v225\n\t"
    "v_mfma_f32_16x16x32_bf16 v[232:235], a[236:239], v[108:111], v[232:235]\n\t"
    "v_add_f32_e32 v36, v225, v36\n\t"
    "v_mfma_f32_16x16x32_bf16 v[236:239], a[240:243], v[96:99], v[48:51]\n\t"
    "v_exp_f32_e32 v226, v226\n\t"
    "v_mfma_f32_16x16x32_bf16 v[236:239], a[244:247], v[100:103], v[236:239]\n\t"
    "v_add_f32_e32 v36, v226, v36\n\t"
    "v_mfma_f32_16x16x32_bf16 v[236:239], a[248:251], v[104:107], v[236:239]\n\t"
    "v_exp_f32_e32 v227, v227\n\t"
    "v_mfma_f32_16x16x32_bf16 v[236:239], a[252:255], v[108:111], v[236:239]\n\t"
    "v_add_f32_e32 v36, v227, v36\n\t"
    "v_mfma_f32_16x16x32_bf16 a[32:35], a[160:163], v[240:243], a[32:35]\n\t"
    "v_exp_f32_e32 v228, v228\n\t"
    "v_mfma_f32_16x16x32_bf16 a[36:39], a[164:167], v[240:243], a[36:39]\n\t"
    "s_add_u32 m0, s14, 0x400\n\t"
    "v_add_f32_e32 v36, v228, v36\n\t"
    "v_cvt_pk_bf16_f32 v224, v224, v225\n\t"
    "v_mfma_f32_16x16x32_bf16 a[40:43], a[168:171], v[240:243], a[40:43]\n\t"
    "buffer_load_dwordx4 v29, s[16:19], s61 offen lds\n\t"
    "v_exp_f32_e32 v229, v229\n\t"
    "v_mfma_f32_16x16x32_bf16 a[44:47], a[172:175], v[240:243], a[44:47]\n\t"
    "v_add_f32_e32 v36, v229, v36\n\t"
    "v_cvt_pk_bf16_f32 v225, v226, v227\n\t"
    "v_mfma_f32_16x16x32_bf16 a[48:51], a[176:179], v[240:243], a[48:51]\n\t"
    "v_exp_f32_e32 v230, v230\n\t"
    "v_mfma_f32_16x16x32_bf16 a[52:55], a[180:183], v[240:243], a[52:55]\n\t"
    "v_add_f32_e32 v36, v230, v36\n\t"
    "v_cvt_pk_bf16_f32 v226, v228, v229\n\t"
    "v_mfma_f32_16x16x32_bf16 a[56:59], a[184:187], v[240:243], a[56:59]\n\t"
    "v_exp_f32_e32 v231, v231\n\t"
    "v_mfma_f32_16x16x32_bf16 a[60:63], a[188:191], v[240:243], a[60:63]\n\t"
    "v_add_f32_e32 v36, v231, v36\n\t"
    "v_cvt_pk_bf16_f32 v227, v230, v231\n\t"
    "v_mfma_f32_16x16x32_bf16 v[240:243], a[192:195], v[144:147], v[60:63]\n\t"
    "v_exp_f32_e32 v232, v232\n\t"
    "v_mfma_f32_16x16x32_bf16 v[240:243], a[196:199], v[148:151], v[240:243]\n\t"
    "v_add_f32_e32 v34, v232, v34\n\t"
    "v_mfma_f32_16x16x32_bf16 v[240:243], a[200:203], v[152:155], v[240:243]\n\t"
    "v_exp_f32_e32 v233, v233\n\t"
    "v_mfma_f32_16x16x32_bf16 v[240:243], a[204:207], v[156:159], v[240:243]\n\t"
    "v_add_f32_e32 v34, v233, v34\n\t"
    "v_mfma_f32_16x16x32_bf16 v[244:247], a[208:211], v[144:147], v[60:63]\n\t"
    "v_exp_f32_e32 v234, v234\n\t"
    "v_mfma_f32_16x16x32_bf16 v[244:247], a[212:215], v[148:151], v[244:247]\n\t"
    "v_add_f32_e32 v34, v234, v34\n\t"
    "v_mfma_f32_16x16x32_bf16 v[244:247], a[216:219], v[152:155], v[244:247]\n\t"
    "s_add_u32 m0, s14, 0x800\n\t"
    "v_exp_f32_e32 v235, v235\n\t"
    "v_mfma_f32_16x16x32_bf16 v[244:247], a[220:223], v[156:159], v[244:247]\n\t"
    "v_add_f32_e32 v34, v235, v34\n\t"
    "v_mfma_f32_16x16x32_bf16 v[160:163], a[128:131], v[224:227], v[160:163]\n\t"
    "buffer_load_dwordx4 v26, s[16:19], s60 offen lds\n\t"
    "v_exp_f32_e32 v236, v236\n\t"
    "v_mfma_f32_16x16x32_bf16 v[164:167], a[132:135], v[224:227], v[164:167]\n\t"
    "v_add_f32_e32 v34, v236, v34\n\t"
    "v_cvt_pk_bf16_f32 v232, v232, v233\n\t"
    "v_mfma_f32_16x16x32_bf16 v[168:171], a[136:139], v[224:227], v[168:171]\n\t"
    "v_exp_f32_e32 v237, v237\n\t"
    "v_mfma_f32_16x16x32_bf16 v[172:175], a[140:143], v[224:227], v[172:175]\n\t"
    "v_add_f32_e32 v34, v237, v34\n\t"
    "v_cvt_pk_bf16_f32 v233, v234, v235\n\t"
    "v_mfma_f32_16x16x32_bf16 v[176:179], a[144:147], v[224:227], v[176:179]\n\t"
    "v_exp_f32_e32 v238, v238\n\t"
    "v_mfma_f32_16x16x32_bf16 v[180:183], a[148:151], v[224:227], v[180:183]\n\t"
    "v_add_f32_e32 v34, v238, v34\n\t"
    "v_cvt_pk_bf16_f32 v234, v236, v237\n\t"
    "v_mfma_f32_16x16x32_bf16 v[184:187], a[152:155], v[224:227], v[184:187]\n\t"
    "v_exp_f32_e32 v239, v239\n\t"
    "v_mfma_f32_16x16x32_bf16 v[188:191], a[156:159], v[224:227], v[188:191]\n\t"
    "v_add_f32_e32 v34, v239, v34\n\t"
    "v_cvt_pk_bf16_f32 v235, v238, v239\n\t"
    "v_mov_b32_e32 v8, v33\n\t"
    "s_mov_b32 s75, 0x5d800000\n\t"
    "s_add_u32 s68, s76, 0x20000\n\t"
    "v_permlane32_swap_b32_e32 v33, v8\n\t"
    "v_accvgpr_read_b32 v0, a32\n\t"
    "v_add_f32_e32 v33, v33, v8\n\t"
    "v_mov_b32_e32 v8, v33\n\t"
    "v_accvgpr_read_b32 v1, a33\n\t"
    "v_accvgpr_read_b32 v2, a34\n\t"
    "v_permlane16_swap_b32_e32 v33, v8\n\t"
    "v_accvgpr_read_b32 v3, a35\n\t"
    "v_add_f32_e32 v33, v33, v8\n\t"
    "v_rcp_f32_e32 v10, v33\n\t"
    "v_cmp_nge_f32_e32 vcc, s75, v33\n\t"
    "v_accvgpr_read_b32 v4, a36\n\t"
    "v_accvgpr_read_b32 v5, a37\n\t"
    "v_accvgpr_read_b32 v6, a38\n\t"
    "v_accvgpr_read_b32 v7, a39\n\t"
    "v_pk_mul_f32 v[0:1], v[0:1], v[10:11] op_sel_hi:[1,0]\n\t"
    "v_pk_mul_f32 v[2:3], v[2:3], v[10:11] op_sel_hi:[1,0]\n\t"
    "v_pk_mul_f32 v[4:5], v[4:5], v[10:11] op_sel_hi:[1,0]\n\t"
    "v_pk_mul_f32 v[6:7], v[6:7], v[10:11] op_sel_hi:[1,0]\n\t"
    "s_or_b64 s[94:95], s[94:95], vcc\n\t"
    "v_cvt_pk_bf16_f32 v248, v0, v1\n\t"
    "v_cvt_pk_bf16_f32 v249, v2, v3\n\t"
    "v_cvt_pk_bf16_f32 v250, v4, v5\n\t"
    "v_cvt_pk_bf16_f32 v251, v6, v7\n\t"
    "buffer_store_dwordx4 v[248:251], v31, s[52:55], s68 offen sc1\n\t"
    "v_accvgpr_write_b32 a32, v248\n\t"
    "v_accvgpr_write_b32 a33, v249\n\t"
    "v_accvgpr_write_b32 a34, v250\n\t"
    "v_accvgpr_write_b32 a35, v251\n\t"
    "v_accvgpr_read_b32 v0, a40\n\t"
    "v_accvgpr_read_b32 v1, a41\n\t"
    "v_accvgpr_read_b32 v2, a42\n\t"
    "v_accvgpr_read_b32 v3, a43\n\t"
    "v_accvgpr_read_b32 v4, a44\n\t"
    "v_accvgpr_read_b32 v5, a45\n\t"
    "v_accvgpr_read_b32 v6, a46\n\t"
    "v_accvgpr_read_b32 v7, a47\n\t"
    "v_pk_mul_f32 v[0:1], v[0:1], v[10:11] op_sel_hi:[1,0]\n\t"
    "v_pk_mul_f32 v[2:3], v[2:3], v[10:11] op_sel_hi:[1,0]\n\t"
    "v_pk_mul_f32 v[4:5], v[4:5], v[10:11] op_sel_hi:[1,0]\n\t"
    "v_pk_mul_f32 v[6:7], v[6:7], v[10:11] op_sel_hi:[1,0]\n\t"
    "v_cvt_pk_bf16_f32 v252, v0, v1\n\t"
    "v_cvt_pk_bf16_f32 v253, v2, v3\n\t"
    "v_cvt_pk_bf16_f32 v254, v4, v5\n\t"
    "v_cvt_pk_bf16_f32 v255, v6, v7\n\t"
    "buffer_store_dwordx4 v[252:255], v31, s[52:55], s68 offen offset:1024 sc1\n\t"
    "v_accvgpr_write_b32 a36, v252\n\t"
    "v_accvgpr_write_b32 a37, v253\n\t"
    "v_accvgpr_write_b32 a38, v254\n\t"
    "v_accvgpr_write_b32 a39, v255\n\t"
    "v_accvgpr_read_b32 v0, a48\n\t"
    "v_accvgpr_read_b32 v1, a49\n\t"
    "v_accvgpr_read_b32 v2, a50\n\t"
    "v_accvgpr_read_b32 v3, a51\n\t"
    "v_accvgpr_read_b32 v4, a52\n\t"
    "v_accvgpr_read_b32 v5, a53\n\t"
    "v_accvgpr_read_b32 v6, a54\n\t"
    "v_accvgpr_read_b32 v7, a55\n\t"
    "v_pk_mul_f32 v[0:1], v[0:1], v[10:11] op_sel_hi:[1,0]\n\t"
    "v_pk_mul_f32 v[2:3], v[2:3], v[10:11] op_sel_hi:[1,0]\n\t"
    "v_pk_mul_f32 v[4:5], v[4:5], v[10:11] op_sel_hi:[1,0]\n\t"
    "v_pk_mul_f32 v[6:7], v[6:7], v[10:11] op_sel_hi:[1,0]\n\t"
    "v_cvt_pk_bf16_f32 v248, v0, v1\n\t"
    "v_cvt_pk_bf16_f32 v249, v2, v3\n\t"
    "v_cvt_pk_bf16_f32 v250, v4, v5\n\t"
    "v_cvt_pk_bf16_f32 v251, v6, v7\n\t"
    "buffer_store_dwordx4 v[248:251], v31, s[52:55], s68 offen offset:2048 sc1\n\t"
    "v_accvgpr_write_b32 a40, v248\n\t"
    "v_accvgpr_write_b32 a41, v249\n\t"
    "v_accvgpr_write_b32 a42, v250\n\t"
    "v_accvgpr_write_b32 a43, v251\n\t"
    "v_accvgpr_read_b32 v0, a56\n\t"
    "v_accvgpr_read_b32 v1, a57\n\t"
    "v_accvgpr_read_b32 v2, a58\n\t"
    "v_accvgpr_read_b32 v3, a59\n\t"
    "v_accvgpr_read_b32 v4, a60\n\t"
    "v_accvgpr_read_b32 v5, a61\n\t"
    "v_accvgpr_read_b32 v6, a62\n\t"
    "v_accvgpr_read_b32 v7, a63\n\t"
    "v_pk_mul_f32 v[0:1], v[0:1], v[10:11] op_sel_hi:[1,0]\n\t"
    "v_pk_mul_f32 v[2:3], v[2:3], v[10:11] op_sel_hi:[1,0]\n\t"
    "v_pk_mul_f32 v[4:5], v[4:5], v[10:11] op_sel_hi:[1,0]\n\t"
    "v_pk_mul_f32 v[6:7], v[6:7], v[10:11] op_sel_hi:[1,0]\n\t"
    "v_cvt_pk_bf16_f32 v252, v0, v1\n\t"
    "v_cvt_pk_bf16_f32 v253, v2, v3\n\t"
    "v_cvt_pk_bf16_f32 v254, v4, v5\n\t"
    "v_cvt_pk_bf16_f32 v255, v6, v7\n\t"
    "buffer_store_dwordx4 v[252:255], v31, s[52:55], s68 offen offset:3072 sc1\n\t"
    "v_accvgpr_write_b32 a44, v252\n\t"
    "v_accvgpr_write_b32 a45, v253\n\t"
    "v_accvgpr_write_b32 a46, v254\n\t"
    "v_accvgpr_write_b32 a47, v255\n\t"
    "v_mfma_f32_16x16x32_bf16 v[224:227], a[224:227], v[112:115], v[52:55]\n\t"
    "v_exp_f32_e32 v240, v240\n\t"
    "v_mfma_f32_16x16x32_bf16 v[224:227], a[228:231], v[116:119], v[224:227]\n\t"
    "v_add_f32_e32 v37, v240, v37\n\t"
    "v_mfma_f32_16x16x32_bf16 v[224:227], a[232:235], v[120:123], v[224:227]\n\t"
    "v_exp_f32_e32 v241, v241\n\t"
    "v_mfma_f32_16x16x32_bf16 v[224:227], a[236:239], v[124:127], v[224:227]\n\t"
    "v_add_f32_e32 v37, v241, v37\n\t"
    "v_mfma_f32_16x16x32_bf16 v[228:231], a[240:243], v[112:115], v[52:55]\n\t"
    "v_exp_f32_e32 v242, v242\n\t"
    "v_mfma_f32_16x16x32_bf16 v[228:231], a[244:247], v[116:119], v[228:231]\n\t"
    "v_add_f32_e32 v37, v242, v37\n\t"
    "v_mfma_f32_16x16x32_bf16 v[228:231], a[248:251], v[120:123], v[228:231]\n\t"
    "s_add_u32 m0, s14, 0xc00\n\t"
    "v_exp_f32_e32 v243, v243\n\t"
    "v_mfma_f32_16x16x32_bf16 v[228:231], a[252:255], v[124:127], v[228:231]\n\t"
    "v_add_f32_e32 v37, v243, v37\n\t"
    "v_mfma_f32_16x16x32_bf16 a[64:67], a[160:163], v[232:235], a[64:67]\n\t"
    "buffer_load_dwordx4 v26, s[16:19], s61 offen lds\n\t"
    "v_exp_f32_e32 v244, v244\n\t"
    "v_mfma_f32_16x16x32_bf16 a[68:71], a[164:167], v[232:235], a[68:71]\n\t"
    "v_add_f32_e32 v37, v244, v37\n\t"
    "v_cvt_pk_bf16_f32 v240, v240, v241\n\t"
    "v_mfma_f32_16x16x32_bf16 a[72:75], a[168:171], v[232:235], a[72:75]\n\t"
    "v_exp_f32_e32 v245, v245\n\t"
    "v_mfma_f32_16x16x32_bf16 a[76:79], a[172:175], v[232:235], a[76:79]\n\t"
    "v_add_f32_e32 v37, v245, v37\n\t"
    "v_cvt_pk_bf16_f32 v241, v242, v243\n\t"
    "v_mfma_f32_16x16x32_bf16 a[80:83], a[176:179], v[232:235], a[80:83]\n\t"
    "v_exp_f32_e32 v246, v246\n\t"
    "v_mfma_f32_16x16x32_bf16 a[84:87], a[180:183], v[232:235], a[84:87]\n\t"
    "s_add_u32 m0, s14, 0x1000\n\t"
    "v_add_f32_e32 v37, v246, v37\n\t"
    "v_cvt_pk_bf16_f32 v242, v244, v245\n\t"
    "v_mfma_f32_16x16x32_bf16 a[88:91], a[184:187], v[232:235], a[88:91]\n\t"
    "buffer_load_dwordx4 v27, s[16:19], s60 offen lds\n\t"
    "v_exp_f32_e32 v247, v247\n\t"
    "v_mfma_f32_16x16x32_bf16 a[92:95], a[188:191], v[232:235], a[92:95]\n\t"
    "v_add_f32_e32 v37, v247, v37\n\t"
    "v_cvt_pk_bf16_f32 v243, v246, v247\n\t"
    "v_mfma_f32_16x16x32_bf16 v[232:235], a[224:227], v[128:131], v[56:59]\n\t"
    "v_exp_f32_e32 v224, v224\n\t"
    "v_mfma_f32_16x16x32_bf16 v[232:235], a[228:231], v[132:135], v[232:235]\n\t"
    "v_add_f32_e32 v35, v224, v35\n\t"
    "v_mfma_f32_16x16x32_bf16 v[232:235], a[232:235], v[136:139], v[232:235]\n\t"
    "v_exp_f32_e32 v225, v225\n\t"
    "v_mfma_f32_16x16x32_bf16 v[232:235], a[236:239], v[140:143], v[232:235]\n\t"
    "v_add_f32_e32 v35, v225, v35\n\t"
    "v_mfma_f32_16x16x32_bf16 v[236:239], a[240:243], v[128:131], v[56:59]\n\t"
    "v_exp_f32_e32 v226, v226\n\t"
    "v_mfma_f32_16x16x32_bf16 v[236:239], a[244:247], v[132:135], v[236:239]\n\t"
    "v_add_f32_e32 v35, v226, v35\n\t"
    "v_mfma_f32_16x16x32_bf16 v[236:239], a[248:251], v[136:139], v[236:239]\n\t"
    "v_exp_f32_e32 v227, v227\n\t"
    "v_mfma_f32_16x16x32_bf16 v[236:239], a[252:255], v[140:143], v[236:239]\n\t"
    "v_add_f32_e32 v35, v227, v35\n\t"
    "v_mfma_f32_16x16x32_bf16 v[192:195], a[128:131], v[240:243], v[192:195]\n\t"
    "v_exp_f32_e32 v228, v228\n\t"
    "v_mfma_f32_16x16x32_bf16 v[196:199], a[132:135], v[240:243], v[196:199]\n\t"
    "v_add_f32_e32 v35, v228, v35\n\t"
    "v_cvt_pk_bf16_f32 v224, v224, v225\n\t"
    "v_mfma_f32_16x16x32_bf16 v[200:203], a[136:139], v[240:243], v[200:203]\n\t"
    "v_exp_f32_e32 v229, v229\n\t"
    "v_mfma_f32_16x16x32_bf16 v[204:207], a[140:143], v[240:243], v[204:207]\n\t"
    "s_add_u32 m0, s14, 0x1400\n\t"
    "v_add_f32_e32 v35, v229, v35\n\t"
    "v_cvt_pk_bf16_f32 v225, v226, v227\n\t"
    "v_mfma_f32_16x16x32_bf16 v[208:211], a[144:147], v[240:243], v[208:211]\n\t"
    "buffer_load_dwordx4 v27, s[16:19], s61 offen lds\n\t"
    "v_exp_f32_e32 v230, v230\n\t"
    "v_mfma_f32_16x16x32_bf16 v[212:215], a[148:151], v[240:243], v[212:215]\n\t"
    "v_add_f32_e32 v35, v230, v35\n\t"
    "v_cvt_pk_bf16_f32 v226, v228, v229\n\t"
    "v_mfma_f32_16x16x32_bf16 v[216:219], a[152:155], v[240:243], v[216:219]\n\t"
    "v_exp_f32_e32 v231, v231\n\t"
    "v_mfma_f32_16x16x32_bf16 v[220:223], a[156:159], v[240:243], v[220:223]\n\t"
    "v_add_f32_e32 v35, v231, v35\n\t"
    "v_cvt_pk_bf16_f32 v227, v230, v231\n\t"
    "v_mov_b32_e32 v8, v34\n\t"
    "s_mov_b32 s75, 0x5d800000\n\t"
    "s_add_u32 s68, s76, 0x40000\n\t"
    "v_permlane32_swap_b32_e32 v34, v8\n\t"
    "v_accvgpr_read_b32 v0, a64\n\t"
    "v_add_f32_e32 v34, v34, v8\n\t"
    "v_mov_b32_e32 v8, v34\n\t"
    "v_accvgpr_read_b32 v1, a65\n\t"
    "v_accvgpr_read_b32 v2, a66\n\t"
    "v_permlane16_swap_b32_e32 v34, v8\n\t"
    "v_accvgpr_read_b32 v3, a67\n\t"
    "v_add_f32_e32 v34, v34, v8\n\t"
    "v_rcp_f32_e32 v10, v34\n\t"
    "v_cmp_nge_f32_e32 vcc, s75, v34\n\t"
    "v_accvgpr_read_b32 v4, a68\n\t"
    "v_accvgpr_read_b32 v5, a69\n\t"
    "v_accvgpr_read_b32 v6, a70\n\t"
    "v_accvgpr_read_b32 v7, a71\n\t"
    "v_pk_mul_f32 v[0:1], v[0:1], v[10:11] op_sel_hi:[1,0]\n\t"
    "v_pk_mul_f32 v[2:3], v[2:3], v[10:11] op_sel_hi:[1,0]\n\t"
    "v_pk_mul_f32 v[4:5], v[4:5], v[10:11] op_sel_hi:[1,0]\n\t"
    "v_pk_mul_f32 v[6:7], v[6:7], v[10:11] op_sel_hi:[1,0]\n\t"
    "s_or_b64 s[94:95], s[94:95], vcc\n\t"
    "v_cvt_pk_bf16_f32 v248, v0, v1\n\t"
    "v_cvt_pk_bf16_f32 v249, v2, v3\n\t"
    "v_cvt_pk_bf16_f32 v250, v4, v5\n\t"
    "v_cvt_pk_bf16_f32 v251, v6, v7\n\t"
    "buffer_store_dwordx4 v[248:251], v31, s[52:55], s68 offen sc1\n\t"
    "v_accvgpr_write_b32 a64, v248\n\t"
    "v_accvgpr_write_b32 a65, v249\n\t"
    "v_accvgpr_write_b32 a66, v250\n\t"
    "v_accvgpr_write_b32 a67, v251\n\t"
    "v_accvgpr_read_b32 v0, a72\n\t"
    "v_accvgpr_read_b32 v1, a73\n\t"
    "v_accvgpr_read_b32 v2, a74\n\t"
    "v_accvgpr_read_b32 v3, a75\n\t"
    "v_accvgpr_read_b32 v4, a76\n\t"
    "v_accvgpr_read_b32 v5, a77\n\t"
    "v_accvgpr_read_b32 v6, a78\n\t"
    "v_accvgpr_read_b32 v7, a79\n\t"
    "v_pk_mul_f32 v[0:1], v[0:1], v[10:11] op_sel_hi:[1,0]\n\t"
    "v_pk_mul_f32 v[2:3], v[2:3], v[10:11] op_sel_hi:[1,0]\n\t"
    "v_pk_mul_f32 v[4:5], v[4:5], v[10:11] op_sel_hi:[1,0]\n\t"
    "v_pk_mul_f32 v[6:7], v[6:7], v[10:11] op_sel_hi:[1,0]\n\t"
    "v_cvt_pk_bf16_f32 v252, v0, v1\n\t"
    "v_cvt_pk_bf16_f32 v253, v2, v3\n\t"
    "v_cvt_pk_bf16_f32 v254, v4, v5\n\t"
    "v_cvt_pk_bf16_f32 v255, v6, v7\n\t"
    "buffer_store_dwordx4 v[252:255], v31, s[52:55], s68 offen offset:1024 sc1\n\t"
    "v_accvgpr_write_b32 a68, v252\n\t"
    "v_accvgpr_write_b32 a69, v253\n\t"
    "v_accvgpr_write_b32 a70, v254\n\t"
    "v_accvgpr_write_b32 a71, v255\n\t"
    "v_accvgpr_read_b32 v0, a80\n\t"
    "v_accvgpr_read_b32 v1, a81\n\t"
    "v_accvgpr_read_b32 v2, a82\n\t"
    "v_accvgpr_read_b32 v3, a83\n\t"
    "v_accvgpr_read_b32 v4, a84\n\t"
    "v_accvgpr_read_b32 v5, a85\n\t"
    "v_accvgpr_read_b32 v6, a86\n\t"
    "v_accvgpr_read_b32 v7, a87\n\t"
    "v_pk_mul_f32 v[0:1], v[0:1], v[10:11] op_sel_hi:[1,0]\n\t"
    "v_pk_mul_f32 v[2:3], v[2:3], v[10:11] op_sel_hi:[1,0]\n\t"
    "v_pk_mul_f32 v[4:5], v[4:5], v[10:11] op_sel_hi:[1,0]\n\t"
    "v_pk_mul_f32 v[6:7], v[6:7], v[10:11] op_sel_hi:[1,0]\n\t"
    "v_cvt_pk_bf16_f32 v248, v0, v1\n\t"
    "v_cvt_pk_bf16_f32 v249, v2, v3\n\t"
    "v_cvt_pk_bf16_f32 v250, v4, v5\n\t"
    "v_cvt_pk_bf16_f32 v251, v6, v7\n\t"
    "buffer_store_dwordx4 v[248:251], v31, s[52:55], s68 offen offset:2048 sc1\n\t"
    "v_accvgpr_write_b32 a72, v248\n\t"
    "v_accvgpr_write_b32 a73, v249\n\t"
    "v_accvgpr_write_b32 a74, v250\n\t"
    "v_accvgpr_write_b32 a75, v251\n\t"
    "v_accvgpr_read_b32 v0, a88\n\t"
    "v_accvgpr_read_b32 v1, a89\n\t"
    "v_accvgpr_read_b32 v2, a90\n\t"
    "v_accvgpr_read_b32 v3, a91\n\t"
    "v_accvgpr_read_b32 v4, a92\n\t"
    "v_accvgpr_read_b32 v5, a93\n\t"
    "v_accvgpr_read_b32 v6, a94\n\t"
    "v_accvgpr_read_b32 v7, a95\n\t"
    "v_pk_mul_f32 v[0:1], v[0:1], v[10:11] op_sel_hi:[1,0]\n\t"
    "v_pk_mul_f32 v[2:3], v[2:3], v[10:11] op_sel_hi:[1,0]\n\t"
    "v_pk_mul_f32 v[4:5], v[4:5], v[10:11] op_sel_hi:[1,0]\n\t"
    "v_pk_mul_f32 v[6:7], v[6:7], v[10:11] op_sel_hi:[1,0]\n\t"
    "v_cvt_pk_bf16_f32 v252, v0, v1\n\t"
    "v_cvt_pk_bf16_f32 v253, v2, v3\n\t"
    "v_cvt_pk_bf16_f32 v254, v4, v5\n\t"
    "v_cvt_pk_bf16_f32 v255, v6, v7\n\t"
    "buffer_store_dwordx4 v[252:255], v31, s[52:55], s68 offen offset:3072 sc1\n\t"
    "v_accvgpr_write_b32 a76, v252\n\t"
    "v_accvgpr_write_b32 a77, v253\n\t"
    "v_accvgpr_write_b32 a78, v254\n\t"
    "v_accvgpr_write_b32 a79, v255\n\t"
    "v_mfma_f32_16x16x32_bf16 v[240:243], a[224:227], v[144:147], v[60:63]\n\t"
    "v_exp_f32_e32 v232, v232\n\t"
    "v_mfma_f32_16x16x32_bf16 v[240:243], a[228:231], v[148:151], v[240:243]\n\t"
    "v_add_f32_e32 v36, v232, v36\n\t"
    "v_mfma_f32_16x16x32_bf16 v[240:243], a[232:235], v[152:155], v[240:243]\n\t"
    "v_exp_f32_e32 v233, v233\n\t"
    "v_mfma_f32_16x16x32_bf16 v[240:243], a[236:239], v[156:159], v[240:243]\n\t"
    "v_add_f32_e32 v36, v233, v36\n\t"
    "v_mfma_f32_16x16x32_bf16 v[244:247], a[240:243], v[144:147], v[60:63]\n\t"
    "v_exp_f32_e32 v234, v234\n\t"
    "v_mfma_f32_16x16x32_bf16 v[244:247], a[244:247], v[148:151], v[244:247]\n\t"
    "v_add_f32_e32 v36, v234, v36\n\t"
    "v_mfma_f32_16x16x32_bf16 v[244:247], a[248:251], v[152:155], v[244:247]\n\t"
    "v_exp_f32_e32 v235, v235\n\t"
    "v_mfma_f32_16x16x32_bf16 v[244:247], a[252:255], v[156:159], v[244:247]\n\t"
    "v_add_f32_e32 v36, v235, v36\n\t"
    "v_mfma_f32_16x16x32_bf16 a[96:99], a[160:163], v[224:227], a[96:99]\n\t"
    "v_exp_f32_e32 v236, v236\n\t"
    "v_mfma_f32_16x16x32_bf16 a[100:103], a[164:167], v[224:227], a[100:103]\n\t"
    "v_add_f32_e32 v36, v236, v36\n\t"
    "v_cvt_pk_bf16_f32 v232, v232, v233\n\t"
    "v_mfma_f32_16x16x32_bf16 a[104:107], a[168:171], v[224:227], a[104:107]\n\t"
    "v_exp_f32_e32 v237, v237\n\t"
    "v_mfma_f32_16x16x32_bf16 a[108:111], a[172:175], v[224:227], a[108:111]\n\t"
    "s_add_u32 m0, s14, 0x1800\n\t"
    "v_add_f32_e32 v36, v237, v36\n\t"
    "v_cvt_pk_bf16_f32 v233, v234, v235\n\t"
    "v_mfma_f32_16x16x32_bf16 a[112:115], a[176:179], v[224:227], a[112:115]\n\t"
    "buffer_load_dwordx4 v28, s[16:19], s60 offen lds\n\t"
    "v_exp_f32_e32 v238, v238\n\t"
    "v_mfma_f32_16x16x32_bf16 a[116:119], a[180:183], v[224:227], a[116:119]\n\t"
    "v_add_f32_e32 v36, v238, v36\n\t"
    "v_cvt_pk_bf16_f32 v234, v236, v237\n\t"
    "v_mfma_f32_16x16x32_bf16 a[120:123], a[184:187], v[224:227], a[120:123]\n\t"
    "v_exp_f32_e32 v239, v239\n\t"
    "v_mfma_f32_16x16x32_bf16 a[124:127], a[188:191], v[224:227], a[124:127]\n\t"
    "v_add_f32_e32 v36, v239, v36\n\t"
    "v_cvt_pk_bf16_f32 v235, v238, v239\n\t"
    "s_nop 1\n\t"
    "v_mfma_f32_16x16x32_bf16 v[160:163], a[160:163], v[232:235], v[160:163]\n\t"
    "v_exp_f32_e32 v240, v240\n\t"
    "v_mfma_f32_16x16x32_bf16 v[164:167], a[164:167], v[232:235], v[164:167]\n\t"
    "v_exp_f32_e32 v241, v241\n\t"
    "v_mfma_f32_16x16x32_bf16 v[168:171], a[168:171], v[232:235], v[168:171]\n\t"
    "v_exp_f32_e32 v242, v242\n\t"
    "v_mfma_f32_16x16x32_bf16 v[172:175], a[172:175], v[232:235], v[172:175]\n\t"
    "v_exp_f32_e32 v243, v243\n\t"
    "v_mfma_f32_16x16x32_bf16 v[176:179], a[176:179], v[232:235], v[176:179]\n\t"
    "v_exp_f32_e32 v244, v244\n\t"
    "v_mfma_f32_16x16x32_bf16 v[180:183], a[180:183], v[232:235], v[180:183]\n\t"
    "v_exp_f32_e32 v245, v245\n\t"
    "v_mfma_f32_16x16x32_bf16 v[184:187], a[184:187], v[232:235], v[184:187]\n\t"
    "v_exp_f32_e32 v246, v246\n\t"
    "v_mfma_f32_16x16x32_bf16 v[188:191], a[188:191], v[232:235], v[188:191]\n\t"
    "v_exp_f32_e32 v247, v247\n\t"
    "s_nop 0\n\t"
    "v_add_f32_e32 v37, v240, v37\n\t"
    "v_add_f32_e32 v37, v241, v37\n\t"
    "v_add_f32_e32 v37, v242, v37\n\t"
    "v_add_f32_e32 v37, v243, v37\n\t"
    "v_add_f32_e32 v37, v244, v37\n\t"
    "v_cvt_pk_bf16_f32 v240, v240, v241\n\t"
    "v_add_f32_e32 v37, v245, v37\n\t"
    "v_cvt_pk_bf16_f32 v241, v242, v243\n\t"
    "v_add_f32_e32 v37, v246, v37\n\t"
    "v_cvt_pk_bf16_f32 v242, v244, v245\n\t"
    "v_add_f32_e32 v37, v247, v37\n\t"
    "v_cvt_pk_bf16_f32 v243, v246, v247\n\t"
    "s_nop 1\n\t"
    "v_mov_b32_e32 v8, v35\n\t"
    "s_mov_b32 s75, 0x5d800000\n\t"
    "s_add_u32 s68, s76, 0x60000\n\t"
    "v_permlane32_swap_b32_e32 v35, v8\n\t"
    "v_accvgpr_read_b32 v0, a96\n\t"
    "v_add_f32_e32 v35, v35, v8\n\t"
    "v_mov_b32_e32 v8, v35\n\t"
    "v_accvgpr_read_b32 v1, a97\n\t"
    "v_accvgpr_read_b32 v2, a98\n\t"
    "v_permlane16_swap_b32_e32 v35, v8\n\t"
    "v_accvgpr_read_b32 v3, a99\n\t"
    "v_add_f32_e32 v35, v35, v8\n\t"
    "v_rcp_f32_e32 v10, v35\n\t"
    "v_cmp_nge_f32_e32 vcc, s75, v35\n\t"
    "v_accvgpr_read_b32 v4, a100\n\t"
    "v_accvgpr_read_b32 v5, a101\n\t"
    "v_accvgpr_read_b32 v6, a102\n\t"
    "v_accvgpr_read_b32 v7, a103\n\t"
    "v_pk_mul_f32 v[0:1], v[0:1], v[10:11] op_sel_hi:[1,0]\n\t"
    "v_pk_mul_f32 v[2:3], v[2:3], v[10:11] op_sel_hi:[1,0]\n\t"
    "v_pk_mul_f32 v[4:5], v[4:5], v[10:11] op_sel_hi:[1,0]\n\t"
    "v_pk_mul_f32 v[6:7], v[6:7], v[10:11] op_sel_hi:[1,0]\n\t"
    "s_or_b64 s[94:95], s[94:95], vcc\n\t"
    "v_cvt_pk_bf16_f32 v248, v0, v1\n\t"
    "v_cvt_pk_bf16_f32 v249, v2, v3\n\t"
    "v_cvt_pk_bf16_f32 v250, v4, v5\n\t"
    "v_cvt_pk_bf16_f32 v251, v6, v7\n\t"
    "buffer_store_dwordx4 v[248:251], v31, s[52:55], s68 offen sc1\n\t"
    "v_accvgpr_write_b32 a96, v248\n\t"
    "v_accvgpr_write_b32 a97, v249\n\t"
    "v_accvgpr_write_b32 a98, v250\n\t"
    "v_accvgpr_write_b32 a99, v251\n\t"
    "v_accvgpr_read_b32 v0, a104\n\t"
    "v_accvgpr_read_b32 v1, a105\n\t"
    "v_accvgpr_read_b32 v2, a106\n\t"
    "v_accvgpr_read_b32 v3, a107\n\t"
    "v_accvgpr_read_b32 v4, a108\n\t"
    "v_accvgpr_read_b32 v5, a109\n\t"
    "v_accvgpr_read_b32 v6, a110\n\t"
    "v_accvgpr_read_b32 v7, a111\n\t"
    "v_pk_mul_f32 v[0:1], v[0:1], v[10:11] op_sel_hi:[1,0]\n\t"
    "v_pk_mul_f32 v[2:3], v[2:3], v[10:11] op_sel_hi:[1,0]\n\t"
    "v_pk_mul_f32 v[4:5], v[4:5], v[10:11] op_sel_hi:[1,0]\n\t"
    "v_pk_mul_f32 v[6:7], v[6:7], v[10:11] op_sel_hi:[1,0]\n\t"
    "v_cvt_pk_bf16_f32 v252, v0, v1\n\t"
    "v_cvt_pk_bf16_f32 v253, v2, v3\n\t"
    "v_cvt_pk_bf16_f32 v254, v4, v5\n\t"
    "v_cvt_pk_bf16_f32 v255, v6, v7\n\t"
    "buffer_store_dwordx4 v[252:255], v31, s[52:55], s68 offen offset:1024 sc1\n\t"
    "v_accvgpr_write_b32 a100, v252\n\t"
    "v_accvgpr_write_b32 a101, v253\n\t"
    "v_accvgpr_write_b32 a102, v254\n\t"
    "v_accvgpr_write_b32 a103, v255\n\t"
    "v_accvgpr_read_b32 v0, a112\n\t"
    "v_accvgpr_read_b32 v1, a113\n\t"
    "v_accvgpr_read_b32 v2, a114\n\t"
    "v_accvgpr_read_b32 v3, a115\n\t"
    "v_accvgpr_read_b32 v4, a116\n\t"
    "v_accvgpr_read_b32 v5, a117\n\t"
    "v_accvgpr_read_b32 v6, a118\n\t"
    "v_accvgpr_read_b32 v7, a119\n\t"
    "v_pk_mul_f32 v[0:1], v[0:1], v[10:11] op_sel_hi:[1,0]\n\t"
    "v_pk_mul_f32 v[2:3], v[2:3], v[10:11] op_sel_hi:[1,0]\n\t"
    "v_pk_mul_f32 v[4:5], v[4:5], v[10:11] op_sel_hi:[1,0]\n\t"
    "v_pk_mul_f32 v[6:7], v[6:7], v[10:11] op_sel_hi:[1,0]\n\t"
    "v_cvt_pk_bf16_f32 v248, v0, v1\n\t"
    "v_cvt_pk_bf16_f32 v249, v2, v3\n\t"
    "v_cvt_pk_bf16_f32 v250, v4, v5\n\t"
    "v_cvt_pk_bf16_f32 v251, v6, v7\n\t"
    "buffer_store_dwordx4 v[248:251], v31, s[52:55], s68 offen offset:2048 sc1\n\t"
    "v_accvgpr_write_b32 a104, v248\n\t"
    "v_accvgpr_write_b32 a105, v249\n\t"
    "v_accvgpr_write_b32 a106, v250\n\t"
    "v_accvgpr_write_b32 a107, v251\n\t"
    "v_accvgpr_read_b32 v0, a120\n\t"
    "v_accvgpr_read_b32 v1, a121\n\t"
    "v_accvgpr_read_b32 v2, a122\n\t"
    "v_accvgpr_read_b32 v3, a123\n\t"
    "v_accvgpr_read_b32 v4, a124\n\t"
    "v_accvgpr_read_b32 v5, a125\n\t"
    "v_accvgpr_read_b32 v6, a126\n\t"
    "v_accvgpr_read_b32 v7, a127\n\t"
    "v_pk_mul_f32 v[0:1], v[0:1], v[10:11] op_sel_hi:[1,0]\n\t"
    "v_pk_mul_f32 v[2:3], v[2:3], v[10:11] op_sel_hi:[1,0]\n\t"
    "v_pk_mul_f32 v[4:5], v[4:5], v[10:11] op_sel_hi:[1,0]\n\t"
    "v_pk_mul_f32 v[6:7], v[6:7], v[10:11] op_sel_hi:[1,0]\n\t"
    "v_cvt_pk_bf16_f32 v252, v0, v1\n\t"
    "v_cvt_pk_bf16_f32 v253, v2, v3\n\t"
    "v_cvt_pk_bf16_f32 v254, v4, v5\n\t"
    "v_cvt_pk_bf16_f32 v255, v6, v7\n\t"
    "buffer_store_dwordx4 v[252:255], v31, s[52:55], s68 offen offset:3072 sc1\n\t"
    "v_accvgpr_write_b32 a108, v252\n\t"
    "v_accvgpr_write_b32 a109, v253\n\t"
    "v_accvgpr_write_b32 a110, v254\n\t"
    "v_accvgpr_write_b32 a111, v255\n\t"
    "v_mfma_f32_16x16x32_bf16 v[192:195], a[160:163], v[240:243], v[192:195]\n\t"
    "v_mfma_f32_16x16x32_bf16 v[196:199], a[164:167], v[240:243], v[196:199]\n\t"
    "v_mfma_f32_16x16x32_bf16 v[200:203], a[168:171], v[240:243], v[200:203]\n\t"
    "v_mfma_f32_16x16x32_bf16 v[204:207], a[172:175], v[240:243], v[204:207]\n\t"
    "v_mfma_f32_16x16x32_bf16 v[208:211], a[176:179], v[240:243], v[208:211]\n\t"
    "v_mfma_f32_16x16x32_bf16 v[212:215], a[180:183], v[240:243], v[212:215]\n\t"
    "v_mfma_f32_16x16x32_bf16 v[216:219], a[184:187], v[240:243], v[216:219]\n\t"
    "v_mfma_f32_16x16x32_bf16 v[220:223], a[188:191], v[240:243], v[220:223]\n\t"
    "v_mov_b32_e32 v8, v36\n\t"
    "s_mov_b32 s75, 0x5d800000\n\t"
    "s_add_u32 s68, s76, 0x80000\n\t"
    "v_permlane32_swap_b32_e32 v36, v8\n\t"
    "v_add_f32_e32 v36, v36, v8\n\t"
    "v_mov_b32_e32 v8, v36\n\t"
    "s_nop 0\n\t"
    "s_nop 0\n\t"
    "v_permlane16_swap_b32_e32 v36, v8\n\t"
    "v_add_f32_e32 v36, v36, v8\n\t"
    "v_rcp_f32_e32 v10, v36\n\t"
    "v_cmp_nge_f32_e32 vcc, s75, v36\n\t"
    "v_pk_mul_f32 v[160:161], v[160:161], v[10:11] op_sel_hi:[1,0]\n\t"
    "v_pk_mul_f32 v[162:163], v[162:163], v[10:11] op_sel_hi:[1,0]\n\t"
    "v_pk_mul_f32 v[164:165], v[164:165], v[10:11] op_sel_hi:[1,0]\n\t"
    "v_pk_mul_f32 v[166:167], v[166:167], v[10:11] op_sel_hi:[1,0]\n\t"
    "s_or_b64 s[94:95], s[94:95], vcc\n\t"
    "v_cvt_pk_bf16_f32 v248, v160, v161\n\t"
    "v_cvt_pk_bf16_f32 v249, v162, v163\n\t"
    "v_cvt_pk_bf16_f32 v250, v164, v165\n\t"
    "v_cvt_pk_bf16_f32 v251, v166, v167\n\t"
    "buffer_store_dwordx4 v[248:251], v31, s[52:55], s68 offen sc1\n\t"
    "v_accvgpr_write_b32 a16, v248\n\t"
    "v_accvgpr_write_b32 a17, v249\n\t"
    "v_accvgpr_write_b32 a18, v250\n\t"
    "v_accvgpr_write_b32 a19, v251\n\t"
    "v_pk_mul_f32 v[168:169], v[168:169], v[10:11] op_sel_hi:[1,0]\n\t"
    "v_pk_mul_f32 v[170:171], v[170:171], v[10:11] op_sel_hi:[1,0]\n\t"
    "v_pk_mul_f32 v[172:173], v[172:173], v[10:11] op_sel_hi:[1,0]\n\t"
    "v_pk_mul_f32 v[174:175], v[174:175], v[10:11] op_sel_hi:[1,0]\n\t"
    "v_cvt_pk_bf16_f32 v252, v168, v169\n\t"
    "v_cvt_pk_bf16_f32 v253, v170, v171\n\t"
    "v_cvt_pk_bf16_f32 v254, v172, v173\n\t"
    "v_cvt_pk_bf16_f32 v255, v174, v175\n\t"
    "buffer_store_dwordx4 v[252:255], v31, s[52:55], s68 offen offset:1024 sc1\n\t"
    "v_accvgpr_write_b32 a20, v252\n\t"
    "v_accvgpr_write_b32 a21, v253\n\t"
    "v_accvgpr_write_b32 a22, v254\n\t"
    "v_accvgpr_write_b32 a23, v255\n\t"
    "v_pk_mul_f32 v[176:177], v[176:177], v[10:11] op_sel_hi:[1,0]\n\t"
    "v_pk_mul_f32 v[178:179], v[178:179], v[10:11] op_sel_hi:[1,0]\n\t"
    "v_pk_mul_f32 v[180:181], v[180:181], v[10:11] op_sel_hi:[1,0]\n\t"
    "v_pk_mul_f32 v[182:183], v[182:183], v[10:11] op_sel_hi:[1,0]\n\t"
    "v_cvt_pk_bf16_f32 v248, v176, v177\n\t"
    "v_cvt_pk_bf16_f32 v249, v178, v179\n\t"
    "v_cvt_pk_bf16_f32 v250, v180, v181\n\t"
    "v_cvt_pk_bf16_f32 v251, v182, v183\n\t"
    "buffer_store_dwordx4 v[248:251], v31, s[52:55], s68 offen offset:2048 sc1\n\t"
    "v_accvgpr_write_b32 a24, v248\n\t"
    "v_accvgpr_write_b32 a25, v249\n\t"
    "v_accvgpr_write_b32 a26, v250\n\t"
    "v_accvgpr_write_b32 a27, v251\n\t"
    "v_pk_mul_f32 v[184:185], v[184:185], v[10:11] op_sel_hi:[1,0]\n\t"
    "v_pk_mul_f32 v[186:187], v[186:187], v[10:11] op_sel_hi:[1,0]\n\t"
    "v_pk_mul_f32 v[188:189], v[188:189], v[10:11] op_sel_hi:[1,0]\n\t"
    "v_pk_mul_f32 v[190:191], v[190:191], v[10:11] op_sel_hi:[1,0]\n\t"
    "v_cvt_pk_bf16_f32 v252, v184, v185\n\t"
    "v_cvt_pk_bf16_f32 v253, v186, v187\n\t"
    "v_cvt_pk_bf16_f32 v254, v188, v189\n\t"
    "v_cvt_pk_bf16_f32 v255, v190, v191\n\t"
    "buffer_store_dwordx4 v[252:255], v31, s[52:55], s68 offen offset:3072 sc1\n\t"
    "v_accvgpr_write_b32 a28, v252\n\t"
    "v_accvgpr_write_b32 a29, v253\n\t"
    "v_accvgpr_write_b32 a30, v254\n\t"
    "v_accvgpr_write_b32 a31, v255\n\t"
    "s_cmp_lg_u32 s77, 0\n\t"
    "s_cbranch_scc1 .Lfa_skip5\n\t"
    "v_mov_b32_e32 v8, v37\n\t"
    "s_mov_b32 s75, 0x5d800000\n\t"
    "s_add_u32 s68, s76, 0xa0000\n\t"
    "v_permlane32_swap_b32_e32 v37, v8\n\t"
    "v_add_f32_e32 v37, v37, v8\n\t"
    "v_mov_b32_e32 v8, v37\n\t"
    "s_nop 0\n\t"
    "s_nop 0\n\t"
    "v_permlane16_swap_b32_e32 v37, v8\n\t"
    "v_add_f32_e32 v37, v37, v8\n\t"
    "v_rcp_f32_e32 v10, v37\n\t"
    "v_cmp_nge_f32_e32 vcc, s75, v37\n\t"
    "v_pk_mul_f32 v[192:193], v[192:193], v[10:11] op_sel_hi:[1,0]\n\t"
    "v_pk_mul_f32 v[194:195], v[194:195], v[10:11] op_sel_hi:[1,0]\n\t"
    "v_pk_mul_f32 v[196:197], v[196:197], v[10:11] op_sel_hi:[1,0]\n\t"
    "v_pk_mul_f32 v[198:199], v[198:199], v[10:11] op_sel_hi:[1,0]\n\t"
    "s_or_b64 s[94:95], s[94:95], vcc\n\t"
    "v_cvt_pk_bf16_f32 v248, v192, v193\n\t"
    "v_cvt_pk_bf16_f32 v249, v194, v195\n\t"
    "v_cvt_pk_bf16_f32 v250, v196, v197\n\t"
    "v_cvt_pk_bf16_f32 v251, v198, v199\n\t"
    "buffer_store_dwordx4 v[248:251], v31, s[52:55], s68 offen sc1\n\t"
    "v_accvgpr_write_b32 a48, v248\n\t"
    "v_accvgpr_write_b32 a49, v249\n\t"
    "v_accvgpr_write_b32 a50, v250\n\t"
    "v_accvgpr_write_b32 a51, v251\n\t"
    "v_pk_mul_f32 v[200:201], v[200:201], v[10:11] op_sel_hi:[1,0]\n\t"
    "v_pk_mul_f32 v[202:203], v[202:203], v[10:11] op_sel_hi:[1,0]\n\t"
    "v_pk_mul_f32 v[204:205], v[204:205], v[10:11] op_sel_hi:[1,0]\n\t"
    "v_pk_mul_f32 v[206:207], v[206:207], v[10:11] op_sel_hi:[1,0]\n\t"
    "v_cvt_pk_bf16_f32 v252, v200, v201\n\t"
    "v_cvt_pk_bf16_f32 v253, v202, v203\n\t"
    "v_cvt_pk_bf16_f32 v254, v204, v205\n\t"
    "v_cvt_pk_bf16_f32 v255, v206, v207\n\t"
    "buffer_store_dwordx4 v[252:255], v31, s[52:55], s68 offen offset:1024 sc1\n\t"
    "v_accvgpr_write_b32 a52, v252\n\t"
    "v_accvgpr_write_b32 a53, v253\n\t"
    "v_accvgpr_write_b32 a54, v254\n\t"
    "v_accvgpr_write_b32 a55, v255\n\t"
    "v_pk_mul_f32 v[208:209], v[208:209], v[10:11] op_sel_hi:[1,0]\n\t"
    "v_pk_mul_f32 v[210:211], v[210:211], v[10:11] op_sel_hi:[1,0]\n\t"
    "v_pk_mul_f32 v[212:213], v[212:213], v[10:11] op_sel_hi:[1,0]\n\t"
    "v_pk_mul_f32 v[214:215], v[214:215], v[10:11] op_sel_hi:[1,0]\n\t"
    "v_cvt_pk_bf16_f32 v248, v208, v209\n\t"
    "v_cvt_pk_bf16_f32 v249, v210, v211\n\t"
    "v_cvt_pk_bf16_f32 v250, v212, v213\n\t"
    "v_cvt_pk_bf16_f32 v251, v214, v215\n\t"
    "buffer_store_dwordx4 v[248:251], v31, s[52:55], s68 offen offset:2048 sc1\n\t"
    "v_accvgpr_write_b32 a56, v248\n\t"
    "v_accvgpr_write_b32 a57, v249\n\t"
    "v_accvgpr_write_b32 a58, v250\n\t"
    "v_accvgpr_write_b32 a59, v251\n\t"
    "v_pk_mul_f32 v[216:217], v[216:217], v[10:11] op_sel_hi:[1,0]\n\t"
    "v_pk_mul_f32 v[218:219], v[218:219], v[10:11] op_sel_hi:[1,0]\n\t"
    "v_pk_mul_f32 v[220:221], v[220:221], v[10:11] op_sel_hi:[1,0]\n\t"
    "v_pk_mul_f32 v[222:223], v[222:223], v[10:11] op_sel_hi:[1,0]\n\t"
    "v_cvt_pk_bf16_f32 v252, v216, v217\n\t"
    "v_cvt_pk_bf16_f32 v253, v218, v219\n\t"
    "v_cvt_pk_bf16_f32 v254, v220, v221\n\t"
    "v_cvt_pk_bf16_f32 v255, v222, v223\n\t"
    "buffer_store_dwordx4 v[252:255], v31, s[52:55], s68 offen offset:3072 sc1\n\t"
    "v_accvgpr_write_b32 a60, v252\n\t"
    "v_accvgpr_write_b32 a61, v253\n\t"
    "v_accvgpr_write_b32 a62, v254\n\t"
    "v_accvgpr_write_b32 a63, v255\n\t"
    "s_branch .Lfa_have5\n\t"
    ".Lfa_skip5:\n\t"
    "v_accvgpr_write_b32 a48, 0\n\t"
    "v_accvgpr_write_b32 a49, 0\n\t"
    "v_accvgpr_write_b32 a50, 0\n\t"
    "v_accvgpr_write_b32 a51, 0\n\t"
    "v_accvgpr_write_b32 a52, 0\n\t"
    "v_accvgpr_write_b32 a53, 0\n\t"
    "v_accvgpr_write_b32 a54, 0\n\t"
    "v_accvgpr_write_b32 a55, 0\n\t"
    "v_accvgpr_write_b32 a56, 0\n\t"
    "v_accvgpr_write_b32 a57, 0\n\t"
    "v_accvgpr_write_b32 a58, 0\n\t"
    "v_accvgpr_write_b32 a59, 0\n\t"
    "v_accvgpr_write_b32 a60, 0\n\t"
    "v_accvgpr_write_b32 a61, 0\n\t"
    "v_accvgpr_write_b32 a62, 0\n\t"
    "v_accvgpr_write_b32 a63, 0\n\t"
    ".Lfa_have5:\n\t"
    "s_or_b32 s75, s94, s95\n\t"
    "v_mov_b32_e32 v0, s75\n\t"
    "v_and_b32_e32 v1, 48, v31\n\t"
    "v_lshlrev_b32_e32 v1, 9, v1\n\t"
    "s_lshl_b32 s68, s8, 2\n\t"
    "s_add_u32 s68, s68, 0\n\t"
    "v_add_u32_e32 v1, s68, v1\n\t"
    "v_mov_b32_e32 v2, s14\n\t"
    "ds_write_b32 v1, v0 offset:7168\n\t"
    "s_lshr_b32 s68, s8, 1\n\t"
    "s_lshl_b32 s68, s68, 13\n\t"
    "s_sub_u32 s68, s68, 0x4000\n\t"
    "v_add_u32_e32 v45, s68, v22\n\t"
    "v_add_u32_e32 v48, s68, v23\n\t"
    "s_waitcnt lgkmcnt(0)\n\t"
    "s_barrier\n\t"
    "ds_read_b128 v[4:7], v2 offset:7168\n\t"
    "ds_read_b64_tr_b16 a[192:193], v45 offset:32768\n\t"
    "ds_read_b64_tr_b16 a[128:129], v45 offset:40960\n\t"
    "ds_read_b64_tr_b16 a[194:195], v45 offset:33792\n\t"
    "ds_read_b64_tr_b16 a[130:131], v45 offset:41984\n\t"
    "ds_read_b64_tr_b16 a[196:197], v48 offset:32768\n\t"
    "ds_read_b64_tr_b16 a[132:133], v48 offset:40960\n\t"
    "ds_read_b64_tr_b16 a[198:199], v48 offset:33792\n\t"
    "ds_read_b64_tr_b16 a[134:135], v48 offset:41984\n\t"
    "ds_read_b64_tr_b16 a[200:201], v45 offset:33024\n\t"
    "ds_read_b64_tr_b16 a[136:137], v45 offset:41216\n\t"
    "ds_read_b64_tr_b16 a[202:203], v45 offset:34048\n\t"
    "ds_read_b64_tr_b16 a[138:139], v45 offset:42240\n\t"
    "ds_read_b64_tr_b16 a[204:205], v48 offset:33024\n\t"
    "ds_read_b64_tr_b16 a[140:141], v48 offset:41216\n\t"
    "s_waitcnt lgkmcnt(14)\n\t"
    "v_or3_b32 v4, v4, v5, v6\n\t"
    "v_or_b32_e32 v4, v4, v7\n\t"
    "s_nop 0\n\t"
    "v_readfirstlane_b32 s68, v4\n\t"
    "s_nop 0\n\t"
    "s_andn2_b32 s68, s68, s15\n\t"
    "s_cbranch_scc1 .Lfa_slow\n\t"
    "s_add_u32 m0, s14, 0x1c00\n\t"
    "ds_read_b64_tr_b16 a[206:207], v48 offset:34048\n\t"
    "ds_read_b64_tr_b16 a[142:143], v48 offset:42240\n\t"
    "ds_read_b64_tr_b16 a[208:209], v45 offset:33280\n\t"
    "ds_read_b64_tr_b16 a[144:145], v45 offset:41472\n\t"
    "ds_read_b64_tr_b16 a[210:211], v45 offset:34304\n\t"
    "ds_read_b64_tr_b16 a[146:147], v45 offset:42496\n\t"
    "ds_read_b64_tr_b16 a[212:213], v48 offset:33280\n\t"
    "ds_read_b64_tr_b16 a[148:149], v48 offset:41472\n\t"
    "ds_read_b64_tr_b16 a[214:215], v48 offset:34304\n\t"
    "ds_read_b64_tr_b16 a[150:151], v48 offset:42496\n\t"
    "ds_read_b64_tr_b16 a[216:217], v45 offset:33536\n\t"
    "ds_read_b64_tr_b16 a[152:153], v45 offset:41728\n\t"
    "ds_read_b64_tr_b16 a[218:219], v45 offset:34560\n\t"
    "ds_read_b64_tr_b16 a[154:155], v45 offset:42752\n\t"
    "ds_read_b64_tr_b16 a[220:221], v48 offset:33536\n\t"
    "ds_read_b64_tr_b16 a[156:157], v48 offset:41728\n\t"
    "ds_read_b64_tr_b16 a[222:223], v48 offset:34560\n\t"
    "ds_read_b64_tr_b16 a[158:159], v48 offset:42752\n\t"
    "buffer_load_dwordx4 v28, s[16:19], s61 offen lds\n\t"
    "v_add_u32_e32 v46, 0x10000, v45\n\t"
    "v_add_u32_e32 v49, 0x10000, v48\n\t"
    "v_add_u32_e32 v47, 0x20000, v45\n\t"
    "v_add_u32_e32 v50, 0x20000, v48\n\t"
    "s_waitcnt lgkmcnt(0)\n\t"
    "s_barrier\n\t"
    "v_mfma_f32_16x16x32_bf16 v[64:67], a[192:195], a[0:3], 0\n\t"
    "s_bfe_u32 s69, s71, 0x30015\n\t"
    "s_bfe_u32 s75, s71, 0x10014\n\t"
    "s_lshl2_add_u32 s69, s69, s75\n\t"
    "v_mfma_f32_16x16x32_bf16 v[64:67], a[128:131], a[4:7], v[64:67]\n\t"
    "s_lshl_b32 s69, s69, 11\n\t"
    "s_add_u32 s69, s69, s25\n\t"
    "s_lshl2_add_u32 s69, s69, s79\n\t"
    "v_mfma_f32_16x16x32_bf16 v[96:99], a[128:131], a[36:39], 0\n\t"
    "s_lshl_b32 s69, s69, 4\n\t"
    "s_add_u32 s70, s69, 0x8000\n\t"
    "v_mfma_f32_16x16x32_bf16 v[96:99], a[192:195], a[32:35], v[96:99]\n\t"
    "ds_read_b64_tr_b16 a[224:225], v46 offset:0\n\t"
    "v_mfma_f32_16x16x32_bf16 v[128:131], a[192:195], a[64:67], 0\n\t"
    "v_mfma_f32_16x16x32_bf16 v[128:131], a[128:131], a[68:71], v[128:131]\n\t"
    "ds_read_b64_tr_b16 a[160:161], v46 offset:8192\n\t"
    "v_mfma_f32_16x16x32_bf16 v[160:163], a[128:131], a[100:103], 0\n\t"
    "v_mfma_f32_16x16x32_bf16 v[160:163], a[192:195], a[96:99], v[160:163]\n\t"
    "ds_read_b64_tr_b16 a[226:227], v46 offset:1024\n\t"
    "v_add_u32_e32 v40, 0x20000, v31\n\t"
    "v_mfma_f32_16x16x32_bf16 v[192:195], a[192:195], a[16:19], 0\n\t"
    "v_add_u32_e32 v41, 0x40000, v31\n\t"
    "v_add_u32_e32 v42, 0x60000, v31\n\t"
    "v_mfma_f32_16x16x32_bf16 v[192:195], a[128:131], a[20:23], v[192:195]\n\t"
    "s_add_u32 m0, s14, 0x8000\n\t"
    "v_add_u32_e32 v43, 0x80000, v31\n\t"
    "v_add_u32_e32 v44, 0xa0000, v31\n\t"
    "v_mfma_f32_16x16x32_bf16 v[224:227], a[128:131], a[52:55], 0\n\t"
    "buffer_load_dwordx4 v29, s[16:19], s69 offen lds\n\t"
    "v_mfma_f32_16x16x32_bf16 v[224:227], a[192:195], a[48:51], v[224:227]\n\t"
    "ds_read_b64_tr_b16 a[162:163], v46 offset:9216\n\t"
    "v_and_b32_e32 v57, 0xf0, v31\n\t"
    "v_mfma_f32_16x16x32_bf16 v[228:231], a[196:199], a[48:51], 0\n\t"
    "v_lshlrev_b32_e32 v57, 8, v57\n\t"
    "v_and_b32_e32 v58, 0x1f0, v31\n\t"
    "v_mfma_f32_16x16x32_bf16 v[228:231], a[132:135], a[52:55], v[228:231]\n\t"
    "ds_read_b64_tr_b16 a[228:229], v49 offset:0\n\t"
    "s_add_u32 m0, s14, 0x8400\n\t"
    "v_mfma_f32_16x16x32_bf16 v[196:199], a[132:135], a[20:23], 0\n\t"
    "buffer_load_dwordx4 v29, s[16:19], s70 offen lds\n\t"
    "v_mfma_f32_16x16x32_bf16 v[196:199], a[196:199], a[16:19], v[196:199]\n\t"
    "v_lshlrev_b32_e32 v58, 3, v58\n\t"
    "v_add_u32_e32 v58, 0x80, v58\n\t"
    "v_mfma_f32_16x16x32_bf16 v[164:167], a[196:199], a[96:99], 0\n\t"
    "v_lshrrev_b32_e32 v3, 4, v31\n\t"
    "v_and_b32_e32 v3, 48, v3\n\t"
    "v_mfma_f32_16x16x32_bf16 v[164:167], a[132:135], a[100:103], v[164:167]\n\t"
    "ds_read_b64_tr_b16 a[164:165], v49 offset:8192\n\t"
    "v_or_b32_e32 v57, v57, v3\n\t"
    "v_mfma_f32_16x16x32_bf16 v[132:135], a[132:135], a[68:71], 0\n\t"
    "s_add_u32 m0, s14, 0x8800\n\t"
    "s_mul_i32 s68, s77, 0x3000\n\t"
    "s_lshr_b32 s75, s8, 1\n\t"
    "v_mfma_f32_16x16x32_bf16 v[132:135], a[196:199], a[64:67], v[132:135]\n\t"
    "buffer_load_dwordx4 v26, s[16:19], s69 offen lds\n\t"
    "v_mfma_f32_16x16x32_bf16 v[100:103], a[196:199], a[32:35], 0\n\t"
    "ds_read_b64_tr_b16 a[230:231], v49 offset:1024\n\t"
    "s_lshl_b32 s75, s75, 16\n\t"
    "v_mfma_f32_16x16x32_bf16 v[100:103], a[132:135], a[36:39], v[100:103]\n\t"
    "s_add_u32 s68, s68, s75\n\t"
    "s_add_u32 s68, s68, 0x10000\n\t"
    "v_add_u32_e32 v51, s68, v31\n\t"
    "v_mfma_f32_16x16x32_bf16 v[68:71], a[132:135], a[4:7], 0\n\t"
    "ds_read_b64_tr_b16 a[166:167], v49 offset:9216\n\t"
    "v_xor_b32_e32 v52, 0x30000, v51\n\t"
    "v_mfma_f32_16x16x32_bf16 v[68:71], a[196:199], a[0:3], v[68:71]\n\t"
    "s_add_u32 m0, s14, 0x8c00\n\t"
    "s_mov_b32 s48, s100\n\t"
    "s_and_b32 s49, s101, 0xffff\n\t"
    "v_mfma_f32_16x16x32_bf16 v[72:75], a[200:203], a[0:3], 0\n\t"
    "buffer_load_dwordx4 v26, s[16:19], s70 offen lds\n\t"
    "v_mfma_f32_16x16x32_bf16 v[72:75], a[136:139], a[4:7], v[72:75]\n\t"
    "s_mov_b32 s50, 0xc0000\n\t"
    "v_mfma_f32_16x16x32_bf16 v[104:107], a[136:139], a[36:39], 0\n\t"
    "ds_read_b64_tr_b16 a[232:233], v46 offset:256\n\t"
    "v_mfma_f32_16x16x32_bf16 v[104:107], a[200:203], a[32:35], v[104:107]\n\t"
    "s_add_u32 m0, s14, 0x9000\n\t"
    "v_mfma_f32_16x16x32_bf16 v[136:139], a[200:203], a[64:67], 0\n\t"
    "buffer_load_dwordx4 v27, s[16:19], s69 offen lds\n\t"
    "v_mfma_f32_16x16x32_bf16 v[136:139], a[136:139], a[68:71], v[136:139]\n\t"
    "ds_read_b64_tr_b16 a[168:169], v46 offset:8448\n\t"
    "v_mfma_f32_16x16x32_bf16 v[168:171], a[136:139], a[100:103], 0\n\t"
    "v_mfma_f32_16x16x32_bf16 v[168:171], a[200:203], a[96:99], v[168:171]\n\t"
    "v_mfma_f32_16x16x32_bf16 v[200:203], a[200:203], a[16:19], 0\n\t"
    "ds_read_b64_tr_b16 a[234:235], v46 offset:1280\n\t"
    "s_add_u32 m0, s14, 0x9400\n\t"
    "v_mfma_f32_16x16x32_bf16 v[200:203], a[136:139], a[20:23], v[200:203]\n\t"
    "buffer_load_dwordx4 v27, s[16:19], s70 offen lds\n\t"
    "v_mfma_f32_16x16x32_bf16 v[232:235], a[136:139], a[52:55], 0\n\t"
    "v_mfma_f32_16x16x32_bf16 v[232:235], a[200:203], a[48:51], v[232:235]\n\t"
    "ds_read_b64_tr_b16 a[170:171], v46 offset:9472\n\t"
    "v_mfma_f32_16x16x32_bf16 v[236:239], a[204:207], a[48:51], 0\n\t"
    "v_mfma_f32_16x16x32_bf16 v[236:239], a[140:143], a[52:55], v[236:239]\n\t"
    "ds_read_b64_tr_b16 a[236:237], v49 offset:256\n\t"
    "s_add_u32 m0, s14, 0x9800\n\t"
    "v_mfma_f32_16x16x32_bf16 v[204:207], a[140:143], a[20:23], 0\n\t"
    "buffer_load_dwordx4 v28, s[16:19], s69 offen lds\n\t"
    "v_mfma_f32_16x16x32_bf16 v[204:207], a[204:207], a[16:19], v[204:207]\n\t"
    "v_mfma_f32_16x16x32_bf16 v[172:175], a[204:207], a[96:99], 0\n\t"
    "v_mfma_f32_16x16x32_bf16 v[172:175], a[140:143], a[100:103], v[172:175]\n\t"
    "ds_read_b64_tr_b16 a[172:173], v49 offset:8448\n\t"
    "s_add_u32 m0, s14, 0x9c00\n\t"
    "v_mfma_f32_16x16x32_bf16 v[140:143], a[140:143], a[68:71], 0\n\t"
    "buffer_load_dwordx4 v28, s[16:19], s70 offen lds\n\t"
    "v_mfma_f32_16x16x32_bf16 v[140:143], a[204:207], a[64:67], v[140:143]\n\t"
    "v_mfma_f32_16x16x32_bf16 v[108:111], a[204:207], a[32:35], 0\n\t"
    "ds_read_b64_tr_b16 a[238:239], v49 offset:1280\n\t"
    "v_mfma_f32_16x16x32_bf16 v[108:111], a[140:143], a[36:39], v[108:111]\n\t"
    "v_mfma_f32_16x16x32_bf16 v[76:79], a[140:143], a[4:7], 0\n\t"
    "v_mfma_f32_16x16x32_bf16 v[76:79], a[204:207], a[0:3], v[76:79]\n\t"
    "ds_read_b64_tr_b16 a[174:175], v49 offset:9472\n\t"
    "v_mfma_f32_16x16x32_bf16 v[80:83], a[208:211], a[0:3], 0\n\t"
    "v_mfma_f32_16x16x32_bf16 v[80:83], a[144:147], a[4:7], v[80:83]\n\t"
    "ds_read_b64_tr_b16 a[240:241], v46 offset:512\n\t"
    "v_mfma_f32_16x16x32_bf16 v[112:115], a[144:147], a[36:39], 0\n\t"
    "v_mfma_f32_16x16x32_bf16 v[112:115], a[208:211], a[32:35], v[112:115]\n\t"
    "ds_read_b64_tr_b16 a[176:177], v46 offset:8704\n\t"
    "v_mfma_f32_16x16x32_bf16 v[144:147], a[208:211], a[64:67], 0\n\t"
    "v_mfma_f32_16x16x32_bf16 v[144:147], a[144:147], a[68:71], v[144:147]\n\t"
    "v_mfma_f32_16x16x32_bf16 v[176:179], a[144:147], a[100:103], 0\n\t"
    "ds_read_b64_tr_b16 a[242:243], v46 offset:1536\n\t"
    "v_mfma_f32_16x16x32_bf16 v[176:179], a[208:211], a[96:99], v[176:179]\n\t"
    "v_mfma_f32_16x16x32_bf16 v[208:211], a[208:211], a[16:19], 0\n\t"
    "ds_read_b64_tr_b16 a[178:179], v46 offset:9728\n\t"
    "v_mfma_f32_16x16x32_bf16 v[208:211], a[144:147], a[20:23], v[208:211]\n\t"
    "v_mfma_f32_16x16x32_bf16 v[240:243], a[144:147], a[52:55], 0\n\t"
    "v_mfma_f32_16x16x32_bf16 v[240:243], a[208:211], a[48:51], v[240:243]\n\t"
    "ds_read_b64_tr_b16 a[244:245], v49 offset:512\n\t"
    "v_mfma_f32_16x16x32_bf16 v[244:247], a[212:215], a[48:51], 0\n\t"
    "v_mfma_f32_16x16x32_bf16 v[244:247], a[148:151], a[52:55], v[244:247]\n\t"
    "ds_read_b64_tr_b16 a[180:181], v49 offset:8704\n\t"
    "v_mfma_f32_16x16x32_bf16 v[212:215], a[148:151], a[20:23], 0\n\t"
    "v_mfma_f32_16x16x32_bf16 v[212:215], a[212:215], a[16:19], v[212:215]\n\t"
    "ds_read_b64_tr_b16 a[246:247], v49 offset:1536\n\t"
    "v_mfma_f32_16x16x32_bf16 v[180:183], a[212:215], a[96:99], 0\n\t"
    "v_mfma_f32_16x16x32_bf16 v[180:183], a[148:151], a[100:103], v[180:183]\n\t"
    "v_mfma_f32_16x16x32_bf16 v[148:151], a[148:151], a[68:71], 0\n\t"
    "ds_read_b64_tr_b16 a[182:183], v49 offset:9728\n\t"
    "v_mfma_f32_16x16x32_bf16 v[148:151], a[212:215], a[64:67], v[148:151]\n\t"
    "v_mfma_f32_16x16x32_bf16 v[116:119], a[212:215], a[32:35], 0\n\t"
    "ds_read_b64_tr_b16 a[248:249], v46 offset:768\n\t"
    "v_mfma_f32_16x16x32_bf16 v[116:119], a[148:151], a[36:39], v[116:119]\n\t"
    "v_mfma_f32_16x16x32_bf16 v[84:87], a[148:151], a[4:7], 0\n\t"
    "v_mfma_f32_16x16x32_bf16 v[84:87], a[212:215], a[0:3], v[84:87]\n\t"
    "ds_read_b64_tr_b16 a[184:185], v46 offset:8960\n\t"
    "v_mfma_f32_16x16x32_bf16 v[88:91], a[216:219], a[0:3], 0\n\t"
    "v_mfma_f32_16x16x32_bf16 v[88:91], a[152:155], a[4:7], v[88:91]\n\t"
    "ds_read_b64_tr_b16 a[250:251], v46 offset:1792\n\t"
    "v_mfma_f32_16x16x32_bf16 v[120:123], a[152:155], a[36:39], 0\n\t"
    "v_mfma_f32_16x16x32_bf16 v[120:123], a[216:219], a[32:35], v[120:123]\n\t"
    "ds_read_b64_tr_b16 a[186:187], v46 offset:9984\n\t"
    "v_mfma_f32_16x16x32_bf16 v[152:155], a[216:219], a[64:67], 0\n\t"
    "v_mfma_f32_16x16x32_bf16 v[152:155], a[152:155], a[68:71], v[152:155]\n\t"
    "v_mfma_f32_16x16x32_bf16 v[184:187], a[152:155], a[100:103], 0\n\t"
    "ds_read_b64_tr_b16 a[252:253], v49 offset:768\n\t"
    "v_mfma_f32_16x16x32_bf16 v[184:187], a[216:219], a[96:99], v[184:187]\n\t"
    "v_mfma_f32_16x16x32_bf16 v[216:219], a[216:219], a[16:19], 0\n\t"
    "ds_read_b64_tr_b16 a[188:189], v49 offset:8960\n\t"
    "v_mfma_f32_16x16x32_bf16 v[216:219], a[152:155], a[20:23], v[216:219]\n\t"
    "v_mfma_f32_16x16x32_bf16 v[248:251], a[152:155], a[52:55], 0\n\t"
    "v_mfma_f32_16x16x32_bf16 v[248:251], a[216:219], a[48:51], v[248:251]\n\t"
    "ds_read_b64_tr_b16 a[254:255], v49 offset:1792\n\t"
    "v_mfma_f32_16x16x32_bf16 v[252:255], a[220:223], a[48:51], 0\n\t"
    "v_mfma_f32_16x16x32_bf16 v[252:255], a[156:159], a[52:55], v[252:255]\n\t"
    "ds_read_b64_tr_b16 a[190:191], v49 offset:9984\n\t"
    "v_mfma_f32_16x16x32_bf16 v[220:223], a[156:159], a[20:23], 0\n\t"
    "v_mfma_f32_16x16x32_bf16 v[220:223], a[220:223], a[16:19], v[220:223]\n\t"
    "v_mfma_f32_16x16x32_bf16 v[188:191], a[220:223], a[96:99], 0\n\t"
    "v_mfma_f32_16x16x32_bf16 v[188:191], a[156:159], a[100:103], v[188:191]\n\t"
    "v_mfma_f32_16x16x32_bf16 v[156:159], a[156:159], a[68:71], 0\n\t"
    "v_mfma_f32_16x16x32_bf16 v[156:159], a[220:223], a[64:67], v[156:159]\n\t"
    "v_mfma_f32_16x16x32_bf16 v[124:127], a[220:223], a[32:35], 0\n\t"
    "v_mfma_f32_16x16x32_bf16 v[124:127], a[156:159], a[36:39], v[124:127]\n\t"
    "v_mfma_f32_16x16x32_bf16 v[92:95], a[156:159], a[4:7], 0\n\t"
    "v_mfma_f32_16x16x32_bf16 v[92:95], a[220:223], a[0:3], v[92:95]\n\t"
    "s_waitcnt vmcnt(0) lgkmcnt(0)\n\t"
    "s_barrier\n\t"
    "s_cmp_lg_u32 s8, 0\n\t"
    "s_cbranch_scc1 .Lfa_pub\n\t"
    "s_lshl_b32 s68, s9, 7\n\t"
    "s_add_u32 s68, s68, 0x80\n\t"
    "v_mov_b32_e32 v2, s68\n\t"
    "v_mov_b32_e32 v0, s96\n\t"
    "v_mov_b32_e32 v1, s97\n\t"
    "s_mov_b64 exec, 1\n\t"
    "global_store_dwordx2 v2, v[0:1], s[26:27] sc1\n\t"
    "s_mov_b64 exec, -1\n\t"
    ".Lfa_pub:\n\t"
    "v_mfma_f32_16x16x32_bf16 v[64:67], a[224:227], a[8:11], v[64:67]\n\t"
    "s_bfe_u32 s60, s71, 0x30019\n\t"
    "s_bfe_u32 s75, s71, 0x10018\n\t"
    "s_lshl2_add_u32 s60, s60, s75\n\t"
    "v_mfma_f32_16x16x32_bf16 v[64:67], a[160:163], a[12:15], v[64:67]\n\t"
    "s_lshl_b32 s60, s60, 11\n\t"
    "s_add_u32 s60, s60, s25\n\t"
    "s_lshl2_add_u32 s60, s60, s79\n\t"
    "v_mfma_f32_16x16x32_bf16 v[96:99], a[160:163], a[44:47], v[96:99]\n\t"
    "s_lshl_b32 s60, s60, 4\n\t"
    "s_add_u32 s61, s60, 0x8000\n\t"
    "s_bfe_u32 s64, s71, 0x30009\n\t"
    "v_mfma_f32_16x16x32_bf16 v[96:99], a[224:227], a[40:43], v[96:99]\n\t"
    "ds_read_b64_tr_b16 a[192:193], v46 offset:32768\n\t"
    "s_bfe_u32 s75, s71, 0x10008\n\t"
    "s_lshl2_add_u32 s64, s64, s75\n\t"
    "s_lshl_b32 s64, s64, 11\n\t"
    "v_mfma_f32_16x16x32_bf16 v[128:131], a[224:227], a[72:75], v[128:131]\n\t"
    "ds_read_b64_tr_b16 a[128:129], v46 offset:40960\n\t"
    "s_add_u32 s64, s64, s25\n\t"
    "v_mfma_f32_16x16x32_bf16 v[128:131], a[160:163], a[76:79], v[128:131]\n\t"
    "v_mfma_f32_16x16x32_bf16 v[160:163], a[160:163], a[108:111], v[160:163]\n\t"
    "ds_read_b64_tr_b16 a[194:195], v46 offset:33792\n\t"
    "v_mfma_f32_16x16x32_bf16 v[160:163], a[224:227], a[104:107], v[160:163]\n\t"
    "v_mfma_f32_16x16x32_bf16 v[192:195], a[224:227], a[24:27], v[192:195]\n\t"
    "ds_read_b64_tr_b16 a[130:131], v46 offset:41984\n\t"
    "s_add_u32 m0, s14, 0x10000\n\t"
    "v_mfma_f32_16x16x32_bf16 v[192:195], a[160:163], a[28:31], v[192:195]\n\t"
    "buffer_load_dwordx4 v29, s[16:19], s60 offen lds\n\t"
    "v_mfma_f32_16x16x32_bf16 v[224:227], a[160:163], a[60:63], v[224:227]\n\t"
    "v_mfma_f32_16x16x32_bf16 v[224:227], a[224:227], a[56:59], v[224:227]\n\t"
    "ds_read_b64_tr_b16 a[196:197], v49 offset:32768\n\t"
    "s_add_u32 m0, s14, 0x10400\n\t"
    "v_mfma_f32_16x16x32_bf16 v[228:231], a[228:231], a[56:59], v[228:231]\n\t"
    "buffer_load_dwordx4 v29, s[16:19], s61 offen lds\n\t"
    "v_mfma_f32_16x16x32_bf16 v[228:231], a[164:167], a[60:63], v[228:231]\n\t"
    "v_mfma_f32_16x16x32_bf16 v[196:199], a[164:167], a[28:31], v[196:199]\n\t"
    "ds_read_b64_tr_b16 a[132:133], v49 offset:40960\n\t"
    "v_mfma_f32_16x16x32_bf16 v[196:199], a[228:231], a[24:27], v[196:199]\n\t"
    "s_add_u32 m0, s14, 0x10800\n\t"
    "v_mfma_f32_16x16x32_bf16 v[164:167], a[228:231], a[104:107], v[164:167]\n\t"
    "buffer_load_dwordx4 v26, s[16:19], s60 offen lds\n\t"
    "v_mfma_f32_16x16x32_bf16 v[164:167], a[164:167], a[108:111], v[164:167]\n\t"
    "ds_read_b64_tr_b16 a[198:199], v49 offset:33792\n\t"
    "v_mfma_f32_16x16x32_bf16 v[132:135], a[164:167], a[76:79], v[132:135]\n\t"
    "s_add_u32 m0, s14, 0x10c00\n\t"
    "v_mfma_f32_16x16x32_bf16 v[132:135], a[228:231], a[72:75], v[132:135]\n\t"
    "buffer_load_dwordx4 v26, s[16:19], s61 offen lds\n\t"
    "v_mfma_f32_16x16x32_bf16 v[100:103], a[228:231], a[40:43], v[100:103]\n\t"
    "ds_read_b64_tr_b16 a[134:135], v49 offset:41984\n\t"
    "v_mfma_f32_16x16x32_bf16 v[100:103], a[164:167], a[44:47], v[100:103]\n\t"
    "s_add_u32 m0, s14, 0x11000\n\t"
    "v_mfma_f32_16x16x32_bf16 v[68:71], a[164:167], a[12:15], v[68:71]\n\t"
    "buffer_load_dwordx4 v27, s[16:19], s60 offen lds\n\t"
    "v_mfma_f32_16x16x32_bf16 v[68:71], a[228:231], a[8:11], v[68:71]\n\t"
    "ds_read_b64_tr_b16 a[200:201], v46 offset:33024\n\t"
    "v_mfma_f32_16x16x32_bf16 v[72:75], a[232:235], a[8:11], v[72:75]\n\t"
    "s_add_u32 m0, s14, 0x11400\n\t"
    "v_mfma_f32_16x16x32_bf16 v[72:75], a[168:171], a[12:15], v[72:75]\n\t"
    "buffer_load_dwordx4 v27, s[16:19], s61 offen lds\n\t"
    "v_mfma_f32_16x16x32_bf16 v[104:107], a[168:171], a[44:47], v[104:107]\n\t"
    "ds_read_b64_tr_b16 a[136:137], v46 offset:41216\n\t"
    "v_mfma_f32_16x16x32_bf16 v[104:107], a[232:235], a[40:43], v[104:107]\n\t"
    "s_add_u32 m0, s14, 0x11800\n\t"
    "v_mfma_f32_16x16x32_bf16 v[136:139], a[232:235], a[72:75], v[136:139]\n\t"
    "buffer_load_dwordx4 v28, s[16:19], s60 offen lds\n\t"
    "v_mfma_f32_16x16x32_bf16 v[136:139], a[168:171], a[76:79], v[136:139]\n\t"
    "ds_read_b64_tr_b16 a[202:203], v46 offset:34048\n\t"
    "v_mfma_f32_16x16x32_bf16 v[168:171], a[168:171], a[108:111], v[168:171]\n\t"
    "v_mfma_f32_16x16x32_bf16 v[168:171], a[232:235], a[104:107], v[168:171]\n\t"
    "ds_read_b64_tr_b16 a[138:139], v46 offset:42240\n\t"
    "s_add_u32 m0, s14, 0x11c00\n\t"
    "v_mfma_f32_16x16x32_bf16 v[200:203], a[232:235], a[24:27], v[200:203]\n\t"
    "buffer_load_dwordx4 v28, s[16:19], s61 offen lds\n\t"
    "v_mfma_f32_16x16x32_bf16 v[200:203], a[168:171], a[28:31], v[200:203]\n\t"
    "v_mfma_f32_16x16x32_bf16 v[232:235], a[168:171], a[60:63], v[232:235]\n\t"
    "ds_read_b64_tr_b16 a[204:205], v49 offset:33024\n\t"
    "v_mfma_f32_16x16x32_bf16 v[232:235], a[232:235], a[56:59], v[232:235]\n\t"
    "global_load_dwordx2 v[60:61], v58, s[26:27] sc1\n\t"
    "v_mfma_f32_16x16x32_bf16 v[236:239], a[236:239], a[56:59], v[236:239]\n\t"
    "v_mfma_f32_16x16x32_bf16 v[236:239], a[172:175], a[60:63], v[236:239]\n\t"
    "ds_read_b64_tr_b16 a[140:141], v49 offset:41216\n\t"
    "v_mfma_f32_16x16x32_bf16 v[204:207], a[172:175], a[28:31], v[204:207]\n\t"
    "v_mfma_f32_16x16x32_bf16 v[204:207], a[236:239], a[24:27], v[204:207]\n\t"
    "ds_read_b64_tr_b16 a[206:207], v49 offset:34048\n\t"
    "v_mfma_f32_16x16x32_bf16 v[172:175], a[236:239], a[104:107], v[172:175]\n\t"
    "v_mfma_f32_16x16x32_bf16 v[172:175], a[172:175], a[108:111], v[172:175]\n\t"
    "ds_read_b64_tr_b16 a[142:143], v49 offset:42240\n\t"
    "v_mfma_f32_16x16x32_bf16 v[140:143], a[172:175], a[76:79], v[140:143]\n\t"
    "v_mfma_f32_16x16x32_bf16 v[140:143], a[236:239], a[72:75], v[140:143]\n\t"
    "ds_read_b64_tr_b16 a[208:209], v46 offset:33280\n\t"
    "v_mfma_f32_16x16x32_bf16 v[108:111], a[236:239], a[40:43], v[108:111]\n\t"
    "v_mfma_f32_16x16x32_bf16 v[108:111], a[172:175], a[44:47], v[108:111]\n\t"
    "ds_read_b64_tr_b16 a[144:145], v46 offset:41472\n\t"
    "v_mfma_f32_16x16x32_bf16 v[76:79], a[172:175], a[12:15], v[76:79]\n\t"
    "v_mfma_f32_16x16x32_bf16 v[76:79], a[236:239], a[8:11], v[76:79]\n\t"
    "ds_read_b64_tr_b16 a[210:211], v46 offset:34304\n\t"
    "v_mfma_f32_16x16x32_bf16 v[80:83], a[240:243], a[8:11], v[80:83]\n\t"
    "v_mfma_f32_16x16x32_bf16 v[80:83], a[176:179], a[12:15], v[80:83]\n\t"
    "ds_read_b64_tr_b16 a[146:147], v46 offset:42496\n\t"
    "v_mfma_f32_16x16x32_bf16 v[112:115], a[176:179], a[44:47], v[112:115]\n\t"
    "v_mfma_f32_16x16x32_bf16 v[112:115], a[240:243], a[40:43], v[112:115]\n\t"
    "ds_read_b64_tr_b16 a[212:213], v49 offset:33280\n\t"
    "v_mfma_f32_16x16x32_bf16 v[144:147], a[240:243], a[72:75], v[144:147]\n\t"
    "v_mfma_f32_16x16x32_bf16 v[144:147], a[176:179], a[76:79], v[144:147]\n\t"
    "ds_read_b64_tr_b16 a[148:149], v49 offset:41472\n\t"
    "v_mfma_f32_16x16x32_bf16 v[176:179], a[176:179], a[108:111], v[176:179]\n\t"
    "v_mfma_f32_16x16x32_bf16 v[176:179], a[240:243], a[104:107], v[176:179]\n\t"
    "ds_read_b64_tr_b16 a[214:215], v49 offset:34304\n\t"
    "v_mfma_f32_16x16x32_bf16 v[208:211], a[240:243], a[24:27], v[208:211]\n\t"
    "s_waitcnt vmcnt(0)\n\t"
    "v_cmp_eq_u64_e32 vcc, s[96:97], v[60:61]\n\t"
    "s_nop 1\n\t"
    "s_and_b32 s68, vcc_lo, s24\n\t"
    "s_cmp_eq_u32 s68, s24\n\t"
    "s_cbranch_scc0 .Lfa_stage1\n\t"
    ".Lfa_back1:\n\t"
    "s_mov_b32 s24, 0xffff\n\t"
    "v_mfma_f32_16x16x32_bf16 v[208:211], a[176:179], a[28:31], v[208:211]\n\t"
    "v_mfma_f32_16x16x32_bf16 v[240:243], a[176:179], a[60:63], v[240:243]\n\t"
    "buffer_load_dwordx4 a[80:83], v31, s[20:23], s64 offen sc1\n\t"
    "v_mfma_f32_16x16x32_bf16 v[240:243], a[240:243], a[56:59], v[240:243]\n\t"
    "ds_read_b64_tr_b16 a[150:151], v49 offset:42496\n\t"
    "v_mfma_f32_16x16x32_bf16 v[244:247], a[244:247], a[56:59], v[244:247]\n\t"
    "buffer_load_dwordx4 a[84:87], v31, s[20:23], s64 offen offset:1024 sc1\n\t"
    "v_mfma_f32_16x16x32_bf16 v[244:247], a[180:183], a[60:63], v[244:247]\n\t"
    "v_mfma_f32_16x16x32_bf16 v[212:215], a[180:183], a[28:31], v[212:215]\n\t"
    "ds_read_b64_tr_b16 a[216:217], v46 offset:33536\n\t"
    "v_mfma_f32_16x16x32_bf16 v[212:215], a[244:247], a[24:27], v[212:215]\n\t"
    "buffer_load_dwordx4 a[88:91], v40, s[20:23], s64 offen sc1\n\t"
    "v_mfma_f32_16x16x32_bf16 v[180:183], a[244:247], a[104:107], v[180:183]\n\t"
    "v_mfma_f32_16x16x32_bf16 v[180:183], a[180:183], a[108:111], v[180:183]\n\t"
    "buffer_load_dwordx4 a[92:95], v40, s[20:23], s64 offen offset:1024 sc1\n\t"
    "v_mfma_f32_16x16x32_bf16 v[148:151], a[180:183], a[76:79], v[148:151]\n\t"
    "ds_read_b64_tr_b16 a[152:153], v46 offset:41728\n\t"
    "v_mfma_f32_16x16x32_bf16 v[148:151], a[244:247], a[72:75], v[148:151]\n\t"
    "v_mfma_f32_16x16x32_bf16 v[116:119], a[244:247], a[40:43], v[116:119]\n\t"
    "buffer_load_dwordx4 a[112:115], v41, s[20:23], s64 offen sc1\n\t"
    "v_mfma_f32_16x16x32_bf16 v[116:119], a[180:183], a[44:47], v[116:119]\n\t"
    "ds_read_b64_tr_b16 a[218:219], v46 offset:34560\n\t"
    "v_mfma_f32_16x16x32_bf16 v[84:87], a[180:183], a[12:15], v[84:87]\n\t"
    "buffer_load_dwordx4 a[116:119], v41, s[20:23], s64 offen offset:1024 sc1\n\t"
    "v_mfma_f32_16x16x32_bf16 v[84:87], a[244:247], a[8:11], v[84:87]\n\t"
    "v_mfma_f32_16x16x32_bf16 v[88:91], a[248:251], a[8:11], v[88:91]\n\t"
    "ds_read_b64_tr_b16 a[154:155], v46 offset:42752\n\t"
    "v_mfma_f32_16x16x32_bf16 v[88:91], a[184:187], a[12:15], v[88:91]\n\t"
    "buffer_load_dwordx4 a[120:123], v42, s[20:23], s64 offen sc1\n\t"
    "v_mfma_f32_16x16x32_bf16 v[120:123], a[184:187], a[44:47], v[120:123]\n\t"
    "v_mfma_f32_16x16x32_bf16 v[120:123], a[248:251], a[40:43], v[120:123]\n\t"
    "buffer_load_dwordx4 a[124:127], v42, s[20:23], s64 offen offset:1024 sc1\n\t"
    "v_mfma_f32_16x16x32_bf16 v[152:155], a[248:251], a[72:75], v[152:155]\n\t"
    "ds_read_b64_tr_b16 a[220:221], v49 offset:33536\n\t"
    "v_mfma_f32_16x16x32_bf16 v[152:155], a[184:187], a[76:79], v[152:155]\n\t"
    "v_mfma_f32_16x16x32_bf16 v[184:187], a[184:187], a[108:111], v[184:187]\n\t"
    "buffer_load_dwordx4 v[4:7], v43, s[20:23], s64 offen sc1\n\t"
    "v_mfma_f32_16x16x32_bf16 v[184:187], a[248:251], a[104:107], v[184:187]\n\t"
    "ds_read_b64_tr_b16 a[156:157], v49 offset:41728\n\t"
    "v_mfma_f32_16x16x32_bf16 v[216:219], a[248:251], a[24:27], v[216:219]\n\t"
    "buffer_load_dwordx4 v[8:11], v43, s[20:23], s64 offen offset:1024 sc1\n\t"
    "v_mfma_f32_16x16x32_bf16 v[216:219], a[184:187], a[28:31], v[216:219]\n\t"
    "v_mfma_f32_16x16x32_bf16 v[248:251], a[184:187], a[60:63], v[248:251]\n\t"
    "ds_read_b64_tr_b16 a[222:223], v49 offset:34560\n\t"
    "v_mfma_f32_16x16x32_bf16 v[248:251], a[248:251], a[56:59], v[248:251]\n\t"
    "buffer_load_dwordx4 v[12:15], v44, s[20:23], s64 offen sc1\n\t"
    "v_mfma_f32_16x16x32_bf16 v[252:255], a[252:255], a[56:59], v[252:255]\n\t"
    "v_mfma_f32_16x16x32_bf16 v[252:255], a[188:191], a[60:63], v[252:255]\n\t"
    "buffer_load_dwordx4 v[16:19], v44, s[20:23], s64 offen offset:1024 sc1\n\t"
    "v_mfma_f32_16x16x32_bf16 v[220:223], a[188:191], a[28:31], v[220:223]\n\t"
    "ds_read_b64_tr_b16 a[158:159], v49 offset:42752\n\t"
    "v_mfma_f32_16x16x32_bf16 v[220:223], a[252:255], a[24:27], v[220:223]\n\t"
    "v_mfma_f32_16x16x32_bf16 v[188:191], a[252:255], a[104:107], v[188:191]\n\t"
    "v_mfma_f32_16x16x32_bf16 v[188:191], a[188:191], a[108:111], v[188:191]\n\t"
    "v_mfma_f32_16x16x32_bf16 v[156:159], a[188:191], a[76:79], v[156:159]\n\t"
    "v_mfma_f32_16x16x32_bf16 v[156:159], a[252:255], a[72:75], v[156:159]\n\t"
    "v_mfma_f32_16x16x32_bf16 v[124:127], a[252:255], a[40:43], v[124:127]\n\t"
    "v_mfma_f32_16x16x32_bf16 v[124:127], a[188:191], a[44:47], v[124:127]\n\t"
    "v_mfma_f32_16x16x32_bf16 v[92:95], a[188:191], a[12:15], v[92:95]\n\t"
    "v_mfma_f32_16x16x32_bf16 v[92:95], a[252:255], a[8:11], v[92:95]\n\t"
    "s_waitcnt vmcnt(0) lgkmcnt(0)\n\t"
    "s_barrier\n\t"
    "v_mfma_f32_16x16x32_bf16 v[64:67], a[192:195], a[80:83], v[64:67]\n\t"
    "s_bfe_u32 s62, s71, 0x30011\n\t"
    "s_bfe_u32 s75, s71, 0x10010\n\t"
    "s_lshl2_add_u32 s62, s62, s75\n\t"
    "v_mfma_f32_16x16x32_bf16 v[64:67], a[128:131], a[84:87], v[64:67]\n\t"
    "s_lshl_b32 s62, s62, 11\n\t"
    "s_add_u32 s62, s62, s25\n\t"
    "s_bfe_u32 s69, s71, 0x3001d\n\t"
    "v_mfma_f32_16x16x32_bf16 v[96:99], a[128:131], a[92:95], v[96:99]\n\t"
    "s_bfe_u32 s75, s71, 0x1001c\n\t"
    "s_lshl2_add_u32 s69, s69, s75\n\t"
    "s_lshl_b32 s69, s69, 11\n\t"
    "v_mfma_f32_16x16x32_bf16 v[96:99], a[192:195], a[88:91], v[96:99]\n\t"
    "ds_read_b64_tr_b16 a[224:225], v47 offset:0\n\t"
    "s_add_u32 s69, s69, s25\n\t"
    "s_lshl2_add_u32 s69, s69, s79\n\t"
    "s_lshl_b32 s69, s69, 4\n\t"
    "v_mfma_f32_16x16x32_bf16 v[128:131], a[192:195], a[112:115], v[128:131]\n\t"
    "ds_read_b64_tr_b16 a[160:161], v47 offset:8192\n\t"
    "s_add_u32 s70, s69, 0x8000\n\t"
    "s_bfe_u32 s64, s71, 0x3000d\n\t"
    "s_bfe_u32 s75, s71, 0x1000c\n\t"
    "v_mfma_f32_16x16x32_bf16 v[128:131], a[128:131], a[116:119], v[128:131]\n\t"
    "s_lshl2_add_u32 s64, s64, s75\n\t"
    "s_lshl_b32 s64, s64, 11\n\t"
    "s_add_u32 s64, s64, s25\n\t"
    "v_mfma_f32_16x16x32_bf16 v[160:163], a[128:131], a[124:127], v[160:163]\n\t"
    "ds_read_b64_tr_b16 a[226:227], v47 offset:1024\n\t"
    "v_mfma_f32_16x16x32_bf16 v[160:163], a[192:195], a[120:123], v[160:163]\n\t"
    "v_mfma_f32_16x16x32_bf16 v[192:195], a[192:195], v[4:7], v[192:195]\n\t"
    "ds_read_b64_tr_b16 a[162:163], v47 offset:9216\n\t"
    "v_mfma_f32_16x16x32_bf16 v[192:195], a[128:131], v[8:11], v[192:195]\n\t"
    "buffer_load_dwordx4 a[0:3], v31, s[20:23], s64 offen sc1\n\t"
    "v_mfma_f32_16x16x32_bf16 v[224:227], a[128:131], v[16:19], v[224:227]\n\t"
    "v_mfma_f32_16x16x32_bf16 v[224:227], a[192:195], v[12:15], v[224:227]\n\t"
    "ds_read_b64_tr_b16 a[228:229], v50 offset:0\n\t"
    "v_mfma_f32_16x16x32_bf16 v[228:231], a[196:199], v[12:15], v[228:231]\n\t"
    "buffer_load_dwordx4 a[4:7], v31, s[20:23], s64 offen offset:1024 sc1\n\t"
    "v_mfma_f32_16x16x32_bf16 v[228:231], a[132:135], v[16:19], v[228:231]\n\t"
    "ds_read_b64_tr_b16 a[164:165], v50 offset:8192\n\t"
    "v_mfma_f32_16x16x32_bf16 v[196:199], a[132:135], v[8:11], v[196:199]\n\t"
    "buffer_load_dwordx4 a[32:35], v40, s[20:23], s64 offen sc1\n\t"
    "v_mfma_f32_16x16x32_bf16 v[196:199], a[196:199], v[4:7], v[196:199]\n\t"
    "v_mfma_f32_16x16x32_bf16 v[164:167], a[196:199], a[120:123], v[164:167]\n\t"
    "ds_read_b64_tr_b16 a[230:231], v50 offset:1024\n\t"
    "v_mfma_f32_16x16x32_bf16 v[164:167], a[132:135], a[124:127], v[164:167]\n\t"
    "buffer_load_dwordx4 a[36:39], v40, s[20:23], s64 offen offset:1024 sc1\n\t"
    "v_mfma_f32_16x16x32_bf16 v[132:135], a[132:135], a[116:119], v[132:135]\n\t"
    "v_mfma_f32_16x16x32_bf16 v[132:135], a[196:199], a[112:115], v[132:135]\n\t"
    "ds_read_b64_tr_b16 a[166:167], v50 offset:9216\n\t"
    "v_mfma_f32_16x16x32_bf16 v[100:103], a[196:199], a[88:91], v[100:103]\n\t"
    "buffer_load_dwordx4 a[64:67], v41, s[20:23], s64 offen sc1\n\t"
    "v_mfma_f32_16x16x32_bf16 v[100:103], a[132:135], a[92:95], v[100:103]\n\t"
    "v_mfma_f32_16x16x32_bf16 v[68:71], a[132:135], a[84:87], v[68:71]\n\t"
    "buffer_load_dwordx4 a[68:71], v41, s[20:23], s64 offen offset:1024 sc1\n\t"
    "v_mfma_f32_16x16x32_bf16 v[68:71], a[196:199], a[80:83], v[68:71]\n\t"
    "ds_read_b64_tr_b16 a[232:233], v47 offset:256\n\t"
    "v_mfma_f32_16x16x32_bf16 v[72:75], a[200:203], a[80:83], v[72:75]\n\t"
    "ds_read_b64_tr_b16 a[168:169], v47 offset:8448\n\t"
    "v_mfma_f32_16x16x32_bf16 v[72:75], a[136:139], a[84:87], v[72:75]\n\t"
    "buffer_load_dwordx4 a[96:99], v42, s[20:23], s64 offen sc1\n\t"
    "v_mfma_f32_16x16x32_bf16 v[104:107], a[136:139], a[92:95], v[104:107]\n\t"
    "v_mfma_f32_16x16x32_bf16 v[104:107], a[200:203], a[88:91], v[104:107]\n\t"
    "ds_read_b64_tr_b16 a[234:235], v47 offset:1280\n\t"
    "v_mfma_f32_16x16x32_bf16 v[136:139], a[200:203], a[112:115], v[136:139]\n\t"
    "buffer_load_dwordx4 a[100:103], v42, s[20:23], s64 offen offset:1024 sc1\n\t"
    "v_mfma_f32_16x16x32_bf16 v[136:139], a[136:139], a[116:119], v[136:139]\n\t"
    "v_mfma_f32_16x16x32_bf16 v[168:171], a[136:139], a[124:127], v[168:171]\n\t"
    "ds_read_b64_tr_b16 a[170:171], v47 offset:9472\n\t"
    "v_mfma_f32_16x16x32_bf16 v[168:171], a[200:203], a[120:123], v[168:171]\n\t"
    "buffer_load_dwordx4 a[16:19], v43, s[20:23], s64 offen sc1\n\t"
    "v_mfma_f32_16x16x32_bf16 v[200:203], a[200:203], v[4:7], v[200:203]\n\t"
    "v_mfma_f32_16x16x32_bf16 v[200:203], a[136:139], v[8:11], v[200:203]\n\t"
    "buffer_load_dwordx4 a[20:23], v43, s[20:23], s64 offen offset:1024 sc1\n\t"
    "v_mfma_f32_16x16x32_bf16 v[232:235], a[136:139], v[16:19], v[232:235]\n\t"
    "ds_read_b64_tr_b16 a[236:237], v50 offset:256\n\t"
    "v_mfma_f32_16x16x32_bf16 v[232:235], a[200:203], v[12:15], v[232:235]\n\t"
    "ds_read_b64_tr_b16 a[172:173], v50 offset:8448\n\t"
    "v_mfma_f32_16x16x32_bf16 v[236:239], a[204:207], v[12:15], v[236:239]\n\t"
    "buffer_load_dwordx4 a[48:51], v44, s[20:23], s64 offen sc1\n\t"
    "v_mfma_f32_16x16x32_bf16 v[236:239], a[140:143], v[16:19], v[236:239]\n\t"
    "v_mfma_f32_16x16x32_bf16 v[204:207], a[140:143], v[8:11], v[204:207]\n\t"
    "ds_read_b64_tr_b16 a[238:239], v50 offset:1280\n\t"
    "v_mfma_f32_16x16x32_bf16 v[204:207], a[204:207], v[4:7], v[204:207]\n\t"
    "buffer_load_dwordx4 a[52:55], v44, s[20:23], s64 offen offset:1024 sc1\n\t"
    "v_mfma_f32_16x16x32_bf16 v[172:175], a[204:207], a[120:123], v[172:175]\n\t"
    "v_mfma_f32_16x16x32_bf16 v[172:175], a[140:143], a[124:127], v[172:175]\n\t"
    "buffer_load_dwordx4 a[8:11], v31, s[20:23], s62 offen sc1\n\t"
    "v_mfma_f32_16x16x32_bf16 v[140:143], a[140:143], a[116:119], v[140:143]\n\t"
    "ds_read_b64_tr_b16 a[174:175], v50 offset:9472\n\t"
    "v_mfma_f32_16x16x32_bf16 v[140:143], a[204:207], a[112:115], v[140:143]\n\t"
    "s_add_u32 m0, s14, 0x18000\n\t"
    "v_mfma_f32_16x16x32_bf16 v[108:111], a[204:207], a[88:91], v[108:111]\n\t"
    "buffer_load_dwordx4 v29, s[16:19], s69 offen lds\n\t"
    "v_mfma_f32_16x16x32_bf16 v[108:111], a[140:143], a[92:95], v[108:111]\n\t"
    "ds_read_b64_tr_b16 a[240:241], v47 offset:512\n\t"
    "v_mfma_f32_16x16x32_bf16 v[76:79], a[140:143], a[84:87], v[76:79]\n\t"
    "ds_read_b64_tr_b16 a[176:177], v47 offset:8704\n\t"
    "v_mfma_f32_16x16x32_bf16 v[76:79], a[204:207], a[80:83], v[76:79]\n\t"
    "buffer_load_dwordx4 a[12:15], v31, s[20:23], s62 offen offset:1024 sc1\n\t"
    "v_mfma_f32_16x16x32_bf16 v[80:83], a[208:211], a[80:83], v[80:83]\n\t"
    "v_mfma_f32_16x16x32_bf16 v[80:83], a[144:147], a[84:87], v[80:83]\n\t"
    "ds_read_b64_tr_b16 a[242:243], v47 offset:1536\n\t"
    "s_add_u32 m0, s14, 0x18400\n\t"
    "v_mfma_f32_16x16x32_bf16 v[112:115], a[144:147], a[92:95], v[112:115]\n\t"
    "buffer_load_dwordx4 v29, s[16:19], s70 offen lds\n\t"
    "v_mfma_f32_16x16x32_bf16 v[112:115], a[208:211], a[88:91], v[112:115]\n\t"
    "v_mfma_f32_16x16x32_bf16 v[144:147], a[208:211], a[112:115], v[144:147]\n\t"
    "buffer_load_dwordx4 a[40:43], v40, s[20:23], s62 offen sc1\n\t"
    "v_mfma_f32_16x16x32_bf16 v[144:147], a[144:147], a[116:119], v[144:147]\n\t"
    "ds_read_b64_tr_b16 a[178:179], v47 offset:9728\n\t"
    "v_mfma_f32_16x16x32_bf16 v[176:179], a[144:147], a[124:127], v[176:179]\n\t"
    "v_mfma_f32_16x16x32_bf16 v[176:179], a[208:211], a[120:123], v[176:179]\n\t"
    "buffer_load_dwordx4 a[44:47], v40, s[20:23], s62 offen offset:1024 sc1\n\t"
    "v_mfma_f32_16x16x32_bf16 v[208:211], a[208:211], v[4:7], v[208:211]\n\t"
    "ds_read_b64_tr_b16 a[244:245], v50 offset:512\n\t"
    "v_mfma_f32_16x16x32_bf16 v[208:211], a[144:147], v[8:11], v[208:211]\n\t"
    "ds_read_b64_tr_b16 a[180:181], v50 offset:8704\n\t"
    "s_add_u32 m0, s14, 0x18800\n\t"
    "v_mfma_f32_16x16x32_bf16 v[240:243], a[144:147], v[16:19], v[240:243]\n\t"
    "buffer_load_dwordx4 v26, s[16:19], s69 offen lds\n\t"
    "v_mfma_f32_16x16x32_bf16 v[240:243], a[208:211], v[12:15], v[240:243]\n\t"
    "v_mfma_f32_16x16x32_bf16 v[244:247], a[212:215], v[12:15], v[244:247]\n\t"
    "ds_read_b64_tr_b16 a[246:247], v50 offset:1536\n\t"
    "v_mfma_f32_16x16x32_bf16 v[244:247], a[148:151], v[16:19], v[244:247]\n\t"
    "buffer_load_dwordx4 a[72:75], v41, s[20:23], s62 offen sc1\n\t"
    "v_mfma_f32_16x16x32_bf16 v[212:215], a[148:151], v[8:11], v[212:215]\n\t"
    "s_add_u32 m0, s14, 0x18c00\n\t"
    "v_mfma_f32_16x16x32_bf16 v[212:215], a[212:215], v[4:7], v[212:215]\n\t"
    "buffer_load_dwordx4 v26, s[16:19], s70 offen lds\n\t"
    "v_mfma_f32_16x16x32_bf16 v[180:183], a[212:215], a[120:123], v[180:183]\n\t"
    "ds_read_b64_tr_b16 a[182:183], v50 offset:9728\n\t"
    "v_mfma_f32_16x16x32_bf16 v[180:183], a[148:151], a[124:127], v[180:183]\n\t"
    "v_mfma_f32_16x16x32_bf16 v[148:151], a[148:151], a[116:119], v[148:151]\n\t"
    "buffer_load_dwordx4 a[76:79], v41, s[20:23], s62 offen offset:1024 sc1\n\t"
    "v_mfma_f32_16x16x32_bf16 v[148:151], a[212:215], a[112:115], v[148:151]\n\t"
    "ds_read_b64_tr_b16 a[248:249], v47 offset:768\n\t"
    "v_mfma_f32_16x16x32_bf16 v[116:119], a[212:215], a[88:91], v[116:119]\n\t"
    "ds_read_b64_tr_b16 a[184:185], v47 offset:8960\n\t"
    "v_mfma_f32_16x16x32_bf16 v[116:119], a[148:151], a[92:95], v[116:119]\n\t"
    "buffer_load_dwordx4 a[104:107], v42, s[20:23], s62 offen sc1\n\t"
    "v_mfma_f32_16x16x32_bf16 v[84:87], a[148:151], a[84:87], v[84:87]\n\t"
    "s_add_u32 m0, s14, 0x19000\n\t"
    "v_mfma_f32_16x16x32_bf16 v[84:87], a[212:215], a[80:83], v[84:87]\n\t"
    "buffer_load_dwordx4 v27, s[16:19], s69 offen lds\n\t"
    "v_mfma_f32_16x16x32_bf16 v[88:91], a[216:219], a[80:83], v[88:91]\n\t"
    "ds_read_b64_tr_b16 a[250:251], v47 offset:1792\n\t"
    "v_mfma_f32_16x16x32_bf16 v[88:91], a[152:155], a[84:87], v[88:91]\n\t"
    "v_mfma_f32_16x16x32_bf16 v[120:123], a[152:155], a[92:95], v[120:123]\n\t"
    "buffer_load_dwordx4 a[108:111], v42, s[20:23], s62 offen offset:1024 sc1\n\t"
    "v_mfma_f32_16x16x32_bf16 v[120:123], a[216:219], a[88:91], v[120:123]\n\t"
    "ds_read_b64_tr_b16 a[186:187], v47 offset:9984\n\t"
    "v_mfma_f32_16x16x32_bf16 v[152:155], a[216:219], a[112:115], v[152:155]\n\t"
    "s_add_u32 m0, s14, 0x19400\n\t"
    "v_mfma_f32_16x16x32_bf16 v[152:155], a[152:155], a[116:119], v[152:155]\n\t"
    "buffer_load_dwordx4 v27, s[16:19], s70 offen lds\n\t"
    "v_mfma_f32_16x16x32_bf16 v[184:187], a[152:155], a[124:127], v[184:187]\n\t"
    "ds_read_b64_tr_b16 a[252:253], v50 offset:768\n\t"
    "v_mfma_f32_16x16x32_bf16 v[184:187], a[216:219], a[120:123], v[184:187]\n\t"
    "ds_read_b64_tr_b16 a[188:189], v50 offset:8960\n\t"
    "v_mfma_f32_16x16x32_bf16 v[216:219], a[216:219], v[4:7], v[216:219]\n\t"
    "buffer_load_dwordx4 a[24:27], v43, s[20:23], s62 offen sc1\n\t"
    "v_mfma_f32_16x16x32_bf16 v[216:219], a[152:155], v[8:11], v[216:219]\n\t"
    "v_mfma_f32_16x16x32_bf16 v[248:251], a[152:155], v[16:19], v[248:251]\n\t"
    "buffer_load_dwordx4 a[28:31], v43, s[20:23], s62 offen offset:1024 sc1\n\t"
    "v_mfma_f32_16x16x32_bf16 v[248:251], a[216:219], v[12:15], v[248:251]\n\t"
    "ds_read_b64_tr_b16 a[254:255], v50 offset:1792\n\t"
    "v_mfma_f32_16x16x32_bf16 v[252:255], a[220:223], v[12:15], v[252:255]\n\t"
    "s_add_u32 m0, s14, 0x19800\n\t"
    "v_mfma_f32_16x16x32_bf16 v[252:255], a[156:159], v[16:19], v[252:255]\n\t"
    "buffer_load_dwordx4 v28, s[16:19], s69 offen lds\n\t"
    "v_mfma_f32_16x16x32_bf16 v[220:223], a[156:159], v[8:11], v[220:223]\n\t"
    "ds_read_b64_tr_b16 a[190:191], v50 offset:9984\n\t"
    "v_mfma_f32_16x16x32_bf16 v[220:223], a[220:223], v[4:7], v[220:223]\n\t"
    "v_mfma_f32_16x16x32_bf16 v[188:191], a[220:223], a[120:123], v[188:191]\n\t"
    "buffer_load_dwordx4 a[56:59], v44, s[20:23], s62 offen sc1\n\t"
    "v_mfma_f32_16x16x32_bf16 v[188:191], a[156:159], a[124:127], v[188:191]\n\t"
    "s_add_u32 m0, s14, 0x19c00\n\t"
    "v_mfma_f32_16x16x32_bf16 v[156:159], a[156:159], a[116:119], v[156:159]\n\t"
    "buffer_load_dwordx4 v28, s[16:19], s70 offen lds\n\t"
    "v_mfma_f32_16x16x32_bf16 v[156:159], a[220:223], a[112:115], v[156:159]\n\t"
    "v_mfma_f32_16x16x32_bf16 v[124:127], a[220:223], a[88:91], v[124:127]\n\t"
    "v_mfma_f32_16x16x32_bf16 v[124:127], a[156:159], a[92:95], v[124:127]\n\t"
    "buffer_load_dwordx4 a[60:63], v44, s[20:23], s62 offen offset:1024 sc1\n\t"
    "v_mfma_f32_16x16x32_bf16 v[92:95], a[156:159], a[84:87], v[92:95]\n\t"
    "v_mfma_f32_16x16x32_bf16 v[92:95], a[220:223], a[80:83], v[92:95]\n\t"
    "s_waitcnt vmcnt(20) lgkmcnt(0)\n\t"
    "s_barrier\n\t"
    "v_mfma_f32_16x16x32_bf16 v[64:67], a[224:227], a[0:3], v[64:67]\n\t"
    "s_bfe_u32 s62, s71, 0x30015\n\t"
    "s_bfe_u32 s75, s71, 0x10014\n\t"
    "s_lshl2_add_u32 s62, s62, s75\n\t"
    "v_mfma_f32_16x16x32_bf16 v[64:67], a[160:163], a[4:7], v[64:67]\n\t"
    "s_lshl_b32 s62, s62, 11\n\t"
    "s_add_u32 s62, s62, s25\n\t"
    "s_bfe_u32 s60, s72, 0x30001\n\t"
    "v_mfma_f32_16x16x32_bf16 v[96:99], a[160:163], a[36:39], v[96:99]\n\t"
    "s_bfe_u32 s75, s72, 0x10000\n\t"
    "s_lshl2_add_u32 s60, s60, s75\n\t"
    "s_lshl_b32 s60, s60, 11\n\t"
    "v_mfma_f32_16x16x32_bf16 v[96:99], a[224:227], a[32:35], v[96:99]\n\t"
    "ds_read_b64_tr_b16 a[192:193], v45 offset:0\n\t"
    "s_add_u32 s60, s60, s25\n\t"
    "s_lshl2_add_u32 s60, s60, s79\n\t"
    "s_lshl_b32 s60, s60, 4\n\t"
    "v_mfma_f32_16x16x32_bf16 v[128:131], a[224:227], a[64:67], v[128:131]\n\t"
    "s_add_u32 s61, s60, 0x8000\n\t"
    "v_mfma_f32_16x16x32_bf16 v[128:131], a[160:163], a[68:71], v[128:131]\n\t"
    "ds_read_b64_tr_b16 a[128:129], v45 offset:8192\n\t"
    "v_mfma_f32_16x16x32_bf16 v[160:163], a[160:163], a[100:103], v[160:163]\n\t"
    "v_mfma_f32_16x16x32_bf16 v[160:163], a[224:227], a[96:99], v[160:163]\n\t"
    "ds_read_b64_tr_b16 a[194:195], v45 offset:1024\n\t"
    "v_mfma_f32_16x16x32_bf16 v[192:195], a[224:227], a[16:19], v[192:195]\n\t"
    "v_mfma_f32_16x16x32_bf16 v[192:195], a[160:163], a[20:23], v[192:195]\n\t"
    "ds_read_b64_tr_b16 a[130:131], v45 offset:9216\n\t"
    "v_mfma_f32_16x16x32_bf16 v[224:227], a[160:163], a[52:55], v[224:227]\n\t"
    "buffer_load_dwordx4 a[80:83], v31, s[20:23], s62 offen sc1\n\t"
    "v_mfma_f32_16x16x32_bf16 v[224:227], a[224:227], a[48:51], v[224:227]\n\t"
    "v_mfma_f32_16x16x32_bf16 v[228:231], a[228:231], a[48:51], v[228:231]\n\t"
    "ds_read_b64_tr_b16 a[196:197], v48 offset:0\n\t"
    "v_mfma_f32_16x16x32_bf16 v[228:231], a[164:167], a[52:55], v[228:231]\n\t"
    "s_add_u32 m0, s14, 0x20000\n\t"
    "v_mfma_f32_16x16x32_bf16 v[196:199], a[164:167], a[20:23], v[196:199]\n\t"
    "buffer_load_dwordx4 v29, s[16:19], s60 offen lds\n\t"
    "v_mfma_f32_16x16x32_bf16 v[196:199], a[228:231], a[16:19], v[196:199]\n\t"
    "ds_read_b64_tr_b16 a[132:133], v48 offset:8192\n\t"
    "v_mfma_f32_16x16x32_bf16 v[164:167], a[228:231], a[96:99], v[164:167]\n\t"
    "v_mfma_f32_16x16x32_bf16 v[164:167], a[164:167], a[100:103], v[164:167]\n\t"
    "ds_read_b64_tr_b16 a[198:199], v48 offset:1024\n\t"
    "v_mfma_f32_16x16x32_bf16 v[132:135], a[164:167], a[68:71], v[132:135]\n\t"
    "buffer_load_dwordx4 a[84:87], v31, s[20:23], s62 offen offset:1024 sc1\n\t"
    "v_mfma_f32_16x16x32_bf16 v[132:135], a[228:231], a[64:67], v[132:135]\n\t"
    "v_mfma_f32_16x16x32_bf16 v[100:103], a[228:231], a[32:35], v[100:103]\n\t"
    "ds_read_b64_tr_b16 a[134:135], v48 offset:9216\n\t"
    "v_mfma_f32_16x16x32_bf16 v[100:103], a[164:167], a[36:39], v[100:103]\n\t"
    "v_mfma_f32_16x16x32_bf16 v[68:71], a[164:167], a[4:7], v[68:71]\n\t"
    "ds_read_b64_tr_b16 a[200:201], v45 offset:256\n\t"
    "s_add_u32 m0, s14, 0x20400\n\t"
    "v_mfma_f32_16x16x32_bf16 v[68:71], a[228:231], a[0:3], v[68:71]\n\t"
    "buffer_load_dwordx4 v29, s[16:19], s61 offen lds\n\t"
    "v_mfma_f32_16x16x32_bf16 v[72:75], a[232:235], a[0:3], v[72:75]\n\t"
    "v_mfma_f32_16x16x32_bf16 v[72:75], a[168:171], a[4:7], v[72:75]\n\t"
    "ds_read_b64_tr_b16 a[136:137], v45 offset:8448\n\t"
    "v_mfma_f32_16x16x32_bf16 v[104:107], a[168:171], a[36:39], v[104:107]\n\t"
    "v_mfma_f32_16x16x32_bf16 v[104:107], a[232:235], a[32:35], v[104:107]\n\t"
    "buffer_load_dwordx4 a[88:91], v40, s[20:23], s62 offen sc1\n\t"
    "v_mfma_f32_16x16x32_bf16 v[136:139], a[232:235], a[64:67], v[136:139]\n\t"
    "ds_read_b64_tr_b16 a[202:203], v45 offset:1280\n\t"
    "v_mfma_f32_16x16x32_bf16 v[136:139], a[168:171], a[68:71], v[136:139]\n\t"
    "v_mfma_f32_16x16x32_bf16 v[168:171], a[168:171], a[100:103], v[168:171]\n\t"
    "v_mfma_f32_16x16x32_bf16 v[168:171], a[232:235], a[96:99], v[168:171]\n\t"
    "buffer_load_dwordx4 a[92:95], v40, s[20:23], s62 offen offset:1024 sc1\n\t"
    "v_mfma_f32_16x16x32_bf16 v[200:203], a[232:235], a[16:19], v[200:203]\n\t"
    "ds_read_b64_tr_b16 a[138:139], v45 offset:9472\n\t"
    "v_mfma_f32_16x16x32_bf16 v[200:203], a[168:171], a[20:23], v[200:203]\n\t"
    "v_mfma_f32_16x16x32_bf16 v[232:235], a[168:171], a[52:55], v[232:235]\n\t"
    "ds_read_b64_tr_b16 a[204:205], v48 offset:256\n\t"
    "v_mfma_f32_16x16x32_bf16 v[232:235], a[232:235], a[48:51], v[232:235]\n\t"
    "s_add_u32 m0, s14, 0x20800\n\t"
    "v_mfma_f32_16x16x32_bf16 v[236:239], a[236:239], a[48:51], v[236:239]\n\t"
    "buffer_load_dwordx4 v26, s[16:19], s60 offen lds\n\t"
    "v_mfma_f32_16x16x32_bf16 v[236:239], a[172:175], a[52:55], v[236:239]\n\t"
    "ds_read_b64_tr_b16 a[140:141], v48 offset:8448\n\t"
    "v_mfma_f32_16x16x32_bf16 v[204:207], a[172:175], a[20:23], v[204:207]\n\t"
    "v_mfma_f32_16x16x32_bf16 v[204:207], a[236:239], a[16:19], v[204:207]\n\t"
    "ds_read_b64_tr_b16 a[206:207], v48 offset:1280\n\t"
    "v_mfma_f32_16x16x32_bf16 v[172:175], a[236:239], a[96:99], v[172:175]\n\t"
    "buffer_load_dwordx4 a[112:115], v41, s[20:23], s62 offen sc1\n\t"
    "v_mfma_f32_16x16x32_bf16 v[172:175], a[172:175], a[100:103], v[172:175]\n\t"
    "v_mfma_f32_16x16x32_bf16 v[140:143], a[172:175], a[68:71], v[140:143]\n\t"
    "ds_read_b64_tr_b16 a[142:143], v48 offset:9472\n\t"
    "v_mfma_f32_16x16x32_bf16 v[140:143], a[236:239], a[64:67], v[140:143]\n\t"
    "s_add_u32 m0, s14, 0x20c00\n\t"
    "v_mfma_f32_16x16x32_bf16 v[108:111], a[236:239], a[32:35], v[108:111]\n\t"
    "buffer_load_dwordx4 v26, s[16:19], s61 offen lds\n\t"
    "v_mfma_f32_16x16x32_bf16 v[108:111], a[172:175], a[36:39], v[108:111]\n\t"
    "ds_read_b64_tr_b16 a[208:209], v45 offset:512\n\t"
    "v_mfma_f32_16x16x32_bf16 v[76:79], a[172:175], a[4:7], v[76:79]\n\t"
    "v_mfma_f32_16x16x32_bf16 v[76:79], a[236:239], a[0:3], v[76:79]\n\t"
    "ds_read_b64_tr_b16 a[144:145], v45 offset:8704\n\t"
    "v_mfma_f32_16x16x32_bf16 v[80:83], a[240:243], a[0:3], v[80:83]\n\t"
    "v_mfma_f32_16x16x32_bf16 v[80:83], a[176:179], a[4:7], v[80:83]\n\t"
    "buffer_load_dwordx4 a[116:119], v41, s[20:23], s62 offen offset:1024 sc1\n\t"
    "v_mfma_f32_16x16x32_bf16 v[112:115], a[176:179], a[36:39], v[112:115]\n\t"
    "ds_read_b64_tr_b16 a[210:211], v45 offset:1536\n\t"
    "v_mfma_f32_16x16x32_bf16 v[112:115], a[240:243], a[32:35], v[112:115]\n\t"
    "v_mfma_f32_16x16x32_bf16 v[144:147], a[240:243], a[64:67], v[144:147]\n\t"
    "ds_read_b64_tr_b16 a[146:147], v45 offset:9728\n\t"
    "v_mfma_f32_16x16x32_bf16 v[144:147], a[176:179], a[68:71], v[144:147]\n\t"
    "buffer_load_dwordx4 a[120:123], v42, s[20:23], s62 offen sc1\n\t"
    "v_mfma_f32_16x16x32_bf16 v[176:179], a[176:179], a[100:103], v[176:179]\n\t"
    "v_mfma_f32_16x16x32_bf16 v[176:179], a[240:243], a[96:99], v[176:179]\n\t"
    "ds_read_b64_tr_b16 a[212:213], v48 offset:512\n\t"
    "v_mfma_f32_16x16x32_bf16 v[208:211], a[240:243], a[16:19], v[208:211]\n\t"
    "v_mfma_f32_16x16x32_bf16 v[208:211], a[176:179], a[20:23], v[208:211]\n\t"
    "ds_read_b64_tr_b16 a[148:149], v48 offset:8704\n\t"
    "s_add_u32 m0, s14, 0x21000\n\t"
    "v_mfma_f32_16x16x32_bf16 v[240:243], a[176:179], a[52:55], v[240:243]\n\t"
    "buffer_load_dwordx4 v27, s[16:19], s60 offen lds\n\t"
    "v_mfma_f32_16x16x32_bf16 v[240:243], a[240:243], a[48:51], v[240:243]\n\t"
    "v_mfma_f32_16x16x32_bf16 v[244:247], a[244:247], a[48:51], v[244:247]\n\t"
    "v_mfma_f32_16x16x32_bf16 v[244:247], a[180:183], a[52:55], v[244:247]\n\t"
    "ds_read_b64_tr_b16 a[214:215], v48 offset:1536\n\t"
    "v_mfma_f32_16x16x32_bf16 v[212:215], a[180:183], a[20:23], v[212:215]\n\t"
    "buffer_load_dwordx4 a[124:127], v42, s[20:23], s62 offen offset:1024 sc1\n\t"
    "v_mfma_f32_16x16x32_bf16 v[212:215], a[244:247], a[16:19], v[212:215]\n\t"
    "v_mfma_f32_16x16x32_bf16 v[180:183], a[244:247], a[96:99], v[180:183]\n\t"
    "ds_read_b64_tr_b16 a[150:151], v48 offset:9728\n\t"
    "v_mfma_f32_16x16x32_bf16 v[180:183], a[180:183], a[100:103], v[180:183]\n\t"
    "s_add_u32 m0, s14, 0x21400\n\t"
    "v_mfma_f32_16x16x32_bf16 v[148:151], a[180:183], a[68:71], v[148:151]\n\t"
    "buffer_load_dwordx4 v27, s[16:19], s61 offen lds\n\t"
    "v_mfma_f32_16x16x32_bf16 v[148:151], a[244:247], a[64:67], v[148:151]\n\t"
    "ds_read_b64_tr_b16 a[216:217], v45 offset:768\n\t"
    "v_mfma_f32_16x16x32_bf16 v[116:119], a[244:247], a[32:35], v[116:119]\n\t"
    "v_mfma_f32_16x16x32_bf16 v[116:119], a[180:183], a[36:39], v[116:119]\n\t"
    "ds_read_b64_tr_b16 a[152:153], v45 offset:8960\n\t"
    "v_mfma_f32_16x16x32_bf16 v[84:87], a[180:183], a[4:7], v[84:87]\n\t"
    "v_mfma_f32_16x16x32_bf16 v[84:87], a[244:247], a[0:3], v[84:87]\n\t"
    "buffer_load_dwordx4 v[4:7], v43, s[20:23], s62 offen sc1\n\t"
    "v_mfma_f32_16x16x32_bf16 v[88:91], a[248:251], a[0:3], v[88:91]\n\t"
    "ds_read_b64_tr_b16 a[218:219], v45 offset:1792\n\t"
    "v_mfma_f32_16x16x32_bf16 v[88:91], a[184:187], a[4:7], v[88:91]\n\t"
    "v_mfma_f32_16x16x32_bf16 v[120:123], a[184:187], a[36:39], v[120:123]\n\t"
    "ds_read_b64_tr_b16 a[154:155], v45 offset:9984\n\t"
    "v_mfma_f32_16x16x32_bf16 v[120:123], a[248:251], a[32:35], v[120:123]\n\t"
    "buffer_load_dwordx4 v[8:11], v43, s[20:23], s62 offen offset:1024 sc1\n\t"
    "v_mfma_f32_16x16x32_bf16 v[152:155], a[248:251], a[64:67], v[152:155]\n\t"
    "v_mfma_f32_16x16x32_bf16 v[152:155], a[184:187], a[68:71], v[152:155]\n\t"
    "ds_read_b64_tr_b16 a[220:221], v48 offset:768\n\t"
    "v_mfma_f32_16x16x32_bf16 v[184:187], a[184:187], a[100:103], v[184:187]\n\t"
    "s_add_u32 m0, s14, 0x21800\n\t"
    "v_mfma_f32_16x16x32_bf16 v[184:187], a[248:251], a[96:99], v[184:187]\n\t"
    "buffer_load_dwordx4 v28, s[16:19], s60 offen lds\n\t"
    "v_mfma_f32_16x16x32_bf16 v[216:219], a[248:251], a[16:19], v[216:219]\n\t"
    "ds_read_b64_tr_b16 a[156:157], v48 offset:8960\n\t"
    "v_mfma_f32_16x16x32_bf16 v[216:219], a[184:187], a[20:23], v[216:219]\n\t"
    "v_mfma_f32_16x16x32_bf16 v[248:251], a[184:187], a[52:55], v[248:251]\n\t"
    "ds_read_b64_tr_b16 a[222:223], v48 offset:1792\n\t"
    "v_mfma_f32_16x16x32_bf16 v[248:251], a[248:251], a[48:51], v[248:251]\n\t"
    "v_mfma_f32_16x16x32_bf16 v[252:255], a[252:255], a[48:51], v[252:255]\n\t"
    "buffer_load_dwordx4 v[12:15], v44, s[20:23], s62 offen sc1\n\t"
    "v_mfma_f32_16x16x32_bf16 v[252:255], a[188:191], a[52:55], v[252:255]\n\t"
    "ds_read_b64_tr_b16 a[158:159], v48 offset:9984\n\t"
    "v_mfma_f32_16x16x32_bf16 v[220:223], a[188:191], a[20:23], v[220:223]\n\t"
    "v_mfma_f32_16x16x32_bf16 v[220:223], a[252:255], a[16:19], v[220:223]\n\t"
    "s_add_u32 m0, s14, 0x21c00\n\t"
    "v_mfma_f32_16x16x32_bf16 v[188:191], a[252:255], a[96:99], v[188:191]\n\t"
    "buffer_load_dwordx4 v28, s[16:19], s61 offen lds\n\t"
    "v_mfma_f32_16x16x32_bf16 v[188:191], a[188:191], a[100:103], v[188:191]\n\t"
    "v_mfma_f32_16x16x32_bf16 v[156:159], a[188:191], a[68:71], v[156:159]\n\t"
    "v_mfma_f32_16x16x32_bf16 v[156:159], a[252:255], a[64:67], v[156:159]\n\t"
    "v_mfma_f32_16x16x32_bf16 v[124:127], a[252:255], a[32:35], v[124:127]\n\t"
    "buffer_load_dwordx4 v[16:19], v44, s[20:23], s62 offen offset:1024 sc1\n\t"
    "v_mfma_f32_16x16x32_bf16 v[124:127], a[188:191], a[36:39], v[124:127]\n\t"
    "v_mfma_f32_16x16x32_bf16 v[92:95], a[188:191], a[4:7], v[92:95]\n\t"
    "v_mfma_f32_16x16x32_bf16 v[92:95], a[252:255], a[0:3], v[92:95]\n\t"
    "s_waitcnt vmcnt(20) lgkmcnt(0)\n\t"
    "s_barrier\n\t"
    "v_mfma_f32_16x16x32_bf16 v[64:67], a[192:195], a[8:11], v[64:67]\n\t"
    "s_bfe_u32 s62, s71, 0x30019\n\t"
    "s_bfe_u32 s75, s71, 0x10018\n\t"
    "s_lshl2_add_u32 s62, s62, s75\n\t"
    "v_mfma_f32_16x16x32_bf16 v[64:67], a[128:131], a[12:15], v[64:67]\n\t"
    "s_lshl_b32 s62, s62, 11\n\t"
    "s_add_u32 s62, s62, s25\n\t"
    "s_bfe_u32 s69, s72, 0x30005\n\t"
    "v_mfma_f32_16x16x32_bf16 v[96:99], a[128:131], a[44:47], v[96:99]\n\t"
    "s_bfe_u32 s75, s72, 0x10004\n\t"
    "s_lshl2_add_u32 s69, s69, s75\n\t"
    "s_lshl_b32 s69, s69, 11\n\t"
    "v_mfma_f32_16x16x32_bf16 v[96:99], a[192:195], a[40:43], v[96:99]\n\t"
    "ds_read_b64_tr_b16 a[224:225], v45 offset:32768\n\t"
    "s_add_u32 s69, s69, s25\n\t"
    "s_lshl2_add_u32 s69, s69, s79\n\t"
    "s_lshl_b32 s69, s69, 4\n\t"
    "v_mfma_f32_16x16x32_bf16 v[128:131], a[192:195], a[72:75], v[128:131]\n\t"
    "s_add_u32 s70, s69, 0x8000\n\t"
    "v_mfma_f32_16x16x32_bf16 v[128:131], a[128:131], a[76:79], v[128:131]\n\t"
    "ds_read_b64_tr_b16 a[160:161], v45 offset:40960\n\t"
    "v_mfma_f32_16x16x32_bf16 v[160:163], a[128:131], a[108:111], v[160:163]\n\t"
    "v_mfma_f32_16x16x32_bf16 v[160:163], a[192:195], a[104:107], v[160:163]\n\t"
    "ds_read_b64_tr_b16 a[226:227], v45 offset:33792\n\t"
    "v_mfma_f32_16x16x32_bf16 v[192:195], a[192:195], a[24:27], v[192:195]\n\t"
    "v_mfma_f32_16x16x32_bf16 v[192:195], a[128:131], a[28:31], v[192:195]\n\t"
    "ds_read_b64_tr_b16 a[162:163], v45 offset:41984\n\t"
    "v_mfma_f32_16x16x32_bf16 v[224:227], a[128:131], a[60:63], v[224:227]\n\t"
    "global_load_dwordx2 v[60:61], v58, s[26:27] sc1\n\t"
    "v_mfma_f32_16x16x32_bf16 v[224:227], a[192:195], a[56:59], v[224:227]\n\t"
    "v_mfma_f32_16x16x32_bf16 v[228:231], a[196:199], a[56:59], v[228:231]\n\t"
    "ds_read_b64_tr_b16 a[228:229], v48 offset:32768\n\t"
    "v_mfma_f32_16x16x32_bf16 v[228:231], a[132:135], a[60:63], v[228:231]\n\t"
    "v_mfma_f32_16x16x32_bf16 v[196:199], a[132:135], a[28:31], v[196:199]\n\t"
    "buffer_load_dwordx4 a[0:3], v31, s[20:23], s62 offen sc1\n\t"
    "v_mfma_f32_16x16x32_bf16 v[196:199], a[196:199], a[24:27], v[196:199]\n\t"
    "ds_read_b64_tr_b16 a[164:165], v48 offset:40960\n\t"
    "v_mfma_f32_16x16x32_bf16 v[164:167], a[196:199], a[104:107], v[164:167]\n\t"
    "v_mfma_f32_16x16x32_bf16 v[164:167], a[132:135], a[108:111], v[164:167]\n\t"
    "ds_read_b64_tr_b16 a[230:231], v48 offset:33792\n\t"
    "s_add_u32 m0, s14, 0\n\t"
    "v_mfma_f32_16x16x32_bf16 v[132:135], a[132:135], a[76:79], v[132:135]\n\t"
    "buffer_load_dwordx4 v29, s[16:19], s69 offen lds\n\t"
    "v_mfma_f32_16x16x32_bf16 v[132:135], a[196:199], a[72:75], v[132:135]\n\t"
    "v_mfma_f32_16x16x32_bf16 v[100:103], a[196:199], a[40:43], v[100:103]\n\t"
    "ds_read_b64_tr_b16 a[166:167], v48 offset:41984\n\t"
    "v_mfma_f32_16x16x32_bf16 v[100:103], a[132:135], a[44:47], v[100:103]\n\t"
    "v_mfma_f32_16x16x32_bf16 v[68:71], a[132:135], a[12:15], v[68:71]\n\t"
    "buffer_load_dwordx4 a[4:7], v31, s[20:23], s62 offen offset:1024 sc1\n\t"
    "v_mfma_f32_16x16x32_bf16 v[68:71], a[196:199], a[8:11], v[68:71]\n\t"
    "ds_read_b64_tr_b16 a[232:233], v45 offset:33024\n\t"
    "v_mfma_f32_16x16x32_bf16 v[72:75], a[200:203], a[8:11], v[72:75]\n\t"
    "v_mfma_f32_16x16x32_bf16 v[72:75], a[136:139], a[12:15], v[72:75]\n\t"
    "ds_read_b64_tr_b16 a[168:169], v45 offset:41216\n\t"
    "s_add_u32 m0, s14, 0x400\n\t"
    "v_mfma_f32_16x16x32_bf16 v[104:107], a[136:139], a[44:47], v[104:107]\n\t"
    "buffer_load_dwordx4 v29, s[16:19], s70 offen lds\n\t"
    "v_mfma_f32_16x16x32_bf16 v[104:107], a[200:203], a[40:43], v[104:107]\n\t"
    "v_mfma_f32_16x16x32_bf16 v[136:139], a[200:203], a[72:75], v[136:139]\n\t"
    "ds_read_b64_tr_b16 a[234:235], v45 offset:34048\n\t"
    "v_mfma_f32_16x16x32_bf16 v[136:139], a[136:139], a[76:79], v[136:139]\n\t"
    "v_mfma_f32_16x16x32_bf16 v[168:171], a[136:139], a[108:111], v[168:171]\n\t"
    "buffer_load_dwordx4 a[32:35], v40, s[20:23], s62 offen sc1\n\t"
    "v_mfma_f32_16x16x32_bf16 v[168:171], a[200:203], a[104:107], v[168:171]\n\t"
    "ds_read_b64_tr_b16 a[170:171], v45 offset:42240\n\t"
    "v_mfma_f32_16x16x32_bf16 v[200:203], a[200:203], a[24:27], v[200:203]\n\t"
    "v_mfma_f32_16x16x32_bf16 v[200:203], a[136:139], a[28:31], v[200:203]\n\t"
    "ds_read_b64_tr_b16 a[236:237], v48 offset:33024\n\t"
    "v_mfma_f32_16x16x32_bf16 v[232:235], a[136:139], a[60:63], v[232:235]\n\t"
    "buffer_load_dwordx4 a[36:39], v40, s[20:23], s62 offen offset:1024 sc1\n\t"
    "v_mfma_f32_16x16x32_bf16 v[232:235], a[200:203], a[56:59], v[232:235]\n\t"
    "v_mfma_f32_16x16x32_bf16 v[236:239], a[204:207], a[56:59], v[236:239]\n\t"
    "ds_read_b64_tr_b16 a[172:173], v48 offset:41216\n\t"
    "v_mfma_f32_16x16x32_bf16 v[236:239], a[140:143], a[60:63], v[236:239]\n\t"
    "v_mfma_f32_16x16x32_bf16 v[204:207], a[140:143], a[28:31], v[204:207]\n\t"
    "ds_read_b64_tr_b16 a[238:239], v48 offset:34048\n\t"
    "s_add_u32 m0, s14, 0x800\n\t"
    "v_mfma_f32_16x16x32_bf16 v[204:207], a[204:207], a[24:27], v[204:207]\n\t"
    "buffer_load_dwordx4 v26, s[16:19], s69 offen lds\n\t"
    "v_mfma_f32_16x16x32_bf16 v[172:175], a[204:207], a[104:107], v[172:175]\n\t"
    "v_mfma_f32_16x16x32_bf16 v[172:175], a[140:143], a[108:111], v[172:175]\n\t"
    "ds_read_b64_tr_b16 a[174:175], v48 offset:42240\n\t"
    "v_mfma_f32_16x16x32_bf16 v[140:143], a[140:143], a[76:79], v[140:143]\n\t"
    "v_mfma_f32_16x16x32_bf16 v[140:143], a[204:207], a[72:75], v[140:143]\n\t"
    "buffer_load_dwordx4 a[64:67], v41, s[20:23], s62 offen sc1\n\t"
    "v_mfma_f32_16x16x32_bf16 v[108:111], a[204:207], a[40:43], v[108:111]\n\t"
    "v_mfma_f32_16x16x32_bf16 v[108:111], a[140:143], a[44:47], v[108:111]\n\t"
    "ds_read_b64_tr_b16 a[240:241], v45 offset:33280\n\t"
    "v_mfma_f32_16x16x32_bf16 v[76:79], a[140:143], a[12:15], v[76:79]\n\t"
    "s_add_u32 m0, s14, 0xc00\n\t"
    "v_mfma_f32_16x16x32_bf16 v[76:79], a[204:207], a[8:11], v[76:79]\n\t"
    "buffer_load_dwordx4 v26, s[16:19], s70 offen lds\n\t"
    "v_mfma_f32_16x16x32_bf16 v[80:83], a[208:211], a[8:11], v[80:83]\n\t"
    "ds_read_b64_tr_b16 a[176:177], v45 offset:41472\n\t"
    "v_mfma_f32_16x16x32_bf16 v[80:83], a[144:147], a[12:15], v[80:83]\n\t"
    "v_mfma_f32_16x16x32_bf16 v[112:115], a[144:147], a[44:47], v[112:115]\n\t"
    "ds_read_b64_tr_b16 a[242:243], v45 offset:34304\n\t"
    "v_mfma_f32_16x16x32_bf16 v[112:115], a[208:211], a[40:43], v[112:115]\n\t"
    "buffer_load_dwordx4 a[68:71], v41, s[20:23], s62 offen offset:1024 sc1\n\t"
    "v_mfma_f32_16x16x32_bf16 v[144:147], a[208:211], a[72:75], v[144:147]\n\t"
    "v_mfma_f32_16x16x32_bf16 v[144:147], a[144:147], a[76:79], v[144:147]\n\t"
    "ds_read_b64_tr_b16 a[178:179], v45 offset:42496\n\t"
    "v_mfma_f32_16x16x32_bf16 v[176:179], a[144:147], a[108:111], v[176:179]\n\t"
    "v_mfma_f32_16x16x32_bf16 v[176:179], a[208:211], a[104:107], v[176:179]\n\t"
    "buffer_load_dwordx4 a[96:99], v42, s[20:23], s62 offen sc1\n\t"
    "v_mfma_f32_16x16x32_bf16 v[208:211], a[208:211], a[24:27], v[208:211]\n\t"
    "ds_read_b64_tr_b16 a[244:245], v48 offset:33280\n\t"
    "v_mfma_f32_16x16x32_bf16 v[208:211], a[144:147], a[28:31], v[208:211]\n\t"
    "v_mfma_f32_16x16x32_bf16 v[240:243], a[144:147], a[60:63], v[240:243]\n\t"
    "ds_read_b64_tr_b16 a[180:181], v48 offset:41472\n\t"
    "s_add_u32 m0, s14, 0x1000\n\t"
    "v_mfma_f32_16x16x32_bf16 v[240:243], a[208:211], a[56:59], v[240:243]\n\t"
    "buffer_load_dwordx4 v27, s[16:19], s69 offen lds\n\t"
    "v_mfma_f32_16x16x32_bf16 v[244:247], a[212:215], a[56:59], v[244:247]\n\t"
    "v_mfma_f32_16x16x32_bf16 v[244:247], a[148:151], a[60:63], v[244:247]\n\t"
    "ds_read_b64_tr_b16 a[246:247], v48 offset:34304\n\t"
    "v_mfma_f32_16x16x32_bf16 v[212:215], a[148:151], a[28:31], v[212:215]\n\t"
    "v_mfma_f32_16x16x32_bf16 v[212:215], a[212:215], a[24:27], v[212:215]\n\t"
    "buffer_load_dwordx4 a[100:103], v42, s[20:23], s62 offen offset:1024 sc1\n\t"
    "v_mfma_f32_16x16x32_bf16 v[180:183], a[212:215], a[104:107], v[180:183]\n\t"
    "ds_read_b64_tr_b16 a[182:183], v48 offset:42496\n\t"
    "v_mfma_f32_16x16x32_bf16 v[180:183], a[148:151], a[108:111], v[180:183]\n\t"
    "v_mfma_f32_16x16x32_bf16 v[148:151], a[148:151], a[76:79], v[148:151]\n\t"
    "ds_read_b64_tr_b16 a[248:249], v45 offset:33536\n\t"
    "v_mfma_f32_16x16x32_bf16 v[148:151], a[212:215], a[72:75], v[148:151]\n\t"
    "s_add_u32 m0, s14, 0x1400\n\t"
    "v_mfma_f32_16x16x32_bf16 v[116:119], a[212:215], a[40:43], v[116:119]\n\t"
    "buffer_load_dwordx4 v27, s[16:19], s70 offen lds\n\t"
    "v_mfma_f32_16x16x32_bf16 v[116:119], a[148:151], a[44:47], v[116:119]\n\t"
    "ds_read_b64_tr_b16 a[184:185], v45 offset:41728\n\t"
    "v_mfma_f32_16x16x32_bf16 v[84:87], a[148:151], a[12:15], v[84:87]\n\t"
    "v_mfma_f32_16x16x32_bf16 v[84:87], a[212:215], a[8:11], v[84:87]\n\t"
    "ds_read_b64_tr_b16 a[250:251], v45 offset:34560\n\t"
    "v_mfma_f32_16x16x32_bf16 v[88:91], a[216:219], a[8:11], v[88:91]\n\t"
    "buffer_load_dwordx4 a[16:19], v43, s[20:23], s62 offen sc1\n\t"
    "v_mfma_f32_16x16x32_bf16 v[88:91], a[152:155], a[12:15], v[88:91]\n\t"
    "v_mfma_f32_16x16x32_bf16 v[120:123], a[152:155], a[44:47], v[120:123]\n\t"
    "ds_read_b64_tr_b16 a[186:187], v45 offset:42752\n\t"
    "v_mfma_f32_16x16x32_bf16 v[120:123], a[216:219], a[40:43], v[120:123]\n\t"
    "v_mfma_f32_16x16x32_bf16 v[152:155], a[216:219], a[72:75], v[152:155]\n\t"
    "buffer_load_dwordx4 a[20:23], v43, s[20:23], s62 offen offset:1024 sc1\n\t"
    "v_mfma_f32_16x16x32_bf16 v[152:155], a[152:155], a[76:79], v[152:155]\n\t"
    "ds_read_b64_tr_b16 a[252:253], v48 offset:33536\n\t"
    "v_mfma_f32_16x16x32_bf16 v[184:187], a[152:155], a[108:111], v[184:187]\n\t"
    "v_mfma_f32_16x16x32_bf16 v[184:187], a[216:219], a[104:107], v[184:187]\n\t"
    "ds_read_b64_tr_b16 a[188:189], v48 offset:41728\n\t"
    "s_add_u32 m0, s14, 0x1800\n\t"
    "v_mfma_f32_16x16x32_bf16 v[216:219], a[216:219], a[24:27], v[216:219]\n\t"
    "buffer_load_dwordx4 v28, s[16:19], s69 offen lds\n\t"
    "v_mfma_f32_16x16x32_bf16 v[216:219], a[152:155], a[28:31], v[216:219]\n\t"
    "v_mfma_f32_16x16x32_bf16 v[248:251], a[152:155], a[60:63], v[248:251]\n\t"
    "ds_read_b64_tr_b16 a[254:255], v48 offset:34560\n\t"
    "v_mfma_f32_16x16x32_bf16 v[248:251], a[216:219], a[56:59], v[248:251]\n\t"
    "v_mfma_f32_16x16x32_bf16 v[252:255], a[220:223], a[56:59], v[252:255]\n\t"
    "buffer_load_dwordx4 a[48:51], v44, s[20:23], s62 offen sc1\n\t"
    "v_mfma_f32_16x16x32_bf16 v[252:255], a[156:159], a[60:63], v[252:255]\n\t"
    "ds_read_b64_tr_b16 a[190:191], v48 offset:42752\n\t"
    "v_mfma_f32_16x16x32_bf16 v[220:223], a[156:159], a[28:31], v[220:223]\n\t"
    "v_mfma_f32_16x16x32_bf16 v[220:223], a[220:223], a[24:27], v[220:223]\n\t"
    "s_add_u32 m0, s14, 0x1c00\n\t"
    "v_mfma_f32_16x16x32_bf16 v[188:191], a[220:223], a[104:107], v[188:191]\n\t"
    "buffer_load_dwordx4 v28, s[16:19], s70 offen lds\n\t"
    "v_mfma_f32_16x16x32_bf16 v[188:191], a[156:159], a[108:111], v[188:191]\n\t"
    "v_mfma_f32_16x16x32_bf16 v[156:159], a[156:159], a[76:79], v[156:159]\n\t"
    "v_mfma_f32_16x16x32_bf16 v[156:159], a[220:223], a[72:75], v[156:159]\n\t"
    "v_mfma_f32_16x16x32_bf16 v[124:127], a[220:223], a[40:43], v[124:127]\n\t"
    "buffer_load_dwordx4 a[52:55], v44, s[20:23], s62 offen offset:1024 sc1\n\t"
    "v_mfma_f32_16x16x32_bf16 v[124:127], a[156:159], a[44:47], v[124:127]\n\t"
    "v_mfma_f32_16x16x32_bf16 v[92:95], a[156:159], a[12:15], v[92:95]\n\t"
    "v_mfma_f32_16x16x32_bf16 v[92:95], a[220:223], a[8:11], v[92:95]\n\t"
    "s_waitcnt vmcnt(20) lgkmcnt(0)\n\t"
    "s_barrier\n\t"
    "v_cmp_eq_u64_e32 vcc, s[96:97], v[60:61]\n\t"
    "s_nop 1\n\t"
    "s_and_b32 s68, vcc_lo, s24\n\t"
    "s_cmp_eq_u32 s68, s24\n\t"
    "s_cbranch_scc0 .Lfa_stage2\n\t"
    ".Lfa_back2:\n\t"
    "s_mov_b32 s24, s78\n\t"
    "s_or_b32 s68, s8, s9\n\t"
    "s_cbranch_scc0 .Lfa_epoch\n\t"
    ".Lfa_back3:\n\t"
    "v_mfma_f32_16x16x32_bf16 v[64:67], a[224:227], a[80:83], v[64:67]\n\t"
    "s_bfe_u32 s62, s71, 0x3001d\n\t"
    "s_bfe_u32 s75, s71, 0x1001c\n\t"
    "s_lshl2_add_u32 s62, s62, s75\n\t"
    "v_mfma_f32_16x16x32_bf16 v[64:67], a[160:163], a[84:87], v[64:67]\n\t"
    "s_lshl_b32 s62, s62, 11\n\t"
    "s_add_u32 s62, s62, s25\n\t"
    "s_bfe_u32 s60, s72, 0x30009\n\t"
    "v_mfma_f32_16x16x32_bf16 v[96:99], a[160:163], a[92:95], v[96:99]\n\t"
    "s_bfe_u32 s75, s72, 0x10008\n\t"
    "s_lshl2_add_u32 s60, s60, s75\n\t"
    "s_lshl_b32 s60, s60, 11\n\t"
    "v_mfma_f32_16x16x32_bf16 v[96:99], a[224:227], a[88:91], v[96:99]\n\t"
    "ds_read_b64_tr_b16 a[192:193], v46 offset:0\n\t"
    "s_add_u32 s60, s60, s25\n\t"
    "s_lshl2_add_u32 s60, s60, s79\n\t"
    "s_lshl_b32 s60, s60, 4\n\t"
    "v_mfma_f32_16x16x32_bf16 v[128:131], a[224:227], a[112:115], v[128:131]\n\t"
    "s_add_u32 s61, s60, 0x8000\n\t"
    "v_mfma_f32_16x16x32_bf16 v[128:131], a[160:163], a[116:119], v[128:131]\n\t"
    "ds_read_b64_tr_b16 a[128:129], v46 offset:8192\n\t"
    "v_mfma_f32_16x16x32_bf16 v[160:163], a[160:163], a[124:127], v[160:163]\n\t"
    "v_mfma_f32_16x16x32_bf16 v[160:163], a[224:227], a[120:123], v[160:163]\n\t"
    "ds_read_b64_tr_b16 a[194:195], v46 offset:1024\n\t"
    "v_mfma_f32_16x16x32_bf16 v[192:195], a[224:227], v[4:7], v[192:195]\n\t"
    "v_mfma_f32_16x16x32_bf16 v[192:195], a[160:163], v[8:11], v[192:195]\n\t"
    "ds_read_b64_tr_b16 a[130:131], v46 offset:9216\n\t"
    "v_mfma_f32_16x16x32_bf16 v[224:227], a[160:163], v[16:19], v[224:227]\n\t"
    "buffer_load_dwordx4 a[8:11], v31, s[20:23], s62 offen sc1\n\t"
    "v_mfma_f32_16x16x32_bf16 v[224:227], a[224:227], v[12:15], v[224:227]\n\t"
    "v_mfma_f32_16x16x32_bf16 v[228:231], a[228:231], v[12:15], v[228:231]\n\t"
    "ds_read_b64_tr_b16 a[196:197], v49 offset:0\n\t"
    "v_mfma_f32_16x16x32_bf16 v[228:231], a[164:167], v[16:19], v[228:231]\n\t"
    "s_add_u32 m0, s14, 0x8000\n\t"
    "v_mfma_f32_16x16x32_bf16 v[196:199], a[164:167], v[8:11], v[196:199]\n\t"
    "buffer_load_dwordx4 v29, s[16:19], s60 offen lds\n\t"
    "v_mfma_f32_16x16x32_bf16 v[196:199], a[228:231], v[4:7], v[196:199]\n\t"
    "ds_read_b64_tr_b16 a[132:133], v49 offset:8192\n\t"
    "v_mfma_f32_16x16x32_bf16 v[164:167], a[228:231], a[120:123], v[164:167]\n\t"
    "v_mfma_f32_16x16x32_bf16 v[164:167], a[164:167], a[124:127], v[164:167]\n\t"
    "ds_read_b64_tr_b16 a[198:199], v49 offset:1024\n\t"
    "v_mfma_f32_16x16x32_bf16 v[132:135], a[164:167], a[116:119], v[132:135]\n\t"
    "buffer_load_dwordx4 a[12:15], v31, s[20:23], s62 offen offset:1024 sc1\n\t"
    "v_mfma_f32_16x16x32_bf16 v[132:135], a[228:231], a[112:115], v[132:135]\n\t"
    "v_mfma_f32_16x16x32_bf16 v[100:103], a[228:231], a[88:91], v[100:103]\n\t"
    "ds_read_b64_tr_b16 a[134:135], v49 offset:9216\n\t"
    "v_mfma_f32_16x16x32_bf16 v[100:103], a[164:167], a[92:95], v[100:103]\n\t"
    "v_mfma_f32_16x16x32_bf16 v[68:71], a[164:167], a[84:87], v[68:71]\n\t"
    "ds_read_b64_tr_b16 a[200:201], v46 offset:256\n\t"
    "s_add_u32 m0, s14, 0x8400\n\t"
    "v_mfma_f32_16x16x32_bf16 v[68:71], a[228:231], a[80:83], v[68:71]\n\t"
    "buffer_load_dwordx4 v29, s[16:19], s61 offen lds\n\t"
    "v_mfma_f32_16x16x32_bf16 v[72:75], a[232:235], a[80:83], v[72:75]\n\t"
    "v_mfma_f32_16x16x32_bf16 v[72:75], a[168:171], a[84:87], v[72:75]\n\t"
    "ds_read_b64_tr_b16 a[136:137], v46 offset:8448\n\t"
    "v_mfma_f32_16x16x32_bf16 v[104:107], a[168:171], a[92:95], v[104:107]\n\t"
    "v_mfma_f32_16x16x32_bf16 v[104:107], a[232:235], a[88:91], v[104:107]\n\t"
    "buffer_load_dwordx4 a[40:43], v40, s[20:23], s62 offen sc1\n\t"
    "v_mfma_f32_16x16x32_bf16 v[136:139], a[232:235], a[112:115], v[136:139]\n\t"
    "ds_read_b64_tr_b16 a[202:203], v46 offset:1280\n\t"
    "v_mfma_f32_16x16x32_bf16 v[136:139], a[168:171], a[116:119], v[136:139]\n\t"
    "v_mfma_f32_16x16x32_bf16 v[168:171], a[168:171], a[124:127], v[168:171]\n\t"
    "v_mfma_f32_16x16x32_bf16 v[168:171], a[232:235], a[120:123], v[168:171]\n\t"
    "buffer_load_dwordx4 a[44:47], v40, s[20:23], s62 offen offset:1024 sc1\n\t"
    "v_mfma_f32_16x16x32_bf16 v[200:203], a[232:235], v[4:7], v[200:203]\n\t"
    "ds_read_b64_tr_b16 a[138:139], v46 offset:9472\n\t"
    "v_mfma_f32_16x16x32_bf16 v[200:203], a[168:171], v[8:11], v[200:203]\n\t"
    "v_mfma_f32_16x16x32_bf16 v[232:235], a[168:171], v[16:19], v[232:235]\n\t"
    "ds_read_b64_tr_b16 a[204:205], v49 offset:256\n\t"
    "v_mfma_f32_16x16x32_bf16 v[232:235], a[232:235], v[12:15], v[232:235]\n\t"
    "s_add_u32 m0, s14, 0x8800\n\t"
    "v_mfma_f32_16x16x32_bf16 v[236:239], a[236:239], v[12:15], v[236:239]\n\t"
    "buffer_load_dwordx4 v26, s[16:19], s60 offen lds\n\t"
    "v_mfma_f32_16x16x32_bf16 v[236:239], a[172:175], v[16:19], v[236:239]\n\t"
    "ds_read_b64_tr_b16 a[140:141], v49 offset:8448\n\t"
    "v_mfma_f32_16x16x32_bf16 v[204:207], a[172:175], v[8:11], v[204:207]\n\t"
    "v_mfma_f32_16x16x32_bf16 v[204:207], a[236:239], v[4:7], v[204:207]\n\t"
    "ds_read_b64_tr_b16 a[206:207], v49 offset:1280\n\t"
    "v_mfma_f32_16x16x32_bf16 v[172:175], a[236:239], a[120:123], v[172:175]\n\t"
    "buffer_load_dwordx4 a[72:75], v41, s[20:23], s62 offen sc1\n\t"
    "v_mfma_f32_16x16x32_bf16 v[172:175], a[172:175], a[124:127], v[172:175]\n\t"
    "v_mfma_f32_16x16x32_bf16 v[140:143], a[172:175], a[116:119], v[140:143]\n\t"
    "ds_read_b64_tr_b16 a[142:143], v49 offset:9472\n\t"
    "v_mfma_f32_16x16x32_bf16 v[140:143], a[236:239], a[112:115], v[140:143]\n\t"
    "s_add_u32 m0, s14, 0x8c00\n\t"
    "v_mfma_f32_16x16x32_bf16 v[108:111], a[236:239], a[88:91], v[108:111]\n\t"
    "buffer_load_dwordx4 v26, s[16:19], s61 offen lds\n\t"
    "v_mfma_f32_16x16x32_bf16 v[108:111], a[172:175], a[92:95], v[108:111]\n\t"
    "ds_read_b64_tr_b16 a[208:209], v46 offset:512\n\t"
    "v_mfma_f32_16x16x32_bf16 v[76:79], a[172:175], a[84:87], v[76:79]\n\t"
    "v_mfma_f32_16x16x32_bf16 v[76:79], a[236:239], a[80:83], v[76:79]\n\t"
    "ds_read_b64_tr_b16 a[144:145], v46 offset:8704\n\t"
    "v_mfma_f32_16x16x32_bf16 v[80:83], a[240:243], a[80:83], v[80:83]\n\t"
    "v_mfma_f32_16x16x32_bf16 v[80:83], a[176:179], a[84:87], v[80:83]\n\t"
    "buffer_load_dwordx4 a[76:79], v41, s[20:23], s62 offen offset:1024 sc1\n\t"
    "v_mfma_f32_16x16x32_bf16 v[112:115], a[176:179], a[92:95], v[112:115]\n\t"
    "ds_read_b64_tr_b16 a[210:211], v46 offset:1536\n\t"
    "v_mfma_f32_16x16x32_bf16 v[112:115], a[240:243], a[88:91], v[112:115]\n\t"
    "v_mfma_f32_16x16x32_bf16 v[144:147], a[240:243], a[112:115], v[144:147]\n\t"
    "ds_read_b64_tr_b16 a[146:147], v46 offset:9728\n\t"
    "v_mfma_f32_16x16x32_bf16 v[144:147], a[176:179], a[116:119], v[144:147]\n\t"
    "buffer_load_dwordx4 a[104:107], v42, s[20:23], s62 offen sc1\n\t"
    "v_mfma_f32_16x16x32_bf16 v[176:179], a[176:179], a[124:127], v[176:179]\n\t"
    "v_mfma_f32_16x16x32_bf16 v[176:179], a[240:243], a[120:123], v[176:179]\n\t"
    "ds_read_b64_tr_b16 a[212:213], v49 offset:512\n\t"
    "v_mfma_f32_16x16x32_bf16 v[208:211], a[240:243], v[4:7], v[208:211]\n\t"
    "v_mfma_f32_16x16x32_bf16 v[208:211], a[176:179], v[8:11], v[208:211]\n\t"
    "ds_read_b64_tr_b16 a[148:149], v49 offset:8704\n\t"
    "s_add_u32 m0, s14, 0x9000\n\t"
    "v_mfma_f32_16x16x32_bf16 v[240:243], a[176:179], v[16:19], v[240:243]\n\t"
    "buffer_load_dwordx4 v27, s[16:19], s60 offen lds\n\t"
    "v_mfma_f32_16x16x32_bf16 v[240:243], a[240:243], v[12:15], v[240:243]\n\t"
    "v_mfma_f32_16x16x32_bf16 v[244:247], a[244:247], v[12:15], v[244:247]\n\t"
    "v_mfma_f32_16x16x32_bf16 v[244:247], a[180:183], v[16:19], v[244:247]\n\t"
    "ds_read_b64_tr_b16 a[214:215], v49 offset:1536\n\t"
    "v_mfma_f32_16x16x32_bf16 v[212:215], a[180:183], v[8:11], v[212:215]\n\t"
    "buffer_load_dwordx4 a[108:111], v42, s[20:23], s62 offen offset:1024 sc1\n\t"
    "v_mfma_f32_16x16x32_bf16 v[212:215], a[244:247], v[4:7], v[212:215]\n\t"
    "v_mfma_f32_16x16x32_bf16 v[180:183], a[244:247], a[120:123], v[180:183]\n\t"
    "ds_read_b64_tr_b16 a[150:151], v49 offset:9728\n\t"
    "v_mfma_f32_16x16x32_bf16 v[180:183], a[180:183], a[124:127], v[180:183]\n\t"
    "s_add_u32 m0, s14, 0x9400\n\t"
    "v_mfma_f32_16x16x32_bf16 v[148:151], a[180:183], a[116:119], v[148:151]\n\t"
    "buffer_load_dwordx4 v27, s[16:19], s61 offen lds\n\t"
    "v_mfma_f32_16x16x32_bf16 v[148:151], a[244:247], a[112:115], v[148:151]\n\t"
    "ds_read_b64_tr_b16 a[216:217], v46 offset:768\n\t"
    "v_mfma_f32_16x16x32_bf16 v[116:119], a[244:247], a[88:91], v[116:119]\n\t"
    "v_mfma_f32_16x16x32_bf16 v[116:119], a[180:183], a[92:95], v[116:119]\n\t"
    "ds_read_b64_tr_b16 a[152:153], v46 offset:8960\n\t"
    "v_mfma_f32_16x16x32_bf16 v[84:87], a[180:183], a[84:87], v[84:87]\n\t"
    "v_mfma_f32_16x16x32_bf16 v[84:87], a[244:247], a[80:83], v[84:87]\n\t"
    "buffer_load_dwordx4 a[24:27], v43, s[20:23], s62 offen sc1\n\t"
    "v_mfma_f32_16x16x32_bf16 v[88:91], a[248:251], a[80:83], v[88:91]\n\t"
    "ds_read_b64_tr_b16 a[218:219], v46 offset:1792\n\t"
    "v_mfma_f32_16x16x32_bf16 v[88:91], a[184:187], a[84:87], v[88:91]\n\t"
    "v_mfma_f32_16x16x32_bf16 v[120:123], a[184:187], a[92:95], v[120:123]\n\t"
    "ds_read_b64_tr_b16 a[154:155], v46 offset:9984\n\t"
    "v_mfma_f32_16x16x32_bf16 v[120:123], a[248:251], a[88:91], v[120:123]\n\t"
    "buffer_load_dwordx4 a[28:31], v43, s[20:23], s62 offen offset:1024 sc1\n\t"
    "v_mfma_f32_16x16x32_bf16 v[152:155], a[248:251], a[112:115], v[152:155]\n\t"
    "v_mfma_f32_16x16x32_bf16 v[152:155], a[184:187], a[116:119], v[152:155]\n\t"
    "ds_read_b64_tr_b16 a[220:221], v49 offset:768\n\t"
    "v_mfma_f32_16x16x32_bf16 v[184:187], a[184:187], a[124:127], v[184:187]\n\t"
    "s_add_u32 m0, s14, 0x9800\n\t"
    "v_mfma_f32_16x16x32_bf16 v[184:187], a[248:251], a[120:123], v[184:187]\n\t"
    "buffer_load_dwordx4 v28, s[16:19], s60 offen lds\n\t"
    "v_mfma_f32_16x16x32_bf16 v[216:219], a[248:251], v[4:7], v[216:219]\n\t"
    "ds_read_b64_tr_b16 a[156:157], v49 offset:8960\n\t"
    "v_mfma_f32_16x16x32_bf16 v[216:219], a[184:187], v[8:11], v[216:219]\n\t"
    "v_mfma_f32_16x16x32_bf16 v[248:251], a[184:187], v[16:19], v[248:251]\n\t"
    "ds_read_b64_tr_b16 a[222:223], v49 offset:1792\n\t"
    "v_mfma_f32_16x16x32_bf16 v[248:251], a[248:251], v[12:15], v[248:251]\n\t"
    "v_mfma_f32_16x16x32_bf16 v[252:255], a[252:255], v[12:15], v[252:255]\n\t"
    "buffer_load_dwordx4 a[56:59], v44, s[20:23], s62 offen sc1\n\t"
    "v_mfma_f32_16x16x32_bf16 v[252:255], a[188:191], v[16:19], v[252:255]\n\t"
    "ds_read_b64_tr_b16 a[158:159], v49 offset:9984\n\t"
    "v_mfma_f32_16x16x32_bf16 v[220:223], a[188:191], v[8:11], v[220:223]\n\t"
    "v_mfma_f32_16x16x32_bf16 v[220:223], a[252:255], v[4:7], v[220:223]\n\t"
    "s_add_u32 m0, s14, 0x9c00\n\t"
    "v_mfma_f32_16x16x32_bf16 v[188:191], a[252:255], a[120:123], v[188:191]\n\t"
    "buffer_load_dwordx4 v28, s[16:19], s61 offen lds\n\t"
    "v_mfma_f32_16x16x32_bf16 v[188:191], a[188:191], a[124:127], v[188:191]\n\t"
    "v_mfma_f32_16x16x32_bf16 v[156:159], a[188:191], a[116:119], v[156:159]\n\t"
    "v_mfma_f32_16x16x32_bf16 v[156:159], a[252:255], a[112:115], v[156:159]\n\t"
    "v_mfma_f32_16x16x32_bf16 v[124:127], a[252:255], a[88:91], v[124:127]\n\t"
    "buffer_load_dwordx4 a[60:63], v44, s[20:23], s62 offen offset:1024 sc1\n\t"
    "v_mfma_f32_16x16x32_bf16 v[124:127], a[188:191], a[92:95], v[124:127]\n\t"
    "v_mfma_f32_16x16x32_bf16 v[92:95], a[188:191], a[84:87], v[92:95]\n\t"
    "v_mfma_f32_16x16x32_bf16 v[92:95], a[252:255], a[80:83], v[92:95]\n\t"
    "s_waitcnt vmcnt(20) lgkmcnt(0)\n\t"
    "s_barrier\n\t"
    "v_mfma_f32_16x16x32_bf16 v[64:67], a[192:195], a[0:3], v[64:67]\n\t"
    "s_bfe_u32 s62, s72, 0x30001\n\t"
    "s_bfe_u32 s75, s72, 0x10000\n\t"
    "s_lshl2_add_u32 s62, s62, s75\n\t"
    "v_mfma_f32_16x16x32_bf16 v[64:67], a[128:131], a[4:7], v[64:67]\n\t"
    "s_lshl_b32 s62, s62, 11\n\t"
    "s_add_u32 s62, s62, s25\n\t"
    "s_bfe_u32 s69, s72, 0x3000d\n\t"
    "v_mfma_f32_16x16x32_bf16 v[96:99], a[128:131], a[36:39], v[96:99]\n\t"
    "s_bfe_u32 s75, s72, 0x1000c\n\t"
    "s_lshl2_add_u32 s69, s69, s75\n\t"
    "s_lshl_b32 s69, s69, 11\n\t"
    "v_mfma_f32_16x16x32_bf16 v[96:99], a[192:195], a[32:35], v[96:99]\n\t"
    "ds_read_b64_tr_b16 a[224:225], v46 offset:32768\n\t"
    "s_add_u32 s69, s69, s25\n\t"
    "s_lshl2_add_u32 s69, s69, s79\n\t"
    "s_lshl_b32 s69, s69, 4\n\t"
    "v_mfma_f32_16x16x32_bf16 v[128:131], a[192:195], a[64:67], v[128:131]\n\t"
    "s_add_u32 s70, s69, 0x8000\n\t"
    "v_mfma_f32_16x16x32_bf16 v[128:131], a[128:131], a[68:71], v[128:131]\n\t"
    "ds_read_b64_tr_b16 a[160:161], v46 offset:40960\n\t"
    "v_mfma_f32_16x16x32_bf16 v[160:163], a[128:131], a[100:103], v[160:163]\n\t"
    "v_mfma_f32_16x16x32_bf16 v[160:163], a[192:195], a[96:99], v[160:163]\n\t"
    "ds_read_b64_tr_b16 a[226:227], v46 offset:33792\n\t"
    "v_mfma_f32_16x16x32_bf16 v[192:195], a[192:195], a[16:19], v[192:195]\n\t"
    "v_mfma_f32_16x16x32_bf16 v[192:195], a[128:131], a[20:23], v[192:195]\n\t"
    "ds_read_b64_tr_b16 a[162:163], v46 offset:41984\n\t"
    "v_mfma_f32_16x16x32_bf16 v[224:227], a[128:131], a[52:55], v[224:227]\n\t"
    "buffer_load_dwordx4 a[80:83], v31, s[20:23], s62 offen sc1\n\t"
    "v_mfma_f32_16x16x32_bf16 v[224:227], a[192:195], a[48:51], v[224:227]\n\t"
    "v_mfma_f32_16x16x32_bf16 v[228:231], a[196:199], a[48:51], v[228:231]\n\t"
    "ds_read_b64_tr_b16 a[228:229], v49 offset:32768\n\t"
    "v_mfma_f32_16x16x32_bf16 v[228:231], a[132:135], a[52:55], v[228:231]\n\t"
    "s_add_u32 m0, s14, 0x10000\n\t"
    "v_mfma_f32_16x16x32_bf16 v[196:199], a[132:135], a[20:23], v[196:199]\n\t"
    "buffer_load_dwordx4 v29, s[16:19], s69 offen lds\n\t"
    "v_mfma_f32_16x16x32_bf16 v[196:199], a[196:199], a[16:19], v[196:199]\n\t"
    "ds_read_b64_tr_b16 a[164:165], v49 offset:40960\n\t"
    "v_mfma_f32_16x16x32_bf16 v[164:167], a[196:199], a[96:99], v[164:167]\n\t"
    "v_mfma_f32_16x16x32_bf16 v[164:167], a[132:135], a[100:103], v[164:167]\n\t"
    "ds_read_b64_tr_b16 a[230:231], v49 offset:33792\n\t"
    "v_mfma_f32_16x16x32_bf16 v[132:135], a[132:135], a[68:71], v[132:135]\n\t"
    "buffer_load_dwordx4 a[84:87], v31, s[20:23], s62 offen offset:1024 sc1\n\t"
    "v_mfma_f32_16x16x32_bf16 v[132:135], a[196:199], a[64:67], v[132:135]\n\t"
    "v_mfma_f32_16x16x32_bf16 v[100:103], a[196:199], a[32:35], v[100:103]\n\t"
    "ds_read_b64_tr_b16 a[166:167], v49 offset:41984\n\t"
    "v_mfma_f32_16x16x32_bf16 v[100:103], a[132:135], a[36:39], v[100:103]\n\t"
    "v_mfma_f32_16x16x32_bf16 v[68:71], a[132:135], a[4:7], v[68:71]\n\t"
    "ds_read_b64_tr_b16 a[232:233], v46 offset:33024\n\t"
    "s_add_u32 m0, s14, 0x10400\n\t"
    "v_mfma_f32_16x16x32_bf16 v[68:71], a[196:199], a[0:3], v[68:71]\n\t"
    "buffer_load_dwordx4 v29, s[16:19], s70 offen lds\n\t"
    "v_mfma_f32_16x16x32_bf16 v[72:75], a[200:203], a[0:3], v[72:75]\n\t"
    "v_mfma_f32_16x16x32_bf16 v[72:75], a[136:139], a[4:7], v[72:75]\n\t"
    "ds_read_b64_tr_b16 a[168:169], v46 offset:41216\n\t"
    "v_mfma_f32_16x16x32_bf16 v[104:107], a[136:139], a[36:39], v[104:107]\n\t"
    "v_mfma_f32_16x16x32_bf16 v[104:107], a[200:203], a[32:35], v[104:107]\n\t"
    "buffer_load_dwordx4 a[88:91], v40, s[20:23], s62 offen sc1\n\t"
    "v_mfma_f32_16x16x32_bf16 v[136:139], a[200:203], a[64:67], v[136:139]\n\t"
    "ds_read_b64_tr_b16 a[234:235], v46 offset:34048\n\t"
    "v_mfma_f32_16x16x32_bf16 v[136:139], a[136:139], a[68:71], v[136:139]\n\t"
    "v_mfma_f32_16x16x32_bf16 v[168:171], a[136:139], a[100:103], v[168:171]\n\t"
    "v_mfma_f32_16x16x32_bf16 v[168:171], a[200:203], a[96:99], v[168:171]\n\t"
    "buffer_load_dwordx4 a[92:95], v40, s[20:23], s62 offen offset:1024 sc1\n\t"
    "v_mfma_f32_16x16x32_bf16 v[200:203], a[200:203], a[16:19], v[200:203]\n\t"
    "ds_read_b64_tr_b16 a[170:171], v46 offset:42240\n\t"
    "v_mfma_f32_16x16x32_bf16 v[200:203], a[136:139], a[20:23], v[200:203]\n\t"
    "v_mfma_f32_16x16x32_bf16 v[232:235], a[136:139], a[52:55], v[232:235]\n\t"
    "ds_read_b64_tr_b16 a[236:237], v49 offset:33024\n\t"
    "v_mfma_f32_16x16x32_bf16 v[232:235], a[200:203], a[48:51], v[232:235]\n\t"
    "s_add_u32 m0, s14, 0x10800\n\t"
    "v_mfma_f32_16x16x32_bf16 v[236:239], a[204:207], a[48:51], v[236:239]\n\t"
    "buffer_load_dwordx4 v26, s[16:19], s69 offen lds\n\t"
    "v_mfma_f32_16x16x32_bf16 v[236:239], a[140:143], a[52:55], v[236:239]\n\t"
    "ds_read_b64_tr_b16 a[172:173], v49 offset:41216\n\t"
    "v_mfma_f32_16x16x32_bf16 v[204:207], a[140:143], a[20:23], v[204:207]\n\t"
    "v_mfma_f32_16x16x32_bf16 v[204:207], a[204:207], a[16:19], v[204:207]\n\t"
    "ds_read_b64_tr_b16 a[238:239], v49 offset:34048\n\t"
    "v_mfma_f32_16x16x32_bf16 v[172:175], a[204:207], a[96:99], v[172:175]\n\t"
    "buffer_load_dwordx4 a[112:115], v41, s[20:23], s62 offen sc1\n\t"
    "v_mfma_f32_16x16x32_bf16 v[172:175], a[140:143], a[100:103], v[172:175]\n\t"
    "v_mfma_f32_16x16x32_bf16 v[140:143], a[140:143], a[68:71], v[140:143]\n\t"
    "ds_read_b64_tr_b16 a[174:175], v49 offset:42240\n\t"
    "v_mfma_f32_16x16x32_bf16 v[140:143], a[204:207], a[64:67], v[140:143]\n\t"
    "s_add_u32 m0, s14, 0x10c00\n\t"
    "v_mfma_f32_16x16x32_bf16 v[108:111], a[204:207], a[32:35], v[108:111]\n\t"
    "buffer_load_dwordx4 v26, s[16:19], s70 offen lds\n\t"
    "v_mfma_f32_16x16x32_bf16 v[108:111], a[140:143], a[36:39], v[108:111]\n\t"
    "ds_read_b64_tr_b16 a[240:241], v46 offset:33280\n\t"
    "v_mfma_f32_16x16x32_bf16 v[76:79], a[140:143], a[4:7], v[76:79]\n\t"
    "v_mfma_f32_16x16x32_bf16 v[76:79], a[204:207], a[0:3], v[76:79]\n\t"
    "ds_read_b64_tr_b16 a[176:177], v46 offset:41472\n\t"
    "v_mfma_f32_16x16x32_bf16 v[80:83], a[208:211], a[0:3], v[80:83]\n\t"
    "v_mfma_f32_16x16x32_bf16 v[80:83], a[144:147], a[4:7], v[80:83]\n\t"
    "buffer_load_dwordx4 a[116:119], v41, s[20:23], s62 offen offset:1024 sc1\n\t"
    "v_mfma_f32_16x16x32_bf16 v[112:115], a[144:147], a[36:39], v[112:115]\n\t"
    "ds_read_b64_tr_b16 a[242:243], v46 offset:34304\n\t"
    "v_mfma_f32_16x16x32_bf16 v[112:115], a[208:211], a[32:35], v[112:115]\n\t"
    "v_mfma_f32_16x16x32_bf16 v[144:147], a[208:211], a[64:67], v[144:147]\n\t"
    "ds_read_b64_tr_b16 a[178:179], v46 offset:42496\n\t"
    "v_mfma_f32_16x16x32_bf16 v[144:147], a[144:147], a[68:71], v[144:147]\n\t"
    "buffer_load_dwordx4 a[120:123], v42, s[20:23], s62 offen sc1\n\t"
    "v_mfma_f32_16x16x32_bf16 v[176:179], a[144:147], a[100:103], v[176:179]\n\t"
    "v_mfma_f32_16x16x32_bf16 v[176:179], a[208:211], a[96:99], v[176:179]\n\t"
    "ds_read_b64_tr_b16 a[244:245], v49 offset:33280\n\t"
    "v_mfma_f32_16x16x32_bf16 v[208:211], a[208:211], a[16:19], v[208:211]\n\t"
    "v_mfma_f32_16x16x32_bf16 v[208:211], a[144:147], a[20:23], v[208:211]\n\t"
    "ds_read_b64_tr_b16 a[180:181], v49 offset:41472\n\t"
    "s_add_u32 m0, s14, 0x11000\n\t"
    "v_mfma_f32_16x16x32_bf16 v[240:243], a[144:147], a[52:55], v[240:243]\n\t"
    "buffer_load_dwordx4 v27, s[16:19], s69 offen lds\n\t"
    "v_mfma_f32_16x16x32_bf16 v[240:243], a[208:211], a[48:51], v[240:243]\n\t"
    "v_mfma_f32_16x16x32_bf16 v[244:247], a[212:215], a[48:51], v[244:247]\n\t"
    "v_mfma_f32_16x16x32_bf16 v[244:247], a[148:151], a[52:55], v[244:247]\n\t"
    "ds_read_b64_tr_b16 a[246:247], v49 offset:34304\n\t"
    "v_mfma_f32_16x16x32_bf16 v[212:215], a[148:151], a[20:23], v[212:215]\n\t"
    "buffer_load_dwordx4 a[124:127], v42, s[20:23], s62 offen offset:1024 sc1\n\t"
    "v_mfma_f32_16x16x32_bf16 v[212:215], a[212:215], a[16:19], v[212:215]\n\t"
    "v_mfma_f32_16x16x32_bf16 v[180:183], a[212:215], a[96:99], v[180:183]\n\t"
    "ds_read_b64_tr_b16 a[182:183], v49 offset:42496\n\t"
    "v_mfma_f32_16x16x32_bf16 v[180:183], a[148:151], a[100:103], v[180:183]\n\t"
    "s_add_u32 m0, s14, 0x11400\n\t"
    "v_mfma_f32_16x16x32_bf16 v[148:151], a[148:151], a[68:71], v[148:151]\n\t"
    "buffer_load_dwordx4 v27, s[16:19], s70 offen lds\n\t"
    "v_mfma_f32_16x16x32_bf16 v[148:151], a[212:215], a[64:67], v[148:151]\n\t"
    "ds_read_b64_tr_b16 a[248:249], v46 offset:33536\n\t"
    "v_mfma_f32_16x16x32_bf16 v[116:119], a[212:215], a[32:35], v[116:119]\n\t"
    "v_mfma_f32_16x16x32_bf16 v[116:119], a[148:151], a[36:39], v[116:119]\n\t"
    "ds_read_b64_tr_b16 a[184:185], v46 offset:41728\n\t"
    "v_mfma_f32_16x16x32_bf16 v[84:87], a[148:151], a[4:7], v[84:87]\n\t"
    "v_mfma_f32_16x16x32_bf16 v[84:87], a[212:215], a[0:3], v[84:87]\n\t"
    "buffer_load_dwordx4 v[4:7], v43, s[20:23], s62 offen sc1\n\t"
    "v_mfma_f32_16x16x32_bf16 v[88:91], a[216:219], a[0:3], v[88:91]\n\t"
    "ds_read_b64_tr_b16 a[250:251], v46 offset:34560\n\t"
    "v_mfma_f32_16x16x32_bf16 v[88:91], a[152:155], a[4:7], v[88:91]\n\t"
    "v_mfma_f32_16x16x32_bf16 v[120:123], a[152:155], a[36:39], v[120:123]\n\t"
    "ds_read_b64_tr_b16 a[186:187], v46 offset:42752\n\t"
    "v_mfma_f32_16x16x32_bf16 v[120:123], a[216:219], a[32:35], v[120:123]\n\t"
    "buffer_load_dwordx4 v[8:11], v43, s[20:23], s62 offen offset:1024 sc1\n\t"
    "v_mfma_f32_16x16x32_bf16 v[152:155], a[216:219], a[64:67], v[152:155]\n\t"
    "v_mfma_f32_16x16x32_bf16 v[152:155], a[152:155], a[68:71], v[152:155]\n\t"
    "ds_read_b64_tr_b16 a[252:253], v49 offset:33536\n\t"
    "v_mfma_f32_16x16x32_bf16 v[184:187], a[152:155], a[100:103], v[184:187]\n\t"
    "s_add_u32 m0, s14, 0x11800\n\t"
    "v_mfma_f32_16x16x32_bf16 v[184:187], a[216:219], a[96:99], v[184:187]\n\t"
    "buffer_load_dwordx4 v28, s[16:19], s69 offen lds\n\t"
    "v_mfma_f32_16x16x32_bf16 v[216:219], a[216:219], a[16:19], v[216:219]\n\t"
    "ds_read_b64_tr_b16 a[188:189], v49 offset:41728\n\t"
    "v_mfma_f32_16x16x32_bf16 v[216:219], a[152:155], a[20:23], v[216:219]\n\t"
    "v_mfma_f32_16x16x32_bf16 v[248:251], a[152:155], a[52:55], v[248:251]\n\t"
    "ds_read_b64_tr_b16 a[254:255], v49 offset:34560\n\t"
    "v_mfma_f32_16x16x32_bf16 v[248:251], a[216:219], a[48:51], v[248:251]\n\t"
    "v_mfma_f32_16x16x32_bf16 v[252:255], a[220:223], a[48:51], v[252:255]\n\t"
    "buffer_load_dwordx4 v[12:15], v44, s[20:23], s62 offen sc1\n\t"
    "v_mfma_f32_16x16x32_bf16 v[252:255], a[156:159], a[52:55], v[252:255]\n\t"
    "ds_read_b64_tr_b16 a[190:191], v49 offset:42752\n\t"
    "v_mfma_f32_16x16x32_bf16 v[220:223], a[156:159], a[20:23], v[220:223]\n\t"
    "v_mfma_f32_16x16x32_bf16 v[220:223], a[220:223], a[16:19], v[220:223]\n\t"
    "s_add_u32 m0, s14, 0x11c00\n\t"
    "v_mfma_f32_16x16x32_bf16 v[188:191], a[220:223], a[96:99], v[188:191]\n\t"
    "buffer_load_dwordx4 v28, s[16:19], s70 offen lds\n\t"
    "v_mfma_f32_16x16x32_bf16 v[188:191], a[156:159], a[100:103], v[188:191]\n\t"
    "v_mfma_f32_16x16x32_bf16 v[156:159], a[156:159], a[68:71], v[156:159]\n\t"
    "v_mfma_f32_16x16x32_bf16 v[156:159], a[220:223], a[64:67], v[156:159]\n\t"
    "v_mfma_f32_16x16x32_bf16 v[124:127], a[220:223], a[32:35], v[124:127]\n\t"
    "buffer_load_dwordx4 v[16:19], v44, s[20:23], s62 offen offset:1024 sc1\n\t"
    "v_mfma_f32_16x16x32_bf16 v[124:127], a[156:159], a[36:39], v[124:127]\n\t"
    "v_mfma_f32_16x16x32_bf16 v[92:95], a[156:159], a[4:7], v[92:95]\n\t"
    "v_mfma_f32_16x16x32_bf16 v[92:95], a[220:223], a[0:3], v[92:95]\n\t"
    "s_waitcnt vmcnt(20) lgkmcnt(0)\n\t"
    "s_barrier\n\t"
    "v_mfma_f32_16x16x32_bf16 v[64:67], a[224:227], a[8:11], v[64:67]\n\t"
    "s_bfe_u32 s62, s72, 0x30005\n\t"
    "s_bfe_u32 s75, s72, 0x10004\n\t"
    "s_lshl2_add_u32 s62, s62, s75\n\t"
    "v_mfma_f32_16x16x32_bf16 v[64:67], a[160:163], a[12:15], v[64:67]\n\t"
    "s_lshl_b32 s62, s62, 11\n\t"
    "s_add_u32 s62, s62, s25\n\t"
    "s_bfe_u32 s60, s72, 0x30011\n\t"
    "v_mfma_f32_16x16x32_bf16 v[96:99], a[160:163], a[44:47], v[96:99]\n\t"
    "s_bfe_u32 s75, s72, 0x10010\n\t"
    "s_lshl2_add_u32 s60, s60, s75\n\t"
    "s_lshl_b32 s60, s60, 11\n\t"
    "v_mfma_f32_16x16x32_bf16 v[96:99], a[224:227], a[40:43], v[96:99]\n\t"
    "ds_read_b64_tr_b16 a[192:193], v47 offset:0\n\t"
    "s_add_u32 s60, s60, s25\n\t"
    "s_lshl2_add_u32 s60, s60, s79\n\t"
    "s_lshl_b32 s60, s60, 4\n\t"
    "v_mfma_f32_16x16x32_bf16 v[128:131], a[224:227], a[72:75], v[128:131]\n\t"
    "s_add_u32 s61, s60, 0x8000\n\t"
    "v_mfma_f32_16x16x32_bf16 v[128:131], a[160:163], a[76:79], v[128:131]\n\t"
    "ds_read_b64_tr_b16 a[128:129], v47 offset:8192\n\t"
    "v_mfma_f32_16x16x32_bf16 v[160:163], a[160:163], a[108:111], v[160:163]\n\t"
    "v_mfma_f32_16x16x32_bf16 v[160:163], a[224:227], a[104:107], v[160:163]\n\t"
    "ds_read_b64_tr_b16 a[194:195], v47 offset:1024\n\t"
    "v_mfma_f32_16x16x32_bf16 v[192:195], a[224:227], a[24:27], v[192:195]\n\t"
    "v_mfma_f32_16x16x32_bf16 v[192:195], a[160:163], a[28:31], v[192:195]\n\t"
    "ds_read_b64_tr_b16 a[130:131], v47 offset:9216\n\t"
    "v_mfma_f32_16x16x32_bf16 v[224:227], a[160:163], a[60:63], v[224:227]\n\t"
    "buffer_load_dwordx4 a[0:3], v31, s[20:23], s62 offen sc1\n\t"
    "v_mfma_f32_16x16x32_bf16 v[224:227], a[224:227], a[56:59], v[224:227]\n\t"
    "v_mfma_f32_16x16x32_bf16 v[228:231], a[228:231], a[56:59], v[228:231]\n\t"
    "ds_read_b64_tr_b16 a[196:197], v50 offset:0\n\t"
    "v_mfma_f32_16x16x32_bf16 v[228:231], a[164:167], a[60:63], v[228:231]\n\t"
    "s_add_u32 m0, s14, 0x18000\n\t"
    "v_mfma_f32_16x16x32_bf16 v[196:199], a[164:167], a[28:31], v[196:199]\n\t"
    "buffer_load_dwordx4 v29, s[16:19], s60 offen lds\n\t"
    "v_mfma_f32_16x16x32_bf16 v[196:199], a[228:231], a[24:27], v[196:199]\n\t"
    "ds_read_b64_tr_b16 a[132:133], v50 offset:8192\n\t"
    "v_mfma_f32_16x16x32_bf16 v[164:167], a[228:231], a[104:107], v[164:167]\n\t"
    "v_mfma_f32_16x16x32_bf16 v[164:167], a[164:167], a[108:111], v[164:167]\n\t"
    "ds_read_b64_tr_b16 a[198:199], v50 offset:1024\n\t"
    "v_mfma_f32_16x16x32_bf16 v[132:135], a[164:167], a[76:79], v[132:135]\n\t"
    "buffer_load_dwordx4 a[4:7], v31, s[20:23], s62 offen offset:1024 sc1\n\t"
    "v_mfma_f32_16x16x32_bf16 v[132:135], a[228:231], a[72:75], v[132:135]\n\t"
    "v_mfma_f32_16x16x32_bf16 v[100:103], a[228:231], a[40:43], v[100:103]\n\t"
    "ds_read_b64_tr_b16 a[134:135], v50 offset:9216\n\t"
    "v_mfma_f32_16x16x32_bf16 v[100:103], a[164:167], a[44:47], v[100:103]\n\t"
    "v_mfma_f32_16x16x32_bf16 v[68:71], a[164:167], a[12:15], v[68:71]\n\t"
    "ds_read_b64_tr_b16 a[200:201], v47 offset:256\n\t"
    "s_add_u32 m0, s14, 0x18400\n\t"
    "v_mfma_f32_16x16x32_bf16 v[68:71], a[228:231], a[8:11], v[68:71]\n\t"
    "buffer_load_dwordx4 v29, s[16:19], s61 offen lds\n\t"
    "v_mfma_f32_16x16x32_bf16 v[72:75], a[232:235], a[8:11], v[72:75]\n\t"
    "v_mfma_f32_16x16x32_bf16 v[72:75], a[168:171], a[12:15], v[72:75]\n\t"
    "ds_read_b64_tr_b16 a[136:137], v47 offset:8448\n\t"
    "v_mfma_f32_16x16x32_bf16 v[104:107], a[168:171], a[44:47], v[104:107]\n\t"
    "v_mfma_f32_16x16x32_bf16 v[104:107], a[232:235], a[40:43], v[104:107]\n\t"
    "buffer_load_dwordx4 a[32:35], v40, s[20:23], s62 offen sc1\n\t"
    "v_mfma_f32_16x16x32_bf16 v[136:139], a[232:235], a[72:75], v[136:139]\n\t"
    "ds_read_b64_tr_b16 a[202:203], v47 offset:1280\n\t"
    "v_mfma_f32_16x16x32_bf16 v[136:139], a[168:171], a[76:79], v[136:139]\n\t"
    "v_mfma_f32_16x16x32_bf16 v[168:171], a[168:171], a[108:111], v[168:171]\n\t"
    "v_mfma_f32_16x16x32_bf16 v[168:171], a[232:235], a[104:107], v[168:171]\n\t"
    "buffer_load_dwordx4 a[36:39], v40, s[20:23], s62 offen offset:1024 sc1\n\t"
    "v_mfma_f32_16x16x32_bf16 v[200:203], a[232:235], a[24:27], v[200:203]\n\t"
    "ds_read_b64_tr_b16 a[138:139], v47 offset:9472\n\t"
    "v_mfma_f32_16x16x32_bf16 v[200:203], a[168:171], a[28:31], v[200:203]\n\t"
    "v_mfma_f32_16x16x32_bf16 v[232:235], a[168:171], a[60:63], v[232:235]\n\t"
    "ds_read_b64_tr_b16 a[204:205], v50 offset:256\n\t"
    "v_mfma_f32_16x16x32_bf16 v[232:235], a[232:235], a[56:59], v[232:235]\n\t"
    "s_add_u32 m0, s14, 0x18800\n\t"
    "v_mfma_f32_16x16x32_bf16 v[236:239], a[236:239], a[56:59], v[236:239]\n\t"
    "buffer_load_dwordx4 v26, s[16:19], s60 offen lds\n\t"
    "v_mfma_f32_16x16x32_bf16 v[236:239], a[172:175], a[60:63], v[236:239]\n\t"
    "ds_read_b64_tr_b16 a[140:141], v50 offset:8448\n\t"
    "v_mfma_f32_16x16x32_bf16 v[204:207], a[172:175], a[28:31], v[204:207]\n\t"
    "v_mfma_f32_16x16x32_bf16 v[204:207], a[236:239], a[24:27], v[204:207]\n\t"
    "ds_read_b64_tr_b16 a[206:207], v50 offset:1280\n\t"
    "v_mfma_f32_16x16x32_bf16 v[172:175], a[236:239], a[104:107], v[172:175]\n\t"
    "buffer_load_dwordx4 a[64:67], v41, s[20:23], s62 offen sc1\n\t"
    "v_mfma_f32_16x16x32_bf16 v[172:175], a[172:175], a[108:111], v[172:175]\n\t"
    "v_mfma_f32_16x16x32_bf16 v[140:143], a[172:175], a[76:79], v[140:143]\n\t"
    "ds_read_b64_tr_b16 a[142:143], v50 offset:9472\n\t"
    "v_mfma_f32_16x16x32_bf16 v[140:143], a[236:239], a[72:75], v[140:143]\n\t"
    "s_add_u32 m0, s14, 0x18c00\n\t"
    "v_mfma_f32_16x16x32_bf16 v[108:111], a[236:239], a[40:43], v[108:111]\n\t"
    "buffer_load_dwordx4 v26, s[16:19], s61 offen lds\n\t"
    "v_mfma_f32_16x16x32_bf16 v[108:111], a[172:175], a[44:47], v[108:111]\n\t"
    "ds_read_b64_tr_b16 a[208:209], v47 offset:512\n\t"
    "v_mfma_f32_16x16x32_bf16 v[76:79], a[172:175], a[12:15], v[76:79]\n\t"
    "v_mfma_f32_16x16x32_bf16 v[76:79], a[236:239], a[8:11], v[76:79]\n\t"
    "ds_read_b64_tr_b16 a[144:145], v47 offset:8704\n\t"
    "v_mfma_f32_16x16x32_bf16 v[80:83], a[240:243], a[8:11], v[80:83]\n\t"
    "v_mfma_f32_16x16x32_bf16 v[80:83], a[176:179], a[12:15], v[80:83]\n\t"
    "buffer_load_dwordx4 a[68:71], v41, s[20:23], s62 offen offset:1024 sc1\n\t"
    "v_mfma_f32_16x16x32_bf16 v[112:115], a[176:179], a[44:47], v[112:115]\n\t"
    "ds_read_b64_tr_b16 a[210:211], v47 offset:1536\n\t"
    "v_mfma_f32_16x16x32_bf16 v[112:115], a[240:243], a[40:43], v[112:115]\n\t"
    "v_mfma_f32_16x16x32_bf16 v[144:147], a[240:243], a[72:75], v[144:147]\n\t"
    "ds_read_b64_tr_b16 a[146:147], v47 offset:9728\n\t"
    "v_mfma_f32_16x16x32_bf16 v[144:147], a[176:179], a[76:79], v[144:147]\n\t"
    "buffer_load_dwordx4 a[96:99], v42, s[20:23], s62 offen sc1\n\t"
    "v_mfma_f32_16x16x32_bf16 v[176:179], a[176:179], a[108:111], v[176:179]\n\t"
    "v_mfma_f32_16x16x32_bf16 v[176:179], a[240:243], a[104:107], v[176:179]\n\t"
    "ds_read_b64_tr_b16 a[212:213], v50 offset:512\n\t"
    "v_mfma_f32_16x16x32_bf16 v[208:211], a[240:243], a[24:27], v[208:211]\n\t"
    "v_mfma_f32_16x16x32_bf16 v[208:211], a[176:179], a[28:31], v[208:211]\n\t"
    "ds_read_b64_tr_b16 a[148:149], v50 offset:8704\n\t"
    "s_add_u32 m0, s14, 0x19000\n\t"
    "v_mfma_f32_16x16x32_bf16 v[240:243], a[176:179], a[60:63], v[240:243]\n\t"
    "buffer_load_dwordx4 v27, s[16:19], s60 offen lds\n\t"
    "v_mfma_f32_16x16x32_bf16 v[240:243], a[240:243], a[56:59], v[240:243]\n\t"
    "v_mfma_f32_16x16x32_bf16 v[244:247], a[244:247], a[56:59], v[244:247]\n\t"
    "v_mfma_f32_16x16x32_bf16 v[244:247], a[180:183], a[60:63], v[244:247]\n\t"
    "ds_read_b64_tr_b16 a[214:215], v50 offset:1536\n\t"
    "v_mfma_f32_16x16x32_bf16 v[212:215], a[180:183], a[28:31], v[212:215]\n\t"
    "buffer_load_dwordx4 a[100:103], v42, s[20:23], s62 offen offset:1024 sc1\n\t"
    "v_mfma_f32_16x16x32_bf16 v[212:215], a[244:247], a[24:27], v[212:215]\n\t"
    "v_mfma_f32_16x16x32_bf16 v[180:183], a[244:247], a[104:107], v[180:183]\n\t"
    "ds_read_b64_tr_b16 a[150:151], v50 offset:9728\n\t"
    "v_mfma_f32_16x16x32_bf16 v[180:183], a[180:183], a[108:111], v[180:183]\n\t"
    "s_add_u32 m0, s14, 0x19400\n\t"
    "v_mfma_f32_16x16x32_bf16 v[148:151], a[180:183], a[76:79], v[148:151]\n\t"
    "buffer_load_dwordx4 v27, s[16:19], s61 offen lds\n\t"
    "v_mfma_f32_16x16x32_bf16 v[148:151], a[244:247], a[72:75], v[148:151]\n\t"
    "ds_read_b64_tr_b16 a[216:217], v47 offset:768\n\t"
    "v_mfma_f32_16x16x32_bf16 v[116:119], a[244:247], a[40:43], v[116:119]\n\t"
    "v_mfma_f32_16x16x32_bf16 v[116:119], a[180:183], a[44:47], v[116:119]\n\t"
    "ds_read_b64_tr_b16 a[152:153], v47 offset:8960\n\t"
    "v_mfma_f32_16x16x32_bf16 v[84:87], a[180:183], a[12:15], v[84:87]\n\t"
    "v_mfma_f32_16x16x32_bf16 v[84:87], a[244:247], a[8:11], v[84:87]\n\t"
    "buffer_load_dwordx4 a[16:19], v43, s[20:23], s62 offen sc1\n\t"
    "v_mfma_f32_16x16x32_bf16 v[88:91], a[248:251], a[8:11], v[88:91]\n\t"
    "ds_read_b64_tr_b16 a[218:219], v47 offset:1792\n\t"
    "v_mfma_f32_16x16x32_bf16 v[88:91], a[184:187], a[12:15], v[88:91]\n\t"
    "v_mfma_f32_16x16x32_bf16 v[120:123], a[184:187], a[44:47], v[120:123]\n\t"
    "ds_read_b64_tr_b16 a[154:155], v47 offset:9984\n\t"
    "v_mfma_f32_16x16x32_bf16 v[120:123], a[248:251], a[40:43], v[120:123]\n\t"
    "buffer_load_dwordx4 a[20:23], v43, s[20:23], s62 offen offset:1024 sc1\n\t"
    "v_mfma_f32_16x16x32_bf16 v[152:155], a[248:251], a[72:75], v[152:155]\n\t"
    "v_mfma_f32_16x16x32_bf16 v[152:155], a[184:187], a[76:79], v[152:155]\n\t"
    "ds_read_b64_tr_b16 a[220:221], v50 offset:768\n\t"
    "v_mfma_f32_16x16x32_bf16 v[184:187], a[184:187], a[108:111], v[184:187]\n\t"
    "s_add_u32 m0, s14, 0x19800\n\t"
    "v_mfma_f32_16x16x32_bf16 v[184:187], a[248:251], a[104:107], v[184:187]\n\t"
    "buffer_load_dwordx4 v28, s[16:19], s60 offen lds\n\t"
    "v_mfma_f32_16x16x32_bf16 v[216:219], a[248:251], a[24:27], v[216:219]\n\t"
    "ds_read_b64_tr_b16 a[156:157], v50 offset:8960\n\t"
    "v_mfma_f32_16x16x32_bf16 v[216:219], a[184:187], a[28:31], v[216:219]\n\t"
    "v_mfma_f32_16x16x32_bf16 v[248:251], a[184:187], a[60:63], v[248:251]\n\t"
    "ds_read_b64_tr_b16 a[222:223], v50 offset:1792\n\t"
    "v_mfma_f32_16x16x32_bf16 v[248:251], a[248:251], a[56:59], v[248:251]\n\t"
    "v_mfma_f32_16x16x32_bf16 v[252:255], a[252:255], a[56:59], v[252:255]\n\t"
    "buffer_load_dwordx4 a[48:51], v44, s[20:23], s62 offen sc1\n\t"
    "v_mfma_f32_16x16x32_bf16 v[252:255], a[188:191], a[60:63], v[252:255]\n\t"
    "ds_read_b64_tr_b16 a[158:159], v50 offset:9984\n\t"
    "v_mfma_f32_16x16x32_bf16 v[220:223], a[188:191], a[28:31], v[220:223]\n\t"
    "v_mfma_f32_16x16x32_bf16 v[220:223], a[252:255], a[24:27], v[220:223]\n\t"
    "s_add_u32 m0, s14, 0x19c00\n\t"
    "v_mfma_f32_16x16x32_bf16 v[188:191], a[252:255], a[104:107], v[188:191]\n\t"
    "buffer_load_dwordx4 v28, s[16:19], s61 offen lds\n\t"
    "v_mfma_f32_16x16x32_bf16 v[188:191], a[188:191], a[108:111], v[188:191]\n\t"
    "v_mfma_f32_16x16x32_bf16 v[156:159], a[188:191], a[76:79], v[156:159]\n\t"
    "v_mfma_f32_16x16x32_bf16 v[156:159], a[252:255], a[72:75], v[156:159]\n\t"
    "v_mfma_f32_16x16x32_bf16 v[124:127], a[252:255], a[40:43], v[124:127]\n\t"
    "buffer_load_dwordx4 a[52:55], v44, s[20:23], s62 offen offset:1024 sc1\n\t"
    "v_mfma_f32_16x16x32_bf16 v[124:127], a[188:191], a[44:47], v[124:127]\n\t"
    "v_mfma_f32_16x16x32_bf16 v[92:95], a[188:191], a[12:15], v[92:95]\n\t"
    "v_mfma_f32_16x16x32_bf16 v[92:95], a[252:255], a[8:11], v[92:95]\n\t"
    "s_waitcnt vmcnt(20) lgkmcnt(0)\n\t"
    "s_barrier\n\t"
    "v_mfma_f32_16x16x32_bf16 v[64:67], a[192:195], a[80:83], v[64:67]\n\t"
    "s_bfe_u32 s62, s72, 0x30009\n\t"
    "s_bfe_u32 s75, s72, 0x10008\n\t"
    "s_lshl2_add_u32 s62, s62, s75\n\t"
    "v_mfma_f32_16x16x32_bf16 v[64:67], a[128:131], a[84:87], v[64:67]\n\t"
    "s_lshl_b32 s62, s62, 11\n\t"
    "s_add_u32 s62, s62, s25\n\t"
    "s_bfe_u32 s69, s72, 0x30015\n\t"
    "v_mfma_f32_16x16x32_bf16 v[96:99], a[128:131], a[92:95], v[96:99]\n\t"
    "s_bfe_u32 s75, s72, 0x10014\n\t"
    "s_lshl2_add_u32 s69, s69, s75\n\t"
    "s_lshl_b32 s69, s69, 11\n\t"
    "v_mfma_f32_16x16x32_bf16 v[96:99], a[192:195], a[88:91], v[96:99]\n\t"
    "ds_read_b64_tr_b16 a[224:225], v45 offset:0\n\t"
    "s_add_u32 s69, s69, s25\n\t"
    "s_lshl2_add_u32 s69, s69, s79\n\t"
    "s_lshl_b32 s69, s69, 4\n\t"
    "v_mfma_f32_16x16x32_bf16 v[128:131], a[192:195], a[112:115], v[128:131]\n\t"
    "s_add_u32 s70, s69, 0x8000\n\t"
    "v_mfma_f32_16x16x32_bf16 v[128:131], a[128:131], a[116:119], v[128:131]\n\t"
    "ds_read_b64_tr_b16 a[160:161], v45 offset:8192\n\t"
    "v_mfma_f32_16x16x32_bf16 v[160:163], a[128:131], a[124:127], v[160:163]\n\t"
    "v_mfma_f32_16x16x32_bf16 v[160:163], a[192:195], a[120:123], v[160:163]\n\t"
    "ds_read_b64_tr_b16 a[226:227], v45 offset:1024\n\t"
    "v_mfma_f32_16x16x32_bf16 v[192:195], a[192:195], v[4:7], v[192:195]\n\t"
    "v_mfma_f32_16x16x32_bf16 v[192:195], a[128:131], v[8:11], v[192:195]\n\t"
    "ds_read_b64_tr_b16 a[162:163], v45 offset:9216\n\t"
    "v_mfma_f32_16x16x32_bf16 v[224:227], a[128:131], v[16:19], v[224:227]\n\t"
    "buffer_load_dwordx4 a[8:11], v31, s[20:23], s62 offen sc1\n\t"
    "v_mfma_f32_16x16x32_bf16 v[224:227], a[192:195], v[12:15], v[224:227]\n\t"
    "v_mfma_f32_16x16x32_bf16 v[228:231], a[196:199], v[12:15], v[228:231]\n\t"
    "ds_read_b64_tr_b16 a[228:229], v48 offset:0\n\t"
    "v_mfma_f32_16x16x32_bf16 v[228:231], a[132:135], v[16:19], v[228:231]\n\t"
    "s_add_u32 m0, s14, 0x20000\n\t"
    "v_mfma_f32_16x16x32_bf16 v[196:199], a[132:135], v[8:11], v[196:199]\n\t"
    "buffer_load_dwordx4 v29, s[16:19], s69 offen lds\n\t"
    "v_mfma_f32_16x16x32_bf16 v[196:199], a[196:199], v[4:7], v[196:199]\n\t"
    "ds_read_b64_tr_b16 a[164:165], v48 offset:8192\n\t"
    "v_mfma_f32_16x16x32_bf16 v[164:167], a[196:199], a[120:123], v[164:167]\n\t"
    "v_mfma_f32_16x16x32_bf16 v[164:167], a[132:135], a[124:127], v[164:167]\n\t"
    "ds_read_b64_tr_b16 a[230:231], v48 offset:1024\n\t"
    "v_mfma_f32_16x16x32_bf16 v[132:135], a[132:135], a[116:119], v[132:135]\n\t"
    "buffer_load_dwordx4 a[12:15], v31, s[20:23], s62 offen offset:1024 sc1\n\t"
    "v_mfma_f32_16x16x32_bf16 v[132:135], a[196:199], a[112:115], v[132:135]\n\t"
    "v_mfma_f32_16x16x32_bf16 v[100:103], a[196:199], a[88:91], v[100:103]\n\t"
    "ds_read_b64_tr_b16 a[166:167], v48 offset:9216\n\t"
    "v_mfma_f32_16x16x32_bf16 v[100:103], a[132:135], a[92:95], v[100:103]\n\t"
    "v_mfma_f32_16x16x32_bf16 v[68:71], a[132:135], a[84:87], v[68:71]\n\t"
    "ds_read_b64_tr_b16 a[232:233], v45 offset:256\n\t"
    "s_add_u32 m0, s14, 0x20400\n\t"
    "v_mfma_f32_16x16x32_bf16 v[68:71], a[196:199], a[80:83], v[68:71]\n\t"
    "buffer_load_dwordx4 v29, s[16:19], s70 offen lds\n\t"
    "v_mfma_f32_16x16x32_bf16 v[72:75], a[200:203], a[80:83], v[72:75]\n\t"
    "v_mfma_f32_16x16x32_bf16 v[72:75], a[136:139], a[84:87], v[72:75]\n\t"
    "ds_read_b64_tr_b16 a[168:169], v45 offset:8448\n\t"
    "v_mfma_f32_16x16x32_bf16 v[104:107], a[136:139], a[92:95], v[104:107]\n\t"
    "v_mfma_f32_16x16x32_bf16 v[104:107], a[200:203], a[88:91], v[104:107]\n\t"
    "buffer_load_dwordx4 a[40:43], v40, s[20:23], s62 offen sc1\n\t"
    "v_mfma_f32_16x16x32_bf16 v[136:139], a[200:203], a[112:115], v[136:139]\n\t"
    "ds_read_b64_tr_b16 a[234:235], v45 offset:1280\n\t"
    "v_mfma_f32_16x16x32_bf16 v[136:139], a[136:139], a[116:119], v[136:139]\n\t"
    "v_mfma_f32_16x16x32_bf16 v[168:171], a[136:139], a[124:127], v[168:171]\n\t"
    "v_mfma_f32_16x16x32_bf16 v[168:171], a[200:203], a[120:123], v[168:171]\n\t"
    "buffer_load_dwordx4 a[44:47], v40, s[20:23], s62 offen offset:1024 sc1\n\t"
    "v_mfma_f32_16x16x32_bf16 v[200:203], a[200:203], v[4:7], v[200:203]\n\t"
    "ds_read_b64_tr_b16 a[170:171], v45 offset:9472\n\t"
    "v_mfma_f32_16x16x32_bf16 v[200:203], a[136:139], v[8:11], v[200:203]\n\t"
    "v_mfma_f32_16x16x32_bf16 v[232:235], a[136:139], v[16:19], v[232:235]\n\t"
    "ds_read_b64_tr_b16 a[236:237], v48 offset:256\n\t"
    "v_mfma_f32_16x16x32_bf16 v[232:235], a[200:203], v[12:15], v[232:235]\n\t"
    "s_add_u32 m0, s14, 0x20800\n\t"
    "v_mfma_f32_16x16x32_bf16 v[236:239], a[204:207], v[12:15], v[236:239]\n\t"
    "buffer_load_dwordx4 v26, s[16:19], s69 offen lds\n\t"
    "v_mfma_f32_16x16x32_bf16 v[236:239], a[140:143], v[16:19], v[236:239]\n\t"
    "ds_read_b64_tr_b16 a[172:173], v48 offset:8448\n\t"
    "v_mfma_f32_16x16x32_bf16 v[204:207], a[140:143], v[8:11], v[204:207]\n\t"
    "v_mfma_f32_16x16x32_bf16 v[204:207], a[204:207], v[4:7], v[204:207]\n\t"
    "ds_read_b64_tr_b16 a[238:239], v48 offset:1280\n\t"
    "v_mfma_f32_16x16x32_bf16 v[172:175], a[204:207], a[120:123], v[172:175]\n\t"
    "buffer_load_dwordx4 a[72:75], v41, s[20:23], s62 offen sc1\n\t"
    "v_mfma_f32_16x16x32_bf16 v[172:175], a[140:143], a[124:127], v[172:175]\n\t"
    "v_mfma_f32_16x16x32_bf16 v[140:143], a[140:143], a[116:119], v[140:143]\n\t"
    "ds_read_b64_tr_b16 a[174:175], v48 offset:9472\n\t"
    "v_mfma_f32_16x16x32_bf16 v[140:143], a[204:207], a[112:115], v[140:143]\n\t"
    "s_add_u32 m0, s14, 0x20c00\n\t"
    "v_mfma_f32_16x16x32_bf16 v[108:111], a[204:207], a[88:91], v[108:111]\n\t"
    "buffer_load_dwordx4 v26, s[16:19], s70 offen lds\n\t"
    "v_mfma_f32_16x16x32_bf16 v[108:111], a[140:143], a[92:95], v[108:111]\n\t"
    "ds_read_b64_tr_b16 a[240:241], v45 offset:512\n\t"
    "v_mfma_f32_16x16x32_bf16 v[76:79], a[140:143], a[84:87], v[76:79]\n\t"
    "v_mfma_f32_16x16x32_bf16 v[76:79], a[204:207], a[80:83], v[76:79]\n\t"
    "ds_read_b64_tr_b16 a[176:177], v45 offset:8704\n\t"
    "v_mfma_f32_16x16x32_bf16 v[80:83], a[208:211], a[80:83], v[80:83]\n\t"
    "v_mfma_f32_16x16x32_bf16 v[80:83], a[144:147], a[84:87], v[80:83]\n\t"
    "buffer_load_dwordx4 a[76:79], v41, s[20:23], s62 offen offset:1024 sc1\n\t"
    "v_mfma_f32_16x16x32_bf16 v[112:115], a[144:147], a[92:95], v[112:115]\n\t"
    "ds_read_b64_tr_b16 a[242:243], v45 offset:1536\n\t"
    "v_mfma_f32_16x16x32_bf16 v[112:115], a[208:211], a[88:91], v[112:115]\n\t"
    "v_mfma_f32_16x16x32_bf16 v[144:147], a[208:211], a[112:115], v[144:147]\n\t"
    "ds_read_b64_tr_b16 a[178:179], v45 offset:9728\n\t"
    "v_mfma_f32_16x16x32_bf16 v[144:147], a[144:147], a[116:119], v[144:147]\n\t"
    "buffer_load_dwordx4 a[104:107], v42, s[20:23], s62 offen sc1\n\t"
    "v_mfma_f32_16x16x32_bf16 v[176:179], a[144:147], a[124:127], v[176:179]\n\t"
    "v_mfma_f32_16x16x32_bf16 v[176:179], a[208:211], a[120:123], v[176:179]\n\t"
    "ds_read_b64_tr_b16 a[244:245], v48 offset:512\n\t"
    "v_mfma_f32_16x16x32_bf16 v[208:211], a[208:211], v[4:7], v[208:211]\n\t"
    "v_mfma_f32_16x16x32_bf16 v[208:211], a[144:147], v[8:11], v[208:211]\n\t"
    "ds_read_b64_tr_b16 a[180:181], v48 offset:8704\n\t"
    "s_add_u32 m0, s14, 0x21000\n\t"
    "v_mfma_f32_16x16x32_bf16 v[240:243], a[144:147], v[16:19], v[240:243]\n\t"
    "buffer_load_dwordx4 v27, s[16:19], s69 offen lds\n\t"
    "v_mfma_f32_16x16x32_bf16 v[240:243], a[208:211], v[12:15], v[240:243]\n\t"
    "v_mfma_f32_16x16x32_bf16 v[244:247], a[212:215], v[12:15], v[244:247]\n\t"
    "v_mfma_f32_16x16x32_bf16 v[244:247], a[148:151], v[16:19], v[244:247]\n\t"
    "ds_read_b64_tr_b16 a[246:247], v48 offset:1536\n\t"
    "v_mfma_f32_16x16x32_bf16 v[212:215], a[148:151], v[8:11], v[212:215]\n\t"
    "buffer_load_dwordx4 a[108:111], v42, s[20:23], s62 offen offset:1024 sc1\n\t"
    "v_mfma_f32_16x16x32_bf16 v[212:215], a[212:215], v[4:7], v[212:215]\n\t"
    "v_mfma_f32_16x16x32_bf16 v[180:183], a[212:215], a[120:123], v[180:183]\n\t"
    "ds_read_b64_tr_b16 a[182:183], v48 offset:9728\n\t"
    "v_mfma_f32_16x16x32_bf16 v[180:183], a[148:151], a[124:127], v[180:183]\n\t"
    "s_add_u32 m0, s14, 0x21400\n\t"
    "v_mfma_f32_16x16x32_bf16 v[148:151], a[148:151], a[116:119], v[148:151]\n\t"
    "buffer_load_dwordx4 v27, s[16:19], s70 offen lds\n\t"
    "v_mfma_f32_16x16x32_bf16 v[148:151], a[212:215], a[112:115], v[148:151]\n\t"
    "ds_read_b64_tr_b16 a[248:249], v45 offset:768\n\t"
    "v_mfma_f32_16x16x32_bf16 v[116:119], a[212:215], a[88:91], v[116:119]\n\t"
    "v_mfma_f32_16x16x32_bf16 v[116:119], a[148:151], a[92:95], v[116:119]\n\t"
    "ds_read_b64_tr_b16 a[184:185], v45 offset:8960\n\t"
    "v_mfma_f32_16x16x32_bf16 v[84:87], a[148:151], a[84:87], v[84:87]\n\t"
    "v_mfma_f32_16x16x32_bf16 v[84:87], a[212:215], a[80:83], v[84:87]\n\t"
    "buffer_load_dwordx4 a[24:27], v43, s[20:23], s62 offen sc1\n\t"
    "v_mfma_f32_16x16x32_bf16 v[88:91], a[216:219], a[80:83], v[88:91]\n\t"
    "ds_read_b64_tr_b16 a[250:251], v45 offset:1792\n\t"
    "v_mfma_f32_16x16x32_bf16 v[88:91], a[152:155], a[84:87], v[88:91]\n\t"
    "v_mfma_f32_16x16x32_bf16 v[120:123], a[152:155], a[92:95], v[120:123]\n\t"
    "ds_read_b64_tr_b16 a[186:187], v45 offset:9984\n\t"
    "v_mfma_f32_16x16x32_bf16 v[120:123], a[216:219], a[88:91], v[120:123]\n\t"
    "buffer_load_dwordx4 a[28:31], v43, s[20:23], s62 offen offset:1024 sc1\n\t"
    "v_mfma_f32_16x16x32_bf16 v[152:155], a[216:219], a[112:115], v[152:155]\n\t"
    "v_mfma_f32_16x16x32_bf16 v[152:155], a[152:155], a[116:119], v[152:155]\n\t"
    "ds_read_b64_tr_b16 a[252:253], v48 offset:768\n\t"
    "v_mfma_f32_16x16x32_bf16 v[184:187], a[152:155], a[124:127], v[184:187]\n\t"
    "s_add_u32 m0, s14, 0x21800\n\t"
    "v_mfma_f32_16x16x32_bf16 v[184:187], a[216:219], a[120:123], v[184:187]\n\t"
    "buffer_load_dwordx4 v28, s[16:19], s69 offen lds\n\t"
    "v_mfma_f32_16x16x32_bf16 v[216:219], a[216:219], v[4:7], v[216:219]\n\t"
    "ds_read_b64_tr_b16 a[188:189], v48 offset:8960\n\t"
    "v_mfma_f32_16x16x32_bf16 v[216:219], a[152:155], v[8:11], v[216:219]\n\t"
    "v_mfma_f32_16x16x32_bf16 v[248:251], a[152:155], v[16:19], v[248:251]\n\t"
    "ds_read_b64_tr_b16 a[254:255], v48 offset:1792\n\t"
    "v_mfma_f32_16x16x32_bf16 v[248:251], a[216:219], v[12:15], v[248:251]\n\t"
    "v_mfma_f32_16x16x32_bf16 v[252:255], a[220:223], v[12:15], v[252:255]\n\t"
    "buffer_load_dwordx4 a[56:59], v44, s[20:23], s62 offen sc1\n\t"
    "v_mfma_f32_16x16x32_bf16 v[252:255], a[156:159], v[16:19], v[252:255]\n\t"
    "ds_read_b64_tr_b16 a[190:191], v48 offset:9984\n\t"
    "v_mfma_f32_16x16x32_bf16 v[220:223], a[156:159], v[8:11], v[220:223]\n\t"
    "v_mfma_f32_16x16x32_bf16 v[220:223], a[220:223], v[4:7], v[220:223]\n\t"
    "s_add_u32 m0, s14, 0x21c00\n\t"
    "v_mfma_f32_16x16x32_bf16 v[188:191], a[220:223], a[120:123], v[188:191]\n\t"
    "buffer_load_dwordx4 v28, s[16:19], s70 offen lds\n\t"
    "v_mfma_f32_16x16x32_bf16 v[188:191], a[156:159], a[124:127], v[188:191]\n\t"
    "v_mfma_f32_16x16x32_bf16 v[156:159], a[156:159], a[116:119], v[156:159]\n\t"
    "v_mfma_f32_16x16x32_bf16 v[156:159], a[220:223], a[112:115], v[156:159]\n\t"
    "v_mfma_f32_16x16x32_bf16 v[124:127], a[220:223], a[88:91], v[124:127]\n\t"
    "buffer_load_dwordx4 a[60:63], v44, s[20:23], s62 offen offset:1024 sc1\n\t"
    "v_mfma_f32_16x16x32_bf16 v[124:127], a[156:159], a[92:95], v[124:127]\n\t"
    "v_mfma_f32_16x16x32_bf16 v[92:95], a[156:159], a[84:87], v[92:95]\n\t"
    "v_mfma_f32_16x16x32_bf16 v[92:95], a[220:223], a[80:83], v[92:95]\n\t"
    "s_waitcnt vmcnt(20) lgkmcnt(0)\n\t"
    "s_barrier\n\t"
    "v_mfma_f32_16x16x32_bf16 v[64:67], a[224:227], a[0:3], v[64:67]\n\t"
    "s_bfe_u32 s62, s72, 0x3000d\n\t"
    "s_bfe_u32 s75, s72, 0x1000c\n\t"
    "s_lshl2_add_u32 s62, s62, s75\n\t"
    "v_mfma_f32_16x16x32_bf16 v[64:67], a[160:163], a[4:7], v[64:67]\n\t"
    "s_lshl_b32 s62, s62, 11\n\t"
    "s_add_u32 s62, s62, s25\n\t"
    "s_bfe_u32 s60, s72, 0x30019\n\t"
    "v_mfma_f32_16x16x32_bf16 v[96:99], a[160:163], a[36:39], v[96:99]\n\t"
    "s_bfe_u32 s75, s72, 0x10018\n\t"
    "s_lshl2_add_u32 s60, s60, s75\n\t"
    "s_lshl_b32 s60, s60, 11\n\t"
    "v_mfma_f32_16x16x32_bf16 v[96:99], a[224:227], a[32:35], v[96:99]\n\t"
    "ds_read_b64_tr_b16 a[192:193], v45 offset:32768\n\t"
    "s_add_u32 s60, s60, s25\n\t"
    "s_lshl2_add_u32 s60, s60, s79\n\t"
    "s_lshl_b32 s60, s60, 4\n\t"
    "v_mfma_f32_16x16x32_bf16 v[128:131], a[224:227], a[64:67], v[128:131]\n\t"
    "s_add_u32 s61, s60, 0x8000\n\t"
    "v_mfma_f32_16x16x32_bf16 v[128:131], a[160:163], a[68:71], v[128:131]\n\t"
    "ds_read_b64_tr_b16 a[128:129], v45 offset:40960\n\t"
    "v_mfma_f32_16x16x32_bf16 v[160:163], a[160:163], a[100:103], v[160:163]\n\t"
    "v_mfma_f32_16x16x32_bf16 v[160:163], a[224:227], a[96:99], v[160:163]\n\t"
    "ds_read_b64_tr_b16 a[194:195], v45 offset:33792\n\t"
    "v_mfma_f32_16x16x32_bf16 v[192:195], a[224:227], a[16:19], v[192:195]\n\t"
    "v_mfma_f32_16x16x32_bf16 v[192:195], a[160:163], a[20:23], v[192:195]\n\t"
    "ds_read_b64_tr_b16 a[130:131], v45 offset:41984\n\t"
    "v_mfma_f32_16x16x32_bf16 v[224:227], a[160:163], a[52:55], v[224:227]\n\t"
    "buffer_load_dwordx4 a[80:83], v31, s[20:23], s62 offen sc1\n\t"
    "v_mfma_f32_16x16x32_bf16 v[224:227], a[224:227], a[48:51], v[224:227]\n\t"
    "v_mfma_f32_16x16x32_bf16 v[228:231], a[228:231], a[48:51], v[228:231]\n\t"
    "ds_read_b64_tr_b16 a[196:197], v48 offset:32768\n\t"
    "v_mfma_f32_16x16x32_bf16 v[228:231], a[164:167], a[52:55], v[228:231]\n\t"
    "s_add_u32 m0, s14, 0\n\t"
    "v_mfma_f32_16x16x32_bf16 v[196:199], a[164:167], a[20:23], v[196:199]\n\t"
    "buffer_load_dwordx4 v29, s[16:19], s60 offen lds\n\t"
    "v_mfma_f32_16x16x32_bf16 v[196:199], a[228:231], a[16:19], v[196:199]\n\t"
    "ds_read_b64_tr_b16 a[132:133], v48 offset:40960\n\t"
    "v_mfma_f32_16x16x32_bf16 v[164:167], a[228:231], a[96:99], v[164:167]\n\t"
    "v_mfma_f32_16x16x32_bf16 v[164:167], a[164:167], a[100:103], v[164:167]\n\t"
    "ds_read_b64_tr_b16 a[198:199], v48 offset:33792\n\t"
    "v_mfma_f32_16x16x32_bf16 v[132:135], a[164:167], a[68:71], v[132:135]\n\t"
    "buffer_load_dwordx4 a[84:87], v31, s[20:23], s62 offen offset:1024 sc1\n\t"
    "v_mfma_f32_16x16x32_bf16 v[132:135], a[228:231], a[64:67], v[132:135]\n\t"
    "v_mfma_f32_16x16x32_bf16 v[100:103], a[228:231], a[32:35], v[100:103]\n\t"
    "ds_read_b64_tr_b16 a[134:135], v48 offset:41984\n\t"
    "v_mfma_f32_16x16x32_bf16 v[100:103], a[164:167], a[36:39], v[100:103]\n\t"
    "v_mfma_f32_16x16x32_bf16 v[68:71], a[164:167], a[4:7], v[68:71]\n\t"
    "ds_read_b64_tr_b16 a[200:201], v45 offset:33024\n\t"
    "s_add_u32 m0, s14, 0x400\n\t"
    "v_mfma_f32_16x16x32_bf16 v[68:71], a[228:231], a[0:3], v[68:71]\n\t"
    "buffer_load_dwordx4 v29, s[16:19], s61 offen lds\n\t"
    "v_mfma_f32_16x16x32_bf16 v[72:75], a[232:235], a[0:3], v[72:75]\n\t"
    "v_mfma_f32_16x16x32_bf16 v[72:75], a[168:171], a[4:7], v[72:75]\n\t"
    "ds_read_b64_tr_b16 a[136:137], v45 offset:41216\n\t"
    "v_mfma_f32_16x16x32_bf16 v[104:107], a[168:171], a[36:39], v[104:107]\n\t"
    "v_mfma_f32_16x16x32_bf16 v[104:107], a[232:235], a[32:35], v[104:107]\n\t"
    "buffer_load_dwordx4 a[88:91], v40, s[20:23], s62 offen sc1\n\t"
    "v_mfma_f32_16x16x32_bf16 v[136:139], a[232:235], a[64:67], v[136:139]\n\t"
    "ds_read_b64_tr_b16 a[202:203], v45 offset:34048\n\t"
    "v_mfma_f32_16x16x32_bf16 v[136:139], a[168:171], a[68:71], v[136:139]\n\t"
    "v_mfma_f32_16x16x32_bf16 v[168:171], a[168:171], a[100:103], v[168:171]\n\t"
    "v_mfma_f32_16x16x32_bf16 v[168:171], a[232:235], a[96:99], v[168:171]\n\t"
    "buffer_load_dwordx4 a[92:95], v40, s[20:23], s62 offen offset:1024 sc1\n\t"
    "v_mfma_f32_16x16x32_bf16 v[200:203], a[232:235], a[16:19], v[200:203]\n\t"
    "ds_read_b64_tr_b16 a[138:139], v45 offset:42240\n\t"
    "v_mfma_f32_16x16x32_bf16 v[200:203], a[168:171], a[20:23], v[200:203]\n\t"
    "v_mfma_f32_16x16x32_bf16 v[232:235], a[168:171], a[52:55], v[232:235]\n\t"
    "ds_read_b64_tr_b16 a[204:205], v48 offset:33024\n\t"
    "v_mfma_f32_16x16x32_bf16 v[232:235], a[232:235], a[48:51], v[232:235]\n\t"
    "s_add_u32 m0, s14, 0x800\n\t"
    "v_mfma_f32_16x16x32_bf16 v[236:239], a[236:239], a[48:51], v[236:239]\n\t"
    "buffer_load_dwordx4 v26, s[16:19], s60 offen lds\n\t"
    "v_mfma_f32_16x16x32_bf16 v[236:239], a[172:175], a[52:55], v[236:239]\n\t"
    "ds_read_b64_tr_b16 a[140:141], v48 offset:41216\n\t"
    "v_mfma_f32_16x16x32_bf16 v[204:207], a[172:175], a[20:23], v[204:207]\n\t"
    "v_mfma_f32_16x16x32_bf16 v[204:207], a[236:239], a[16:19], v[204:207]\n\t"
    "ds_read_b64_tr_b16 a[206:207], v48 offset:34048\n\t"
    "v_mfma_f32_16x16x32_bf16 v[172:175], a[236:239], a[96:99], v[172:175]\n\t"
    "buffer_load_dwordx4 a[112:115], v41, s[20:23], s62 offen sc1\n\t"
    "v_mfma_f32_16x16x32_bf16 v[172:175], a[172:175], a[100:103], v[172:175]\n\t"
    "v_mfma_f32_16x16x32_bf16 v[140:143], a[172:175], a[68:71], v[140:143]\n\t"
    "ds_read_b64_tr_b16 a[142:143], v48 offset:42240\n\t"
    "v_mfma_f32_16x16x32_bf16 v[140:143], a[236:239], a[64:67], v[140:143]\n\t"
    "s_add_u32 m0, s14, 0xc00\n\t"
    "v_mfma_f32_16x16x32_bf16 v[108:111], a[236:239], a[32:35], v[108:111]\n\t"
    "buffer_load_dwordx4 v26, s[16:19], s61 offen lds\n\t"
    "v_mfma_f32_16x16x32_bf16 v[108:111], a[172:175], a[36:39], v[108:111]\n\t"
    "ds_read_b64_tr_b16 a[208:209], v45 offset:33280\n\t"
    "v_mfma_f32_16x16x32_bf16 v[76:79], a[172:175], a[4:7], v[76:79]\n\t"
    "v_mfma_f32_16x16x32_bf16 v[76:79], a[236:239], a[0:3], v[76:79]\n\t"
    "ds_read_b64_tr_b16 a[144:145], v45 offset:41472\n\t"
    "v_mfma_f32_16x16x32_bf16 v[80:83], a[240:243], a[0:3], v[80:83]\n\t"
    "v_mfma_f32_16x16x32_bf16 v[80:83], a[176:179], a[4:7], v[80:83]\n\t"
    "buffer_load_dwordx4 a[116:119], v41, s[20:23], s62 offen offset:1024 sc1\n\t"
    "v_mfma_f32_16x16x32_bf16 v[112:115], a[176:179], a[36:39], v[112:115]\n\t"
    "ds_read_b64_tr_b16 a[210:211], v45 offset:34304\n\t"
    "v_mfma_f32_16x16x32_bf16 v[112:115], a[240:243], a[32:35], v[112:115]\n\t"
    "v_mfma_f32_16x16x32_bf16 v[144:147], a[240:243], a[64:67], v[144:147]\n\t"
    "ds_read_b64_tr_b16 a[146:147], v45 offset:42496\n\t"
    "v_mfma_f32_16x16x32_bf16 v[144:147], a[176:179], a[68:71], v[144:147]\n\t"
    "buffer_load_dwordx4 a[120:123], v42, s[20:23], s62 offen sc1\n\t"
    "v_mfma_f32_16x16x32_bf16 v[176:179], a[176:179], a[100:103], v[176:179]\n\t"
    "v_mfma_f32_16x16x32_bf16 v[176:179], a[240:243], a[96:99], v[176:179]\n\t"
    "ds_read_b64_tr_b16 a[212:213], v48 offset:33280\n\t"
    "v_mfma_f32_16x16x32_bf16 v[208:211], a[240:243], a[16:19], v[208:211]\n\t"
    "v_mfma_f32_16x16x32_bf16 v[208:211], a[176:179], a[20:23], v[208:211]\n\t"
    "ds_read_b64_tr_b16 a[148:149], v48 offset:41472\n\t"
    "s_add_u32 m0, s14, 0x1000\n\t"
    "v_mfma_f32_16x16x32_bf16 v[240:243], a[176:179], a[52:55], v[240:243]\n\t"
    "buffer_load_dwordx4 v27, s[16:19], s60 offen lds\n\t"
    "v_mfma_f32_16x16x32_bf16 v[240:243], a[240:243], a[48:51], v[240:243]\n\t"
    "v_mfma_f32_16x16x32_bf16 v[244:247], a[244:247], a[48:51], v[244:247]\n\t"
    "v_mfma_f32_16x16x32_bf16 v[244:247], a[180:183], a[52:55], v[244:247]\n\t"
    "ds_read_b64_tr_b16 a[214:215], v48 offset:34304\n\t"
    "v_mfma_f32_16x16x32_bf16 v[212:215], a[180:183], a[20:23], v[212:215]\n\t"
    "buffer_load_dwordx4 a[124:127], v42, s[20:23], s62 offen offset:1024 sc1\n\t"
    "v_mfma_f32_16x16x32_bf16 v[212:215], a[244:247], a[16:19], v[212:215]\n\t"
    "v_mfma_f32_16x16x32_bf16 v[180:183], a[244:247], a[96:99], v[180:183]\n\t"
    "ds_read_b64_tr_b16 a[150:151], v48 offset:42496\n\t"
    "v_mfma_f32_16x16x32_bf16 v[180:183], a[180:183], a[100:103], v[180:183]\n\t"
    "s_add_u32 m0, s14, 0x1400\n\t"
    "v_mfma_f32_16x16x32_bf16 v[148:151], a[180:183], a[68:71], v[148:151]\n\t"
    "buffer_load_dwordx4 v27, s[16:19], s61 offen lds\n\t"
    "v_mfma_f32_16x16x32_bf16 v[148:151], a[244:247], a[64:67], v[148:151]\n\t"
    "ds_read_b64_tr_b16 a[216:217], v45 offset:33536\n\t"
    "v_mfma_f32_16x16x32_bf16 v[116:119], a[244:247], a[32:35], v[116:119]\n\t"
    "v_mfma_f32_16x16x32_bf16 v[116:119], a[180:183], a[36:39], v[116:119]\n\t"
    "ds_read_b64_tr_b16 a[152:153], v45 offset:41728\n\t"
    "v_mfma_f32_16x16x32_bf16 v[84:87], a[180:183], a[4:7], v[84:87]\n\t"
    "v_mfma_f32_16x16x32_bf16 v[84:87], a[244:247], a[0:3], v[84:87]\n\t"
    "buffer_load_dwordx4 v[4:7], v43, s[20:23], s62 offen sc1\n\t"
    "v_mfma_f32_16x16x32_bf16 v[88:91], a[248:251], a[0:3], v[88:91]\n\t"
    "ds_read_b64_tr_b16 a[218:219], v45 offset:34560\n\t"
    "v_mfma_f32_16x16x32_bf16 v[88:91], a[184:187], a[4:7], v[88:91]\n\t"
    "v_mfma_f32_16x16x32_bf16 v[120:123], a[184:187], a[36:39], v[120:123]\n\t"
    "ds_read_b64_tr_b16 a[154:155], v45 offset:42752\n\t"
    "v_mfma_f32_16x16x32_bf16 v[120:123], a[248:251], a[32:35], v[120:123]\n\t"
    "buffer_load_dwordx4 v[8:11], v43, s[20:23], s62 offen offset:1024 sc1\n\t"
    "v_mfma_f32_16x16x32_bf16 v[152:155], a[248:251], a[64:67], v[152:155]\n\t"
    "v_mfma_f32_16x16x32_bf16 v[152:155], a[184:187], a[68:71], v[152:155]\n\t"
    "ds_read_b64_tr_b16 a[220:221], v48 offset:33536\n\t"
    "v_mfma_f32_16x16x32_bf16 v[184:187], a[184:187], a[100:103], v[184:187]\n\t"
    "s_add_u32 m0, s14, 0x1800\n\t"
    "v_mfma_f32_16x16x32_bf16 v[184:187], a[248:251], a[96:99], v[184:187]\n\t"
    "buffer_load_dwordx4 v28, s[16:19], s60 offen lds\n\t"
    "v_mfma_f32_16x16x32_bf16 v[216:219], a[248:251], a[16:19], v[216:219]\n\t"
    "ds_read_b64_tr_b16 a[156:157], v48 offset:41728\n\t"
    "v_mfma_f32_16x16x32_bf16 v[216:219], a[184:187], a[20:23], v[216:219]\n\t"
    "v_mfma_f32_16x16x32_bf16 v[248:251], a[184:187], a[52:55], v[248:251]\n\t"
    "ds_read_b64_tr_b16 a[222:223], v48 offset:34560\n\t"
    "v_mfma_f32_16x16x32_bf16 v[248:251], a[248:251], a[48:51], v[248:251]\n\t"
    "v_mfma_f32_16x16x32_bf16 v[252:255], a[252:255], a[48:51], v[252:255]\n\t"
    "buffer_load_dwordx4 v[12:15], v44, s[20:23], s62 offen sc1\n\t"
    "v_mfma_f32_16x16x32_bf16 v[252:255], a[188:191], a[52:55], v[252:255]\n\t"
    "ds_read_b64_tr_b16 a[158:159], v48 offset:42752\n\t"
    "v_mfma_f32_16x16x32_bf16 v[220:223], a[188:191], a[20:23], v[220:223]\n\t"
    "v_mfma_f32_16x16x32_bf16 v[220:223], a[252:255], a[16:19], v[220:223]\n\t"
    "s_add_u32 m0, s14, 0x1c00\n\t"
    "v_mfma_f32_16x16x32_bf16 v[188:191], a[252:255], a[96:99], v[188:191]\n\t"
    "buffer_load_dwordx4 v28, s[16:19], s61 offen lds\n\t"
    "v_mfma_f32_16x16x32_bf16 v[188:191], a[188:191], a[100:103], v[188:191]\n\t"
    "v_mfma_f32_16x16x32_bf16 v[156:159], a[188:191], a[68:71], v[156:159]\n\t"
    "v_mfma_f32_16x16x32_bf16 v[156:159], a[252:255], a[64:67], v[156:159]\n\t"
    "v_mfma_f32_16x16x32_bf16 v[124:127], a[252:255], a[32:35], v[124:127]\n\t"
    "buffer_load_dwordx4 v[16:19], v44, s[20:23], s62 offen offset:1024 sc1\n\t"
    "v_mfma_f32_16x16x32_bf16 v[124:127], a[188:191], a[36:39], v[124:127]\n\t"
    "v_mfma_f32_16x16x32_bf16 v[92:95], a[188:191], a[4:7], v[92:95]\n\t"
    "v_mfma_f32_16x16x32_bf16 v[92:95], a[252:255], a[0:3], v[92:95]\n\t"
    "s_waitcnt vmcnt(20) lgkmcnt(0)\n\t"
    "s_barrier\n\t"
    "v_mfma_f32_16x16x32_bf16 v[64:67], a[192:195], a[8:11], v[64:67]\n\t"
    "s_bfe_u32 s62, s72, 0x30011\n\t"
    "s_bfe_u32 s75, s72, 0x10010\n\t"
    "s_lshl2_add_u32 s62, s62, s75\n\t"
    "v_mfma_f32_16x16x32_bf16 v[64:67], a[128:131], a[12:15], v[64:67]\n\t"
    "s_lshl_b32 s62, s62, 11\n\t"
    "s_add_u32 s62, s62, s25\n\t"
    "s_bfe_u32 s69, s72, 0x3001d\n\t"
    "v_mfma_f32_16x16x32_bf16 v[96:99], a[128:131], a[44:47], v[96:99]\n\t"
    "s_bfe_u32 s75, s72, 0x1001c\n\t"
    "s_lshl2_add_u32 s69, s69, s75\n\t"
    "s_lshl_b32 s69, s69, 11\n\t"
    "v_mfma_f32_16x16x32_bf16 v[96:99], a[192:195], a[40:43], v[96:99]\n\t"
    "ds_read_b64_tr_b16 a[224:225], v46 offset:0\n\t"
    "s_add_u32 s69, s69, s25\n\t"
    "s_lshl2_add_u32 s69, s69, s79\n\t"
    "s_lshl_b32 s69, s69, 4\n\t"
    "v_mfma_f32_16x16x32_bf16 v[128:131], a[192:195], a[72:75], v[128:131]\n\t"
    "s_add_u32 s70, s69, 0x8000\n\t"
    "v_mfma_f32_16x16x32_bf16 v[128:131], a[128:131], a[76:79], v[128:131]\n\t"
    "ds_read_b64_tr_b16 a[160:161], v46 offset:8192\n\t"
    "v_mfma_f32_16x16x32_bf16 v[160:163], a[128:131], a[108:111], v[160:163]\n\t"
    "v_mfma_f32_16x16x32_bf16 v[160:163], a[192:195], a[104:107], v[160:163]\n\t"
    "ds_read_b64_tr_b16 a[226:227], v46 offset:1024\n\t"
    "v_mfma_f32_16x16x32_bf16 v[192:195], a[192:195], a[24:27], v[192:195]\n\t"
    "v_mfma_f32_16x16x32_bf16 v[192:195], a[128:131], a[28:31], v[192:195]\n\t"
    "ds_read_b64_tr_b16 a[162:163], v46 offset:9216\n\t"
    "v_mfma_f32_16x16x32_bf16 v[224:227], a[128:131], a[60:63], v[224:227]\n\t"
    "buffer_load_dwordx4 a[0:3], v31, s[20:23], s62 offen sc1\n\t"
    "v_mfma_f32_16x16x32_bf16 v[224:227], a[192:195], a[56:59], v[224:227]\n\t"
    "v_mfma_f32_16x16x32_bf16 v[228:231], a[196:199], a[56:59], v[228:231]\n\t"
    "ds_read_b64_tr_b16 a[228:229], v49 offset:0\n\t"
    "v_mfma_f32_16x16x32_bf16 v[228:231], a[132:135], a[60:63], v[228:231]\n\t"
    "s_add_u32 m0, s14, 0x8000\n\t"
    "v_mfma_f32_16x16x32_bf16 v[196:199], a[132:135], a[28:31], v[196:199]\n\t"
    "buffer_load_dwordx4 v29, s[16:19], s69 offen lds\n\t"
    "v_mfma_f32_16x16x32_bf16 v[196:199], a[196:199], a[24:27], v[196:199]\n\t"
    "ds_read_b64_tr_b16 a[164:165], v49 offset:8192\n\t"
    "v_mfma_f32_16x16x32_bf16 v[164:167], a[196:199], a[104:107], v[164:167]\n\t"
    "v_mfma_f32_16x16x32_bf16 v[164:167], a[132:135], a[108:111], v[164:167]\n\t"
    "ds_read_b64_tr_b16 a[230:231], v49 offset:1024\n\t"
    "v_mfma_f32_16x16x32_bf16 v[132:135], a[132:135], a[76:79], v[132:135]\n\t"
    "buffer_load_dwordx4 a[4:7], v31, s[20:23], s62 offen offset:1024 sc1\n\t"
    "v_mfma_f32_16x16x32_bf16 v[132:135], a[196:199], a[72:75], v[132:135]\n\t"
    "v_mfma_f32_16x16x32_bf16 v[100:103], a[196:199], a[40:43], v[100:103]\n\t"
    "ds_read_b64_tr_b16 a[166:167], v49 offset:9216\n\t"
    "v_mfma_f32_16x16x32_bf16 v[100:103], a[132:135], a[44:47], v[100:103]\n\t"
    "v_mfma_f32_16x16x32_bf16 v[68:71], a[132:135], a[12:15], v[68:71]\n\t"
    "ds_read_b64_tr_b16 a[232:233], v46 offset:256\n\t"
    "s_add_u32 m0, s14, 0x8400\n\t"
    "v_mfma_f32_16x16x32_bf16 v[68:71], a[196:199], a[8:11], v[68:71]\n\t"
    "buffer_load_dwordx4 v29, s[16:19], s70 offen lds\n\t"
    "v_mfma_f32_16x16x32_bf16 v[72:75], a[200:203], a[8:11], v[72:75]\n\t"
    "v_mfma_f32_16x16x32_bf16 v[72:75], a[136:139], a[12:15], v[72:75]\n\t"
    "ds_read_b64_tr_b16 a[168:169], v46 offset:8448\n\t"
    "v_mfma_f32_16x16x32_bf16 v[104:107], a[136:139], a[44:47], v[104:107]\n\t"
    "v_mfma_f32_16x16x32_bf16 v[104:107], a[200:203], a[40:43], v[104:107]\n\t"
    "buffer_load_dwordx4 a[32:35], v40, s[20:23], s62 offen sc1\n\t"
    "v_mfma_f32_16x16x32_bf16 v[136:139], a[200:203], a[72:75], v[136:139]\n\t"
    "ds_read_b64_tr_b16 a[234:235], v46 offset:1280\n\t"
    "v_mfma_f32_16x16x32_bf16 v[136:139], a[136:139], a[76:79], v[136:139]\n\t"
    "v_mfma_f32_16x16x32_bf16 v[168:171], a[136:139], a[108:111], v[168:171]\n\t"
    "v_mfma_f32_16x16x32_bf16 v[168:171], a[200:203], a[104:107], v[168:171]\n\t"
    "buffer_load_dwordx4 a[36:39], v40, s[20:23], s62 offen offset:1024 sc1\n\t"
    "v_mfma_f32_16x16x32_bf16 v[200:203], a[200:203], a[24:27], v[200:203]\n\t"
    "ds_read_b64_tr_b16 a[170:171], v46 offset:9472\n\t"
    "v_mfma_f32_16x16x32_bf16 v[200:203], a[136:139], a[28:31], v[200:203]\n\t"
    "v_mfma_f32_16x16x32_bf16 v[232:235], a[136:139], a[60:63], v[232:235]\n\t"
    "ds_read_b64_tr_b16 a[236:237], v49 offset:256\n\t"
    "v_mfma_f32_16x16x32_bf16 v[232:235], a[200:203], a[56:59], v[232:235]\n\t"
    "s_add_u32 m0, s14, 0x8800\n\t"
    "v_mfma_f32_16x16x32_bf16 v[236:239], a[204:207], a[56:59], v[236:239]\n\t"
    "buffer_load_dwordx4 v26, s[16:19], s69 offen lds\n\t"
    "v_mfma_f32_16x16x32_bf16 v[236:239], a[140:143], a[60:63], v[236:239]\n\t"
    "ds_read_b64_tr_b16 a[172:173], v49 offset:8448\n\t"
    "v_mfma_f32_16x16x32_bf16 v[204:207], a[140:143], a[28:31], v[204:207]\n\t"
    "v_mfma_f32_16x16x32_bf16 v[204:207], a[204:207], a[24:27], v[204:207]\n\t"
    "ds_read_b64_tr_b16 a[238:239], v49 offset:1280\n\t"
    "v_mfma_f32_16x16x32_bf16 v[172:175], a[204:207], a[104:107], v[172:175]\n\t"
    "buffer_load_dwordx4 a[64:67], v41, s[20:23], s62 offen sc1\n\t"
    "v_mfma_f32_16x16x32_bf16 v[172:175], a[140:143], a[108:111], v[172:175]\n\t"
    "v_mfma_f32_16x16x32_bf16 v[140:143], a[140:143], a[76:79], v[140:143]\n\t"
    "ds_read_b64_tr_b16 a[174:175], v49 offset:9472\n\t"
    "v_mfma_f32_16x16x32_bf16 v[140:143], a[204:207], a[72:75], v[140:143]\n\t"
    "s_add_u32 m0, s14, 0x8c00\n\t"
    "v_mfma_f32_16x16x32_bf16 v[108:111], a[204:207], a[40:43], v[108:111]\n\t"
    "buffer_load_dwordx4 v26, s[16:19], s70 offen lds\n\t"
    "v_mfma_f32_16x16x32_bf16 v[108:111], a[140:143], a[44:47], v[108:111]\n\t"
    "ds_read_b64_tr_b16 a[240:241], v46 offset:512\n\t"
    "v_mfma_f32_16x16x32_bf16 v[76:79], a[140:143], a[12:15], v[76:79]\n\t"
    "v_mfma_f32_16x16x32_bf16 v[76:79], a[204:207], a[8:11], v[76:79]\n\t"
    "ds_read_b64_tr_b16 a[176:177], v46 offset:8704\n\t"
    "v_mfma_f32_16x16x32_bf16 v[80:83], a[208:211], a[8:11], v[80:83]\n\t"
    "v_mfma_f32_16x16x32_bf16 v[80:83], a[144:147], a[12:15], v[80:83]\n\t"
    "buffer_load_dwordx4 a[68:71], v41, s[20:23], s62 offen offset:1024 sc1\n\t"
    "v_mfma_f32_16x16x32_bf16 v[112:115], a[144:147], a[44:47], v[112:115]\n\t"
    "ds_read_b64_tr_b16 a[242:243], v46 offset:1536\n\t"
    "v_mfma_f32_16x16x32_bf16 v[112:115], a[208:211], a[40:43], v[112:115]\n\t"
    "v_mfma_f32_16x16x32_bf16 v[144:147], a[208:211], a[72:75], v[144:147]\n\t"
    "ds_read_b64_tr_b16 a[178:179], v46 offset:9728\n\t"
    "v_mfma_f32_16x16x32_bf16 v[144:147], a[144:147], a[76:79], v[144:147]\n\t"
    "buffer_load_dwordx4 a[96:99], v42, s[20:23], s62 offen sc1\n\t"
    "v_mfma_f32_16x16x32_bf16 v[176:179], a[144:147], a[108:111], v[176:179]\n\t"
    "v_mfma_f32_16x16x32_bf16 v[176:179], a[208:211], a[104:107], v[176:179]\n\t"
    "ds_read_b64_tr_b16 a[244:245], v49 offset:512\n\t"
    "v_mfma_f32_16x16x32_bf16 v[208:211], a[208:211], a[24:27], v[208:211]\n\t"
    "v_mfma_f32_16x16x32_bf16 v[208:211], a[144:147], a[28:31], v[208:211]\n\t"
    "ds_read_b64_tr_b16 a[180:181], v49 offset:8704\n\t"
    "s_add_u32 m0, s14, 0x9000\n\t"
    "v_mfma_f32_16x16x32_bf16 v[240:243], a[144:147], a[60:63], v[240:243]\n\t"
    "buffer_load_dwordx4 v27, s[16:19], s69 offen lds\n\t"
    "v_mfma_f32_16x16x32_bf16 v[240:243], a[208:211], a[56:59], v[240:243]\n\t"
    "v_mfma_f32_16x16x32_bf16 v[244:247], a[212:215], a[56:59], v[244:247]\n\t"
    "v_mfma_f32_16x16x32_bf16 v[244:247], a[148:151], a[60:63], v[244:247]\n\t"
    "ds_read_b64_tr_b16 a[246:247], v49 offset:1536\n\t"
    "v_mfma_f32_16x16x32_bf16 v[212:215], a[148:151], a[28:31], v[212:215]\n\t"
    "buffer_load_dwordx4 a[100:103], v42, s[20:23], s62 offen offset:1024 sc1\n\t"
    "v_mfma_f32_16x16x32_bf16 v[212:215], a[212:215], a[24:27], v[212:215]\n\t"
    "v_mfma_f32_16x16x32_bf16 v[180:183], a[212:215], a[104:107], v[180:183]\n\t"
    "ds_read_b64_tr_b16 a[182:183], v49 offset:9728\n\t"
    "v_mfma_f32_16x16x32_bf16 v[180:183], a[148:151], a[108:111], v[180:183]\n\t"
    "s_add_u32 m0, s14, 0x9400\n\t"
    "v_mfma_f32_16x16x32_bf16 v[148:151], a[148:151], a[76:79], v[148:151]\n\t"
    "buffer_load_dwordx4 v27, s[16:19], s70 offen lds\n\t"
    "v_mfma_f32_16x16x32_bf16 v[148:151], a[212:215], a[72:75], v[148:151]\n\t"
    "ds_read_b64_tr_b16 a[248:249], v46 offset:768\n\t"
    "v_mfma_f32_16x16x32_bf16 v[116:119], a[212:215], a[40:43], v[116:119]\n\t"
    "v_mfma_f32_16x16x32_bf16 v[116:119], a[148:151], a[44:47], v[116:119]\n\t"
    "ds_read_b64_tr_b16 a[184:185], v46 offset:8960\n\t"
    "v_mfma_f32_16x16x32_bf16 v[84:87], a[148:151], a[12:15], v[84:87]\n\t"
    "v_mfma_f32_16x16x32_bf16 v[84:87], a[212:215], a[8:11], v[84:87]\n\t"
    "buffer_load_dwordx4 a[16:19], v43, s[20:23], s62 offen sc1\n\t"
    "v_mfma_f32_16x16x32_bf16 v[88:91], a[216:219], a[8:11], v[88:91]\n\t"
    "ds_read_b64_tr_b16 a[250:251], v46 offset:1792\n\t"
    "v_mfma_f32_16x16x32_bf16 v[88:91], a[152:155], a[12:15], v[88:91]\n\t"
    "v_mfma_f32_16x16x32_bf16 v[120:123], a[152:155], a[44:47], v[120:123]\n\t"
    "ds_read_b64_tr_b16 a[186:187], v46 offset:9984\n\t"
    "v_mfma_f32_16x16x32_bf16 v[120:123], a[216:219], a[40:43], v[120:123]\n\t"
    "buffer_load_dwordx4 a[20:23], v43, s[20:23], s62 offen offset:1024 sc1\n\t"
    "v_mfma_f32_16x16x32_bf16 v[152:155], a[216:219], a[72:75], v[152:155]\n\t"
    "v_mfma_f32_16x16x32_bf16 v[152:155], a[152:155], a[76:79], v[152:155]\n\t"
    "ds_read_b64_tr_b16 a[252:253], v49 offset:768\n\t"
    "v_mfma_f32_16x16x32_bf16 v[184:187], a[152:155], a[108:111], v[184:187]\n\t"
    "s_add_u32 m0, s14, 0x9800\n\t"
    "v_mfma_f32_16x16x32_bf16 v[184:187], a[216:219], a[104:107], v[184:187]\n\t"
    "buffer_load_dwordx4 v28, s[16:19], s69 offen lds\n\t"
    "v_mfma_f32_16x16x32_bf16 v[216:219], a[216:219], a[24:27], v[216:219]\n\t"
    "ds_read_b64_tr_b16 a[188:189], v49 offset:8960\n\t"
    "v_mfma_f32_16x16x32_bf16 v[216:219], a[152:155], a[28:31], v[216:219]\n\t"
    "v_mfma_f32_16x16x32_bf16 v[248:251], a[152:155], a[60:63], v[248:251]\n\t"
    "ds_read_b64_tr_b16 a[254:255], v49 offset:1792\n\t"
    "v_mfma_f32_16x16x32_bf16 v[248:251], a[216:219], a[56:59], v[248:251]\n\t"
    "v_mfma_f32_16x16x32_bf16 v[252:255], a[220:223], a[56:59], v[252:255]\n\t"
    "buffer_load_dwordx4 a[48:51], v44, s[20:23], s62 offen sc1\n\t"
    "v_mfma_f32_16x16x32_bf16 v[252:255], a[156:159], a[60:63], v[252:255]\n\t"
    "ds_read_b64_tr_b16 a[190:191], v49 offset:9984\n\t"
    "v_mfma_f32_16x16x32_bf16 v[220:223], a[156:159], a[28:31], v[220:223]\n\t"
    "v_mfma_f32_16x16x32_bf16 v[220:223], a[220:223], a[24:27], v[220:223]\n\t"
    "s_add_u32 m0, s14, 0x9c00\n\t"
    "v_mfma_f32_16x16x32_bf16 v[188:191], a[220:223], a[104:107], v[188:191]\n\t"
    "buffer_load_dwordx4 v28, s[16:19], s70 offen lds\n\t"
    "v_mfma_f32_16x16x32_bf16 v[188:191], a[156:159], a[108:111], v[188:191]\n\t"
    "v_mfma_f32_16x16x32_bf16 v[156:159], a[156:159], a[76:79], v[156:159]\n\t"
    "v_mfma_f32_16x16x32_bf16 v[156:159], a[220:223], a[72:75], v[156:159]\n\t"
    "v_mfma_f32_16x16x32_bf16 v[124:127], a[220:223], a[40:43], v[124:127]\n\t"
    "buffer_load_dwordx4 a[52:55], v44, s[20:23], s62 offen offset:1024 sc1\n\t"
    "v_mfma_f32_16x16x32_bf16 v[124:127], a[156:159], a[44:47], v[124:127]\n\t"
    "v_mfma_f32_16x16x32_bf16 v[92:95], a[156:159], a[12:15], v[92:95]\n\t"
    "v_mfma_f32_16x16x32_bf16 v[92:95], a[220:223], a[8:11], v[92:95]\n\t"
    "s_waitcnt vmcnt(20) lgkmcnt(0)\n\t"
    "s_barrier\n\t"
    "v_mfma_f32_16x16x32_bf16 v[64:67], a[224:227], a[80:83], v[64:67]\n\t"
    "s_bfe_u32 s62, s72, 0x30015\n\t"
    "s_bfe_u32 s75, s72, 0x10014\n\t"
    "s_lshl2_add_u32 s62, s62, s75\n\t"
    "v_mfma_f32_16x16x32_bf16 v[64:67], a[160:163], a[84:87], v[64:67]\n\t"
    "s_lshl_b32 s62, s62, 11\n\t"
    "s_add_u32 s62, s62, s25\n\t"
    "v_mfma_f32_16x16x32_bf16 v[96:99], a[160:163], a[92:95], v[96:99]\n\t"
    "v_mfma_f32_16x16x32_bf16 v[96:99], a[224:227], a[88:91], v[96:99]\n\t"
    "ds_read_b64_tr_b16 a[222:223], v49 offset:34560\n\t"
    "v_mfma_f32_16x16x32_bf16 v[128:131], a[224:227], a[112:115], v[128:131]\n\t"
    "v_mfma_f32_16x16x32_bf16 v[128:131], a[160:163], a[116:119], v[128:131]\n\t"
    "v_mfma_f32_16x16x32_bf16 v[160:163], a[160:163], a[124:127], v[160:163]\n\t"
    "v_mfma_f32_16x16x32_bf16 v[160:163], a[224:227], a[120:123], v[160:163]\n\t"
    "ds_read_b64_tr_b16 a[158:159], v49 offset:42752\n\t"
    "v_mfma_f32_16x16x32_bf16 v[192:195], a[224:227], v[4:7], v[192:195]\n\t"
    "v_mfma_f32_16x16x32_bf16 v[192:195], a[160:163], v[8:11], v[192:195]\n\t"
    "v_mfma_f32_16x16x32_bf16 v[224:227], a[160:163], v[16:19], v[224:227]\n\t"
    "v_mfma_f32_16x16x32_bf16 v[224:227], a[224:227], v[12:15], v[224:227]\n\t"
    "buffer_load_dwordx4 a[8:11], v31, s[20:23], s62 offen sc1\n\t"
    "v_mfma_f32_16x16x32_bf16 v[228:231], a[228:231], v[12:15], v[228:231]\n\t"
    "v_mfma_f32_16x16x32_bf16 v[228:231], a[164:167], v[16:19], v[228:231]\n\t"
    "ds_read_b64_tr_b16 a[220:221], v49 offset:33536\n\t"
    "v_mfma_f32_16x16x32_bf16 v[196:199], a[164:167], v[8:11], v[196:199]\n\t"
    "v_mfma_f32_16x16x32_bf16 v[196:199], a[228:231], v[4:7], v[196:199]\n\t"
    "v_mfma_f32_16x16x32_bf16 v[164:167], a[228:231], a[120:123], v[164:167]\n\t"
    "v_mfma_f32_16x16x32_bf16 v[164:167], a[164:167], a[124:127], v[164:167]\n\t"
    "ds_read_b64_tr_b16 a[156:157], v49 offset:41728\n\t"
    "v_mfma_f32_16x16x32_bf16 v[132:135], a[164:167], a[116:119], v[132:135]\n\t"
    "buffer_load_dwordx4 a[12:15], v31, s[20:23], s62 offen offset:1024 sc1\n\t"
    "v_mfma_f32_16x16x32_bf16 v[132:135], a[228:231], a[112:115], v[132:135]\n\t"
    "v_mfma_f32_16x16x32_bf16 v[100:103], a[228:231], a[88:91], v[100:103]\n\t"
    "v_mfma_f32_16x16x32_bf16 v[100:103], a[164:167], a[92:95], v[100:103]\n\t"
    "v_mfma_f32_16x16x32_bf16 v[68:71], a[164:167], a[84:87], v[68:71]\n\t"
    "v_mfma_f32_16x16x32_bf16 v[68:71], a[228:231], a[80:83], v[68:71]\n\t"
    "ds_read_b64_tr_b16 a[218:219], v46 offset:34560\n\t"
    "v_mfma_f32_16x16x32_bf16 v[72:75], a[232:235], a[80:83], v[72:75]\n\t"
    "v_mfma_f32_16x16x32_bf16 v[72:75], a[168:171], a[84:87], v[72:75]\n\t"
    "v_mfma_f32_16x16x32_bf16 v[104:107], a[168:171], a[92:95], v[104:107]\n\t"
    "buffer_load_dwordx4 a[40:43], v40, s[20:23], s62 offen sc1\n\t"
    "v_mfma_f32_16x16x32_bf16 v[104:107], a[232:235], a[88:91], v[104:107]\n\t"
    "v_mfma_f32_16x16x32_bf16 v[136:139], a[232:235], a[112:115], v[136:139]\n\t"
    "v_mfma_f32_16x16x32_bf16 v[136:139], a[168:171], a[116:119], v[136:139]\n\t"
    "ds_read_b64_tr_b16 a[154:155], v46 offset:42752\n\t"
    "v_mfma_f32_16x16x32_bf16 v[168:171], a[168:171], a[124:127], v[168:171]\n\t"
    "v_mfma_f32_16x16x32_bf16 v[168:171], a[232:235], a[120:123], v[168:171]\n\t"
    "v_mfma_f32_16x16x32_bf16 v[200:203], a[232:235], v[4:7], v[200:203]\n\t"
    "v_mfma_f32_16x16x32_bf16 v[200:203], a[168:171], v[8:11], v[200:203]\n\t"
    "buffer_load_dwordx4 a[44:47], v40, s[20:23], s62 offen offset:1024 sc1\n\t"
    "v_mfma_f32_16x16x32_bf16 v[232:235], a[168:171], v[16:19], v[232:235]\n\t"
    "ds_read_b64_tr_b16 a[216:217], v46 offset:33536\n\t"
    "v_mfma_f32_16x16x32_bf16 v[232:235], a[232:235], v[12:15], v[232:235]\n\t"
    "v_mfma_f32_16x16x32_bf16 v[236:239], a[236:239], v[12:15], v[236:239]\n\t"
    "v_mfma_f32_16x16x32_bf16 v[236:239], a[172:175], v[16:19], v[236:239]\n\t"
    "v_mfma_f32_16x16x32_bf16 v[204:207], a[172:175], v[8:11], v[204:207]\n\t"
    "v_mfma_f32_16x16x32_bf16 v[204:207], a[236:239], v[4:7], v[204:207]\n\t"
    "ds_read_b64_tr_b16 a[152:153], v46 offset:41728\n\t"
    "v_mfma_f32_16x16x32_bf16 v[172:175], a[236:239], a[120:123], v[172:175]\n\t"
    "buffer_load_dwordx4 a[72:75], v41, s[20:23], s62 offen sc1\n\t"
    "v_mfma_f32_16x16x32_bf16 v[172:175], a[172:175], a[124:127], v[172:175]\n\t"
    "v_mfma_f32_16x16x32_bf16 v[140:143], a[172:175], a[116:119], v[140:143]\n\t"
    "v_mfma_f32_16x16x32_bf16 v[140:143], a[236:239], a[112:115], v[140:143]\n\t"
    "v_mfma_f32_16x16x32_bf16 v[108:111], a[236:239], a[88:91], v[108:111]\n\t"
    "v_mfma_f32_16x16x32_bf16 v[108:111], a[172:175], a[92:95], v[108:111]\n\t"
    "ds_read_b64_tr_b16 a[214:215], v49 offset:34304\n\t"
    "v_mfma_f32_16x16x32_bf16 v[76:79], a[172:175], a[84:87], v[76:79]\n\t"
    "v_mfma_f32_16x16x32_bf16 v[76:79], a[236:239], a[80:83], v[76:79]\n\t"
    "buffer_load_dwordx4 a[76:79], v41, s[20:23], s62 offen offset:1024 sc1\n\t"
    "v_mfma_f32_16x16x32_bf16 v[80:83], a[240:243], a[80:83], v[80:83]\n\t"
    "v_mfma_f32_16x16x32_bf16 v[80:83], a[176:179], a[84:87], v[80:83]\n\t"
    "v_mfma_f32_16x16x32_bf16 v[112:115], a[176:179], a[92:95], v[112:115]\n\t"
    "ds_read_b64_tr_b16 a[150:151], v49 offset:42496\n\t"
    "v_mfma_f32_16x16x32_bf16 v[112:115], a[240:243], a[88:91], v[112:115]\n\t"
    "v_mfma_f32_16x16x32_bf16 v[144:147], a[240:243], a[112:115], v[144:147]\n\t"
    "v_mfma_f32_16x16x32_bf16 v[144:147], a[176:179], a[116:119], v[144:147]\n\t"
    "v_mfma_f32_16x16x32_bf16 v[176:179], a[176:179], a[124:127], v[176:179]\n\t"
    "v_mfma_f32_16x16x32_bf16 v[176:179], a[240:243], a[120:123], v[176:179]\n\t"
    "buffer_load_dwordx4 a[104:107], v42, s[20:23], s62 offen sc1\n\t"
    "v_mfma_f32_16x16x32_bf16 v[208:211], a[240:243], v[4:7], v[208:211]\n\t"
    "ds_read_b64_tr_b16 a[212:213], v49 offset:33280\n\t"
    "v_mfma_f32_16x16x32_bf16 v[208:211], a[176:179], v[8:11], v[208:211]\n\t"
    "v_mfma_f32_16x16x32_bf16 v[240:243], a[176:179], v[16:19], v[240:243]\n\t"
    "v_mfma_f32_16x16x32_bf16 v[240:243], a[240:243], v[12:15], v[240:243]\n\t"
    "v_mfma_f32_16x16x32_bf16 v[244:247], a[244:247], v[12:15], v[244:247]\n\t"
    "ds_read_b64_tr_b16 a[148:149], v49 offset:41472\n\t"
    "v_mfma_f32_16x16x32_bf16 v[244:247], a[180:183], v[16:19], v[244:247]\n\t"
    "v_mfma_f32_16x16x32_bf16 v[212:215], a[180:183], v[8:11], v[212:215]\n\t"
    "buffer_load_dwordx4 a[108:111], v42, s[20:23], s62 offen offset:1024 sc1\n\t"
    "v_mfma_f32_16x16x32_bf16 v[212:215], a[244:247], v[4:7], v[212:215]\n\t"
    "v_mfma_f32_16x16x32_bf16 v[180:183], a[244:247], a[120:123], v[180:183]\n\t"
    "v_mfma_f32_16x16x32_bf16 v[180:183], a[180:183], a[124:127], v[180:183]\n\t"
    "v_mfma_f32_16x16x32_bf16 v[148:151], a[180:183], a[116:119], v[148:151]\n\t"
    "ds_read_b64_tr_b16 a[210:211], v46 offset:34304\n\t"
    "v_mfma_f32_16x16x32_bf16 v[148:151], a[244:247], a[112:115], v[148:151]\n\t"
    "v_mfma_f32_16x16x32_bf16 v[116:119], a[244:247], a[88:91], v[116:119]\n\t"
    "v_mfma_f32_16x16x32_bf16 v[116:119], a[180:183], a[92:95], v[116:119]\n\t"
    "buffer_load_dwordx4 a[24:27], v43, s[20:23], s62 offen sc1\n\t"
    "v_mfma_f32_16x16x32_bf16 v[84:87], a[180:183], a[84:87], v[84:87]\n\t"
    "v_mfma_f32_16x16x32_bf16 v[84:87], a[244:247], a[80:83], v[84:87]\n\t"
    "v_mfma_f32_16x16x32_bf16 v[88:91], a[248:251], a[80:83], v[88:91]\n\t"
    "ds_read_b64_tr_b16 a[146:147], v46 offset:42496\n\t"
    "v_mfma_f32_16x16x32_bf16 v[88:91], a[184:187], a[84:87], v[88:91]\n\t"
    "v_mfma_f32_16x16x32_bf16 v[120:123], a[184:187], a[92:95], v[120:123]\n\t"
    "v_mfma_f32_16x16x32_bf16 v[120:123], a[248:251], a[88:91], v[120:123]\n\t"
    "v_mfma_f32_16x16x32_bf16 v[152:155], a[248:251], a[112:115], v[152:155]\n\t"
    "buffer_load_dwordx4 a[28:31], v43, s[20:23], s62 offen offset:1024 sc1\n\t"
    "v_mfma_f32_16x16x32_bf16 v[152:155], a[184:187], a[116:119], v[152:155]\n\t"
    "ds_read_b64_tr_b16 a[208:209], v46 offset:33280\n\t"
    "v_mfma_f32_16x16x32_bf16 v[184:187], a[184:187], a[124:127], v[184:187]\n\t"
    "v_mfma_f32_16x16x32_bf16 v[184:187], a[248:251], a[120:123], v[184:187]\n\t"
    "v_mfma_f32_16x16x32_bf16 v[216:219], a[248:251], v[4:7], v[216:219]\n\t"
    "v_mfma_f32_16x16x32_bf16 v[216:219], a[184:187], v[8:11], v[216:219]\n\t"
    "v_mfma_f32_16x16x32_bf16 v[248:251], a[184:187], v[16:19], v[248:251]\n\t"
    "ds_read_b64_tr_b16 a[144:145], v46 offset:41472\n\t"
    "v_mfma_f32_16x16x32_bf16 v[248:251], a[248:251], v[12:15], v[248:251]\n\t"
    "v_mfma_f32_16x16x32_bf16 v[252:255], a[252:255], v[12:15], v[252:255]\n\t"
    "buffer_load_dwordx4 a[56:59], v44, s[20:23], s62 offen sc1\n\t"
    "v_mfma_f32_16x16x32_bf16 v[252:255], a[188:191], v[16:19], v[252:255]\n\t"
    "v_mfma_f32_16x16x32_bf16 v[220:223], a[188:191], v[8:11], v[220:223]\n\t"
    "v_mfma_f32_16x16x32_bf16 v[220:223], a[252:255], v[4:7], v[220:223]\n\t"
    "v_mfma_f32_16x16x32_bf16 v[188:191], a[252:255], a[120:123], v[188:191]\n\t"
    "v_mfma_f32_16x16x32_bf16 v[188:191], a[188:191], a[124:127], v[188:191]\n\t"
    "v_mfma_f32_16x16x32_bf16 v[156:159], a[188:191], a[116:119], v[156:159]\n\t"
    "v_mfma_f32_16x16x32_bf16 v[156:159], a[252:255], a[112:115], v[156:159]\n\t"
    "buffer_load_dwordx4 a[60:63], v44, s[20:23], s62 offen offset:1024 sc1\n\t"
    "v_mfma_f32_16x16x32_bf16 v[124:127], a[252:255], a[88:91], v[124:127]\n\t"
    "v_mfma_f32_16x16x32_bf16 v[124:127], a[188:191], a[92:95], v[124:127]\n\t"
    "v_mfma_f32_16x16x32_bf16 v[92:95], a[188:191], a[84:87], v[92:95]\n\t"
    "v_mfma_f32_16x16x32_bf16 v[92:95], a[252:255], a[80:83], v[92:95]\n\t"
    "s_waitcnt vmcnt(12) lgkmcnt(0)\n\t"
    "s_barrier\n\t"
    "v_mfma_f32_16x16x32_bf16 v[252:255], a[220:223], a[48:51], v[252:255]\n\t"
    "s_bfe_u32 s62, s72, 0x30019\n\t"
    "s_bfe_u32 s75, s72, 0x10018\n\t"
    "s_lshl2_add_u32 s62, s62, s75\n\t"
    "v_mfma_f32_16x16x32_bf16 v[252:255], a[156:159], a[52:55], v[252:255]\n\t"
    "ds_read_b64_tr_b16 a[254:255], v50 offset:1792\n\t"
    "s_lshl_b32 s62, s62, 11\n\t"
    "s_add_u32 s62, s62, s25\n\t"
    "s_bfe_u32 s64, s72, 0x3001d\n\t"
    "v_mfma_f32_16x16x32_bf16 v[220:223], a[156:159], a[20:23], v[220:223]\n\t"
    "s_bfe_u32 s75, s72, 0x1001c\n\t"
    "s_lshl2_add_u32 s64, s64, s75\n\t"
    "s_lshl_b32 s64, s64, 11\n\t"
    "v_mfma_f32_16x16x32_bf16 v[220:223], a[220:223], a[16:19], v[220:223]\n\t"
    "ds_read_b64_tr_b16 a[190:191], v50 offset:9984\n\t"
    "s_add_u32 s64, s64, s25\n\t"
    "v_mfma_f32_16x16x32_bf16 v[188:191], a[220:223], a[96:99], v[188:191]\n\t"
    "v_mfma_f32_16x16x32_bf16 v[188:191], a[156:159], a[100:103], v[188:191]\n\t"
    "ds_read_b64_tr_b16 a[252:253], v50 offset:768\n\t"
    "v_mfma_f32_16x16x32_bf16 v[156:159], a[156:159], a[68:71], v[156:159]\n\t"
    "v_mfma_f32_16x16x32_bf16 v[156:159], a[220:223], a[64:67], v[156:159]\n\t"
    "buffer_load_dwordx4 a[80:83], v31, s[20:23], s62 offen sc1\n\t"
    "v_mfma_f32_16x16x32_bf16 v[124:127], a[220:223], a[32:35], v[124:127]\n\t"
    "ds_read_b64_tr_b16 a[188:189], v50 offset:8960\n\t"
    "v_mfma_f32_16x16x32_bf16 v[124:127], a[156:159], a[36:39], v[124:127]\n\t"
    "v_mfma_f32_16x16x32_bf16 v[92:95], a[156:159], a[4:7], v[92:95]\n\t"
    "ds_read_b64_tr_b16 a[250:251], v47 offset:1792\n\t"
    "v_mfma_f32_16x16x32_bf16 v[92:95], a[220:223], a[0:3], v[92:95]\n\t"
    "v_mfma_f32_16x16x32_bf16 v[88:91], a[216:219], a[0:3], v[88:91]\n\t"
    "buffer_load_dwordx4 a[84:87], v31, s[20:23], s62 offen offset:1024 sc1\n\t"
    "v_mfma_f32_16x16x32_bf16 v[88:91], a[152:155], a[4:7], v[88:91]\n\t"
    "ds_read_b64_tr_b16 a[186:187], v47 offset:9984\n\t"
    "v_mfma_f32_16x16x32_bf16 v[120:123], a[152:155], a[36:39], v[120:123]\n\t"
    "v_mfma_f32_16x16x32_bf16 v[120:123], a[216:219], a[32:35], v[120:123]\n\t"
    "ds_read_b64_tr_b16 a[248:249], v47 offset:768\n\t"
    "v_mfma_f32_16x16x32_bf16 v[152:155], a[216:219], a[64:67], v[152:155]\n\t"
    "buffer_load_dwordx4 a[88:91], v40, s[20:23], s62 offen sc1\n\t"
    "v_mfma_f32_16x16x32_bf16 v[152:155], a[152:155], a[68:71], v[152:155]\n\t"
    "v_mfma_f32_16x16x32_bf16 v[184:187], a[152:155], a[100:103], v[184:187]\n\t"
    "ds_read_b64_tr_b16 a[184:185], v47 offset:8960\n\t"
    "v_mfma_f32_16x16x32_bf16 v[184:187], a[216:219], a[96:99], v[184:187]\n\t"
    "v_mfma_f32_16x16x32_bf16 v[216:219], a[216:219], a[16:19], v[216:219]\n\t"
    "ds_read_b64_tr_b16 a[246:247], v50 offset:1536\n\t"
    "v_mfma_f32_16x16x32_bf16 v[216:219], a[152:155], a[20:23], v[216:219]\n\t"
    "buffer_load_dwordx4 a[92:95], v40, s[20:23], s62 offen offset:1024 sc1\n\t"
    "v_mfma_f32_16x16x32_bf16 v[248:251], a[152:155], a[52:55], v[248:251]\n\t"
    "v_mfma_f32_16x16x32_bf16 v[248:251], a[216:219], a[48:51], v[248:251]\n\t"
    "ds_read_b64_tr_b16 a[182:183], v50 offset:9728\n\t"
    "v_mfma_f32_16x16x32_bf16 v[244:247], a[212:215], a[48:51], v[244:247]\n\t"
    "v_mfma_f32_16x16x32_bf16 v[244:247], a[148:151], a[52:55], v[244:247]\n\t"
    "ds_read_b64_tr_b16 a[244:245], v50 offset:512\n\t"
    "v_mfma_f32_16x16x32_bf16 v[212:215], a[148:151], a[20:23], v[212:215]\n\t"
    "buffer_load_dwordx4 a[112:115], v41, s[20:23], s62 offen sc1\n\t"
    "v_mfma_f32_16x16x32_bf16 v[212:215], a[212:215], a[16:19], v[212:215]\n\t"
    "v_mfma_f32_16x16x32_bf16 v[180:183], a[212:215], a[96:99], v[180:183]\n\t"
    "ds_read_b64_tr_b16 a[180:181], v50 offset:8704\n\t"
    "v_mfma_f32_16x16x32_bf16 v[180:183], a[148:151], a[100:103], v[180:183]\n\t"
    "v_mfma_f32_16x16x32_bf16 v[148:151], a[148:151], a[68:71], v[148:151]\n\t"
    "buffer_load_dwordx4 a[116:119], v41, s[20:23], s62 offen offset:1024 sc1\n\t"
    "v_mfma_f32_16x16x32_bf16 v[148:151], a[212:215], a[64:67], v[148:151]\n\t"
    "ds_read_b64_tr_b16 a[242:243], v47 offset:1536\n\t"
    "v_mfma_f32_16x16x32_bf16 v[116:119], a[212:215], a[32:35], v[116:119]\n\t"
    "v_mfma_f32_16x16x32_bf16 v[116:119], a[148:151], a[36:39], v[116:119]\n\t"
    "ds_read_b64_tr_b16 a[178:179], v47 offset:9728\n\t"
    "v_mfma_f32_16x16x32_bf16 v[84:87], a[148:151], a[4:7], v[84:87]\n\t"
    "v_mfma_f32_16x16x32_bf16 v[84:87], a[212:215], a[0:3], v[84:87]\n\t"
    "buffer_load_dwordx4 a[120:123], v42, s[20:23], s62 offen sc1\n\t"
    "v_mfma_f32_16x16x32_bf16 v[80:83], a[208:211], a[0:3], v[80:83]\n\t"
    "ds_read_b64_tr_b16 a[240:241], v47 offset:512\n\t"
    "v_mfma_f32_16x16x32_bf16 v[80:83], a[144:147], a[4:7], v[80:83]\n\t"
    "v_mfma_f32_16x16x32_bf16 v[112:115], a[144:147], a[36:39], v[112:115]\n\t"
    "ds_read_b64_tr_b16 a[176:177], v47 offset:8704\n\t"
    "v_mfma_f32_16x16x32_bf16 v[112:115], a[208:211], a[32:35], v[112:115]\n\t"
    "v_mfma_f32_16x16x32_bf16 v[144:147], a[208:211], a[64:67], v[144:147]\n\t"
    "buffer_load_dwordx4 a[124:127], v42, s[20:23], s62 offen offset:1024 sc1\n\t"
    "v_mfma_f32_16x16x32_bf16 v[144:147], a[144:147], a[68:71], v[144:147]\n\t"
    "v_mfma_f32_16x16x32_bf16 v[176:179], a[144:147], a[100:103], v[176:179]\n\t"
    "v_mfma_f32_16x16x32_bf16 v[176:179], a[208:211], a[96:99], v[176:179]\n\t"
    "v_mfma_f32_16x16x32_bf16 v[208:211], a[208:211], a[16:19], v[208:211]\n\t"
    "buffer_load_dwordx4 v[4:7], v43, s[20:23], s62 offen sc1\n\t"
    "v_mfma_f32_16x16x32_bf16 v[208:211], a[144:147], a[20:23], v[208:211]\n\t"
    "v_mfma_f32_16x16x32_bf16 v[240:243], a[144:147], a[52:55], v[240:243]\n\t"
    "v_mfma_f32_16x16x32_bf16 v[240:243], a[208:211], a[48:51], v[240:243]\n\t"
    "s_waitcnt vmcnt(9) lgkmcnt(0)\n\t"
    "v_mfma_f32_16x16x32_bf16 v[252:255], a[252:255], a[56:59], v[252:255]\n\t"
    "ds_read_b64_tr_b16 a[222:223], v48 offset:1792\n\t"
    "v_mfma_f32_16x16x32_bf16 v[252:255], a[188:191], a[60:63], v[252:255]\n\t"
    "buffer_load_dwordx4 v[8:11], v43, s[20:23], s62 offen offset:1024 sc1\n\t"
    "v_mfma_f32_16x16x32_bf16 v[220:223], a[188:191], a[28:31], v[220:223]\n\t"
    "ds_read_b64_tr_b16 a[158:159], v48 offset:9984\n\t"
    "v_mfma_f32_16x16x32_bf16 v[220:223], a[252:255], a[24:27], v[220:223]\n\t"
    "ds_read_b64_tr_b16 a[220:221], v48 offset:768\n\t"
    "v_mfma_f32_16x16x32_bf16 v[188:191], a[252:255], a[104:107], v[188:191]\n\t"
    "ds_read_b64_tr_b16 a[156:157], v48 offset:8960\n\t"
    "v_mfma_f32_16x16x32_bf16 v[188:191], a[188:191], a[108:111], v[188:191]\n\t"
    "ds_read_b64_tr_b16 a[218:219], v45 offset:1792\n\t"
    "v_mfma_f32_16x16x32_bf16 v[156:159], a[188:191], a[76:79], v[156:159]\n\t"
    "buffer_load_dwordx4 v[12:15], v44, s[20:23], s62 offen sc1\n\t"
    "v_mfma_f32_16x16x32_bf16 v[156:159], a[252:255], a[72:75], v[156:159]\n\t"
    "ds_read_b64_tr_b16 a[154:155], v45 offset:9984\n\t"
    "v_mfma_f32_16x16x32_bf16 v[124:127], a[252:255], a[40:43], v[124:127]\n\t"
    "ds_read_b64_tr_b16 a[216:217], v45 offset:768\n\t"
    "v_mfma_f32_16x16x32_bf16 v[124:127], a[188:191], a[44:47], v[124:127]\n\t"
    "ds_read_b64_tr_b16 a[152:153], v45 offset:8960\n\t"
    "v_mfma_f32_16x16x32_bf16 v[92:95], a[188:191], a[12:15], v[92:95]\n\t"
    "buffer_load_dwordx4 v[16:19], v44, s[20:23], s62 offen offset:1024 sc1\n\t"
    "v_mfma_f32_16x16x32_bf16 v[92:95], a[252:255], a[8:11], v[92:95]\n\t"
    "ds_read_b64_tr_b16 a[214:215], v48 offset:1536\n\t"
    "v_mfma_f32_16x16x32_bf16 v[88:91], a[248:251], a[8:11], v[88:91]\n\t"
    "ds_read_b64_tr_b16 a[150:151], v48 offset:9728\n\t"
    "v_mfma_f32_16x16x32_bf16 v[88:91], a[184:187], a[12:15], v[88:91]\n\t"
    "ds_read_b64_tr_b16 a[212:213], v48 offset:512\n\t"
    "v_mfma_f32_16x16x32_bf16 v[120:123], a[184:187], a[44:47], v[120:123]\n\t"
    "ds_read_b64_tr_b16 a[148:149], v48 offset:8704\n\t"
    "v_mfma_f32_16x16x32_bf16 v[120:123], a[248:251], a[40:43], v[120:123]\n\t"
    "ds_read_b64_tr_b16 a[210:211], v45 offset:1536\n\t"
    "v_mfma_f32_16x16x32_bf16 v[152:155], a[248:251], a[72:75], v[152:155]\n\t"
    "buffer_load_dwordx4 a[0:3], v31, s[20:23], s64 offen sc1\n\t"
    "v_mfma_f32_16x16x32_bf16 v[152:155], a[184:187], a[76:79], v[152:155]\n\t"
    "ds_read_b64_tr_b16 a[146:147], v45 offset:9728\n\t"
    "v_mfma_f32_16x16x32_bf16 v[184:187], a[184:187], a[108:111], v[184:187]\n\t"
    "ds_read_b64_tr_b16 a[208:209], v45 offset:512\n\t"
    "v_mfma_f32_16x16x32_bf16 v[184:187], a[248:251], a[104:107], v[184:187]\n\t"
    "ds_read_b64_tr_b16 a[144:145], v45 offset:8704\n\t"
    "v_mfma_f32_16x16x32_bf16 v[216:219], a[248:251], a[24:27], v[216:219]\n\t"
    "buffer_load_dwordx4 a[4:7], v31, s[20:23], s64 offen offset:1024 sc1\n\t"
    "v_mfma_f32_16x16x32_bf16 v[216:219], a[184:187], a[28:31], v[216:219]\n\t"
    "ds_read_b64_tr_b16 a[238:239], v50 offset:1280\n\t"
    "v_mfma_f32_16x16x32_bf16 v[248:251], a[184:187], a[60:63], v[248:251]\n\t"
    "ds_read_b64_tr_b16 a[174:175], v50 offset:9472\n\t"
    "v_mfma_f32_16x16x32_bf16 v[248:251], a[248:251], a[56:59], v[248:251]\n\t"
    "ds_read_b64_tr_b16 a[236:237], v50 offset:256\n\t"
    "v_mfma_f32_16x16x32_bf16 v[244:247], a[244:247], a[56:59], v[244:247]\n\t"
    "ds_read_b64_tr_b16 a[172:173], v50 offset:8448\n\t"
    "v_mfma_f32_16x16x32_bf16 v[244:247], a[180:183], a[60:63], v[244:247]\n\t"
    "buffer_load_dwordx4 a[32:35], v40, s[20:23], s64 offen sc1\n\t"
    "v_mfma_f32_16x16x32_bf16 v[212:215], a[180:183], a[28:31], v[212:215]\n\t"
    "ds_read_b64_tr_b16 a[234:235], v47 offset:1280\n\t"
    "v_mfma_f32_16x16x32_bf16 v[212:215], a[244:247], a[24:27], v[212:215]\n\t"
    "ds_read_b64_tr_b16 a[170:171], v47 offset:9472\n\t"
    "v_mfma_f32_16x16x32_bf16 v[180:183], a[244:247], a[104:107], v[180:183]\n\t"
    "ds_read_b64_tr_b16 a[232:233], v47 offset:256\n\t"
    "v_mfma_f32_16x16x32_bf16 v[180:183], a[180:183], a[108:111], v[180:183]\n\t"
    "ds_read_b64_tr_b16 a[168:169], v47 offset:8448\n\t"
    "v_mfma_f32_16x16x32_bf16 v[148:151], a[180:183], a[76:79], v[148:151]\n\t"
    "buffer_load_dwordx4 a[36:39], v40, s[20:23], s64 offen offset:1024 sc1\n\t"
    "v_mfma_f32_16x16x32_bf16 v[148:151], a[244:247], a[72:75], v[148:151]\n\t"
    "ds_read_b64_tr_b16 a[230:231], v50 offset:1024\n\t"
    "v_mfma_f32_16x16x32_bf16 v[116:119], a[244:247], a[40:43], v[116:119]\n\t"
    "ds_read_b64_tr_b16 a[166:167], v50 offset:9216\n\t"
    "v_mfma_f32_16x16x32_bf16 v[116:119], a[180:183], a[44:47], v[116:119]\n\t"
    "ds_read_b64_tr_b16 a[228:229], v50 offset:0\n\t"
    "v_mfma_f32_16x16x32_bf16 v[84:87], a[180:183], a[12:15], v[84:87]\n\t"
    "buffer_load_dwordx4 a[64:67], v41, s[20:23], s64 offen sc1\n\t"
    "v_mfma_f32_16x16x32_bf16 v[84:87], a[244:247], a[8:11], v[84:87]\n\t"
    "ds_read_b64_tr_b16 a[164:165], v50 offset:8192\n\t"
    "v_mfma_f32_16x16x32_bf16 v[80:83], a[240:243], a[8:11], v[80:83]\n\t"
    "ds_read_b64_tr_b16 a[226:227], v47 offset:1024\n\t"
    "v_mfma_f32_16x16x32_bf16 v[80:83], a[176:179], a[12:15], v[80:83]\n\t"
    "ds_read_b64_tr_b16 a[162:163], v47 offset:9216\n\t"
    "v_mfma_f32_16x16x32_bf16 v[112:115], a[176:179], a[44:47], v[112:115]\n\t"
    "ds_read_b64_tr_b16 a[224:225], v47 offset:0\n\t"
    "v_mfma_f32_16x16x32_bf16 v[112:115], a[240:243], a[40:43], v[112:115]\n\t"
    "buffer_load_dwordx4 a[68:71], v41, s[20:23], s64 offen offset:1024 sc1\n\t"
    "v_mfma_f32_16x16x32_bf16 v[144:147], a[240:243], a[72:75], v[144:147]\n\t"
    "ds_read_b64_tr_b16 a[160:161], v47 offset:8192\n\t"
    "v_mfma_f32_16x16x32_bf16 v[144:147], a[176:179], a[76:79], v[144:147]\n\t"
    "v_mfma_f32_16x16x32_bf16 v[176:179], a[176:179], a[108:111], v[176:179]\n\t"
    "v_mfma_f32_16x16x32_bf16 v[176:179], a[240:243], a[104:107], v[176:179]\n\t"
    "buffer_load_dwordx4 a[96:99], v42, s[20:23], s64 offen sc1\n\t"
    "v_mfma_f32_16x16x32_bf16 v[208:211], a[240:243], a[24:27], v[208:211]\n\t"
    "v_mfma_f32_16x16x32_bf16 v[208:211], a[176:179], a[28:31], v[208:211]\n\t"
    "v_mfma_f32_16x16x32_bf16 v[240:243], a[176:179], a[60:63], v[240:243]\n\t"
    "v_mfma_f32_16x16x32_bf16 v[240:243], a[240:243], a[56:59], v[240:243]\n\t"
    "s_waitcnt vmcnt(7) lgkmcnt(0)\n\t"
    "v_mfma_f32_16x16x32_bf16 v[252:255], a[220:223], v[12:15], v[252:255]\n\t"
    "buffer_load_dwordx4 a[100:103], v42, s[20:23], s64 offen offset:1024 sc1\n\t"
    "v_mfma_f32_16x16x32_bf16 v[252:255], a[156:159], v[16:19], v[252:255]\n\t"
    "ds_read_b64_tr_b16 a[254:255], v48 offset:34560\n\t"
    "v_mfma_f32_16x16x32_bf16 v[220:223], a[156:159], v[8:11], v[220:223]\n\t"
    "ds_read_b64_tr_b16 a[190:191], v48 offset:42752\n\t"
    "v_mfma_f32_16x16x32_bf16 v[220:223], a[220:223], v[4:7], v[220:223]\n\t"
    "ds_read_b64_tr_b16 a[252:253], v48 offset:33536\n\t"
    "v_mfma_f32_16x16x32_bf16 v[188:191], a[220:223], a[120:123], v[188:191]\n\t"
    "buffer_load_dwordx4 a[16:19], v43, s[20:23], s64 offen sc1\n\t"
    "v_mfma_f32_16x16x32_bf16 v[188:191], a[156:159], a[124:127], v[188:191]\n\t"
    "ds_read_b64_tr_b16 a[188:189], v48 offset:41728\n\t"
    "v_mfma_f32_16x16x32_bf16 v[156:159], a[156:159], a[116:119], v[156:159]\n\t"
    "ds_read_b64_tr_b16 a[250:251], v45 offset:34560\n\t"
    "v_mfma_f32_16x16x32_bf16 v[156:159], a[220:223], a[112:115], v[156:159]\n\t"
    "ds_read_b64_tr_b16 a[186:187], v45 offset:42752\n\t"
    "v_mfma_f32_16x16x32_bf16 v[124:127], a[220:223], a[88:91], v[124:127]\n\t"
    "ds_read_b64_tr_b16 a[248:249], v45 offset:33536\n\t"
    "v_mfma_f32_16x16x32_bf16 v[124:127], a[156:159], a[92:95], v[124:127]\n\t"
    "buffer_load_dwordx4 a[20:23], v43, s[20:23], s64 offen offset:1024 sc1\n\t"
    "v_mfma_f32_16x16x32_bf16 v[92:95], a[156:159], a[84:87], v[92:95]\n\t"
    "v_mfma_f32_16x16x32_bf16 v[92:95], a[220:223], a[80:83], v[92:95]\n\t"
    "ds_read_b64_tr_b16 a[184:185], v45 offset:41728\n\t"
    "v_mfma_f32_16x16x32_bf16 v[88:91], a[216:219], a[80:83], v[88:91]\n\t"
    "ds_read_b64_tr_b16 a[246:247], v48 offset:34304\n\t"
    "v_mfma_f32_16x16x32_bf16 v[88:91], a[152:155], a[84:87], v[88:91]\n\t"
    "ds_read_b64_tr_b16 a[182:183], v48 offset:42496\n\t"
    "v_mfma_f32_16x16x32_bf16 v[120:123], a[152:155], a[92:95], v[120:123]\n\t"
    "buffer_load_dwordx4 a[48:51], v44, s[20:23], s64 offen sc1\n\t"
    "v_mfma_f32_16x16x32_bf16 v[120:123], a[216:219], a[88:91], v[120:123]\n\t"
    "ds_read_b64_tr_b16 a[244:245], v48 offset:33280\n\t"
    "v_mfma_f32_16x16x32_bf16 v[152:155], a[216:219], a[112:115], v[152:155]\n\t"
    "ds_read_b64_tr_b16 a[180:181], v48 offset:41472\n\t"
    "v_mfma_f32_16x16x32_bf16 v[152:155], a[152:155], a[116:119], v[152:155]\n\t"
    "ds_read_b64_tr_b16 a[242:243], v45 offset:34304\n\t"
    "v_mfma_f32_16x16x32_bf16 v[184:187], a[152:155], a[124:127], v[184:187]\n\t"
    "buffer_load_dwordx4 a[52:55], v44, s[20:23], s64 offen offset:1024 sc1\n\t"
    "v_mfma_f32_16x16x32_bf16 v[184:187], a[216:219], a[120:123], v[184:187]\n\t"
    "v_mfma_f32_16x16x32_bf16 v[216:219], a[216:219], v[4:7], v[216:219]\n\t"
    "ds_read_b64_tr_b16 a[178:179], v45 offset:42496\n\t"
    "v_mfma_f32_16x16x32_bf16 v[216:219], a[152:155], v[8:11], v[216:219]\n\t"
    "ds_read_b64_tr_b16 a[240:241], v45 offset:33280\n\t"
    "v_mfma_f32_16x16x32_bf16 v[248:251], a[152:155], v[16:19], v[248:251]\n\t"
    "ds_read_b64_tr_b16 a[176:177], v45 offset:41472\n\t"
    "v_mfma_f32_16x16x32_bf16 v[248:251], a[216:219], v[12:15], v[248:251]\n\t"
    "ds_read_b64_tr_b16 a[206:207], v48 offset:1280\n\t"
    "v_mfma_f32_16x16x32_bf16 v[244:247], a[212:215], v[12:15], v[244:247]\n\t"
    "ds_read_b64_tr_b16 a[142:143], v48 offset:9472\n\t"
    "v_mfma_f32_16x16x32_bf16 v[244:247], a[148:151], v[16:19], v[244:247]\n\t"
    "ds_read_b64_tr_b16 a[204:205], v48 offset:256\n\t"
    "v_mfma_f32_16x16x32_bf16 v[212:215], a[148:151], v[8:11], v[212:215]\n\t"
    "ds_read_b64_tr_b16 a[140:141], v48 offset:8448\n\t"
    "v_mfma_f32_16x16x32_bf16 v[212:215], a[212:215], v[4:7], v[212:215]\n\t"
    "v_mfma_f32_16x16x32_bf16 v[180:183], a[212:215], a[120:123], v[180:183]\n\t"
    "ds_read_b64_tr_b16 a[202:203], v45 offset:1280\n\t"
    "v_mfma_f32_16x16x32_bf16 v[180:183], a[148:151], a[124:127], v[180:183]\n\t"
    "ds_read_b64_tr_b16 a[138:139], v45 offset:9472\n\t"
    "v_mfma_f32_16x16x32_bf16 v[148:151], a[148:151], a[116:119], v[148:151]\n\t"
    "ds_read_b64_tr_b16 a[200:201], v45 offset:256\n\t"
    "v_mfma_f32_16x16x32_bf16 v[148:151], a[212:215], a[112:115], v[148:151]\n\t"
    "ds_read_b64_tr_b16 a[136:137], v45 offset:8448\n\t"
    "v_mfma_f32_16x16x32_bf16 v[116:119], a[212:215], a[88:91], v[116:119]\n\t"
    "ds_read_b64_tr_b16 a[198:199], v48 offset:1024\n\t"
    "v_mfma_f32_16x16x32_bf16 v[116:119], a[148:151], a[92:95], v[116:119]\n\t"
    "ds_read_b64_tr_b16 a[134:135], v48 offset:9216\n\t"
    "v_mfma_f32_16x16x32_bf16 v[84:87], a[148:151], a[84:87], v[84:87]\n\t"
    "v_mfma_f32_16x16x32_bf16 v[84:87], a[212:215], a[80:83], v[84:87]\n\t"
    "ds_read_b64_tr_b16 a[196:197], v48 offset:0\n\t"
    "v_mfma_f32_16x16x32_bf16 v[80:83], a[208:211], a[80:83], v[80:83]\n\t"
    "ds_read_b64_tr_b16 a[132:133], v48 offset:8192\n\t"
    "v_mfma_f32_16x16x32_bf16 v[80:83], a[144:147], a[84:87], v[80:83]\n\t"
    "ds_read_b64_tr_b16 a[194:195], v45 offset:1024\n\t"
    "v_mfma_f32_16x16x32_bf16 v[112:115], a[144:147], a[92:95], v[112:115]\n\t"
    "ds_read_b64_tr_b16 a[130:131], v45 offset:9216\n\t"
    "v_mfma_f32_16x16x32_bf16 v[112:115], a[208:211], a[88:91], v[112:115]\n\t"
    "ds_read_b64_tr_b16 a[192:193], v45 offset:0\n\t"
    "v_mfma_f32_16x16x32_bf16 v[144:147], a[208:211], a[112:115], v[144:147]\n\t"
    "ds_read_b64_tr_b16 a[128:129], v45 offset:8192\n\t"
    "v_mfma_f32_16x16x32_bf16 v[144:147], a[144:147], a[116:119], v[144:147]\n\t"
    "v_mfma_f32_16x16x32_bf16 v[176:179], a[144:147], a[124:127], v[176:179]\n\t"
    "v_mfma_f32_16x16x32_bf16 v[176:179], a[208:211], a[120:123], v[176:179]\n\t"
    "s_bfe_u32 s62, s72, 0x30011\n\t"
    "s_bfe_u32 s75, s72, 0x10010\n\t"
    "s_lshl2_add_u32 s62, s62, s75\n\t"
    "v_mfma_f32_16x16x32_bf16 v[208:211], a[208:211], v[4:7], v[208:211]\n\t"
    "s_lshl_b32 s62, s62, 11\n\t"
    "s_add_u32 s62, s62, s25\n\t"
    "v_mfma_f32_16x16x32_bf16 v[208:211], a[144:147], v[8:11], v[208:211]\n\t"
    "v_mfma_f32_16x16x32_bf16 v[240:243], a[144:147], v[16:19], v[240:243]\n\t"
    "v_mfma_f32_16x16x32_bf16 v[240:243], a[208:211], v[12:15], v[240:243]\n\t"
    "s_waitcnt vmcnt(0) lgkmcnt(0)\n\t"
    "s_barrier\n\t"
    "v_mfma_f32_16x16x32_bf16 v[252:255], a[252:255], a[48:51], v[252:255]\n\t"
    "v_mfma_f32_16x16x32_bf16 v[252:255], a[188:191], a[52:55], v[252:255]\n\t"
    "v_mfma_f32_16x16x32_bf16 v[220:223], a[188:191], a[20:23], v[220:223]\n\t"
    "v_mfma_f32_16x16x32_bf16 v[220:223], a[252:255], a[16:19], v[220:223]\n\t"
    "buffer_load_dwordx4 a[208:211], v31, s[20:23], s62 offen sc1\n\t"
    "v_mfma_f32_16x16x32_bf16 v[188:191], a[252:255], a[96:99], v[188:191]\n\t"
    "v_mfma_f32_16x16x32_bf16 v[188:191], a[188:191], a[100:103], v[188:191]\n\t"
    "v_mfma_f32_16x16x32_bf16 v[156:159], a[188:191], a[68:71], v[156:159]\n\t"
    "v_mfma_f32_16x16x32_bf16 v[156:159], a[252:255], a[64:67], v[156:159]\n\t"
    "v_mfma_f32_16x16x32_bf16 v[124:127], a[252:255], a[32:35], v[124:127]\n\t"
    "buffer_load_dwordx4 a[212:215], v31, s[20:23], s62 offen offset:1024 sc1\n\t"
    "v_mfma_f32_16x16x32_bf16 v[124:127], a[188:191], a[36:39], v[124:127]\n\t"
    "ds_write_b128 v51, v[252:255] offset:11264\n\t"
    "v_mfma_f32_16x16x32_bf16 v[92:95], a[188:191], a[4:7], v[92:95]\n\t"
    "v_mfma_f32_16x16x32_bf16 v[92:95], a[252:255], a[0:3], v[92:95]\n\t"
    "ds_write_b128 v51, v[220:223] offset:9216\n\t"
    "v_mfma_f32_16x16x32_bf16 v[88:91], a[248:251], a[0:3], v[88:91]\n\t"
    "v_mfma_f32_16x16x32_bf16 v[88:91], a[184:187], a[4:7], v[88:91]\n\t"
    "ds_write_b128 v51, v[188:191] offset:7168\n\t"
    "v_mfma_f32_16x16x32_bf16 v[120:123], a[184:187], a[36:39], v[120:123]\n\t"
    "buffer_load_dwordx4 a[216:219], v40, s[20:23], s62 offen sc1\n\t"
    "v_mfma_f32_16x16x32_bf16 v[120:123], a[248:251], a[32:35], v[120:123]\n\t"
    "ds_write_b128 v51, v[156:159] offset:5120\n\t"
    "v_mfma_f32_16x16x32_bf16 v[152:155], a[248:251], a[64:67], v[152:155]\n\t"
    "v_mfma_f32_16x16x32_bf16 v[152:155], a[184:187], a[68:71], v[152:155]\n\t"
    "ds_write_b128 v51, v[124:127] offset:3072\n\t"
    "v_mfma_f32_16x16x32_bf16 v[184:187], a[184:187], a[100:103], v[184:187]\n\t"
    "v_mfma_f32_16x16x32_bf16 v[184:187], a[248:251], a[96:99], v[184:187]\n\t"
    "ds_write_b128 v51, v[92:95] offset:1024\n\t"
    "v_mfma_f32_16x16x32_bf16 v[216:219], a[248:251], a[16:19], v[216:219]\n\t"
    "buffer_load_dwordx4 a[220:223], v40, s[20:23], s62 offen offset:1024 sc1\n\t"
    "v_mfma_f32_16x16x32_bf16 v[216:219], a[184:187], a[20:23], v[216:219]\n\t"
    "ds_write_b128 v51, v[88:91] offset:0\n\t"
    "v_mfma_f32_16x16x32_bf16 v[248:251], a[184:187], a[52:55], v[248:251]\n\t"
    "v_mfma_f32_16x16x32_bf16 v[248:251], a[248:251], a[48:51], v[248:251]\n\t"
    "ds_write_b128 v51, v[120:123] offset:2048\n\t"
    "v_mfma_f32_16x16x32_bf16 v[244:247], a[244:247], a[48:51], v[244:247]\n\t"
    "v_mfma_f32_16x16x32_bf16 v[244:247], a[180:183], a[52:55], v[244:247]\n\t"
    "ds_write_b128 v51, v[152:155] offset:4096\n\t"
    "v_mfma_f32_16x16x32_bf16 v[212:215], a[180:183], a[20:23], v[212:215]\n\t"
    "buffer_load_dwordx4 a[144:147], v41, s[20:23], s62 offen sc1\n\t"
    "v_mfma_f32_16x16x32_bf16 v[212:215], a[244:247], a[16:19], v[212:215]\n\t"
    "ds_write_b128 v51, v[184:187] offset:6144\n\t"
    "v_mfma_f32_16x16x32_bf16 v[180:183], a[244:247], a[96:99], v[180:183]\n\t"
    "v_mfma_f32_16x16x32_bf16 v[180:183], a[180:183], a[100:103], v[180:183]\n\t"
    "ds_write_b128 v51, v[216:219] offset:8192\n\t"
    "v_mfma_f32_16x16x32_bf16 v[148:151], a[180:183], a[68:71], v[148:151]\n\t"
    "buffer_load_dwordx4 a[148:151], v41, s[20:23], s62 offen offset:1024 sc1\n\t"
    "v_mfma_f32_16x16x32_bf16 v[148:151], a[244:247], a[64:67], v[148:151]\n\t"
    "ds_write_b128 v51, v[248:251] offset:10240\n\t"
    "v_mfma_f32_16x16x32_bf16 v[116:119], a[244:247], a[32:35], v[116:119]\n\t"
    "v_mfma_f32_16x16x32_bf16 v[116:119], a[180:183], a[36:39], v[116:119]\n\t"
    "v_mfma_f32_16x16x32_bf16 v[84:87], a[180:183], a[4:7], v[84:87]\n\t"
    "v_mfma_f32_16x16x32_bf16 v[84:87], a[244:247], a[0:3], v[84:87]\n\t"
    "v_mfma_f32_16x16x32_bf16 v[80:83], a[240:243], a[0:3], v[80:83]\n\t"
    "v_mfma_f32_16x16x32_bf16 v[80:83], a[176:179], a[4:7], v[80:83]\n\t"
    "buffer_load_dwordx4 a[152:155], v42, s[20:23], s62 offen sc1\n\t"
    "v_mfma_f32_16x16x32_bf16 v[112:115], a[176:179], a[36:39], v[112:115]\n\t"
    "v_mfma_f32_16x16x32_bf16 v[112:115], a[240:243], a[32:35], v[112:115]\n\t"
    "v_mfma_f32_16x16x32_bf16 v[144:147], a[240:243], a[64:67], v[144:147]\n\t"
    "v_mfma_f32_16x16x32_bf16 v[144:147], a[176:179], a[68:71], v[144:147]\n\t"
    "v_mfma_f32_16x16x32_bf16 v[176:179], a[176:179], a[100:103], v[176:179]\n\t"
    "v_mfma_f32_16x16x32_bf16 v[176:179], a[240:243], a[96:99], v[176:179]\n\t"
    "buffer_load_dwordx4 a[156:159], v42, s[20:23], s62 offen offset:1024 sc1\n\t"
    "v_mfma_f32_16x16x32_bf16 v[208:211], a[240:243], a[16:19], v[208:211]\n\t"
    "v_mfma_f32_16x16x32_bf16 v[208:211], a[176:179], a[20:23], v[208:211]\n\t"
    "v_mfma_f32_16x16x32_bf16 v[240:243], a[176:179], a[52:55], v[240:243]\n\t"
    "v_mfma_f32_16x16x32_bf16 v[240:243], a[240:243], a[48:51], v[240:243]\n\t"
    "s_waitcnt lgkmcnt(0)\n\t"
    "s_barrier\n\t"
    "v_mfma_f32_16x16x32_bf16 v[236:239], a[236:239], a[56:59], v[236:239]\n\t"
    "ds_read_b128 v[88:91], v52 offset:0\n\t"
    "v_mfma_f32_16x16x32_bf16 v[236:239], a[172:175], a[60:63], v[236:239]\n\t"
    "ds_read_b128 v[92:95], v52 offset:1024\n\t"
    "v_mfma_f32_16x16x32_bf16 v[204:207], a[172:175], a[28:31], v[204:207]\n\t"
    "ds_read_b128 v[120:123], v52 offset:2048\n\t"
    "v_mfma_f32_16x16x32_bf16 v[204:207], a[236:239], a[24:27], v[204:207]\n\t"
    "ds_read_b128 v[124:127], v52 offset:3072\n\t"
    "v_mfma_f32_16x16x32_bf16 v[172:175], a[236:239], a[104:107], v[172:175]\n\t"
    "ds_read_b128 v[152:155], v52 offset:4096\n\t"
    "v_mfma_f32_16x16x32_bf16 v[172:175], a[172:175], a[108:111], v[172:175]\n\t"
    "ds_read_b128 v[156:159], v52 offset:5120\n\t"
    "v_mfma_f32_16x16x32_bf16 v[140:143], a[172:175], a[76:79], v[140:143]\n\t"
    "ds_read_b128 v[184:187], v52 offset:6144\n\t"
    "v_mfma_f32_16x16x32_bf16 v[140:143], a[236:239], a[72:75], v[140:143]\n\t"
    "ds_read_b128 v[188:191], v52 offset:7168\n\t"
    "v_mfma_f32_16x16x32_bf16 v[108:111], a[236:239], a[40:43], v[108:111]\n\t"
    "ds_read_b128 v[216:219], v52 offset:8192\n\t"
    "v_mfma_f32_16x16x32_bf16 v[108:111], a[172:175], a[44:47], v[108:111]\n\t"
    "ds_read_b128 v[220:223], v52 offset:9216\n\t"
    "v_mfma_f32_16x16x32_bf16 v[76:79], a[172:175], a[12:15], v[76:79]\n\t"
    "ds_read_b128 v[248:251], v52 offset:10240\n\t"
    "v_mfma_f32_16x16x32_bf16 v[76:79], a[236:239], a[8:11], v[76:79]\n\t"
    "ds_read_b128 v[252:255], v52 offset:11264\n\t"
    "v_mfma_f32_16x16x32_bf16 v[72:75], a[232:235], a[8:11], v[72:75]\n\t"
    "v_mfma_f32_16x16x32_bf16 v[72:75], a[168:171], a[12:15], v[72:75]\n\t"
    "buffer_load_dwordx4 a[240:243], v43, s[20:23], s62 offen sc1\n\t"
    "v_mfma_f32_16x16x32_bf16 v[104:107], a[168:171], a[44:47], v[104:107]\n\t"
    "v_mfma_f32_16x16x32_bf16 v[104:107], a[232:235], a[40:43], v[104:107]\n\t"
    "buffer_load_dwordx4 a[244:247], v43, s[20:23], s62 offen offset:1024 sc1\n\t"
    "v_mfma_f32_16x16x32_bf16 v[136:139], a[232:235], a[72:75], v[136:139]\n\t"
    "v_mfma_f32_16x16x32_bf16 v[136:139], a[168:171], a[76:79], v[136:139]\n\t"
    "buffer_load_dwordx4 a[248:251], v44, s[20:23], s62 offen sc1\n\t"
    "v_mfma_f32_16x16x32_bf16 v[168:171], a[168:171], a[108:111], v[168:171]\n\t"
    "v_mfma_f32_16x16x32_bf16 v[168:171], a[232:235], a[104:107], v[168:171]\n\t"
    "buffer_load_dwordx4 a[252:255], v44, s[20:23], s62 offen offset:1024 sc1\n\t"
    "v_mfma_f32_16x16x32_bf16 v[200:203], a[232:235], a[24:27], v[200:203]\n\t"
    "v_mfma_f32_16x16x32_bf16 v[200:203], a[168:171], a[28:31], v[200:203]\n\t"
    "s_waitcnt lgkmcnt(0)\n\t"
    "v_add_f32_e32 v80, v88, v80\n\t"
    "v_add_f32_e32 v81, v89, v81\n\t"
    "v_mfma_f32_16x16x32_bf16 v[232:235], a[168:171], a[60:63], v[232:235]\n\t"
    "v_add_f32_e32 v82, v90, v82\n\t"
    "v_add_f32_e32 v83, v91, v83\n\t"
    "v_mfma_f32_16x16x32_bf16 v[232:235], a[232:235], a[56:59], v[232:235]\n\t"
    "v_add_f32_e32 v84, v92, v84\n\t"
    "v_add_f32_e32 v85, v93, v85\n\t"
    "v_mfma_f32_16x16x32_bf16 v[228:231], a[228:231], a[56:59], v[228:231]\n\t"
    "v_add_f32_e32 v86, v94, v86\n\t"
    "v_add_f32_e32 v87, v95, v87\n\t"
    "v_mfma_f32_16x16x32_bf16 v[228:231], a[164:167], a[60:63], v[228:231]\n\t"
    "v_cvt_pk_bf16_f32 v88, v80, v81\n\t"
    "v_cvt_pk_bf16_f32 v89, v82, v83\n\t"
    "v_mfma_f32_16x16x32_bf16 v[196:199], a[164:167], a[28:31], v[196:199]\n\t"
    "v_cvt_pk_bf16_f32 v90, v84, v85\n\t"
    "v_cvt_pk_bf16_f32 v91, v86, v87\n\t"
    "v_mfma_f32_16x16x32_bf16 v[196:199], a[228:231], a[24:27], v[196:199]\n\t"
    "buffer_store_dwordx4 v[88:91], v31, s[48:51], s73 offen sc1\n\t"
    "v_mfma_f32_16x16x32_bf16 v[164:167], a[228:231], a[104:107], v[164:167]\n\t"
    "v_add_f32_e32 v112, v120, v112\n\t"
    "v_add_f32_e32 v113, v121, v113\n\t"
    "v_mfma_f32_16x16x32_bf16 v[164:167], a[164:167], a[108:111], v[164:167]\n\t"
    "v_add_f32_e32 v114, v122, v114\n\t"
    "v_add_f32_e32 v115, v123, v115\n\t"
    "v_mfma_f32_16x16x32_bf16 v[132:135], a[164:167], a[76:79], v[132:135]\n\t"
    "v_add_f32_e32 v116, v124, v116\n\t"
    "v_add_f32_e32 v117, v125, v117\n\t"
    "v_mfma_f32_16x16x32_bf16 v[132:135], a[228:231], a[72:75], v[132:135]\n\t"
    "v_add_f32_e32 v118, v126, v118\n\t"
    "v_add_f32_e32 v119, v127, v119\n\t"
    "v_mfma_f32_16x16x32_bf16 v[100:103], a[228:231], a[40:43], v[100:103]\n\t"
    "v_cvt_pk_bf16_f32 v120, v112, v113\n\t"
    "v_cvt_pk_bf16_f32 v121, v114, v115\n\t"
    "v_mfma_f32_16x16x32_bf16 v[100:103], a[164:167], a[44:47], v[100:103]\n\t"
    "v_cvt_pk_bf16_f32 v122, v116, v117\n\t"
    "v_cvt_pk_bf16_f32 v123, v118, v119\n\t"
    "v_mfma_f32_16x16x32_bf16 v[68:71], a[164:167], a[12:15], v[68:71]\n\t"
    "buffer_store_dwordx4 v[120:123], v31, s[48:51], s73 offen offset:1024 sc1\n\t"
    "v_mfma_f32_16x16x32_bf16 v[68:71], a[228:231], a[8:11], v[68:71]\n\t"
    "v_add_f32_e32 v144, v152, v144\n\t"
    "v_add_f32_e32 v145, v153, v145\n\t"
    "v_mfma_f32_16x16x32_bf16 v[64:67], a[224:227], a[8:11], v[64:67]\n\t"
    "v_add_f32_e32 v146, v154, v146\n\t"
    "v_add_f32_e32 v147, v155, v147\n\t"
    "v_mfma_f32_16x16x32_bf16 v[64:67], a[160:163], a[12:15], v[64:67]\n\t"
    "v_add_f32_e32 v148, v156, v148\n\t"
    "v_add_f32_e32 v149, v157, v149\n\t"
    "v_mfma_f32_16x16x32_bf16 v[96:99], a[160:163], a[44:47], v[96:99]\n\t"
    "v_add_f32_e32 v150, v158, v150\n\t"
    "v_add_f32_e32 v151, v159, v151\n\t"
    "v_mfma_f32_16x16x32_bf16 v[96:99], a[224:227], a[40:43], v[96:99]\n\t"
    "v_cvt_pk_bf16_f32 v152, v144, v145\n\t"
    "v_cvt_pk_bf16_f32 v153, v146, v147\n\t"
    "v_mfma_f32_16x16x32_bf16 v[128:131], a[224:227], a[72:75], v[128:131]\n\t"
    "v_cvt_pk_bf16_f32 v154, v148, v149\n\t"
    "v_cvt_pk_bf16_f32 v155, v150, v151\n\t"
    "v_mfma_f32_16x16x32_bf16 v[128:131], a[160:163], a[76:79], v[128:131]\n\t"
    "buffer_store_dwordx4 v[152:155], v31, s[48:51], s73 offen offset:2048 sc1\n\t"
    "v_mfma_f32_16x16x32_bf16 v[160:163], a[160:163], a[108:111], v[160:163]\n\t"
    "v_add_f32_e32 v176, v184, v176\n\t"
    "v_add_f32_e32 v177, v185, v177\n\t"
    "v_mfma_f32_16x16x32_bf16 v[160:163], a[224:227], a[104:107], v[160:163]\n\t"
    "v_add_f32_e32 v178, v186, v178\n\t"
    "v_add_f32_e32 v179, v187, v179\n\t"
    "v_mfma_f32_16x16x32_bf16 v[192:195], a[224:227], a[24:27], v[192:195]\n\t"
    "v_add_f32_e32 v180, v188, v180\n\t"
    "v_add_f32_e32 v181, v189, v181\n\t"
    "v_mfma_f32_16x16x32_bf16 v[192:195], a[160:163], a[28:31], v[192:195]\n\t"
    "v_add_f32_e32 v182, v190, v182\n\t"
    "v_add_f32_e32 v183, v191, v183\n\t"
    "v_mfma_f32_16x16x32_bf16 v[224:227], a[160:163], a[60:63], v[224:227]\n\t"
    "v_cvt_pk_bf16_f32 v184, v176, v177\n\t"
    "v_cvt_pk_bf16_f32 v185, v178, v179\n\t"
    "v_mfma_f32_16x16x32_bf16 v[224:227], a[224:227], a[56:59], v[224:227]\n\t"
    "v_cvt_pk_bf16_f32 v186, v180, v181\n\t"
    "v_cvt_pk_bf16_f32 v187, v182, v183\n\t"
    "v_mfma_f32_16x16x32_bf16 v[236:239], a[204:207], v[12:15], v[236:239]\n\t"
    "buffer_store_dwordx4 v[184:187], v31, s[48:51], s73 offen offset:3072 sc1\n\t"
    "v_mfma_f32_16x16x32_bf16 v[236:239], a[140:143], v[16:19], v[236:239]\n\t"
    "s_add_u32 s73, s73, 0x1000\n\t"
    "v_add_f32_e32 v208, v216, v208\n\t"
    "v_add_f32_e32 v209, v217, v209\n\t"
    "v_mfma_f32_16x16x32_bf16 v[204:207], a[140:143], v[8:11], v[204:207]\n\t"
    "v_add_f32_e32 v210, v218, v210\n\t"
    "v_add_f32_e32 v211, v219, v211\n\t"
    "v_mfma_f32_16x16x32_bf16 v[204:207], a[204:207], v[4:7], v[204:207]\n\t"
    "v_add_f32_e32 v212, v220, v212\n\t"
    "v_add_f32_e32 v213, v221, v213\n\t"
    "v_mfma_f32_16x16x32_bf16 v[172:175], a[204:207], a[120:123], v[172:175]\n\t"
    "v_add_f32_e32 v214, v222, v214\n\t"
    "v_add_f32_e32 v215, v223, v215\n\t"
    "v_mfma_f32_16x16x32_bf16 v[172:175], a[140:143], a[124:127], v[172:175]\n\t"
    "v_cvt_pk_bf16_f32 v216, v208, v209\n\t"
    "v_cvt_pk_bf16_f32 v217, v210, v211\n\t"
    "v_mfma_f32_16x16x32_bf16 v[140:143], a[140:143], a[116:119], v[140:143]\n\t"
    "v_cvt_pk_bf16_f32 v218, v212, v213\n\t"
    "v_cvt_pk_bf16_f32 v219, v214, v215\n\t"
    "v_mfma_f32_16x16x32_bf16 v[140:143], a[204:207], a[112:115], v[140:143]\n\t"
    "buffer_store_dwordx4 v[216:219], v31, s[48:51], s73 offen sc1\n\t"
    "v_mfma_f32_16x16x32_bf16 v[108:111], a[204:207], a[88:91], v[108:111]\n\t"
    "v_add_f32_e32 v240, v248, v240\n\t"
    "v_add_f32_e32 v241, v249, v241\n\t"
    "v_mfma_f32_16x16x32_bf16 v[108:111], a[140:143], a[92:95], v[108:111]\n\t"
    "v_add_f32_e32 v242, v250, v242\n\t"
    "v_add_f32_e32 v243, v251, v243\n\t"
    "v_mfma_f32_16x16x32_bf16 v[76:79], a[140:143], a[84:87], v[76:79]\n\t"
    "v_add_f32_e32 v244, v252, v244\n\t"
    "v_add_f32_e32 v245, v253, v245\n\t"
    "v_mfma_f32_16x16x32_bf16 v[76:79], a[204:207], a[80:83], v[76:79]\n\t"
    "v_add_f32_e32 v246, v254, v246\n\t"
    "v_add_f32_e32 v247, v255, v247\n\t"
    "v_mfma_f32_16x16x32_bf16 v[72:75], a[200:203], a[80:83], v[72:75]\n\t"
    "v_cvt_pk_bf16_f32 v248, v240, v241\n\t"
    "v_cvt_pk_bf16_f32 v249, v242, v243\n\t"
    "v_mfma_f32_16x16x32_bf16 v[72:75], a[136:139], a[84:87], v[72:75]\n\t"
    "v_cvt_pk_bf16_f32 v250, v244, v245\n\t"
    "v_cvt_pk_bf16_f32 v251, v246, v247\n\t"
    "v_mfma_f32_16x16x32_bf16 v[104:107], a[136:139], a[92:95], v[104:107]\n\t"
    "buffer_store_dwordx4 v[248:251], v31, s[48:51], s73 offen offset:1024 sc1\n\t"
    "v_mfma_f32_16x16x32_bf16 v[104:107], a[200:203], a[88:91], v[104:107]\n\t"
    "ds_read_b64_tr_b16 a[238:239], v48 offset:34048\n\t"
    "v_mfma_f32_16x16x32_bf16 v[136:139], a[200:203], a[112:115], v[136:139]\n\t"
    "ds_read_b64_tr_b16 a[174:175], v48 offset:42240\n\t"
    "v_mfma_f32_16x16x32_bf16 v[136:139], a[136:139], a[116:119], v[136:139]\n\t"
    "v_mfma_f32_16x16x32_bf16 v[168:171], a[136:139], a[124:127], v[168:171]\n\t"
    "ds_read_b64_tr_b16 a[236:237], v48 offset:33024\n\t"
    "v_mfma_f32_16x16x32_bf16 v[168:171], a[200:203], a[120:123], v[168:171]\n\t"
    "v_mfma_f32_16x16x32_bf16 v[200:203], a[200:203], v[4:7], v[200:203]\n\t"
    "ds_read_b64_tr_b16 a[172:173], v48 offset:41216\n\t"
    "v_mfma_f32_16x16x32_bf16 v[200:203], a[136:139], v[8:11], v[200:203]\n\t"
    "v_mfma_f32_16x16x32_bf16 v[232:235], a[136:139], v[16:19], v[232:235]\n\t"
    "ds_read_b64_tr_b16 a[234:235], v45 offset:34048\n\t"
    "v_mfma_f32_16x16x32_bf16 v[232:235], a[200:203], v[12:15], v[232:235]\n\t"
    "ds_read_b64_tr_b16 a[170:171], v45 offset:42240\n\t"
    "v_mfma_f32_16x16x32_bf16 v[228:231], a[196:199], v[12:15], v[228:231]\n\t"
    "v_mfma_f32_16x16x32_bf16 v[228:231], a[132:135], v[16:19], v[228:231]\n\t"
    "ds_read_b64_tr_b16 a[232:233], v45 offset:33024\n\t"
    "v_mfma_f32_16x16x32_bf16 v[196:199], a[132:135], v[8:11], v[196:199]\n\t"
    "v_mfma_f32_16x16x32_bf16 v[196:199], a[196:199], v[4:7], v[196:199]\n\t"
    "ds_read_b64_tr_b16 a[168:169], v45 offset:41216\n\t"
    "v_mfma_f32_16x16x32_bf16 v[164:167], a[196:199], a[120:123], v[164:167]\n\t"
    "v_mfma_f32_16x16x32_bf16 v[164:167], a[132:135], a[124:127], v[164:167]\n\t"
    "ds_read_b64_tr_b16 a[230:231], v48 offset:33792\n\t"
    "v_mfma_f32_16x16x32_bf16 v[132:135], a[132:135], a[116:119], v[132:135]\n\t"
    "ds_read_b64_tr_b16 a[166:167], v48 offset:41984\n\t"
    "v_mfma_f32_16x16x32_bf16 v[132:135], a[196:199], a[112:115], v[132:135]\n\t"
    "v_mfma_f32_16x16x32_bf16 v[100:103], a[196:199], a[88:91], v[100:103]\n\t"
    "ds_read_b64_tr_b16 a[228:229], v48 offset:32768\n\t"
    "v_mfma_f32_16x16x32_bf16 v[100:103], a[132:135], a[92:95], v[100:103]\n\t"
    "v_mfma_f32_16x16x32_bf16 v[68:71], a[132:135], a[84:87], v[68:71]\n\t"
    "ds_read_b64_tr_b16 a[164:165], v48 offset:40960\n\t"
    "v_mfma_f32_16x16x32_bf16 v[68:71], a[196:199], a[80:83], v[68:71]\n\t"
    "v_mfma_f32_16x16x32_bf16 v[64:67], a[192:195], a[80:83], v[64:67]\n\t"
    "ds_read_b64_tr_b16 a[226:227], v45 offset:33792\n\t"
    "v_mfma_f32_16x16x32_bf16 v[64:67], a[128:131], a[84:87], v[64:67]\n\t"
    "ds_read_b64_tr_b16 a[162:163], v45 offset:41984\n\t"
    "v_mfma_f32_16x16x32_bf16 v[96:99], a[128:131], a[92:95], v[96:99]\n\t"
    "v_mfma_f32_16x16x32_bf16 v[96:99], a[192:195], a[88:91], v[96:99]\n\t"
    "ds_read_b64_tr_b16 a[224:225], v45 offset:32768\n\t"
    "v_mfma_f32_16x16x32_bf16 v[128:131], a[192:195], a[112:115], v[128:131]\n\t"
    "v_mfma_f32_16x16x32_bf16 v[128:131], a[128:131], a[116:119], v[128:131]\n\t"
    "ds_read_b64_tr_b16 a[160:161], v45 offset:40960\n\t"
    "v_mfma_f32_16x16x32_bf16 v[160:163], a[128:131], a[124:127], v[160:163]\n\t"
    "v_mfma_f32_16x16x32_bf16 v[160:163], a[192:195], a[120:123], v[160:163]\n\t"
    "v_mfma_f32_16x16x32_bf16 v[192:195], a[192:195], v[4:7], v[192:195]\n\t"
    "v_mfma_f32_16x16x32_bf16 v[192:195], a[128:131], v[8:11], v[192:195]\n\t"
    "v_mfma_f32_16x16x32_bf16 v[224:227], a[128:131], v[16:19], v[224:227]\n\t"
    "v_mfma_f32_16x16x32_bf16 v[224:227], a[192:195], v[12:15], v[224:227]\n\t"
    "s_waitcnt lgkmcnt(0)\n\t"
    "v_mfma_f32_16x16x32_bf16 v[236:239], a[236:239], a[48:51], v[236:239]\n\t"
    "ds_read_b64_tr_b16 a[206:207], v49 offset:34048\n\t"
    "v_mfma_f32_16x16x32_bf16 v[236:239], a[172:175], a[52:55], v[236:239]\n\t"
    "v_mfma_f32_16x16x32_bf16 v[204:207], a[172:175], a[20:23], v[204:207]\n\t"
    "ds_read_b64_tr_b16 a[142:143], v49 offset:42240\n\t"
    "v_mfma_f32_16x16x32_bf16 v[204:207], a[236:239], a[16:19], v[204:207]\n\t"
    "v_mfma_f32_16x16x32_bf16 v[172:175], a[236:239], a[96:99], v[172:175]\n\t"
    "v_mfma_f32_16x16x32_bf16 v[172:175], a[172:175], a[100:103], v[172:175]\n\t"
    "ds_read_b64_tr_b16 a[204:205], v49 offset:33024\n\t"
    "v_mfma_f32_16x16x32_bf16 v[140:143], a[172:175], a[68:71], v[140:143]\n\t"
    "v_mfma_f32_16x16x32_bf16 v[140:143], a[236:239], a[64:67], v[140:143]\n\t"
    "ds_read_b64_tr_b16 a[140:141], v49 offset:41216\n\t"
    "v_mfma_f32_16x16x32_bf16 v[108:111], a[236:239], a[32:35], v[108:111]\n\t"
    "v_mfma_f32_16x16x32_bf16 v[108:111], a[172:175], a[36:39], v[108:111]\n\t"
    "v_mfma_f32_16x16x32_bf16 v[76:79], a[172:175], a[4:7], v[76:79]\n\t"
    "ds_read_b64_tr_b16 a[202:203], v46 offset:34048\n\t"
    "v_mfma_f32_16x16x32_bf16 v[76:79], a[236:239], a[0:3], v[76:79]\n\t"
    "v_mfma_f32_16x16x32_bf16 v[72:75], a[232:235], a[0:3], v[72:75]\n\t"
    "v_mfma_f32_16x16x32_bf16 v[72:75], a[168:171], a[4:7], v[72:75]\n\t"
    "ds_read_b64_tr_b16 a[138:139], v46 offset:42240\n\t"
    "v_mfma_f32_16x16x32_bf16 v[104:107], a[168:171], a[36:39], v[104:107]\n\t"
    "v_mfma_f32_16x16x32_bf16 v[104:107], a[232:235], a[32:35], v[104:107]\n\t"
    "ds_read_b64_tr_b16 a[200:201], v46 offset:33024\n\t"
    "v_mfma_f32_16x16x32_bf16 v[136:139], a[232:235], a[64:67], v[136:139]\n\t"
    "v_mfma_f32_16x16x32_bf16 v[136:139], a[168:171], a[68:71], v[136:139]\n\t"
    "v_mfma_f32_16x16x32_bf16 v[168:171], a[168:171], a[100:103], v[168:171]\n\t"
    "ds_read_b64_tr_b16 a[136:137], v46 offset:41216\n\t"
    "v_mfma_f32_16x16x32_bf16 v[168:171], a[232:235], a[96:99], v[168:171]\n\t"
    "v_mfma_f32_16x16x32_bf16 v[200:203], a[232:235], a[16:19], v[200:203]\n\t"
    "v_mfma_f32_16x16x32_bf16 v[200:203], a[168:171], a[20:23], v[200:203]\n\t"
    "ds_read_b64_tr_b16 a[198:199], v49 offset:33792\n\t"
    "v_mfma_f32_16x16x32_bf16 v[232:235], a[168:171], a[52:55], v[232:235]\n\t"
    "s_waitcnt vmcnt(0) lgkmcnt(0)\n\t"
    "s_barrier\n\t"
    "s_cmp_lg_u32 s8, 0\n\t"
    "s_cbranch_scc1 .Lfa_pubx\n\t"
    "s_lshl_b32 s68, s9, 7\n\t"
    "s_add_u32 s68, s68, 0x880\n\t"
    "v_mov_b32_e32 v2, s68\n\t"
    "v_mov_b32_e32 v0, s96\n\t"
    "v_mov_b32_e32 v1, s97\n\t"
    "s_mov_b64 exec, 1\n\t"
    "global_store_dwordx2 v2, v[0:1], s[26:27] sc1\n\t"
    "s_mov_b64 exec, -1\n\t"
    ".Lfa_pubx:\n\t"
    "v_mfma_f32_16x16x32_bf16 v[232:235], a[232:235], a[48:51], v[232:235]\n\t"
    "ds_read_b64_tr_b16 a[134:135], v49 offset:41984\n\t"
    "v_mfma_f32_16x16x32_bf16 v[228:231], a[228:231], a[48:51], v[228:231]\n\t"
    "v_mfma_f32_16x16x32_bf16 v[228:231], a[164:167], a[52:55], v[228:231]\n\t"
    "v_mfma_f32_16x16x32_bf16 v[196:199], a[164:167], a[20:23], v[196:199]\n\t"
    "ds_read_b64_tr_b16 a[196:197], v49 offset:32768\n\t"
    "v_mfma_f32_16x16x32_bf16 v[196:199], a[228:231], a[16:19], v[196:199]\n\t"
    "v_mfma_f32_16x16x32_bf16 v[164:167], a[228:231], a[96:99], v[164:167]\n\t"
    "ds_read_b64_tr_b16 a[132:133], v49 offset:40960\n\t"
    "v_mfma_f32_16x16x32_bf16 v[164:167], a[164:167], a[100:103], v[164:167]\n\t"
    "v_mfma_f32_16x16x32_bf16 v[132:135], a[164:167], a[68:71], v[132:135]\n\t"
    "v_mfma_f32_16x16x32_bf16 v[132:135], a[228:231], a[64:67], v[132:135]\n\t"
    "ds_read_b64_tr_b16 a[194:195], v46 offset:33792\n\t"
    "v_mfma_f32_16x16x32_bf16 v[100:103], a[228:231], a[32:35], v[100:103]\n\t"
    "v_mfma_f32_16x16x32_bf16 v[100:103], a[164:167], a[36:39], v[100:103]\n\t"
    "v_mfma_f32_16x16x32_bf16 v[68:71], a[164:167], a[4:7], v[68:71]\n\t"
    "ds_read_b64_tr_b16 a[130:131], v46 offset:41984\n\t"
    "v_mfma_f32_16x16x32_bf16 v[68:71], a[228:231], a[0:3], v[68:71]\n\t"
    "v_mfma_f32_16x16x32_bf16 v[64:67], a[224:227], a[0:3], v[64:67]\n\t"
    "ds_read_b64_tr_b16 a[192:193], v46 offset:32768\n\t"
    "v_mfma_f32_16x16x32_bf16 v[64:67], a[160:163], a[4:7], v[64:67]\n\t"
    "v_mfma_f32_16x16x32_bf16 v[96:99], a[160:163], a[36:39], v[96:99]\n\t"
    "v_mfma_f32_16x16x32_bf16 v[96:99], a[224:227], a[32:35], v[96:99]\n\t"
    "ds_read_b64_tr_b16 a[128:129], v46 offset:40960\n\t"
    "v_mfma_f32_16x16x32_bf16 v[128:131], a[224:227], a[64:67], v[128:131]\n\t"
    "v_mfma_f32_16x16x32_bf16 v[128:131], a[160:163], a[68:71], v[128:131]\n\t"
    "v_mfma_f32_16x16x32_bf16 v[160:163], a[160:163], a[100:103], v[160:163]\n\t"
    "v_mfma_f32_16x16x32_bf16 v[160:163], a[224:227], a[96:99], v[160:163]\n\t"
    "v_mfma_f32_16x16x32_bf16 v[192:195], a[224:227], a[16:19], v[192:195]\n\t"
    "v_mfma_f32_16x16x32_bf16 v[192:195], a[160:163], a[20:23], v[192:195]\n\t"
    "v_mfma_f32_16x16x32_bf16 v[224:227], a[160:163], a[52:55], v[224:227]\n\t"
    "v_mfma_f32_16x16x32_bf16 v[224:227], a[224:227], a[48:51], v[224:227]\n\t"
    "s_waitcnt vmcnt(6) lgkmcnt(0)\n\t"
    "v_mfma_f32_16x16x32_bf16 v[236:239], a[204:207], a[248:251], v[236:239]\n\t"
    "v_mfma_f32_16x16x32_bf16 v[236:239], a[140:143], a[252:255], v[236:239]\n\t"
    "v_mfma_f32_16x16x32_bf16 v[204:207], a[140:143], a[244:247], v[204:207]\n\t"
    "v_mfma_f32_16x16x32_bf16 v[204:207], a[204:207], a[240:243], v[204:207]\n\t"
    "v_mfma_f32_16x16x32_bf16 v[172:175], a[204:207], a[152:155], v[172:175]\n\t"
    "v_mfma_f32_16x16x32_bf16 v[172:175], a[140:143], a[156:159], v[172:175]\n\t"
    "v_mfma_f32_16x16x32_bf16 v[140:143], a[140:143], a[148:151], v[140:143]\n\t"
    "v_mfma_f32_16x16x32_bf16 v[140:143], a[204:207], a[144:147], v[140:143]\n\t"
    "v_mfma_f32_16x16x32_bf16 v[108:111], a[204:207], a[216:219], v[108:111]\n\t"
    "v_mfma_f32_16x16x32_bf16 v[108:111], a[140:143], a[220:223], v[108:111]\n\t"
    "ds_write_b128 v51, v[236:239] offset:11264\n\t"
    "v_mfma_f32_16x16x32_bf16 v[76:79], a[140:143], a[212:215], v[76:79]\n\t"
    "v_mfma_f32_16x16x32_bf16 v[76:79], a[204:207], a[208:211], v[76:79]\n\t"
    "ds_write_b128 v51, v[204:207] offset:9216\n\t"
    "v_mfma_f32_16x16x32_bf16 v[72:75], a[200:203], a[208:211], v[72:75]\n\t"
    "v_mfma_f32_16x16x32_bf16 v[72:75], a[136:139], a[212:215], v[72:75]\n\t"
    "ds_write_b128 v51, v[172:175] offset:7168\n\t"
    "v_mfma_f32_16x16x32_bf16 v[104:107], a[136:139], a[220:223], v[104:107]\n\t"
    "v_mfma_f32_16x16x32_bf16 v[104:107], a[200:203], a[216:219], v[104:107]\n\t"
    "ds_write_b128 v51, v[140:143] offset:5120\n\t"
    "v_mfma_f32_16x16x32_bf16 v[136:139], a[200:203], a[144:147], v[136:139]\n\t"
    "v_mfma_f32_16x16x32_bf16 v[136:139], a[136:139], a[148:151], v[136:139]\n\t"
    "ds_write_b128 v51, v[108:111] offset:3072\n\t"
    "v_mfma_f32_16x16x32_bf16 v[168:171], a[136:139], a[156:159], v[168:171]\n\t"
    "v_mfma_f32_16x16x32_bf16 v[168:171], a[200:203], a[152:155], v[168:171]\n\t"
    "ds_write_b128 v51, v[76:79] offset:1024\n\t"
    "v_mfma_f32_16x16x32_bf16 v[200:203], a[200:203], a[240:243], v[200:203]\n\t"
    "v_mfma_f32_16x16x32_bf16 v[200:203], a[136:139], a[244:247], v[200:203]\n\t"
    "ds_write_b128 v51, v[72:75] offset:0\n\t"
    "v_mfma_f32_16x16x32_bf16 v[232:235], a[136:139], a[252:255], v[232:235]\n\t"
    "v_mfma_f32_16x16x32_bf16 v[232:235], a[200:203], a[248:251], v[232:235]\n\t"
    "ds_write_b128 v51, v[104:107] offset:2048\n\t"
    "v_mfma_f32_16x16x32_bf16 v[228:231], a[196:199], a[248:251], v[228:231]\n\t"
    "v_mfma_f32_16x16x32_bf16 v[228:231], a[132:135], a[252:255], v[228:231]\n\t"
    "ds_write_b128 v51, v[136:139] offset:4096\n\t"
    "v_mfma_f32_16x16x32_bf16 v[196:199], a[132:135], a[244:247], v[196:199]\n\t"
    "v_mfma_f32_16x16x32_bf16 v[196:199], a[196:199], a[240:243], v[196:199]\n\t"
    "ds_write_b128 v51, v[168:171] offset:6144\n\t"
    "v_mfma_f32_16x16x32_bf16 v[164:167], a[196:199], a[152:155], v[164:167]\n\t"
    "v_mfma_f32_16x16x32_bf16 v[164:167], a[132:135], a[156:159], v[164:167]\n\t"
    "ds_write_b128 v51, v[200:203] offset:8192\n\t"
    "v_mfma_f32_16x16x32_bf16 v[132:135], a[132:135], a[148:151], v[132:135]\n\t"
    "v_mfma_f32_16x16x32_bf16 v[132:135], a[196:199], a[144:147], v[132:135]\n\t"
    "ds_write_b128 v51, v[232:235] offset:10240\n\t"
    "v_mfma_f32_16x16x32_bf16 v[100:103], a[196:199], a[216:219], v[100:103]\n\t"
    "v_mfma_f32_16x16x32_bf16 v[100:103], a[132:135], a[220:223], v[100:103]\n\t"
    "v_mfma_f32_16x16x32_bf16 v[68:71], a[132:135], a[212:215], v[68:71]\n\t"
    "global_load_dwordx2 v[60:61], v58, s[26:27] sc1\n\t"
    "v_mfma_f32_16x16x32_bf16 v[68:71], a[196:199], a[208:211], v[68:71]\n\t"
    "v_mfma_f32_16x16x32_bf16 v[64:67], a[192:195], a[208:211], v[64:67]\n\t"
    "v_mfma_f32_16x16x32_bf16 v[64:67], a[128:131], a[212:215], v[64:67]\n\t"
    "v_mfma_f32_16x16x32_bf16 v[96:99], a[128:131], a[220:223], v[96:99]\n\t"
    "v_mfma_f32_16x16x32_bf16 v[96:99], a[192:195], a[216:219], v[96:99]\n\t"
    "v_mfma_f32_16x16x32_bf16 v[128:131], a[192:195], a[144:147], v[128:131]\n\t"
    "v_mfma_f32_16x16x32_bf16 v[128:131], a[128:131], a[148:151], v[128:131]\n\t"
    "v_mfma_f32_16x16x32_bf16 v[160:163], a[128:131], a[156:159], v[160:163]\n\t"
    "v_mfma_f32_16x16x32_bf16 v[160:163], a[192:195], a[152:155], v[160:163]\n\t"
    "v_mfma_f32_16x16x32_bf16 v[192:195], a[192:195], a[240:243], v[192:195]\n\t"
    "v_mfma_f32_16x16x32_bf16 v[192:195], a[128:131], a[244:247], v[192:195]\n\t"
    "v_mfma_f32_16x16x32_bf16 v[224:227], a[128:131], a[252:255], v[224:227]\n\t"
    "v_mfma_f32_16x16x32_bf16 v[224:227], a[192:195], a[248:251], v[224:227]\n\t"
    "s_waitcnt vmcnt(0)\n\t"
    "v_cmp_eq_u64_e32 vcc, s[96:97], v[60:61]\n\t"
    "s_nop 1\n\t"
    "s_and_b32 s68, vcc_lo, s24\n\t"
    "s_cmp_eq_u32 s68, s24\n\t"
    "s_cbranch_scc0 .Lfa_stage3\n\t"
    ".Lfa_back4:\n\t"
    "buffer_load_dwordx4 v[92:95], v31, s[48:51], s74 offen sc1\n\t"
    "buffer_load_dwordx4 v[124:127], v31, s[48:51], s74 offen offset:1024 sc1\n\t"
    "buffer_load_dwordx4 v[156:159], v31, s[48:51], s74 offen offset:2048 sc1\n\t"
    "buffer_load_dwordx4 v[188:191], v31, s[48:51], s74 offen offset:3072 sc1\n\t"
    "s_add_u32 s74, s74, 0x1000\n\t"
    "buffer_load_dwordx4 v[220:223], v31, s[48:51], s74 offen sc1\n\t"
    "buffer_load_dwordx4 v[252:255], v31, s[48:51], s74 offen offset:1024 sc1\n\t"
    "s_waitcnt lgkmcnt(0)\n\t"
    "s_barrier\n\t"
    "ds_read_b128 v[72:75], v52 offset:0\n\t"
    "ds_read_b128 v[76:79], v52 offset:1024\n\t"
    "ds_read_b128 v[104:107], v52 offset:2048\n\t"
    "ds_read_b128 v[108:111], v52 offset:3072\n\t"
    "ds_read_b128 v[136:139], v52 offset:4096\n\t"
    "ds_read_b128 v[140:143], v52 offset:5120\n\t"
    "ds_read_b128 v[168:171], v52 offset:6144\n\t"
    "ds_read_b128 v[172:175], v52 offset:7168\n\t"
    "ds_read_b128 v[200:203], v52 offset:8192\n\t"
    "ds_read_b128 v[204:207], v52 offset:9216\n\t"
    "ds_read_b128 v[232:235], v52 offset:10240\n\t"
    "ds_read_b128 v[236:239], v52 offset:11264\n\t"
    "s_waitcnt lgkmcnt(10)\n\t"
    "v_pk_add_f32 v[64:65], v[64:65], v[72:73]\n\t"
    "v_pk_add_f32 v[66:67], v[66:67], v[74:75]\n\t"
    "v_pk_add_f32 v[68:69], v[68:69], v[76:77]\n\t"
    "v_pk_add_f32 v[70:71], v[70:71], v[78:79]\n\t"
    "s_waitcnt lgkmcnt(8)\n\t"
    "v_pk_add_f32 v[96:97], v[96:97], v[104:105]\n\t"
    "v_pk_add_f32 v[98:99], v[98:99], v[106:107]\n\t"
    "v_pk_add_f32 v[100:101], v[100:101], v[108:109]\n\t"
    "v_pk_add_f32 v[102:103], v[102:103], v[110:111]\n\t"
    "s_waitcnt lgkmcnt(6)\n\t"
    "v_pk_add_f32 v[128:129], v[128:129], v[136:137]\n\t"
    "v_pk_add_f32 v[130:131], v[130:131], v[138:139]\n\t"
    "v_pk_add_f32 v[132:133], v[132:133], v[140:141]\n\t"
    "v_pk_add_f32 v[134:135], v[134:135], v[142:143]\n\t"
    "s_waitcnt lgkmcnt(4)\n\t"
    "v_pk_add_f32 v[160:161], v[160:161], v[168:169]\n\t"
    "v_pk_add_f32 v[162:163], v[162:163], v[170:171]\n\t"
    "v_pk_add_f32 v[164:165], v[164:165], v[172:173]\n\t"
    "v_pk_add_f32 v[166:167], v[166:167], v[174:175]\n\t"
    "s_waitcnt lgkmcnt(2)\n\t"
    "v_pk_add_f32 v[192:193], v[192:193], v[200:201]\n\t"
    "v_pk_add_f32 v[194:195], v[194:195], v[202:203]\n\t"
    "v_pk_add_f32 v[196:197], v[196:197], v[204:205]\n\t"
    "v_pk_add_f32 v[198:199], v[198:199], v[206:207]\n\t"
    "s_waitcnt lgkmcnt(0)\n\t"
    "v_pk_add_f32 v[224:225], v[224:225], v[232:233]\n\t"
    "v_pk_add_f32 v[226:227], v[226:227], v[234:235]\n\t"
    "v_pk_add_f32 v[228:229], v[228:229], v[236:237]\n\t"
    "v_pk_add_f32 v[230:231], v[230:231], v[238:239]\n\t"
    "s_waitcnt vmcnt(5)\n\t"
    "v_lshlrev_b32_e32 v80, 16, v92\n\t"
    "v_and_b32_e32 v81, 0xffff0000, v92\n\t"
    "v_lshlrev_b32_e32 v82, 16, v93\n\t"
    "v_and_b32_e32 v83, 0xffff0000, v93\n\t"
    "v_lshlrev_b32_e32 v84, 16, v94\n\t"
    "v_and_b32_e32 v85, 0xffff0000, v94\n\t"
    "v_lshlrev_b32_e32 v86, 16, v95\n\t"
    "v_and_b32_e32 v87, 0xffff0000, v95\n\t"
    "v_pk_add_f32 v[64:65], v[64:65], v[80:81]\n\t"
    "v_pk_add_f32 v[66:67], v[66:67], v[82:83]\n\t"
    "v_pk_add_f32 v[68:69], v[68:69], v[84:85]\n\t"
    "v_pk_add_f32 v[70:71], v[70:71], v[86:87]\n\t"
    "v_cvt_pk_bf16_f32 v88, v64, v65\n\t"
    "v_cvt_pk_bf16_f32 v89, v66, v67\n\t"
    "v_cvt_pk_bf16_f32 v90, v68, v69\n\t"
    "v_cvt_pk_bf16_f32 v91, v70, v71\n\t"
    "s_mov_b32 s68, 0\n\t"
    "s_nop 0\n\t"
    "v_permlane32_swap_b32_e32 v88, v90\n\t"
    "v_permlane32_swap_b32_e32 v89, v91\n\t"
    "s_nop 0\n\t"
    "v_permlane16_swap_b32_e32 v88, v90\n\t"
    "v_permlane16_swap_b32_e32 v89, v91\n\t"
    "buffer_store_dwordx4 v[88:91], v57, s[4:7], s68 offen sc1\n\t"
    "s_waitcnt vmcnt(5)\n\t"
    "v_lshlrev_b32_e32 v112, 16, v124\n\t"
    "v_and_b32_e32 v113, 0xffff0000, v124\n\t"
    "v_lshlrev_b32_e32 v114, 16, v125\n\t"
    "v_and_b32_e32 v115, 0xffff0000, v125\n\t"
    "v_lshlrev_b32_e32 v116, 16, v126\n\t"
    "v_and_b32_e32 v117, 0xffff0000, v126\n\t"
    "v_lshlrev_b32_e32 v118, 16, v127\n\t"
    "v_and_b32_e32 v119, 0xffff0000, v127\n\t"
    "v_pk_add_f32 v[96:97], v[96:97], v[112:113]\n\t"
    "v_pk_add_f32 v[98:99], v[98:99], v[114:115]\n\t"
    "v_pk_add_f32 v[100:101], v[100:101], v[116:117]\n\t"
    "v_pk_add_f32 v[102:103], v[102:103], v[118:119]\n\t"
    "v_cvt_pk_bf16_f32 v120, v96, v97\n\t"
    "v_cvt_pk_bf16_f32 v121, v98, v99\n\t"
    "v_cvt_pk_bf16_f32 v122, v100, v101\n\t"
    "v_cvt_pk_bf16_f32 v123, v102, v103\n\t"
    "s_mov_b32 s68, 0x10000\n\t"
    "s_nop 0\n\t"
    "v_permlane32_swap_b32_e32 v120, v122\n\t"
    "v_permlane32_swap_b32_e32 v121, v123\n\t"
    "s_nop 0\n\t"
    "v_permlane16_swap_b32_e32 v120, v122\n\t"
    "v_permlane16_swap_b32_e32 v121, v123\n\t"
    "buffer_store_dwordx4 v[120:123], v57, s[4:7], s68 offen sc1\n\t"
    "s_waitcnt vmcnt(5)\n\t"
    "v_lshlrev_b32_e32 v144, 16, v156\n\t"
    "v_and_b32_e32 v145, 0xffff0000, v156\n\t"
    "v_lshlrev_b32_e32 v146, 16, v157\n\t"
    "v_and_b32_e32 v147, 0xffff0000, v157\n\t"
    "v_lshlrev_b32_e32 v148, 16, v158\n\t"
    "v_and_b32_e32 v149, 0xffff0000, v158\n\t"
    "v_lshlrev_b32_e32 v150, 16, v159\n\t"
    "v_and_b32_e32 v151, 0xffff0000, v159\n\t"
    "v_pk_add_f32 v[128:129], v[128:129], v[144:145]\n\t"
    "v_pk_add_f32 v[130:131], v[130:131], v[146:147]\n\t"
    "v_pk_add_f32 v[132:133], v[132:133], v[148:149]\n\t"
    "v_pk_add_f32 v[134:135], v[134:135], v[150:151]\n\t"
    "v_cvt_pk_bf16_f32 v152, v128, v129\n\t"
    "v_cvt_pk_bf16_f32 v153, v130, v131\n\t"
    "v_cvt_pk_bf16_f32 v154, v132, v133\n\t"
    "v_cvt_pk_bf16_f32 v155, v134, v135\n\t"
    "s_mov_b32 s68, 0x20000\n\t"
    "s_nop 0\n\t"
    "v_permlane32_swap_b32_e32 v152, v154\n\t"
    "v_permlane32_swap_b32_e32 v153, v155\n\t"
    "s_nop 0\n\t"
    "v_permlane16_swap_b32_e32 v152, v154\n\t"
    "v_permlane16_swap_b32_e32 v153, v155\n\t"
    "buffer_store_dwordx4 v[152:155], v57, s[4:7], s68 offen sc1\n\t"
    "s_waitcnt vmcnt(5)\n\t"
    "v_lshlrev_b32_e32 v176, 16, v188\n\t"
    "v_and_b32_e32 v177, 0xffff0000, v188\n\t"
    "v_lshlrev_b32_e32 v178, 16, v189\n\t"
    "v_and_b32_e32 v179, 0xffff0000, v189\n\t"
    "v_lshlrev_b32_e32 v180, 16, v190\n\t"
    "v_and_b32_e32 v181, 0xffff0000, v190\n\t"
    "v_lshlrev_b32_e32 v182, 16, v191\n\t"
    "v_and_b32_e32 v183, 0xffff0000, v191\n\t"
    "v_pk_add_f32 v[160:161], v[160:161], v[176:177]\n\t"
    "v_pk_add_f32 v[162:163], v[162:163], v[178:179]\n\t"
    "v_pk_add_f32 v[164:165], v[164:165], v[180:181]\n\t"
    "v_pk_add_f32 v[166:167], v[166:167], v[182:183]\n\t"
    "v_cvt_pk_bf16_f32 v184, v160, v161\n\t"
    "v_cvt_pk_bf16_f32 v185, v162, v163\n\t"
    "v_cvt_pk_bf16_f32 v186, v164, v165\n\t"
    "v_cvt_pk_bf16_f32 v187, v166, v167\n\t"
    "s_mov_b32 s68, 0x30000\n\t"
    "s_nop 0\n\t"
    "v_permlane32_swap_b32_e32 v184, v186\n\t"
    "v_permlane32_swap_b32_e32 v185, v187\n\t"
    "s_nop 0\n\t"
    "v_permlane16_swap_b32_e32 v184, v186\n\t"
    "v_permlane16_swap_b32_e32 v185, v187\n\t"
    "buffer_store_dwordx4 v[184:187], v57, s[4:7], s68 offen sc1\n\t"
    "s_waitcnt vmcnt(5)\n\t"
    "v_lshlrev_b32_e32 v208, 16, v220\n\t"
    "v_and_b32_e32 v209, 0xffff0000, v220\n\t"
    "v_lshlrev_b32_e32 v210, 16, v221\n\t"
    "v_and_b32_e32 v211, 0xffff0000, v221\n\t"
    "v_lshlrev_b32_e32 v212, 16, v222\n\t"
    "v_and_b32_e32 v213, 0xffff0000, v222\n\t"
    "v_lshlrev_b32_e32 v214, 16, v223\n\t"
    "v_and_b32_e32 v215, 0xffff0000, v223\n\t"
    "v_pk_add_f32 v[192:193], v[192:193], v[208:209]\n\t"
    "v_pk_add_f32 v[194:195], v[194:195], v[210:211]\n\t"
    "v_pk_add_f32 v[196:197], v[196:197], v[212:213]\n\t"
    "v_pk_add_f32 v[198:199], v[198:199], v[214:215]\n\t"
    "v_cvt_pk_bf16_f32 v216, v192, v193\n\t"
    "v_cvt_pk_bf16_f32 v217, v194, v195\n\t"
    "v_cvt_pk_bf16_f32 v218, v196, v197\n\t"
    "v_cvt_pk_bf16_f32 v219, v198, v199\n\t"
    "s_mov_b32 s68, 0x40000\n\t"
    "s_nop 0\n\t"
    "v_permlane32_swap_b32_e32 v216, v218\n\t"
    "v_permlane32_swap_b32_e32 v217, v219\n\t"
    "s_nop 0\n\t"
    "v_permlane16_swap_b32_e32 v216, v218\n\t"
    "v_permlane16_swap_b32_e32 v217, v219\n\t"
    "buffer_store_dwordx4 v[216:219], v57, s[4:7], s68 offen sc1\n\t"
    "s_cmp_lg_u32 s77, 0\n\t"
    "s_cbranch_scc1 .Lfa_skipc\n\t"
    "s_waitcnt vmcnt(5)\n\t"
    "v_lshlrev_b32_e32 v240, 16, v252\n\t"
    "v_and_b32_e32 v241, 0xffff0000, v252\n\t"
    "v_lshlrev_b32_e32 v242, 16, v253\n\t"
    "v_and_b32_e32 v243, 0xffff0000, v253\n\t"
    "v_lshlrev_b32_e32 v244, 16, v254\n\t"
    "v_and_b32_e32 v245, 0xffff0000, v254\n\t"
    "v_lshlrev_b32_e32 v246, 16, v255\n\t"
    "v_and_b32_e32 v247, 0xffff0000, v255\n\t"
    "v_pk_add_f32 v[224:225], v[224:225], v[240:241]\n\t"
    "v_pk_add_f32 v[226:227], v[226:227], v[242:243]\n\t"
    "v_pk_add_f32 v[228:229], v[228:229], v[244:245]\n\t"
    "v_pk_add_f32 v[230:231], v[230:231], v[246:247]\n\t"
    "v_cvt_pk_bf16_f32 v248, v224, v225\n\t"
    "v_cvt_pk_bf16_f32 v249, v226, v227\n\t"
    "v_cvt_pk_bf16_f32 v250, v228, v229\n\t"
    "v_cvt_pk_bf16_f32 v251, v230, v231\n\t"
    "s_mov_b32 s68, 0x50000\n\t"
    "s_nop 0\n\t"
    "v_permlane32_swap_b32_e32 v248, v250\n\t"
    "v_permlane32_swap_b32_e32 v249, v251\n\t"
    "s_nop 0\n\t"
    "v_permlane16_swap_b32_e32 v248, v250\n\t"
    "v_permlane16_swap_b32_e32 v249, v251\n\t"
    "buffer_store_dwordx4 v[248:251], v57, s[4:7], s68 offen sc1\n\t"
    ".Lfa_skipc:\n\t"
    "s_endpgm\n\t"
    ".Lfa_stage1:\n\t"
    "s_mov_b32 s86, 0x100000\n\t"
    ".Lfa_poll1:\n\t"
    "global_load_dwordx2 v[60:61], v58, s[26:27] sc1\n\t"
    "s_waitcnt vmcnt(0)\n\t"
    "v_cmp_eq_u64_e32 vcc, s[96:97], v[60:61]\n\t"
    "s_nop 1\n\t"
    "s_and_b32 s68, vcc_lo, s24\n\t"
    "s_cmp_eq_u32 s68, s24\n\t"
    "s_cbranch_scc1 .Lfa_poll1_ok\n\t"
    "s_sleep 2\n\t"
    "s_sub_u32 s86, s86, 1\n\t"
    "s_cmp_lg_u32 s86, 0\n\t"
    "s_cbranch_scc1 .Lfa_poll1\n\t"
    ".Lfa_poll1_ok:\n\t"
    "s_branch .Lfa_back1\n\t"
    ".Lfa_stage3:\n\t"
    "s_mov_b32 s86, 0x100000\n\t"
    ".Lfa_poll3:\n\t"
    "global_load_dwordx2 v[60:61], v58, s[26:27] sc1\n\t"
    "s_waitcnt vmcnt(0)\n\t"
    "v_cmp_eq_u64_e32 vcc, s[96:97], v[60:61]\n\t"
    "s_nop 1\n\t"
    "s_and_b32 s68, vcc_lo, s24\n\t"
    "s_cmp_eq_u32 s68, s24\n\t"
    "s_cbranch_scc1 .Lfa_poll3_ok\n\t"
    "s_sleep 2\n\t"
    "s_sub_u32 s86, s86, 1\n\t"
    "s_cmp_lg_u32 s86, 0\n\t"
    "s_cbranch_scc1 .Lfa_poll3\n\t"
    ".Lfa_poll3_ok:\n\t"
    "s_branch .Lfa_back4\n\t"
    ".Lfa_stage2:\n\t"
    "s_mov_b32 s86, 0x100000\n\t"
    ".Lfa_poll2:\n\t"
    "global_load_dwordx2 v[60:61], v58, s[26:27] sc1\n\t"
    "s_waitcnt vmcnt(0)\n\t"
    "v_cmp_eq_u64_e32 vcc, s[96:97], v[60:61]\n\t"
    "s_nop 1\n\t"
    "s_and_b32 s68, vcc_lo, s24\n\t"
    "s_cmp_eq_u32 s68, s24\n\t"
    "s_cbranch_scc1 .Lfa_poll2_ok\n\t"
    "s_sleep 2\n\t"
    "s_sub_u32 s86, s86, 1\n\t"
    "s_cmp_lg_u32 s86, 0\n\t"
    "s_cbranch_scc1 .Lfa_poll2\n\t"
    ".Lfa_poll2_ok:\n\t"
    "s_branch .Lfa_back2\n\t"
    ".Lfa_epoch:\n\t"
    "v_mov_b32_e32 v2, 0\n\t"
    "v_mov_b32_e32 v0, s96\n\t"
    "v_mov_b32_e32 v1, s97\n\t"
    "s_mov_b64 exec, 1\n\t"
    "global_store_dwordx2 v2, v[0:1], s[26:27] sc1\n\t"
    "s_mov_b64 exec, -1\n\t"
    "s_nop 3\n\t"
    "s_branch .Lfa_back3\n\t"
    ".Lfa_slow:\n\t"
    "s_waitcnt vmcnt(0) lgkmcnt(0)\n\t"
    "s_mov_b32 s15, -1\n\t"
    "s_mov_b32 s87, 0\n\t"
    "s_cmp_lg_u32 s77, 0\n\t"
    "s_mov_b32 s75, 0x7149f2ca\n\t"
    "s_cselect_b32 s75, s75, 0xff7fffff\n\t"
    "v_mov_b32_e32 v0, 0xff7fffff\n\t"
    "v_mov_b32_e32 v1, 0xff7fffff\n\t"
    "v_mov_b32_e32 v2, 0xff7fffff\n\t"
    "v_mov_b32_e32 v3, 0xff7fffff\n\t"
    "v_mov_b32_e32 v4, 0xff7fffff\n\t"
    "v_mov_b32_e32 v5, s75\n\t"
    ".Lfa_slowloop:\n\t"
    "s_barrier\n\t"
    "s_add_u32 m0, s65, 0\n\t"
    "s_add_u32 s68, s87, 0\n\t"
    "buffer_load_dwordx4 v16, s[44:47], s68 offen lds\n\t"
    "buffer_load_dwordx4 v16, s[44:47], s68 offen offset:1024 lds\n\t"
    "s_add_u32 m0, s65, 0x2000\n\t"
    "s_add_u32 s68, s87, 0x20000\n\t"
    "buffer_load_dwordx4 v16, s[44:47], s68 offen lds\n\t"
    "buffer_load_dwordx4 v16, s[44:47], s68 offen offset:1024 lds\n\t"
    "s_waitcnt vmcnt(0)\n\t"
    "s_barrier\n\t"
    "ds_read_b128 a[192:195], v30 offset:0\n\t"
    "ds_read_b128 a[196:199], v30 offset:256\n\t"
    "ds_read_b128 a[200:203], v30 offset:512\n\t"
    "ds_read_b128 a[204:207], v30 offset:768\n\t"
    "ds_read_b128 a[208:211], v30 offset:1024\n\t"
    "ds_read_b128 a[212:215], v30 offset:1280\n\t"
    "ds_read_b128 a[216:219], v30 offset:1536\n\t"
    "ds_read_b128 a[220:223], v30 offset:1792\n\t"
    "s_waitcnt lgkmcnt(0)\n\t"
    "v_mfma_f32_16x16x32_bf16 v[160:163], a[192:195], v[64:67], 0\n\t"
    "v_mfma_f32_16x16x32_bf16 v[164:167], a[208:211], v[64:67], 0\n\t"
    "v_mfma_f32_16x16x32_bf16 v[160:163], a[196:199], v[68:71], v[160:163]\n\t"
    "v_mfma_f32_16x16x32_bf16 v[164:167], a[212:215], v[68:71], v[164:167]\n\t"
    "v_mfma_f32_16x16x32_bf16 v[160:163], a[200:203], v[72:75], v[160:163]\n\t"
    "v_mfma_f32_16x16x32_bf16 v[164:167], a[216:219], v[72:75], v[164:167]\n\t"
    "v_mfma_f32_16x16x32_bf16 v[160:163], a[204:207], v[76:79], v[160:163]\n\t"
    "v_mfma_f32_16x16x32_bf16 v[164:167], a[220:223], v[76:79], v[164:167]\n\t"
    "v_mfma_f32_16x16x32_bf16 v[168:171], a[192:195], v[80:83], 0\n\t"
    "v_mfma_f32_16x16x32_bf16 v[172:175], a[208:211], v[80:83], 0\n\t"
    "v_mfma_f32_16x16x32_bf16 v[168:171], a[196:199], v[84:87], v[168:171]\n\t"
    "v_mfma_f32_16x16x32_bf16 v[172:175], a[212:215], v[84:87], v[172:175]\n\t"
    "v_mfma_f32_16x16x32_bf16 v[168:171], a[200:203], v[88:91], v[168:171]\n\t"
    "v_mfma_f32_16x16x32_bf16 v[172:175], a[216:219], v[88:91], v[172:175]\n\t"
    "v_mfma_f32_16x16x32_bf16 v[168:171], a[204:207], v[92:95], v[168:171]\n\t"
    "v_mfma_f32_16x16x32_bf16 v[172:175], a[220:223], v[92:95], v[172:175]\n\t"
    "v_mfma_f32_16x16x32_bf16 v[176:179], a[192:195], v[96:99], 0\n\t"
    "v_mfma_f32_16x16x32_bf16 v[180:183], a[208:211], v[96:99], 0\n\t"
    "v_mfma_f32_16x16x32_bf16 v[176:179], a[196:199], v[100:103], v[176:179]\n\t"
    "v_mfma_f32_16x16x32_bf16 v[180:183], a[212:215], v[100:103], v[180:183]\n\t"
    "v_mfma_f32_16x16x32_bf16 v[176:179], a[200:203], v[104:107], v[176:179]\n\t"
    "v_mfma_f32_16x16x32_bf16 v[180:183], a[216:219], v[104:107], v[180:183]\n\t"
    "v_mfma_f32_16x16x32_bf16 v[176:179], a[204:207], v[108:111], v[176:179]\n\t"
    "v_mfma_f32_16x16x32_bf16 v[180:183], a[220:223], v[108:111], v[180:183]\n\t"
    "v_mfma_f32_16x16x32_bf16 v[184:187], a[192:195], v[112:115], 0\n\t"
    "v_mfma_f32_16x16x32_bf16 v[188:191], a[208:211], v[112:115], 0\n\t"
    "v_mfma_f32_16x16x32_bf16 v[184:187], a[196:199], v[116:119], v[184:187]\n\t"
    "v_mfma_f32_16x16x32_bf16 v[188:191], a[212:215], v[116:119], v[188:191]\n\t"
    "v_mfma_f32_16x16x32_bf16 v[184:187], a[200:203], v[120:123], v[184:187]\n\t"
    "v_mfma_f32_16x16x32_bf16 v[188:191], a[216:219], v[120:123], v[188:191]\n\t"
    "v_mfma_f32_16x16x32_bf16 v[184:187], a[204:207], v[124:127], v[184:187]\n\t"
    "v_mfma_f32_16x16x32_bf16 v[188:191], a[220:223], v[124:127], v[188:191]\n\t"
    "v_mfma_f32_16x16x32_bf16 v[192:195], a[192:195], v[128:131], 0\n\t"
    "v_mfma_f32_16x16x32_bf16 v[196:199], a[208:211], v[128:131], 0\n\t"
    "v_mfma_f32_16x16x32_bf16 v[192:195], a[196:199], v[132:135], v[192:195]\n\t"
    "v_mfma_f32_16x16x32_bf16 v[196:199], a[212:215], v[132:135], v[196:199]\n\t"
    "v_mfma_f32_16x16x32_bf16 v[192:195], a[200:203], v[136:139], v[192:195]\n\t"
    "v_mfma_f32_16x16x32_bf16 v[196:199], a[216:219], v[136:139], v[196:199]\n\t"
    "v_mfma_f32_16x16x32_bf16 v[192:195], a[204:207], v[140:143], v[192:195]\n\t"
    "v_mfma_f32_16x16x32_bf16 v[196:199], a[220:223], v[140:143], v[196:199]\n\t"
    "v_mfma_f32_16x16x32_bf16 v[200:203], a[192:195], v[144:147], 0\n\t"
    "v_mfma_f32_16x16x32_bf16 v[204:207], a[208:211], v[144:147], 0\n\t"
    "v_mfma_f32_16x16x32_bf16 v[200:203], a[196:199], v[148:151], v[200:203]\n\t"
    "v_mfma_f32_16x16x32_bf16 v[204:207], a[212:215], v[148:151], v[204:207]\n\t"
    "v_mfma_f32_16x16x32_bf16 v[200:203], a[200:203], v[152:155], v[200:203]\n\t"
    "v_mfma_f32_16x16x32_bf16 v[204:207], a[216:219], v[152:155], v[204:207]\n\t"
    "v_mfma_f32_16x16x32_bf16 v[200:203], a[204:207], v[156:159], v[200:203]\n\t"
    "v_mfma_f32_16x16x32_bf16 v[204:207], a[220:223], v[156:159], v[204:207]\n\t"
    "s_nop 7\n\t"
    "v_max3_f32 v0, v0, v160, v161\n\t"
    "v_max3_f32 v0, v0, v162, v163\n\t"
    "v_max3_f32 v0, v0, v164, v165\n\t"
    "v_max3_f32 v0, v0, v166, v167\n\t"
    "v_max3_f32 v1, v1, v168, v169\n\t"
    "v_max3_f32 v1, v1, v170, v171\n\t"
    "v_max3_f32 v1, v1, v172, v173\n\t"
    "v_max3_f32 v1, v1, v174, v175\n\t"
    "v_max3_f32 v2, v2, v176, v177\n\t"
    "v_max3_f32 v2, v2, v178, v179\n\t"
    "v_max3_f32 v2, v2, v180, v181\n\t"
    "v_max3_f32 v2, v2, v182, v183\n\t"
    "v_max3_f32 v3, v3, v184, v185\n\t"
    "v_max3_f32 v3, v3, v186, v187\n\t"
    "v_max3_f32 v3, v3, v188, v189\n\t"
    "v_max3_f32 v3, v3, v190, v191\n\t"
    "v_max3_f32 v4, v4, v192, v193\n\t"
    "v_max3_f32 v4, v4, v194, v195\n\t"
    "v_max3_f32 v4, v4, v196, v197\n\t"
    "v_max3_f32 v4, v4, v198, v199\n\t"
    "v_max3_f32 v5, v5, v200, v201\n\t"
    "v_max3_f32 v5, v5, v202, v203\n\t"
    "v_max3_f32 v5, v5, v204, v205\n\t"
    "v_max3_f32 v5, v5, v206, v207\n\t"
    "s_add_u32 s87, s87, 0x2000\n\t"
    "s_cmp_lt_u32 s87, 0x20000\n\t"
    "s_cbranch_scc1 .Lfa_slowloop\n\t"
    "v_mov_b32_e32 v8, v0\n\t"
    "v_mov_b32_e32 v9, v1\n\t"
    "v_mov_b32_e32 v10, v2\n\t"
    "v_mov_b32_e32 v11, v3\n\t"
    "v_mov_b32_e32 v12, v4\n\t"
    "v_mov_b32_e32 v13, v5\n\t"
    "v_permlane32_swap_b32_e32 v0, v8\n\t"
    "v_permlane32_swap_b32_e32 v1, v9\n\t"
    "v_permlane32_swap_b32_e32 v2, v10\n\t"
    "v_permlane32_swap_b32_e32 v3, v11\n\t"
    "v_permlane32_swap_b32_e32 v4, v12\n\t"
    "v_permlane32_swap_b32_e32 v5, v13\n\t"
    "v_max_f32_e32 v0, v0, v8\n\t"
    "v_max_f32_e32 v1, v1, v9\n\t"
    "v_max_f32_e32 v2, v2, v10\n\t"
    "v_max_f32_e32 v3, v3, v11\n\t"
    "v_max_f32_e32 v4, v4, v12\n\t"
    "v_max_f32_e32 v5, v5, v13\n\t"
    "v_mov_b32_e32 v8, v0\n\t"
    "v_mov_b32_e32 v9, v1\n\t"
    "v_mov_b32_e32 v10, v2\n\t"
    "v_mov_b32_e32 v11, v3\n\t"
    "v_mov_b32_e32 v12, v4\n\t"
    "v_mov_b32_e32 v13, v5\n\t"
    "v_permlane16_swap_b32_e32 v0, v8\n\t"
    "v_permlane16_swap_b32_e32 v1, v9\n\t"
    "v_permlane16_swap_b32_e32 v2, v10\n\t"
    "v_permlane16_swap_b32_e32 v3, v11\n\t"
    "v_permlane16_swap_b32_e32 v4, v12\n\t"
    "v_permlane16_swap_b32_e32 v5, v13\n\t"
    "v_max_f32_e32 v0, v0, v8\n\t"
    "v_max_f32_e32 v1, v1, v9\n\t"
    "v_max_f32_e32 v2, v2, v10\n\t"
    "v_max_f32_e32 v3, v3, v11\n\t"
    "v_max_f32_e32 v4, v4, v12\n\t"
    "v_max_f32_e32 v5, v5, v13\n\t"
    "v_xor_b32_e32 v40, 0x80000000, v0\n\t"
    "v_xor_b32_e32 v41, 0x80000000, v0\n\t"
    "v_xor_b32_e32 v42, 0x80000000, v0\n\t"
    "v_xor_b32_e32 v43, 0x80000000, v0\n\t"
    "v_xor_b32_e32 v44, 0x80000000, v1\n\t"
    "v_xor_b32_e32 v45, 0x80000000, v1\n\t"
    "v_xor_b32_e32 v46, 0x80000000, v1\n\t"
    "v_xor_b32_e32 v47, 0x80000000, v1\n\t"
    "v_xor_b32_e32 v48, 0x80000000, v2\n\t"
    "v_xor_b32_e32 v49, 0x80000000, v2\n\t"
    "v_xor_b32_e32 v50, 0x80000000, v2\n\t"
    "v_xor_b32_e32 v51, 0x80000000, v2\n\t"
    "v_xor_b32_e32 v52, 0x80000000, v3\n\t"
    "v_xor_b32_e32 v53, 0x80000000, v3\n\t"
    "v_xor_b32_e32 v54, 0x80000000, v3\n\t"
    "v_xor_b32_e32 v55, 0x80000000, v3\n\t"
    "v_xor_b32_e32 v56, 0x80000000, v4\n\t"
    "v_xor_b32_e32 v57, 0x80000000, v4\n\t"
    "v_xor_b32_e32 v58, 0x80000000, v4\n\t"
    "v_xor_b32_e32 v59, 0x80000000, v4\n\t"
    "v_xor_b32_e32 v60, 0x80000000, v5\n\t"
    "v_xor_b32_e32 v61, 0x80000000, v5\n\t"
    "v_xor_b32_e32 v62, 0x80000000, v5\n\t"
    "v_xor_b32_e32 v63, 0x80000000, v5\n\t"
    "s_barrier\n\t"
    "s_add_u32 m0, s65, 0\n\t"
    "s_mov_b32 s68, 0\n\t"
    "buffer_load_dwordx4 v16, s[44:47], s68 offen nt lds\n\t"
    "buffer_load_dwordx4 v16, s[44:47], s68 offen offset:1024 nt lds\n\t"
    "s_add_u32 m0, s65, 0x2000\n\t"
    "s_mov_b32 s68, 0x20000\n\t"
    "buffer_load_dwordx4 v16, s[44:47], s68 offen nt lds\n\t"
    "buffer_load_dwordx4 v16, s[44:47], s68 offen offset:1024 nt lds\n\t"
    "s_add_u32 m0, s65, 0x4000\n\t"
    "s_mov_b32 s68, 0\n\t"
    "buffer_load_dwordx4 v16, s[48:51], s68 offen nt lds\n\t"
    "buffer_load_dwordx4 v16, s[48:51], s68 offen offset:1024 nt lds\n\t"
    "s_add_u32 m0, s65, 0x6000\n\t"
    "s_mov_b32 s68, 0x20000\n\t"
    "buffer_load_dwordx4 v16, s[48:51], s68 offen nt lds\n\t"
    "buffer_load_dwordx4 v16, s[48:51], s68 offen offset:1024 nt lds\n\t"
    "s_add_u32 m0, s65, 0x8000\n\t"
    "s_mov_b32 s68, 0x2000\n\t"
    "buffer_load_dwordx4 v16, s[44:47], s68 offen nt lds\n\t"
    "buffer_load_dwordx4 v16, s[44:47], s68 offen offset:1024 nt lds\n\t"
    "s_add_u32 m0, s65, 0xa000\n\t"
    "s_mov_b32 s68, 0x22000\n\t"
    "buffer_load_dwordx4 v16, s[44:47], s68 offen nt lds\n\t"
    "buffer_load_dwordx4 v16, s[44:47], s68 offen offset:1024 nt lds\n\t"
    "s_add_u32 m0, s65, 0xc000\n\t"
    "s_mov_b32 s68, 0x2000\n\t"
    "buffer_load_dwordx4 v16, s[48:51], s68 offen nt lds\n\t"
    "buffer_load_dwordx4 v16, s[48:51], s68 offen offset:1024 nt lds\n\t"
    "s_add_u32 m0, s65, 0xe000\n\t"
    "s_mov_b32 s68, 0x22000\n\t"
    "buffer_load_dwordx4 v16, s[48:51], s68 offen nt lds\n\t"
    "buffer_load_dwordx4 v16, s[48:51], s68 offen offset:1024 nt lds\n\t"
    "s_add_u32 m0, s65, 0x10000\n\t"
    "s_mov_b32 s68, 0x4000\n\t"
    "buffer_load_dwordx4 v16, s[44:47], s68 offen nt lds\n\t"
    "buffer_load_dwordx4 v16, s[44:47], s68 offen offset:1024 nt lds\n\t"
    "s_add_u32 m0, s65, 0x12000\n\t"
    "s_mov_b32 s68, 0x24000\n\t"
    "buffer_load_dwordx4 v16, s[44:47], s68 offen nt lds\n\t"
    "buffer_load_dwordx4 v16, s[44:47], s68 offen offset:1024 nt lds\n\t"
    "s_add_u32 m0, s65, 0x14000\n\t"
    "s_mov_b32 s68, 0x4000\n\t"
    "buffer_load_dwordx4 v16, s[48:51], s68 offen nt lds\n\t"
    "buffer_load_dwordx4 v16, s[48:51], s68 offen offset:1024 nt lds\n\t"
    "s_add_u32 m0, s65, 0x16000\n\t"
    "s_mov_b32 s68, 0x24000\n\t"
    "buffer_load_dwordx4 v16, s[48:51], s68 offen nt lds\n\t"
    "buffer_load_dwordx4 v16, s[48:51], s68 offen offset:1024 nt lds\n\t"
    "s_add_u32 m0, s65, 0x18000\n\t"
    "s_mov_b32 s68, 0x6000\n\t"
    "buffer_load_dwordx4 v16, s[44:47], s68 offen nt lds\n\t"
    "buffer_load_dwordx4 v16, s[44:47], s68 offen offset:1024 nt lds\n\t"
    "s_add_u32 m0, s65, 0x1a000\n\t"
    "s_mov_b32 s68, 0x26000\n\t"
    "buffer_load_dwordx4 v16, s[44:47], s68 offen nt lds\n\t"
    "buffer_load_dwordx4 v16, s[44:47], s68 offen offset:1024 nt lds\n\t"
    "s_add_u32 m0, s65, 0x1c000\n\t"
    "s_mov_b32 s68, 0x6000\n\t"
    "buffer_load_dwordx4 v16, s[48:51], s68 offen nt lds\n\t"
    "buffer_load_dwordx4 v16, s[48:51], s68 offen offset:1024 nt lds\n\t"
    "s_add_u32 m0, s65, 0x1e000\n\t"
    "s_mov_b32 s68, 0x26000\n\t"
    "buffer_load_dwordx4 v16, s[48:51], s68 offen nt lds\n\t"
    "buffer_load_dwordx4 v16, s[48:51], s68 offen offset:1024 nt lds\n\t"
    "s_add_u32 m0, s65, 0x20000\n\t"
    "s_mov_b32 s68, 0x8000\n\t"
    "buffer_load_dwordx4 v16, s[44:47], s68 offen nt lds\n\t"
    "buffer_load_dwordx4 v16, s[44:47], s68 offen offset:1024 nt lds\n\t"
    "s_add_u32 m0, s65, 0x22000\n\t"
    "s_mov_b32 s68, 0x28000\n\t"
    "buffer_load_dwordx4 v16, s[44:47], s68 offen nt lds\n\t"
    "buffer_load_dwordx4 v16, s[44:47], s68 offen offset:1024 nt lds\n\t"
    "s_add_u32 m0, s65, 0x24000\n\t"
    "s_mov_b32 s68, 0x8000\n\t"
    "buffer_load_dwordx4 v16, s[48:51], s68 offen nt lds\n\t"
    "buffer_load_dwordx4 v16, s[48:51], s68 offen offset:1024 nt lds\n\t"
    "s_waitcnt vmcnt(0)\n\t"
    "s_barrier\n\t"
    "ds_read_b128 a[192:195], v30 offset:0\n\t"
    "ds_read_b128 a[196:199], v30 offset:256\n\t"
    "ds_read_b128 a[200:203], v30 offset:512\n\t"
    "ds_read_b128 a[204:207], v30 offset:768\n\t"
    "ds_read_b128 a[208:211], v30 offset:1024\n\t"
    "ds_read_b128 a[212:215], v30 offset:1280\n\t"
    "ds_read_b128 a[216:219], v30 offset:1536\n\t"
    "ds_read_b128 a[220:223], v30 offset:1792\n\t"
    "ds_read_b64_tr_b16 a[128:129], v22 offset:0\n\t"
    "ds_read_b64_tr_b16 a[130:131], v22 offset:1024\n\t"
    "ds_read_b64_tr_b16 a[132:133], v23 offset:0\n\t"
    "ds_read_b64_tr_b16 a[134:135], v23 offset:1024\n\t"
    "ds_read_b64_tr_b16 a[136:137], v22 offset:256\n\t"
    "ds_read_b64_tr_b16 a[138:139], v22 offset:1280\n\t"
    "ds_read_b64_tr_b16 a[140:141], v23 offset:256\n\t"
    "ds_read_b64_tr_b16 a[142:143], v23 offset:1280\n\t"
    "ds_read_b64_tr_b16 a[144:145], v22 offset:512\n\t"
    "ds_read_b64_tr_b16 a[146:147], v22 offset:1536\n\t"
    "ds_read_b64_tr_b16 a[148:149], v23 offset:512\n\t"
    "ds_read_b64_tr_b16 a[150:151], v23 offset:1536\n\t"
    "ds_read_b64_tr_b16 a[152:153], v22 offset:768\n\t"
    "ds_read_b64_tr_b16 a[154:155], v22 offset:1792\n\t"
    "ds_read_b64_tr_b16 a[156:157], v23 offset:768\n\t"
    "ds_read_b64_tr_b16 a[158:159], v23 offset:1792\n\t"
    "s_branch .Lfa_att\n\t"
    ".Lfa_end:\n\t"
      :
      : "v"(threadIdx.x), "s"(__builtin_amdgcn_kernarg_segment_ptr()), "s"(blockIdx.x)
      :
      "v1","v2","v3","v4","v5","v6","v7","v8","v9","v10","v11","v12","v13","v14","v15","v16","v17","v18","v19","v20","v21","v22","v23","v24","v25","v26","v27","v28","v29","v30","v31","v32",
      "v33","v34","v35","v36","v37","v38","v39","v40","v41","v42","v43","v44","v45","v46","v47","v48","v49","v50","v51","v52","v53","v54","v55","v56","v57","v58","v59","v60","v61","v62","v63","v64",
      "v65","v66","v67","v68","v69","v70","v71","v72","v73","v74","v75","v76","v77","v78","v79","v80","v81","v82","v83","v84","v85","v86","v87","v88","v89","v90","v91","v92","v93","v94","v95","v96",
      "v97","v98","v99","v100","v101","v102","v103","v104","v105","v106","v107","v108","v109","v110","v111","v112","v113","v114","v115","v116","v117","v118","v119","v120","v121","v122","v123","v124","v125","v126","v127","v128",
      "v129","v130","v131","v132","v133","v134","v135","v136","v137","v138","v139","v140","v141","v142","v143","v144","v145","v146","v147","v148","v149","v150","v151","v152","v153","v154","v155","v156","v157","v158","v159","v160",
      "v161","v162","v163","v164","v165","v166","v167","v168","v169","v170","v171","v172","v173","v174","v175","v176","v177","v178","v179","v180","v181","v182","v183","v184","v185","v186","v187","v188","v189","v190","v191","v192",
      "v193","v194","v195","v196","v197","v198","v199","v200","v201","v202","v203","v204","v205","v206","v207","v208","v209","v210","v211","v212","v213","v214","v215","v216","v217","v218","v219","v220","v221","v222","v223","v224",
      "v225","v226","v227","v228","v229","v230","v231","v232","v233","v234","v235","v236","v237","v238","v239","v240","v241","v242","v243","v244","v245","v246","v247","v248","v249","v250","v251","v252","v253","v254","v255","a0",
      "a1","a2","a3","a4","a5","a6","a7","a8","a9","a10","a11","a12","a13","a14","a15","a16","a17","a18","a19","a20","a21","a22","a23","a24","a25","a26","a27","a28","a29","a30","a31","a32",
      "a33","a34","a35","a36","a37","a38","a39","a40","a41","a42","a43","a44","a45","a46","a47","a48","a49","a50","a51","a52","a53","a54","a55","a56","a57","a58","a59","a60","a61","a62","a63","a64",
      "a65","a66","a67","a68","a69","a70","a71","a72","a73","a74","a75","a76","a77","a78","a79","a80","a81","a82","a83","a84","a85","a86","a87","a88","a89","a90","a91","a92","a93","a94","a95","a96",
      "a97","a98","a99","a100","a101","a102","a103","a104","a105","a106","a107","a108","a109","a110","a111","a112","a113","a114","a115","a116","a117","a118","a119","a120","a121","a122","a123","a124","a125","a126","a127","a128",
      "a129","a130","a131","a132","a133","a134","a135","a136","a137","a138","a139","a140","a141","a142","a143","a144","a145","a146","a147","a148","a149","a150","a151","a152","a153","a154","a155","a156","a157","a158","a159","a160",
      "a161","a162","a163","a164","a165","a166","a167","a168","a169","a170","a171","a172","a173","a174","a175","a176","a177","a178","a179","a180","a181","a182","a183","a184","a185","a186","a187","a188","a189","a190","a191","a192",
      "a193","a194","a195","a196","a197","a198","a199","a200","a201","a202","a203","a204","a205","a206","a207","a208","a209","a210","a211","a212","a213","a214","a215","a216","a217","a218","a219","a220","a221","a222","a223","a224",
      "a225","a226","a227","a228","a229","a230","a231","a232","a233","a234","a235","a236","a237","a238","a239","a240","a241","a242","a243","a244","a245","a246","a247","a248","a249","a250","a251","a252","a253","a254","a255","s4",
      "s5","s6","s7","s8","s9","s10","s11","s12","s13","s14","s15","s16","s17","s18","s19","s20","s21","s22","s23","s24","s25","s26","s27","s28","s29","s30","s31","s36","s37","s38","s39","s40",
      "s41","s42","s43","s44","s45","s46","s47","s48","s49","s50","s51","s52","s53","s54","s55","s56","s57","s58","s59","s60","s61","s62","s63","s64","s65","s66","s67","s68","s69","s70","s71","s72",
      "s73","s74","s75","s76","s77","s78","s79","s80","s81","s82","s83","s84","s85","s86","s87","s88","s89","s90","s91","s92","s93","s94","s95","s96","s97","s98","s99","s100","s101","vcc","scc","memory");
}

extern "C" void kernel_launch(void* const* d_in, const int* in_sizes, int n_in, void* d_out, int out_size, void* d_ws,
                              size_t ws_size, hipStream_t stream) {
  (void)hipFuncSetAttribute((const void*)fused_attn_pair, hipFuncAttributeMaxDynamicSharedMemorySize, LDS_BYTES);
  fused_attn_pair<<<256, 256, LDS_BYTES, stream>>>((const char*)d_in[0], (const char*)d_in[1], (const char*)d_in[2], (const char*)d_in[3], (const char*)d_in[4],
                                                   (const char*)d_in[5], (const char*)d_in[6], (const char*)d_in[7], (const char*)d_in[8], (const char*)d_in[9],
                                                   (char*)d_ws, (char*)d_out);
}
```
